```python
import torch

def _softmax(x, axis=-1):
    return torch.softmax(x, dim=axis)
def _gelu(x):
    return torch.nn.functional.gelu(x, approximate="tanh")
def _silu(x):
    return torch.nn.functional.silu(x)
def _sigmoid(x):
    return torch.sigmoid(x)
def _layernorm(x, axis=-1, eps=1e-5):
    m = x.mean(dim=axis, keepdim=True)
    v = x.var(dim=axis, keepdim=True, unbiased=False)
    return (x - m) / torch.sqrt(v + eps)
def _scatter_add(tgt, idx, src, axis=0):
    return tgt.clone().index_add_(axis, idx, src)
def _segment_sum(src, seg, n):
    out = torch.zeros((n,) + src.shape[1:], dtype=src.dtype)
    return out.index_add_(0, seg, src)
def _pad(x, pw):
    flat = tuple(v for pair in reversed(pw) for v in pair)
    return torch.nn.functional.pad(x, flat)

kernel_name = 'standalone__mha_block_cached__near_ridge'

def setup_inputs():

    _w = (lambda a: (lambda t: t.to(torch.bfloat16) if t.is_floating_point() else t)(torch.as_tensor(a)))
    B, Sq, D = 8, 224, 576
    H, Sk, Dh = 32, 512, 128
    return {
        "x": _w((lambda *s: torch.randn(*s, dtype=torch.bfloat16) * 0.8)(B, Sq, D)),
        "k_cache": _w((lambda *s: torch.randn(*s, dtype=torch.bfloat16) * 0.8)(B, H, Sk, Dh)),
        "v_cache": _w((lambda *s: torch.randn(*s, dtype=torch.bfloat16) * 0.05)(B, H, Sk, Dh)),
        "wq": _w((lambda *s: torch.randn(*s, dtype=torch.bfloat16) * 0.8)(D, H * Dh) * (D ** -0.5)),
        "wo": _w((lambda *s: torch.randn(*s, dtype=torch.bfloat16) * 0.05)(H * Dh, D) * ((H * Dh) ** -0.5)),
    }

def reference(x, k_cache, v_cache, wq, wo):
    B, Sq, D = x.shape
    H, Dh = 32, 128
    q = (lambda x, *d: x.permute(*d))((x @ wq).reshape(B, Sq, H, Dh), 0, 2, 1, 3)
    scores = torch.einsum("bhqd,bhkd->bhqk", q, k_cache) * (Dh ** -0.5)
    p = _softmax(scores, axis=-1)
    o = torch.einsum("bhqk,bhkd->bhqd", p, v_cache)
    return (lambda x, *d: x.permute(*d))(o, 0, 2, 1, 3).reshape(B, Sq, H * Dh) @ wo
```

```cpp
#include <hip/hip_runtime.h>
#include <stdint.h>
#include <utility>

typedef short    bf16x8 __attribute__((ext_vector_type(8)));
typedef short    s16x4  __attribute__((ext_vector_type(4)));
typedef float    f32x16 __attribute__((ext_vector_type(16)));
typedef float    f32x4  __attribute__((ext_vector_type(4)));
typedef float    f32x2  __attribute__((ext_vector_type(2)));
typedef __bf16   bf16x2 __attribute__((ext_vector_type(2)));
typedef unsigned u32x4  __attribute__((ext_vector_type(4)));
typedef __attribute__((address_space(3))) const char* lds_cptr;
typedef __attribute__((address_space(3))) char* lds_ptr;
typedef __amdgpu_buffer_rsrc_t srd_t;

#define DI __device__ __forceinline__
#define MFMA(a, b, c) __builtin_amdgcn_mfma_f32_32x32x16_bf16((a), (b), (c), 0, 0, 0)
#define SBAR() __builtin_amdgcn_sched_barrier(0)

constexpr int NB = 8, SQ = 224, DM = 576, NH = 32, SK = 512, DH = 128;
constexpr int LDS_BYTES = 163840;
constexpr float QSCALE = 0.08838834764831845f * 1.4426950408889634f;
constexpr float SUM_THR = 4096.f;

template <int... Is, class F> DI void static_for_impl(std::integer_sequence<int, Is...>, F&& f) { (f(std::integral_constant<int, Is>{}), ...); }
template <int N, class F> DI void static_for(F&& f) { static_for_impl(std::make_integer_sequence<int, N>{}, f); }

DI unsigned rfl(unsigned x) { return __builtin_amdgcn_readfirstlane(x); }
DI unsigned cvtpk(float lo, float hi) { f32x2 v = {lo, hi}; bf16x2 b = __builtin_convertvector(v, bf16x2); return __builtin_bit_cast(unsigned, b); }
DI srd_t mk_srd(const void* p, unsigned bytes) { return __builtin_amdgcn_make_buffer_rsrc((void*)p, (short)0, (int)bytes, 0x00020000); }
DI void dma(unsigned lds, unsigned voff, srd_t srd, unsigned soff) {
  asm volatile("s_mov_b32 m0, %0\n\ts_nop 0\n\tbuffer_load_dwordx4 %1, %2, %3 offen lds" :: "s"(lds), "v"(voff), "s"(srd), "s"(soff) : "memory");
}
template <int OFF> DI void gload(bf16x8& d, unsigned voff, const void* sbase) { asm volatile("global_load_dwordx4 %0, %1, %2 offset:%3" : "=v"(d) : "v"(voff), "s"(sbase), "n"(OFF) : "memory"); }
DI void bload(bf16x8& d, unsigned voff, srd_t srd, unsigned soff) { asm volatile("buffer_load_dwordx4 %0, %1, %2, %3 offen" : "=v"(d) : "v"(voff), "s"(srd), "s"(soff) : "memory"); }
DI void pin4(bf16x8& a, bf16x8& b, bf16x8& c, bf16x8& d) { asm volatile("" : "+v"(a), "+v"(b), "+v"(c), "+v"(d)); }
template <int N> DI void wait_vm2(bf16x8& a, bf16x8& b) { asm volatile("s_waitcnt vmcnt(%2)" : "+v"(a), "+v"(b) : "n"(N) : "memory"); }
template <int N> DI void wait_bar() { asm volatile("s_waitcnt vmcnt(%0) lgkmcnt(0)\n\ts_barrier" :: "n"(N) : "memory"); }
template <int N> DI void wait_vm() { asm volatile("s_waitcnt vmcnt(%0)" :: "n"(N) : "memory"); }
typedef short v4i16_t __attribute__((ext_vector_type(4)));
DI s16x4 trd(lds_cptr p) { return __builtin_amdgcn_ds_read_tr16_b64_v4i16((__attribute__((address_space(3))) v4i16_t*)p); }
DI bf16x8 trd2(lds_cptr p, int o0, int o1) { s16x4 a = trd(p + o0), b = trd(p + o1); return __builtin_shufflevector(a, b, 0, 1, 2, 3, 4, 5, 6, 7); }
DI bf16x8 rd128(lds_cptr p) { return *(const __attribute__((address_space(3))) bf16x8*)p; }
DI float swap_max(float m) { auto rr = __builtin_amdgcn_permlane32_swap(__float_as_uint(m), __float_as_uint(m), false, false); return fmaxf(__uint_as_float(rr[0]), __uint_as_float(rr[1])); }
DI float swap_add(float m) { auto rr = __builtin_amdgcn_permlane32_swap(__float_as_uint(m), __float_as_uint(m), false, false); return __uint_as_float(rr[0]) + __uint_as_float(rr[1]); }
DI float max16(const f32x16& s) { float a = fmaxf(fmaxf(s[0], s[1]), s[2]), b = fmaxf(fmaxf(s[3], s[4]), s[5]);
  a = fmaxf(fmaxf(a, s[6]), s[7]); b = fmaxf(fmaxf(b, s[8]), s[9]); a = fmaxf(fmaxf(a, s[10]), s[11]); b = fmaxf(fmaxf(b, s[12]), s[13]); a = fmaxf(fmaxf(a, s[14]), s[15]); return fmaxf(a, b); }

DI float expo(const f32x16& s, bf16x8 (&p)[2]) {
  float e[16]; float s0 = 0.f, s1 = 0.f;
#pragma unroll
  for (int i = 0; i < 16; ++i) e[i] = __builtin_amdgcn_exp2f(s[i]);
#pragma unroll
  for (int i = 0; i < 16; i += 2) { s0 += e[i]; s1 += e[i + 1]; }
  u32x4 a = {cvtpk(e[0], e[1]), cvtpk(e[2], e[3]), cvtpk(e[4], e[5]), cvtpk(e[6], e[7])};
  u32x4 b = {cvtpk(e[8], e[9]), cvtpk(e[10], e[11]), cvtpk(e[12], e[13]), cvtpk(e[14], e[15])};
  p[0] = __builtin_bit_cast(bf16x8, a); p[1] = __builtin_bit_cast(bf16x8, b);
  return s0 + s1;
}

constexpr unsigned SLOTK = 0x97204976u;
constexpr unsigned SLOTV = 0x53861538u;

__global__ __launch_bounds__(512, 1) void attn_kernel(const unsigned short* __restrict__ x, const unsigned short* __restrict__ kc, const unsigned short* __restrict__ vc,
                                                      const unsigned short* __restrict__ wq, unsigned short* __restrict__ ows) {
  extern __shared__ __attribute__((aligned(16))) char lds[];
  const unsigned tid = threadIdx.x, lane = tid & 63u, r = lane & 31u, hh = lane >> 5;
  const unsigned wid = rfl(tid >> 6);
  const unsigned bid = blockIdx.x, xcd = bid & 7u, ix = bid >> 3;
  const unsigned b = (xcd & 1u) * 4u + (ix & 3u), h = (xcd >> 1) * 8u + (ix >> 2);
  const unsigned lds0 = (unsigned)(uintptr_t)lds;
  const srd_t sK = mk_srd(kc + (size_t)(b * NH + h) * SK * DH, SK * DH * 2);
  const srd_t sV = mk_srd(vc + (size_t)(b * NH + h) * SK * DH, SK * DH * 2);
  const srd_t sW = mk_srd(wq, DM * NH * DH * 2);
  const unsigned prow = (lane >> 2) & 7u, pch = 4u * (lane >> 5) + (lane & 3u);
  const unsigned voffV = prow * 256u + pch * 16u;
  const unsigned voffK = prow * 256u + (4u * (lane >> 5) + ((lane & 3u) ^ ((2u * wid + ((lane >> 4) & 1u)) & 3u))) * 16u;
  const unsigned voffW = prow * 8192u + pch * 16u;
  const unsigned ldsw = lds0 + wid * 2048u;
  auto fillW = [&](unsigned c, unsigned slot) { const unsigned so = (c * 64u + 8u * wid) * 8192u + h * 256u;
    dma(ldsw + slot * 16384u, voffW, sW, so); dma(ldsw + slot * 16384u + 1024u, voffW, sW, so + 128u); };
  auto fillK = [&](unsigned t, unsigned slot) { const unsigned so = t * 16384u + wid * 2048u;
    dma(ldsw + slot * 16384u, voffK, sK, so); dma(ldsw + slot * 16384u + 1024u, voffK, sK, so + 128u); };
  auto fillV = [&](unsigned t, unsigned slot) { const unsigned so = t * 16384u + wid * 2048u;
    dma(ldsw + slot * 16384u, voffV, sV, so); dma(ldsw + slot * 16384u + 1024u, voffV, sV, so + 128u); };
  auto fillQ = [&](auto C) { constexpr int c = C.value;
    if constexpr (c == 1) fillW(6, 0); else if constexpr (c == 2) fillW(7, 1); else if constexpr (c == 3) fillW(8, 2);
    else if constexpr (c == 4) fillV(1, 3); else if constexpr (c == 5) fillK(3, 4); else if constexpr (c == 6) fillV(2, 5);
    else if constexpr (c == 7) fillK(4, 0); else if constexpr (c == 8) fillV(3, 1); };
  auto fillA = [&](unsigned t) {
    if (t <= 3u) fillK(t + 4u, (SLOTK >> (4u * (t + 4u))) & 15u);
    if (t <= 4u) fillV(t + 3u, (SLOTV >> (4u * (t + 3u))) & 15u); };
  auto endA = [&](unsigned t) {
    if (t <= 3u) wait_bar<12>(); else if (t == 4u) wait_bar<10>(); else if (t == 5u) wait_bar<6>(); else if (t == 6u) wait_bar<2>(); else wait_bar<0>(); };

  if (wid == 7u) {
    static_for<6>([&](auto C) { fillW(C.value, C.value); });
    fillK(0, 6); fillK(1, 7); fillV(0, 8); fillK(2, 9);
    wait_bar<18>();
    static_for<9>([&](auto C) { constexpr int c = C.value; fillQ(C); if constexpr (c <= 4) wait_bar<16>(); else wait_bar<8>(); });
    wait_bar<8>();
    for (unsigned t = 1; t < 8; ++t) { fillA(t); endA(t); }
    return;
  }

  bf16x8 xf[36];
  { const unsigned short* xb = x + (size_t)(b * SQ + wid * 32u) * DM;
    const unsigned xoff = r * (DM * 2u) + hh * 16u;
    static_for<9>([&](auto C) { constexpr int c = C.value;
      if constexpr (c < 6) fillW(c, c);
      static_for<4>([&](auto S) { constexpr int i = c * 4 + S.value;
        gload<i * 32>(xf[i], xoff, xb); }); });
    fillK(0, 6); fillK(1, 7); fillV(0, 8); fillK(2, 9); }
  const unsigned q4 = (lane & 15u) >> 2, p4 = lane & 3u, blk = (lane >> 4) & 1u;
  const unsigned trcol = 32u * blk + 16u * (p4 & 1u) + 8u * (p4 >> 1);
  const lds_cptr wbase = (lds_cptr)lds + 2048u * hh + 64u * q4 + trcol;
  f32x16 qa[4] = {};
  wait_bar<50>();
  static_for<9>([&](auto C) { constexpr int c = C.value; constexpr int slot = c < 6 ? c : c - 6;
    pin4(xf[4 * c], xf[4 * c + 1], xf[4 * c + 2], xf[4 * c + 3]);
    if (wid >= 4u) fillQ(C);
    const lds_cptr wp = wbase + slot * 16384;
#pragma unroll
    for (int s = 0; s < 4; ++s)
#pragma unroll
      for (int db = 0; db < 4; ++db) qa[db] = MFMA(trd2(wp, 4096 * s + 512 * db, 4096 * s + 512 * db + 256), xf[4 * c + s], qa[db]);
    if (wid < 4u) fillQ(C);
    if constexpr (c <= 4) wait_bar<44 - 4 * c>(); else wait_bar<8>(); });
  bf16x8 qf[8];
#pragma unroll
  for (int db = 0; db < 4; ++db)
#pragma unroll
    for (int s = 0; s < 2; ++s) { u32x4 w;
#pragma unroll
      for (int e = 0; e < 4; ++e) w[e] = cvtpk(qa[db][8 * s + 2 * e] * QSCALE, qa[db][8 * s + 2 * e + 1] * QSCALE);
      qf[2 * db + s] = __builtin_bit_cast(bf16x8, w); }

  const lds_cptr kb0 = (lds_cptr)lds + 2048u * (r >> 3) + 64u * (r & 7u) + 16u * ((hh) ^ ((r >> 2) & 3u));
  const lds_cptr kb1 = (lds_cptr)lds + 2048u * (r >> 3) + 64u * (r & 7u) + 16u * ((2u + hh) ^ ((r >> 2) & 3u));
  const lds_cptr vb = (lds_cptr)lds + 64u * (4u * hh + q4) + trcol;
  auto qk = [&](unsigned slotoff, int kb, const f32x16& c0) { f32x16 s = c0;
#pragma unroll
    for (int i = 0; i < 8; ++i) s = MFMA(rd128(((i & 1) ? kb1 : kb0) + slotoff + 8192 * kb + 512 * (i >> 1)), qf[i], s);
    return s; };
  f32x16 o[4] = {};
  auto pv = [&](unsigned slotoff, int kb, const bf16x8 (&p)[2]) {
#pragma unroll
    for (int ks = 0; ks < 2; ++ks)
#pragma unroll
      for (int db = 0; db < 4; ++db) o[db] = MFMA(trd2(vb + slotoff, 8192 * kb + 4096 * ks + 512 * db, 8192 * kb + 4096 * ks + 2048 + 512 * db), p[ks], o[db]); };
  f32x16 negm, S0, S1; bf16x8 P0[2], P1[2]; float l = 0.f;
  auto sm_fix = [&](float& ps, f32x16& Sc, f32x16& Sn, bf16x8 (&P)[2]) {
    if (__builtin_expect(__any(!(ps <= SUM_THR)), 0)) {
      const float dl = fmaxf(swap_max(max16(Sc)), 0.f), f = __builtin_amdgcn_exp2f(-dl);
#pragma unroll
      for (int i = 0; i < 16; ++i) { Sc[i] -= dl; Sn[i] -= dl; negm[i] -= dl; }
#pragma unroll
      for (int db = 0; db < 4; ++db)
#pragma unroll
        for (int i = 0; i < 16; ++i) o[db][i] *= f;
      l *= f; ps = expo(Sc, P); }
    l += ps; };
  { const f32x16 z = {}; S0 = qk(6 * 16384, 0, z);
    const float m = swap_max(max16(S0));
#pragma unroll
    for (int i = 0; i < 16; ++i) { S0[i] -= m; negm[i] = -m; }
    S1 = qk(6 * 16384, 1, negm);
    l = expo(S0, P0); }
  wait_bar<8>();
  for (unsigned t = 1; t < 8; ++t) {
    const unsigned ko = ((SLOTK >> (4u * t)) & 15u) * 16384u, vo = ((SLOTV >> (4u * (t - 1u))) & 15u) * 16384u;
    if (wid >= 4u) fillA(t);
    { S0 = qk(ko, 0, negm); pv(vo, 0, P0); float ps = expo(S1, P1); sm_fix(ps, S1, S0, P1); }
    { S1 = qk(ko, 1, negm); pv(vo, 1, P1); float ps = expo(S0, P0); sm_fix(ps, S0, S1, P0); }
    if (wid < 4u) fillA(t);
    endA(t);
  }
  { const unsigned vo = ((SLOTV >> 28) & 15u) * 16384u;
    pv(vo, 0, P0); float ps = expo(S1, P1); sm_fix(ps, S1, S0, P1); pv(vo, 1, P1); }
  const float inv = __builtin_amdgcn_rcpf(swap_add(l));
  char* op = (char*)ows + (size_t)(((b * 7u + wid) * NH + h) * 8u) * 1024u + lane * 16u;
#pragma unroll
  for (int db = 0; db < 4; ++db)
#pragma unroll
    for (int s = 0; s < 2; ++s) { u32x4 w;
#pragma unroll
      for (int e = 0; e < 4; ++e) w[e] = cvtpk(o[db][8 * s + 2 * e] * inv, o[db][8 * s + 2 * e + 1] * inv);
      *(u32x4*)(op + (2 * db + s) * 1024) = w; }
}

__global__ __launch_bounds__(512, 1) void oproj_kernel(const unsigned short* __restrict__ ows, const unsigned short* __restrict__ wo, unsigned short* __restrict__ out) {
  extern __shared__ __attribute__((aligned(16))) char lds[];
  const unsigned tid = threadIdx.x, lane = tid & 63u, hh = lane >> 5;
  const unsigned wid = rfl(tid >> 6);
  const unsigned bid = blockIdx.x, xcd = bid & 7u, ix = bid >> 3;
  const unsigned t0 = (xcd * 63u) >> 1, t1 = ((xcd + 1u) * 63u) >> 1;
  if (t0 + ix >= t1) return;
  const unsigned T = t0 + ix, rt = T / 9u, ct = T - rt * 9u;
  const unsigned lds0 = (unsigned)(uintptr_t)lds;
  const srd_t sW = mk_srd(wo, NH * DH * DM * 2);
  const srd_t sO = mk_srd(ows, NB * SQ * NH * DH * 2);
  const unsigned voffW = ((lane >> 2) & 7u) * (DM * 2u) + (4u * (lane >> 5) + (lane & 3u)) * 16u;
  const unsigned soW = wid * 512u * (DM * 2u) + ct * 128u, ldsw = lds0 + wid * 16384u;
  const unsigned voffO = lane * 16u, soO = (2u * rt * NH + 4u * wid) * 8192u;
  bf16x8 of[8][2];
  auto issue = [&](auto S) { constexpr int st = S.value;
    dma(ldsw + (st & 7) * 2048u, voffW, sW, soW + st * 16u * (DM * 2u)); dma(ldsw + (st & 7) * 2048u + 1024u, voffW, sW, soW + (st * 16u + 8u) * (DM * 2u));
    bload(of[st & 7][0], voffO, sO, soO + st * 1024u);
    bload(of[st & 7][1], voffO, sO, soO + NH * 8192u + st * 1024u); };
  static_for<8>(issue);
  const unsigned q4 = (lane & 15u) >> 2, p4 = lane & 3u, blk = (lane >> 4) & 1u;
  const lds_cptr wb = (lds_cptr)lds + wid * 16384u + 1024u * hh + 64u * q4 + 32u * blk + 16u * (p4 & 1u) + 8u * (p4 >> 1);
  f32x16 acc[2][2] = {};
  static_for<32>([&](auto S) { constexpr int st = S.value;
    constexpr int N = st < 24 ? 28 : 4 * (31 - st);
    wait_vm2<N>(of[st & 7][0], of[st & 7][1]);
    const bf16x8 a0 = trd2(wb, (st & 7) * 2048, (st & 7) * 2048 + 256), a1 = trd2(wb, (st & 7) * 2048 + 512, (st & 7) * 2048 + 512 + 256);
    acc[0][0] = MFMA(a0, of[st & 7][0], acc[0][0]); acc[0][1] = MFMA(a0, of[st & 7][1], acc[0][1]);
    acc[1][0] = MFMA(a1, of[st & 7][0], acc[1][0]); acc[1][1] = MFMA(a1, of[st & 7][1], acc[1][1]);
    SBAR();
    if constexpr (st + 8 < 32) issue(std::integral_constant<int, st + 8>{});
    SBAR(); });
  { lds_ptr pw = (lds_ptr)lds + wid * 16384u;
#pragma unroll
    for (int nb = 0; nb < 2; ++nb)
#pragma unroll
      for (int qb = 0; qb < 2; ++qb)
#pragma unroll
        for (int s = 0; s < 2; ++s)
#pragma unroll
          for (int e = 0; e < 2; ++e) { const f32x4 v = {acc[nb][qb][8 * s + 4 * e], acc[nb][qb][8 * s + 4 * e + 1], acc[nb][qb][8 * s + 4 * e + 2], acc[nb][qb][8 * s + 4 * e + 3]};
            const unsigned pos = lane ^ ((4u * nb + 2u * s + hh) << 1);
            *(__attribute__((address_space(3))) f32x4*)(pw + (((nb * 2 + qb) * 2 + s) * 2 + e) * 1024 + pos * 16u) = v; } }
  asm volatile("s_waitcnt lgkmcnt(0)\n\ts_barrier" ::: "memory");
  { const unsigned q = tid >> 3, c = tid & 7u, nb = c >> 2, s = (c >> 1) & 1u, h2 = c & 1u, qb = q >> 5;
    const unsigned pos = ((q & 31u) + 32u * h2) ^ (c << 1);
    const lds_cptr pr = (lds_cptr)lds + (((nb * 2u + qb) * 2u + s) * 2u) * 1024u + pos * 16u;
    f32x4 a = {}, bq = {};
#pragma unroll
    for (int w = 0; w < 8; ++w) { a += *(const __attribute__((address_space(3))) f32x4*)(pr + w * 16384); bq += *(const __attribute__((address_space(3))) f32x4*)(pr + w * 16384 + 1024); }
    const u32x4 w4 = {cvtpk(a[0], a[1]), cvtpk(a[2], a[3]), cvtpk(bq[0], bq[1]), cvtpk(bq[2], bq[3])};
    *(u32x4*)((char*)out + (size_t)(rt * 64u + q) * (DM * 2u) + ct * 128u + c * 16u) = w4; }
}

extern "C" void kernel_launch(void* const* d_in, const int* in_sizes, int n_in, void* d_out, int out_size, void* d_ws, size_t ws_size, hipStream_t stream) {
  (void)in_sizes; (void)n_in; (void)out_size; (void)ws_size;
  (void)hipFuncSetAttribute((const void*)attn_kernel, hipFuncAttributeMaxDynamicSharedMemorySize, LDS_BYTES);
  (void)hipFuncSetAttribute((const void*)oproj_kernel, hipFuncAttributeMaxDynamicSharedMemorySize, 131072);
  const unsigned short* x = (const unsigned short*)d_in[0]; const unsigned short* kc = (const unsigned short*)d_in[1]; const unsigned short* vc = (const unsigned short*)d_in[2];
  const unsigned short* wq = (const unsigned short*)d_in[3]; const unsigned short* wo = (const unsigned short*)d_in[4];
  unsigned short* ows = (unsigned short*)d_ws;
  attn_kernel<<<256, 512, LDS_BYTES, stream>>>(x, kc, vc, wq, ows);
  oproj_kernel<<<256, 512, 131072, stream>>>(ows, wo, (unsigned short*)d_out);
}
```

```cpp
#include <hip/hip_runtime.h>
#include <stdint.h>
#include <utility>

typedef short    bf16x8 __attribute__((ext_vector_type(8)));
typedef short    s16x4  __attribute__((ext_vector_type(4)));
typedef float    f32x16 __attribute__((ext_vector_type(16)));
typedef float    f32x4  __attribute__((ext_vector_type(4)));
typedef float    f32x2  __attribute__((ext_vector_type(2)));
typedef __bf16   bf16x2 __attribute__((ext_vector_type(2)));
typedef unsigned u32x4  __attribute__((ext_vector_type(4)));
typedef __attribute__((address_space(3))) const char* lds_cptr;
typedef __attribute__((address_space(3))) char* lds_ptr;
typedef __amdgpu_buffer_rsrc_t srd_t;

#define DI __device__ __forceinline__
#define MFMA(a, b, c) __builtin_amdgcn_mfma_f32_32x32x16_bf16((a), (b), (c), 0, 0, 0)
#define SBAR() __builtin_amdgcn_sched_barrier(0)

constexpr int NB = 8, SQ = 224, DM = 576, NH = 32, SK = 512, DH = 128;
constexpr int LDS_BYTES = 163840;
constexpr float QSCALE = 0.08838834764831845f * 1.4426950408889634f;
constexpr float SUM_THR = 4096.f;

template <int... Is, class F> DI void static_for_impl(std::integer_sequence<int, Is...>, F&& f) { (f(std::integral_constant<int, Is>{}), ...); }
template <int N, class F> DI void static_for(F&& f) { static_for_impl(std::make_integer_sequence<int, N>{}, f); }

DI unsigned rfl(unsigned x) { return __builtin_amdgcn_readfirstlane(x); }
DI unsigned cvtpk(float lo, float hi) { f32x2 v = {lo, hi}; bf16x2 b = __builtin_convertvector(v, bf16x2); return __builtin_bit_cast(unsigned, b); }
DI srd_t mk_srd(const void* p, unsigned bytes) { return __builtin_amdgcn_make_buffer_rsrc((void*)p, (short)0, (int)bytes, 0x00020000); }
DI void dma(unsigned lds, unsigned voff, srd_t srd, unsigned soff) {
  asm volatile("s_mov_b32 m0, %0\n\ts_nop 0\n\tbuffer_load_dwordx4 %1, %2, %3 offen lds" :: "s"(lds), "v"(voff), "s"(srd), "s"(soff) : "memory");
}
DI void dma_nt(unsigned lds, unsigned voff, srd_t srd, unsigned soff) {
  asm volatile("s_mov_b32 m0, %0\n\ts_nop 0\n\tbuffer_load_dwordx4 %1, %2, %3 offen nt lds" :: "s"(lds), "v"(voff), "s"(srd), "s"(soff) : "memory");
}
DI void dma4(unsigned lds, unsigned v0, unsigned v1, unsigned v2, unsigned v3, srd_t srd, unsigned soff) {
  asm volatile("s_mov_b32 m0, %0\n\ts_nop 0\n\tbuffer_load_dwordx4 %1, %5, %6 offen lds\n\tbuffer_load_dwordx4 %2, %5, %6 offen offset:1024 lds\n\t"
               "buffer_load_dwordx4 %3, %5, %6 offen offset:2048 lds\n\tbuffer_load_dwordx4 %4, %5, %6 offen offset:3072 lds"
               :: "s"(lds), "v"(v0), "v"(v1), "v"(v2), "v"(v3), "s"(srd), "s"(soff) : "memory");
}
DI void dma4_nt(unsigned lds, unsigned v0, unsigned v1, unsigned v2, unsigned v3, srd_t srd, unsigned soff) {
  asm volatile("s_mov_b32 m0, %0\n\ts_nop 0\n\tbuffer_load_dwordx4 %1, %5, %6 offen nt lds\n\tbuffer_load_dwordx4 %2, %5, %6 offen offset:1024 nt lds\n\t"
               "buffer_load_dwordx4 %3, %5, %6 offen offset:2048 nt lds\n\tbuffer_load_dwordx4 %4, %5, %6 offen offset:3072 nt lds"
               :: "s"(lds), "v"(v0), "v"(v1), "v"(v2), "v"(v3), "s"(srd), "s"(soff) : "memory");
}
DI void bstore_sc1(const u32x4& d, unsigned voff, srd_t srd, unsigned soff) { asm volatile("buffer_store_dwordx4 %0, %1, %2, %3 offen sc1\n\ts_nop 1" :: "v"(d), "v"(voff), "s"(srd), "s"(soff) : "memory"); }
DI void bload_sc1(bf16x8& d, unsigned voff, srd_t srd, unsigned soff) { asm volatile("buffer_load_dwordx4 %0, %1, %2, %3 offen sc1" : "=v"(d) : "v"(voff), "s"(srd), "s"(soff) : "memory"); }
DI void bload(bf16x8& d, unsigned voff, srd_t srd, unsigned soff) { asm volatile("buffer_load_dwordx4 %0, %1, %2, %3 offen" : "=v"(d) : "v"(voff), "s"(srd), "s"(soff) : "memory"); }
DI void bload_nt(bf16x8& d, unsigned voff, srd_t srd, unsigned soff) { asm volatile("buffer_load_dwordx4 %0, %1, %2, %3 offen nt" : "=v"(d) : "v"(voff), "s"(srd), "s"(soff) : "memory"); }
DI void touch(unsigned& d, unsigned voff, srd_t srd, unsigned soff) { asm volatile("buffer_load_dword %0, %1, %2, %3 offen" : "+v"(d) : "v"(voff), "s"(srd), "s"(soff) : "memory"); }
DI void pin(bf16x8& a) { asm volatile("" : "+v"(a)); }
DI void pinf(float& a) { asm volatile("" : "+v"(a)); }
DI void pinu(unsigned& a) { asm volatile("" : "+v"(a)); }
template <int N> DI void wait_vm2(bf16x8& a, bf16x8& b) { asm volatile("s_waitcnt vmcnt(%2)" : "+v"(a), "+v"(b) : "n"(N) : "memory"); }
template <int N> DI void wait_bar() { asm volatile("s_waitcnt vmcnt(%0) lgkmcnt(0)\n\ts_barrier" :: "n"(N) : "memory"); }
template <int N> DI void wait_vm() { asm volatile("s_waitcnt vmcnt(%0)" :: "n"(N) : "memory"); }
DI void bar() { asm volatile("s_waitcnt lgkmcnt(0)\n\ts_barrier" ::: "memory"); }
typedef short v4i16_t __attribute__((ext_vector_type(4)));
DI s16x4 trd(lds_cptr p) { return __builtin_amdgcn_ds_read_tr16_b64_v4i16((__attribute__((address_space(3))) v4i16_t*)p); }
DI bf16x8 trd2(lds_cptr p, int o0, int o1) { s16x4 a = trd(p + o0), b = trd(p + o1); return __builtin_shufflevector(a, b, 0, 1, 2, 3, 4, 5, 6, 7); }
DI bf16x8 rd128(lds_cptr p) { return *(const __attribute__((address_space(3))) bf16x8*)p; }
DI float swap_max(float m) { auto rr = __builtin_amdgcn_permlane32_swap(__float_as_uint(m), __float_as_uint(m), false, false); return fmaxf(__uint_as_float(rr[0]), __uint_as_float(rr[1])); }
DI float swap_add(float m) { auto rr = __builtin_amdgcn_permlane32_swap(__float_as_uint(m), __float_as_uint(m), false, false); return __uint_as_float(rr[0]) + __uint_as_float(rr[1]); }
DI float max16(const f32x16& s) { float a = fmaxf(fmaxf(s[0], s[1]), s[2]), b = fmaxf(fmaxf(s[3], s[4]), s[5]);
  a = fmaxf(fmaxf(a, s[6]), s[7]); b = fmaxf(fmaxf(b, s[8]), s[9]); a = fmaxf(fmaxf(a, s[10]), s[11]); b = fmaxf(fmaxf(b, s[12]), s[13]); a = fmaxf(fmaxf(a, s[14]), s[15]); return fmaxf(a, b); }
DI float fadd(float a, float b) { float r; asm("v_add_f32_e32 %0, %1, %2" : "=v"(r) : "v"(a), "v"(b)); return r; }

DI float expo(const f32x16& s, u32x4 (&p)[2]) {
  float e[16]; float s0 = 0.f, s1 = 0.f;
#pragma unroll
  for (int i = 0; i < 16; ++i) e[i] = __builtin_amdgcn_exp2f(s[i]);
#pragma unroll
  for (int i = 0; i < 16; i += 2) { s0 += e[i]; s1 += e[i + 1]; }
#pragma unroll
  for (int i = 0; i < 8; ++i) p[i >> 2][i & 3] = cvtpk(e[2 * i], e[2 * i + 1]);
  return s0 + s1;
}

#define ST_DECL
#define ST(k)
#define ST_OUT(ws, lo, hi)

constexpr unsigned SLOTK = 0x04204209u;
constexpr unsigned SLOTV = 0x31531531u;
constexpr unsigned STG = 45056u, STG_W = 28672u;
constexpr unsigned O_BYTES = NB * SQ * NH * DH * 2;
constexpr unsigned FLAG_OFF = O_BYTES, FLAG_STRIDE = 128, EPOCH_OFF = FLAG_OFF + 256 * FLAG_STRIDE;
constexpr unsigned SPIN_CAP = 1u << 20;
#define RA 3
constexpr unsigned SLOTW = 0x05429876u;
#define WO 6
#define QROT 1
#define TQ 0
#define HROT 1
constexpr int p2_wait(int st) { int n = 0; for (int j = st + 1; j <= st + WO - 1 && j < 32; ++j) n += 2; for (int k = (st - WO + 1 > 0 ? st - WO + 1 : 0); k < st; ++k) if (k >= 8 && k < 16) n += 2; return n; }
typedef __attribute__((address_space(1))) unsigned long long gu64;
DI unsigned long long mix64(unsigned long long z) { z += 0x9E3779B97F4A7C15ull; z = (z ^ (z >> 30)) * 0xBF58476D1CE4E5B9ull; z = (z ^ (z >> 27)) * 0x94D049BB133111EBull; return z ^ (z >> 31); }
DI unsigned long long ld_agent(const char* p) { return __hip_atomic_load((gu64*)p, __ATOMIC_RELAXED, __HIP_MEMORY_SCOPE_AGENT); }
DI void st_agent(char* p, unsigned long long v) { __hip_atomic_store((gu64*)p, v, __ATOMIC_RELAXED, __HIP_MEMORY_SCOPE_AGENT); }
DI void poll_flags(const char* p, unsigned long long tag) {
  for (unsigned spins = 0; spins < SPIN_CAP; ++spins) { if (__all(ld_agent(p) == tag)) break; __builtin_amdgcn_s_sleep(2); }
  asm volatile("" ::: "memory");
}

__global__ __launch_bounds__(512, 1) void mha_kernel(const unsigned short* __restrict__ x, const unsigned short* __restrict__ kc, const unsigned short* __restrict__ vc,
                                                     const unsigned short* __restrict__ wq, const unsigned short* __restrict__ wo, char* ws, unsigned short* __restrict__ out) {
  extern __shared__ __attribute__((aligned(16))) char lds[];
  const unsigned tid = threadIdx.x, lane = tid & 63u, r = lane & 31u, hh = lane >> 5;
  const unsigned wid = rfl(tid >> 6);
  ST_DECL ST(0);
  const unsigned bid = blockIdx.x, xcd = bid & 7u, ix = bid >> 3;
  const unsigned b = (xcd & 1u) * 4u + (ix & 3u), h = (xcd >> 1) * 8u + (ix >> 2);
  const unsigned lds0 = (unsigned)(uintptr_t)lds;
  const unsigned prow = (lane >> 2) & 7u, pch = 4u * hh + (lane & 3u);
  const unsigned q4 = (lane & 15u) >> 2, p4 = lane & 3u, blk = (lane >> 4) & 1u;
  const unsigned trcol = 32u * blk + 16u * (p4 & 1u) + 8u * (p4 >> 1);
  const srd_t sO = mk_srd(ws, O_BYTES);
  unsigned long long tag;
  const unsigned t0 = (xcd * 63u) >> 1, t1 = ((xcd + 1u) * 63u) >> 1;
  const bool has_tile = t0 + ix < t1;
  const unsigned T = has_tile ? t0 + ix : t0, rt = T / 9u, ct = T - rt * 9u;
  const unsigned hx = HROT ? ((rt + ct) & 1u) * 4u : 0u;
  const srd_t sWo = mk_srd(wo, NH * DH * DM * 2);
  const unsigned vWo = prow * (DM * 2u) + pch * 16u;
  auto fillWo = [&](unsigned head, unsigned slot) {
#pragma unroll
    for (int i = 0; i < 4; ++i) dma4(lds0 + slot * 16384u + i * 4096u, vWo, vWo + 8192u, vWo + 16384u, vWo + 24576u, sWo, (head * 128u + i * 32u) * (DM * 2u) + ct * 128u); };
  const unsigned myslot = (SLOTW >> (4u * wid)) & 15u;
  const lds_cptr wb = (lds_cptr)lds + myslot * 16384u + 1024u * hh + 64u * q4 + trcol;
  auto rdWo = [&](int i) { return trd2(wb, (i >> 1) * 2048 + (i & 1) * 512, (i >> 1) * 2048 + (i & 1) * 512 + 256); };
  bf16x8 wa[16];

  if (wid == 7u) {
    const srd_t sK = mk_srd(kc + (size_t)(b * NH + h) * SK * DH, SK * DH * 2);
    const srd_t sV = mk_srd(vc + (size_t)(b * NH + h) * SK * DH, SK * DH * 2);
    const srd_t sW = mk_srd(wq, DM * NH * DH * 2);
    const srd_t sX = mk_srd(x + (size_t)b * SQ * DM, SQ * DM * 2);
    const unsigned pchx0 = 4u * hh + ((lane & 3u) ^ ((lane >> 4) & 1u)), pchx1 = pchx0 ^ 2u;
    const unsigned vV = prow * 256u + pch * 16u, vK0 = prow * 256u + pchx0 * 16u, vK1 = prow * 256u + pchx1 * 16u;
    const unsigned vW = prow * 8192u + pch * 16u, vX0 = prow * (DM * 2u) + pchx0 * 16u, vX1 = prow * (DM * 2u) + pchx1 * 16u;
    auto fillK = [&](unsigned t) { const unsigned so = t * 16384u, ld = lds0 + ((SLOTK >> (4u * t)) & 15u) * 16384u;
      dma_nt(ld, vK0, sK, so); dma_nt(ld + 1024u, vK0, sK, so + 128u); dma_nt(ld + 2048u, vK1, sK, so + 2048u); dma_nt(ld + 3072u, vK1, sK, so + 2176u);
#pragma unroll
      for (int i = 1; i < 4; ++i) dma4_nt(ld + i * 4096u, vK0 + 1024u, vK0 + 128u, vK1 + 1024u, vK1 + 128u, sK, so + i * 4096u - 1024u); };
    auto fillV = [&](unsigned t) { const unsigned so = t * 16384u, ld = lds0 + ((SLOTV >> (4u * t)) & 15u) * 16384u;
      dma_nt(ld, vV, sV, so); dma_nt(ld + 1024u, vV, sV, so + 128u); dma_nt(ld + 2048u, vV, sV, so + 2048u); dma_nt(ld + 3072u, vV, sV, so + 2176u);
#pragma unroll
      for (int i = 1; i < 4; ++i) dma4_nt(ld + i * 4096u, vV + 1024u, vV + 128u, vV + 1024u, vV + 128u, sV, so + i * 4096u - 1024u); };
    auto fillQ = [&](unsigned c, unsigned stage) {
      const unsigned lw = lds0 + stage + STG_W, sw = c * 64u * 8192u + h * 256u, lx = lds0 + stage, sx = c * 128u;
      dma(lw, vW, sW, sw); dma(lw + 1024u, vW, sW, sw + 128u); dma(lw + 2048u, vW, sW, sw + 65536u); dma(lw + 3072u, vW, sW, sw + 65664u);
#pragma unroll
      for (int i = 1; i < 4; ++i) dma4(lw + i * 4096u, vW + 1024u, vW + 128u, vW + 65536u - 1024u, vW + 65664u - 2048u, sW, sw + i * 131072u - 1024u);
#pragma unroll
      for (int i = 0; i < 7; ++i) dma4(lx + i * 4096u, vX0, vX1 + 8192u, vX0 + 16384u, vX1 + 24576u, sX, sx + i * 32u * DM * 2u); };
    auto fillA = [&](unsigned w) { fillWo(4u * (w ^ hx), (SLOTW >> (4u * w)) & 15u); };
    unsigned dummy = 0;
    const unsigned third = rt - (rt / 3u) * 3u;
    auto warm = [&](unsigned i) { const unsigned ln = (i * 3u + third) * 64u, grp = ln >> 10, hd = ((ln >> 7) & 7u) * 4u + (grp == 0u ? 2u : grp == 1u ? 1u : 3u);
      touch(dummy, lane * (DM * 2u), sWo, (hd * 128u + (ln & 127u)) * (DM * 2u) + ct * 128u); };
    const unsigned rot = QROT ? 3u * (((ix & 3u) + (ix >> 2)) % 3u) : 0u;
    auto chunk = [&](unsigned c) { const unsigned k = c + rot; return k >= 9u ? k - 9u : k; };
    fillQ(chunk(0), 0);
    { unsigned stage = STG;
#pragma nounroll
      for (unsigned c = 1; c < 8; ++c) {
        if (TQ && (c == 5u || c == 7u)) { wait_vm<0>(); warm(c == 5u ? 0u : 3u); warm(c == 5u ? 1u : 4u); warm(c == 5u ? 2u : 5u); }
        fillQ(chunk(c), stage); stage = stage == 2 * STG ? 0u : stage + STG; wait_vm<TQ ? 47 : 44>(); if (TQ && c != 5u && c != 7u) wait_vm<44>(); bar(); ST(1 + c); } }
    fillQ(chunk(8), 2 * STG); fillK(0); wait_vm<60>(); bar(); ST(9);
    fillK(1); fillV(0); wait_vm<48>(); bar(); ST(10);
    fillK(2); wait_vm<48>(); bar(); ST(11);
    fillV(1); wait_vm<32>(); bar(); ST(12);
#pragma nounroll
    for (unsigned t = 1; t < 5; ++t) { fillA(t - 1u); fillK(t + 2u); fillV(t + 1u); wait_vm<48>(); bar(); ST(12 + t); }
    fillK(7); fillV(6); wait_vm<32>(); bar(); ST(17);
    fillV(7); fillA(4); warm(TQ ? 6 : 0); warm(TQ ? 7 : 1); warm(TQ ? 8 : 2); wait_vm<35>(); bar(); ST(18);
    fillA(5); fillA(6); warm(TQ ? 9 : 3); warm(TQ ? 10 : 4); warm(TQ ? 11 : 5); warm(TQ ? 12 : 6); wait_vm<4>(); bar(); ST(19);
    fillA(7);
    tag = mix64(ld_agent(ws + EPOCH_OFF));
    wait_vm<0>(); pinu(dummy);
#pragma unroll
    for (int i = 0; i < 16; ++i) wa[i] = rdWo(i);
    ST(20); ST(21);
  } else {
  tag = mix64(ld_agent(ws + EPOCH_OFF));
  tag = ((unsigned long long)rfl((unsigned)(tag >> 32)) << 32) | rfl((unsigned)tag);
  const unsigned rsw0 = 64u * (r & 7u) + 16u * (hh ^ ((r >> 2) & 3u)), rsw1 = rsw0 ^ 32u;
  f32x16 qa[4] = {}, o[4] = {};
  asm volatile("" : "+v"(qa[0]), "+v"(qa[1]), "+v"(qa[2]), "+v"(qa[3]), "+v"(o[0]), "+v"(o[1]), "+v"(o[2]), "+v"(o[3]));
  __builtin_amdgcn_s_waitcnt(0x0070);
  ST(1);
  bar();
  ST(2);
  { lds_cptr wp = (lds_cptr)lds + STG_W + 2048u * hh + 64u * q4 + trcol;
    lds_cptr xp0 = (lds_cptr)lds + wid * 4096u + 1024u * (r >> 3) + rsw0, xp1 = (lds_cptr)lds + wid * 4096u + 1024u * (r >> 3) + rsw1;
    bf16x8 xf[4], af[5];
    auto rdA = [&](int i) { return trd2(wp, 4096 * (i >> 2) + 512 * (i & 3), 4096 * (i >> 2) + 512 * (i & 3) + 256); };
    xf[0] = rd128(xp0); af[0] = rdA(0); af[1] = rdA(1); af[2] = rdA(2); af[3] = rdA(3);
    unsigned stage = 0;
#pragma nounroll
    for (unsigned c = 0; c < 9; ++c) {
      SBAR();
      static_for<12>([&](auto I) { constexpr int i = I.value;
        af[(i + 4) % 5] = rdA(i + 4);
        if constexpr (i == 0) xf[1] = rd128(xp1); if constexpr (i == 3) xf[2] = rd128(xp0 + 512); if constexpr (i == 7) xf[3] = rd128(xp1 + 512);
        qa[i & 3] = MFMA(af[i % 5], xf[i >> 2], qa[i & 3]); SBAR(); });
      qa[0] = MFMA(af[12 % 5], xf[3], qa[0]); SBAR();
      bar();
      const unsigned nstage = stage == 2 * STG ? 0u : stage + STG; const int dlt = (int)nstage - (int)stage; stage = nstage;
      wp += dlt; xp0 += dlt; xp1 += dlt;
      const bf16x8 xl = xf[3], a13 = af[13 % 5], a14 = af[14 % 5], a15 = af[15 % 5];
      xf[0] = rd128(xp0); af[0] = rdA(0); af[1] = rdA(1);
      SBAR();
      qa[1] = MFMA(a13, xl, qa[1]); SBAR();
      af[2] = rdA(2);
      qa[2] = MFMA(a14, xl, qa[2]); SBAR();
      af[3] = rdA(3);
      qa[3] = MFMA(a15, xl, qa[3]); SBAR();
      ST(3 + c);
    } }

  const lds_cptr kb0 = (lds_cptr)lds + 2048u * (r >> 3) + rsw0, kb1 = (lds_cptr)lds + 2048u * (r >> 3) + rsw1;
  const lds_cptr vb = (lds_cptr)lds + 64u * (4u * hh + q4) + trcol;
  bf16x8 qf[8], pre[RA];
  f32x16 negm, S0, S1; u32x4 P0[2], P1[2]; float l = 0.f;
  auto rdK = [&](lds_cptr k0, lds_cptr k1, int kb, int i) { return rd128(((i & 1) ? k1 : k0) + 8192 * kb + 512 * (i >> 1)); };
  auto rdV = [&](lds_cptr v, int kb, int j) { return trd2(v, 8192 * kb + 4096 * (j >> 2) + 512 * (j & 3), 8192 * kb + 4096 * (j >> 2) + 2048 + 512 * (j & 3)); };
  auto sm_fix = [&](float ps, f32x16& Sc, f32x16& Sn, u32x4 (&P)[2]) {
    if (__builtin_expect(__any(!(ps <= SUM_THR)), 0)) {
      const float dl = fmaxf(swap_max(max16(Sc)), 0.f), f = __builtin_amdgcn_exp2f(-dl);
#pragma unroll
      for (int i = 0; i < 16; ++i) { Sc[i] -= dl; Sn[i] -= dl; negm[i] -= dl; }
#pragma unroll
      for (int db = 0; db < 4; ++db)
#pragma unroll
        for (int i = 0; i < 16; ++i) o[db][i] *= f;
      l *= f; ps = expo(Sc, P); }
    l += ps; };
  {
    lds_cptr k0 = kb0 + 9 * 16384, k1 = kb1 + 9 * 16384; asm volatile("" : "+v"(k0), "+v"(k1));
    bf16x8 f[16];
#pragma unroll
    for (int i = 0; i < 4; ++i) f[i] = rdK(k0, k1, 0, i);
    auto cvq = [&](int g) { u32x4 w;
#pragma unroll
      for (int e = 0; e < 4; ++e) w[e] = cvtpk(qa[g >> 1][8 * (g & 1) + 2 * e] * QSCALE, qa[g >> 1][8 * (g & 1) + 2 * e + 1] * QSCALE);
      qf[g] = __builtin_bit_cast(bf16x8, w); pin(qf[g]); };
    cvq(0); SBAR();
    static_for<8>([&](auto G) { constexpr int g = G.value;
      f[g + 4] = rdK(k0, k1, (g + 4) >> 3, (g + 4) & 7);
      if constexpr (g == 0) { const f32x16 z = {}; S0 = MFMA(f[0], qf[0], z); } else S0 = MFMA(f[g], qf[g], S0);
      if constexpr (g < 7) cvq(g + 1);
      SBAR(); });
#pragma unroll
    for (int i = 12; i < 16; ++i) f[i] = rdK(k0, k1, 1, i & 7);
    const float m = swap_max(max16(S0));
#pragma unroll
    for (int i = 0; i < 16; ++i) negm[i] = -m;
    bar();
    { const unsigned ko = ((SLOTK >> 4) & 15u) * 16384u;
#pragma unroll
      for (int i = 0; i < RA; ++i) pre[i] = rdK(kb0 + ko, kb1 + ko, 0, i); }
    SBAR();
    float e[16]; float s0 = 0.f, s1 = 0.f;
    static_for<9>([&](auto G) { constexpr int g = G.value;
      if constexpr (g == 0) S1 = MFMA(f[8], qf[0], negm); else if constexpr (g < 8) S1 = MFMA(f[8 + g], qf[g], S1);
      if constexpr (g < 8) { e[2 * g] = __builtin_amdgcn_exp2f(S0[2 * g] - m); e[2 * g + 1] = __builtin_amdgcn_exp2f(S0[2 * g + 1] - m); pinf(e[2 * g]); pinf(e[2 * g + 1]); }
      if constexpr (g == 1) { s0 = e[0]; s1 = e[1]; } else if constexpr (g > 1) { s0 = fadd(s0, e[2 * g - 2]); s1 = fadd(s1, e[2 * g - 1]); pinf(s0); pinf(s1); }
      if constexpr (g >= 1) { unsigned w = cvtpk(e[2 * g - 2], e[2 * g - 1]); pinu(w); P0[(g - 1) >> 2][(g - 1) & 3] = w; }
      SBAR(); });
    l = s0 + s1; }
  ST(12);
  auto unit = [&](auto KB, auto LAST, lds_cptr k0, lds_cptr k1, lds_cptr v, lds_cptr nk0, lds_cptr nk1, f32x16& Sn, f32x16& Sc, const u32x4 (&Pp)[2], u32x4 (&Pc)[2]) {
    constexpr int kb = KB.value; constexpr bool last = LAST.value;
    float e[16]; float s0 = 0.f, s1 = 0.f; bf16x8 f[16 + RA];
#pragma unroll
    for (int i = 0; i < RA; ++i) f[i] = pre[i];
    static_for<16>([&](auto G) { constexpr int g = G.value; constexpr int n = g + RA; constexpr int gb = 16 - RA + 1;
      if constexpr (last && g == gb) { bar(); f[16] = rdK(nk0, nk1, 0, 0); }
      if constexpr (n < 8) f[n] = rdK(k0, k1, kb, n); else if constexpr (n < 16) f[n] = rdV(v, kb, n - 8);
      else if constexpr (!last) f[n] = rdK(k0, k1, kb + 1, n - 16);
      else if constexpr (n > 16) f[n] = rdK(nk0, nk1, 0, n - 16);
      if constexpr (g == 0) Sn = MFMA(f[0], qf[0], negm); else if constexpr (g < 8) Sn = MFMA(f[g], qf[g], Sn);
      else o[g & 3] = MFMA(f[g], __builtin_bit_cast(bf16x8, Pp[(g >> 2) & 1]), o[g & 3]);
      constexpr int e0 = g < 12 ? (g / 3) * 4 + (g % 3 == 0 ? 0 : g % 3 + 1) : 16, e1 = g + 1 < 12 ? ((g + 1) / 3) * 4 + ((g + 1) % 3 == 0 ? 0 : (g + 1) % 3 + 1) : 16;
      constexpr int p0 = g >= 1 ? (g - 1 < 12 ? ((g - 1) / 3) * 4 + ((g - 1) % 3 == 0 ? 0 : (g - 1) % 3 + 1) : 16) : 0, p1 = g >= 1 ? e0 : 0;
      static_for<e1 - e0>([&](auto J) { constexpr int i = e0 + J.value; e[i] = __builtin_amdgcn_exp2f(Sc[i]); pinf(e[i]); });
      static_for<p1 - p0>([&](auto J) { constexpr int i = p0 + J.value;
        if constexpr (i == 0) s0 = e[0]; else if constexpr (i == 1) s1 = e[1]; else if constexpr (i & 1) { s1 = fadd(s1, e[i]); pinf(s1); } else { s0 = fadd(s0, e[i]); pinf(s0); }
        if constexpr (i & 1) { unsigned w = cvtpk(e[i - 1], e[i]); pinu(w); Pc[i >> 3][(i >> 1) & 3] = w; } });
      SBAR(); });
#pragma unroll
    for (int i = 0; i < RA; ++i) pre[i] = f[16 + i];
    sm_fix(s0 + s1, Sc, Sn, Pc); };
#pragma nounroll
  for (unsigned t = 1; t < 8; ++t) {
    const unsigned ko = ((SLOTK >> (4u * t)) & 15u) * 16384u, vo = ((SLOTV >> (4u * (t - 1u))) & 15u) * 16384u, nko = ((SLOTK >> (4u * ((t + 1u) & 7u))) & 15u) * 16384u;
    SBAR();
    unit(std::integral_constant<int, 0>{}, std::false_type{}, kb0 + ko, kb1 + ko, vb + vo, kb0, kb1, S0, S1, P0, P1);
    unit(std::integral_constant<int, 1>{}, std::true_type{}, kb0 + ko, kb1 + ko, vb + vo, kb0 + nko, kb1 + nko, S1, S0, P1, P0);
    ST(12 + t);
  }
  {
    const lds_cptr v = vb + ((SLOTV >> 28) & 15u) * 16384u;
    bf16x8 f[16];
#pragma unroll
    for (int i = 0; i < 3; ++i) f[i] = rdV(v, 0, i);
    float e[16]; float s0 = 0.f, s1 = 0.f;
    SBAR();
    static_for<9>([&](auto G) { constexpr int g = G.value; constexpr int n = g + 3;
      if constexpr (n < 8) f[n] = rdV(v, 0, n); else if constexpr (g < 8) f[n] = rdV(v, 1, ((n - 8) >> 1) + 4 * ((n - 8) & 1));
      if constexpr (g < 8) { o[g & 3] = MFMA(f[g], __builtin_bit_cast(bf16x8, P0[g >> 2]), o[g & 3]);
        e[2 * g] = __builtin_amdgcn_exp2f(S1[2 * g]); e[2 * g + 1] = __builtin_amdgcn_exp2f(S1[2 * g + 1]); pinf(e[2 * g]); pinf(e[2 * g + 1]); }
      if constexpr (g == 1) { s0 = e[0]; s1 = e[1]; } else if constexpr (g > 1) { s0 = fadd(s0, e[2 * g - 2]); s1 = fadd(s1, e[2 * g - 1]); pinf(s0); pinf(s1); }
      if constexpr (g >= 1) { unsigned w = cvtpk(e[2 * g - 2], e[2 * g - 1]); pinu(w); P1[(g - 1) >> 2][(g - 1) & 3] = w; }
      SBAR(); });
    sm_fix(s0 + s1, S1, S0, P1);
    const float inv = __builtin_amdgcn_rcpf(swap_add(l));
    const unsigned so = ((b * 7u + wid) * NH + h) * 8192u, vo = lane * 16u;
    auto put = [&](int k8) { u32x4 w;
#pragma unroll
      for (int e2 = 0; e2 < 4; ++e2) w[e2] = cvtpk(o[k8 >> 1][8 * (k8 & 1) + 2 * e2] * inv, o[k8 >> 1][8 * (k8 & 1) + 2 * e2 + 1] * inv);
      bstore_sc1(w, vo, sO, so + k8 * 1024u); };
    static_for<8>([&](auto G) { constexpr int g = G.value; constexpr int n = g + 11;
      if constexpr (n < 16) f[n] = rdV(v, 1, ((n - 8) >> 1) + 4 * ((n - 8) & 1));
      o[g >> 1] = MFMA(f[8 + g], __builtin_bit_cast(bf16x8, P1[g & 1]), o[g >> 1]);
      if constexpr (g >= 4) put(g - 4);
      SBAR(); });
    ST(20);
    put(4); put(5); put(6); put(7); }
  SBAR();
#pragma unroll
  for (int i = 0; i < 16; ++i) wa[i] = rdWo(i);
  ST(21);
  }
  wait_vm<0>();
  ST(22);
  bar();
  if (tid == 0) st_agent(ws + FLAG_OFF + (b * NH + h) * FLAG_STRIDE, tag);
  ST(23);

  if (!has_tile) {
    if (bid == 248u && wid == 0u) {
#pragma unroll
      for (int i = 0; i < 4; ++i) poll_flags(ws + FLAG_OFF + (i * 64u + lane) * FLAG_STRIDE, tag);
      if (lane == 0) st_agent(ws + EPOCH_OFF, tag); }
    ST(24); ST(25); ST(26); ST(27); ST(28); ST(29); ST(30); ST(31); ST_OUT(ws, 0, 32); return; }
  const unsigned head0 = 4u * (wid ^ hx);
  bf16x8 wc[16];
  fillWo(head0 + 2u, myslot);
  wait_vm<0>();
#pragma unroll
  for (int i = 0; i < 16; ++i) wc[i] = rdWo(i);
#pragma unroll
  for (int i = 0; i < 16; ++i) pin(wc[i]);
  fillWo(head0 + 1u, myslot);
  ST(24);
  { const unsigned g = 2u * rt + ((lane >> 2) & 1u), fb = (g * 37u) >> 8;
    poll_flags(ws + FLAG_OFF + (fb * NH + head0 + (lane & 3u)) * FLAG_STRIDE, tag); }
  wait_vm<0>();
  ST(25);
  const unsigned voffO = lane * 16u, soO = (2u * rt * NH + head0) * 8192u;
  const unsigned soD = (head0 + 3u) * 128u * (DM * 2u) + ct * 128u, ldsw = lds0 + myslot * 16384u;
  bf16x8 of[32][2], lf[2][2];
  auto issueO = [&](auto S) { constexpr int st = S.value;
    bload_sc1(of[st][0], voffO, sO, soO + st * 1024u);
    bload_sc1(of[st][1], voffO, sO, soO + NH * 8192u + st * 1024u); };
  static_for<WO>(issueO);
  f32x16 acc[2][2] = {};
  static_for<32>([&](auto S) { constexpr int st = S.value; constexpr int nx = st + 1;
    wait_vm2<p2_wait(st)>(of[st][0], of[st][1]);
    if constexpr (st == 8) ST(26); if constexpr (st == 16) ST(27); if constexpr (st == 24) ST(28);
    if constexpr (nx < 32 && ((nx >> 3) & 1)) { lf[nx & 1][0] = rdWo(2 * (nx & 7)); lf[nx & 1][1] = rdWo(2 * (nx & 7) + 1); }
    bf16x8 a0, a1;
    if constexpr (st < 8) { a0 = wa[2 * st]; a1 = wa[2 * st + 1]; } else if constexpr (st >= 16 && st < 24) { a0 = wc[2 * (st - 16)]; a1 = wc[2 * (st - 16) + 1]; } else { a0 = lf[st & 1][0]; a1 = lf[st & 1][1]; }
    acc[0][0] = MFMA(a0, of[st][0], acc[0][0]); acc[0][1] = MFMA(a0, of[st][1], acc[0][1]);
    acc[1][0] = MFMA(a1, of[st][0], acc[1][0]); acc[1][1] = MFMA(a1, of[st][1], acc[1][1]);
    SBAR();
    if constexpr (st >= 8 && st < 16) { dma(ldsw + (st & 7) * 2048u, vWo, sWo, soD + (st & 7) * 16u * (DM * 2u)); dma(ldsw + (st & 7) * 2048u + 1024u, vWo, sWo, soD + ((st & 7) * 16u + 8u) * (DM * 2u)); }
    if constexpr (st + WO < 32) issueO(std::integral_constant<int, st + WO>{});
    SBAR(); });
  ST(29);
  { lds_ptr pw = (lds_ptr)lds + myslot * 16384u;
#pragma unroll
    for (int nb = 0; nb < 2; ++nb)
#pragma unroll
      for (int qb = 0; qb < 2; ++qb)
#pragma unroll
        for (int s = 0; s < 2; ++s)
#pragma unroll
          for (int e = 0; e < 2; ++e) { const f32x4 v = {acc[nb][qb][8 * s + 4 * e], acc[nb][qb][8 * s + 4 * e + 1], acc[nb][qb][8 * s + 4 * e + 2], acc[nb][qb][8 * s + 4 * e + 3]};
            const unsigned pos = lane ^ ((4u * nb + 2u * s + hh) << 1);
            *(__attribute__((address_space(3))) f32x4*)(pw + (((nb * 2 + qb) * 2 + s) * 2 + e) * 1024 + pos * 16u) = v; } }
  bar();
  ST(30);
  { const unsigned q = tid >> 3, c = tid & 7u, nb = c >> 2, s = (c >> 1) & 1u, h2 = c & 1u, qb = q >> 5;
    const unsigned pos = ((q & 31u) + 32u * h2) ^ (c << 1);
    const lds_cptr pr = (lds_cptr)lds + (((nb * 2u + qb) * 2u + s) * 2u) * 1024u + pos * 16u;
    f32x4 a = {}, bq = {};
#pragma unroll
    for (int w = 0; w < 8; ++w) { const int so = ((SLOTW >> (4 * w)) & 15) * 16384; a += *(const __attribute__((address_space(3))) f32x4*)(pr + so); bq += *(const __attribute__((address_space(3))) f32x4*)(pr + so + 1024); }
    const u32x4 w4 = {cvtpk(a[0], a[1]), cvtpk(a[2], a[3]), cvtpk(bq[0], bq[1]), cvtpk(bq[2], bq[3])};
    *(u32x4*)((char*)out + (size_t)(rt * 64u + q) * (DM * 2u) + ct * 128u + c * 16u) = w4; }
  ST(31); ST_OUT(ws, 0, 32);
}

extern "C" void kernel_launch(void* const* d_in, const int* in_sizes, int n_in, void* d_out, int out_size, void* d_ws, size_t ws_size, hipStream_t stream) {
  (void)in_sizes; (void)n_in; (void)out_size; (void)ws_size;
  (void)hipFuncSetAttribute((const void*)mha_kernel, hipFuncAttributeMaxDynamicSharedMemorySize, LDS_BYTES);
  const unsigned short* x = (const unsigned short*)d_in[0]; const unsigned short* kc = (const unsigned short*)d_in[1]; const unsigned short* vc = (const unsigned short*)d_in[2];
  const unsigned short* wq = (const unsigned short*)d_in[3]; const unsigned short* wo = (const unsigned short*)d_in[4];
  mha_kernel<<<256, 512, LDS_BYTES, stream>>>(x, kc, vc, wq, wo, (char*)d_ws, (unsigned short*)d_out);
}
```

```cpp
#include <hip/hip_runtime.h>
#include <stdint.h>
#include <utility>

typedef short    bf16x8 __attribute__((ext_vector_type(8)));
typedef short    s16x4  __attribute__((ext_vector_type(4)));
typedef float    f32x16 __attribute__((ext_vector_type(16)));
typedef float    f32x4  __attribute__((ext_vector_type(4)));
typedef float    f32x2  __attribute__((ext_vector_type(2)));
typedef __bf16   bf16x2 __attribute__((ext_vector_type(2)));
typedef unsigned u32x4  __attribute__((ext_vector_type(4)));
typedef __attribute__((address_space(3))) const char* lds_cptr;
typedef __attribute__((address_space(3))) char* lds_ptr;
typedef __amdgpu_buffer_rsrc_t srd_t;

#define KV_AUX "nt "
#define CODEPF 0
#define TOUCH_AUX ""
#define ST_AUX "sc1"
#define LD_AUX "sc1"
#define DI __device__ __forceinline__
#define MFMA(a, b, c) __builtin_amdgcn_mfma_f32_32x32x16_bf16((a), (b), (c), 0, 0, 0)
#define SBAR() __builtin_amdgcn_sched_barrier(0)

constexpr int NB = 8, SQ = 224, DM = 576, NH = 32, SK = 512, DH = 128;
constexpr int LDS_BYTES = 163840;
constexpr float QSCALE = 0.08838834764831845f * 1.4426950408889634f;
constexpr float SUM_THR = 4096.f;

template <int... Is, class F> DI void static_for_impl(std::integer_sequence<int, Is...>, F&& f) { (f(std::integral_constant<int, Is>{}), ...); }
template <int N, class F> DI void static_for(F&& f) { static_for_impl(std::make_integer_sequence<int, N>{}, f); }

DI unsigned rfl(unsigned x) { return __builtin_amdgcn_readfirstlane(x); }
DI unsigned cvtpk(float lo, float hi) { f32x2 v = {lo, hi}; bf16x2 b = __builtin_convertvector(v, bf16x2); return __builtin_bit_cast(unsigned, b); }
DI srd_t mk_srd(const void* p, unsigned bytes) { return __builtin_amdgcn_make_buffer_rsrc((void*)p, (short)0, (int)bytes, 0x00020000); }
DI void dma(unsigned lds, unsigned voff, srd_t srd, unsigned soff) {
  asm volatile("s_mov_b32 m0, %0\n\ts_nop 0\n\tbuffer_load_dwordx4 %1, %2, %3 offen lds" :: "s"(lds), "v"(voff), "s"(srd), "s"(soff) : "memory");
}
DI void dma_nt(unsigned lds, unsigned voff, srd_t srd, unsigned soff) {
  asm volatile("s_mov_b32 m0, %0\n\ts_nop 0\n\tbuffer_load_dwordx4 %1, %2, %3 offen " KV_AUX "lds" :: "s"(lds), "v"(voff), "s"(srd), "s"(soff) : "memory");
}
DI void dma4(unsigned lds, unsigned v0, unsigned v1, unsigned v2, unsigned v3, srd_t srd, unsigned soff) {
  asm volatile("s_mov_b32 m0, %0\n\ts_nop 0\n\tbuffer_load_dwordx4 %1, %5, %6 offen lds\n\tbuffer_load_dwordx4 %2, %5, %6 offen offset:1024 lds\n\t"
               "buffer_load_dwordx4 %3, %5, %6 offen offset:2048 lds\n\tbuffer_load_dwordx4 %4, %5, %6 offen offset:3072 lds"
               :: "s"(lds), "v"(v0), "v"(v1), "v"(v2), "v"(v3), "s"(srd), "s"(soff) : "memory");
}
DI void dma4_nt(unsigned lds, unsigned v0, unsigned v1, unsigned v2, unsigned v3, srd_t srd, unsigned soff) {
  asm volatile("s_mov_b32 m0, %0\n\ts_nop 0\n\tbuffer_load_dwordx4 %1, %5, %6 offen " KV_AUX "lds\n\tbuffer_load_dwordx4 %2, %5, %6 offen offset:1024 " KV_AUX "lds\n\t"
               "buffer_load_dwordx4 %3, %5, %6 offen offset:2048 " KV_AUX "lds\n\tbuffer_load_dwordx4 %4, %5, %6 offen offset:3072 " KV_AUX "lds"
               :: "s"(lds), "v"(v0), "v"(v1), "v"(v2), "v"(v3), "s"(srd), "s"(soff) : "memory");
}
DI void bstore_sc1(const u32x4& d, unsigned voff, srd_t srd, unsigned soff) { asm volatile("buffer_store_dwordx4 %0, %1, %2, %3 offen " ST_AUX "\n\ts_nop 1" :: "v"(d), "v"(voff), "s"(srd), "s"(soff) : "memory"); }
DI void bload_sc1(bf16x8& d, unsigned voff, srd_t srd, unsigned soff) { asm volatile("buffer_load_dwordx4 %0, %1, %2, %3 offen " LD_AUX : "=v"(d) : "v"(voff), "s"(srd), "s"(soff) : "memory"); }
DI void bload(bf16x8& d, unsigned voff, srd_t srd, unsigned soff) { asm volatile("buffer_load_dwordx4 %0, %1, %2, %3 offen" : "=v"(d) : "v"(voff), "s"(srd), "s"(soff) : "memory"); }
DI void bload_nt(bf16x8& d, unsigned voff, srd_t srd, unsigned soff) { asm volatile("buffer_load_dwordx4 %0, %1, %2, %3 offen nt" : "=v"(d) : "v"(voff), "s"(srd), "s"(soff) : "memory"); }
DI void touch(unsigned& d, unsigned voff, srd_t srd, unsigned soff) { asm volatile("buffer_load_dword %0, %1, %2, %3 offen " TOUCH_AUX : "+v"(d) : "v"(voff), "s"(srd), "s"(soff) : "memory"); }
DI void pin(bf16x8& a) { asm volatile("" : "+v"(a)); }
DI void pinf(float& a) { asm volatile("" : "+v"(a)); }
DI void pinu(unsigned& a) { asm volatile("" : "+v"(a)); }
template <int N> DI void wait_vm2(bf16x8& a, bf16x8& b) { asm volatile("s_waitcnt vmcnt(%2)" : "+v"(a), "+v"(b) : "n"(N) : "memory"); }
template <int N> DI void wait_vm1(bf16x8& a) { asm volatile("s_waitcnt vmcnt(%1)" : "+v"(a) : "n"(N) : "memory"); }
template <int N> DI void wait_bar() { asm volatile("s_waitcnt vmcnt(%0) lgkmcnt(0)\n\ts_barrier" :: "n"(N) : "memory"); }
template <int N> DI void wait_vm() { asm volatile("s_waitcnt vmcnt(%0)" :: "n"(N) : "memory"); }
DI void bar() { asm volatile("s_waitcnt lgkmcnt(0)\n\ts_barrier" ::: "memory"); }
typedef short v4i16_t __attribute__((ext_vector_type(4)));
DI s16x4 trd(lds_cptr p) { return __builtin_amdgcn_ds_read_tr16_b64_v4i16((__attribute__((address_space(3))) v4i16_t*)p); }
DI bf16x8 trd2(lds_cptr p, int o0, int o1) { s16x4 a = trd(p + o0), b = trd(p + o1); return __builtin_shufflevector(a, b, 0, 1, 2, 3, 4, 5, 6, 7); }
DI bf16x8 rd128(lds_cptr p) { return *(const __attribute__((address_space(3))) bf16x8*)p; }
DI float swap_max(float m) { auto rr = __builtin_amdgcn_permlane32_swap(__float_as_uint(m), __float_as_uint(m), false, false); return fmaxf(__uint_as_float(rr[0]), __uint_as_float(rr[1])); }
DI float swap_add(float m) { auto rr = __builtin_amdgcn_permlane32_swap(__float_as_uint(m), __float_as_uint(m), false, false); return __uint_as_float(rr[0]) + __uint_as_float(rr[1]); }
DI float max16(const f32x16& s) { float a = fmaxf(fmaxf(s[0], s[1]), s[2]), b = fmaxf(fmaxf(s[3], s[4]), s[5]);
  a = fmaxf(fmaxf(a, s[6]), s[7]); b = fmaxf(fmaxf(b, s[8]), s[9]); a = fmaxf(fmaxf(a, s[10]), s[11]); b = fmaxf(fmaxf(b, s[12]), s[13]); a = fmaxf(fmaxf(a, s[14]), s[15]); return fmaxf(a, b); }
DI float fadd(float a, float b) { float r; asm("v_add_f32_e32 %0, %1, %2" : "=v"(r) : "v"(a), "v"(b)); return r; }

DI float expo(const f32x16& s, u32x4 (&p)[2]) {
  float e[16]; float s0 = 0.f, s1 = 0.f;
#pragma unroll
  for (int i = 0; i < 16; ++i) e[i] = __builtin_amdgcn_exp2f(s[i]);
#pragma unroll
  for (int i = 0; i < 16; i += 2) { s0 += e[i]; s1 += e[i + 1]; }
#pragma unroll
  for (int i = 0; i < 8; ++i) p[i >> 2][i & 3] = cvtpk(e[2 * i], e[2 * i + 1]);
  return s0 + s1;
}

#define ST_DECL
#define ST(k)
#define ST_OUT(ws, lo, hi)

constexpr unsigned SLOTK = 0x04204209u;
constexpr unsigned SLOTV = 0x31531531u;
constexpr unsigned STG = 45056u, STG_W = 28672u;
constexpr unsigned O_BYTES = NB * SQ * NH * DH * 2;
constexpr unsigned X_OFF = O_BYTES, X_BYTES = 63u * 8u * 4u * 8192u;
constexpr unsigned FLAG_OFF = X_OFF + X_BYTES, FLAG_STRIDE = 128, FLAG2_OFF = FLAG_OFF + 256 * FLAG_STRIDE, EPOCH_OFF = FLAG2_OFF + 256 * FLAG_STRIDE;
constexpr unsigned SPIN_CAP = 1u << 20;
#define RA 3
constexpr unsigned SLOTW = 0x05429876u;
#define WO 6
#define QROT 1
#define HROT 1
#define RETOUCH 1
typedef __attribute__((address_space(1))) unsigned long long gu64;
DI unsigned long long mix64(unsigned long long z) { z += 0x9E3779B97F4A7C15ull; z = (z ^ (z >> 30)) * 0xBF58476D1CE4E5B9ull; z = (z ^ (z >> 27)) * 0x94D049BB133111EBull; return z ^ (z >> 31); }
DI unsigned long long ld_agent(const char* p) { return __hip_atomic_load((gu64*)p, __ATOMIC_RELAXED, __HIP_MEMORY_SCOPE_AGENT); }
DI void st_agent(char* p, unsigned long long v) { __hip_atomic_store((gu64*)p, v, __ATOMIC_RELAXED, __HIP_MEMORY_SCOPE_AGENT); }
DI void poll_flags(const char* p, unsigned long long tag) {
  for (unsigned spins = 0; spins < SPIN_CAP; ++spins) { if (__all(ld_agent(p) == tag)) break; __builtin_amdgcn_s_sleep(2); }
  asm volatile("" ::: "memory");
}

__global__ __launch_bounds__(512, 1) void mha_kernel(const unsigned short* __restrict__ x, const unsigned short* __restrict__ kc, const unsigned short* __restrict__ vc,
                                                     const unsigned short* __restrict__ wq, const unsigned short* __restrict__ wo, char* ws, unsigned short* __restrict__ out) {
  extern __shared__ __attribute__((aligned(16))) char lds[];
  unsigned long long pc0; asm volatile("s_getpc_b64 %0" : "=s"(pc0));
  const unsigned tid = threadIdx.x, lane = tid & 63u, r = lane & 31u, hh = lane >> 5;
  const unsigned wid = rfl(tid >> 6);
  ST_DECL ST(0);
  const unsigned bid = blockIdx.x, xcd = bid & 7u, ix = bid >> 3;
  const unsigned b = (xcd & 1u) * 4u + (ix & 3u), h = (xcd >> 1) * 8u + (ix >> 2);
  const unsigned lds0 = (unsigned)(uintptr_t)lds;
  const unsigned prow = (lane >> 2) & 7u, pch = 4u * hh + (lane & 3u);
  const unsigned q4 = (lane & 15u) >> 2, p4 = lane & 3u, blk = (lane >> 4) & 1u;
  const unsigned trcol = 32u * blk + 16u * (p4 & 1u) + 8u * (p4 >> 1);
  const srd_t sO = mk_srd(ws, O_BYTES);
  unsigned long long tag;
  const unsigned g4 = xcd >> 1, u0 = (xcd & 1u) * 32u + ix;
  const bool has_tile = u0 < 63u;
  const unsigned u = has_tile ? u0 : 62u, rt = u / 9u, ct = u - rt * 9u;

  if (wid == 7u) {
    const srd_t sK = mk_srd(kc + (size_t)(b * NH + h) * SK * DH, SK * DH * 2);
    const srd_t sV = mk_srd(vc + (size_t)(b * NH + h) * SK * DH, SK * DH * 2);
    const srd_t sW = mk_srd(wq, DM * NH * DH * 2);
    const srd_t sX = mk_srd(x + (size_t)b * SQ * DM, SQ * DM * 2);
    const unsigned pchx0 = 4u * hh + ((lane & 3u) ^ ((lane >> 4) & 1u)), pchx1 = pchx0 ^ 2u;
    const unsigned vV = prow * 256u + pch * 16u, vK0 = prow * 256u + pchx0 * 16u, vK1 = prow * 256u + pchx1 * 16u;
    const unsigned vW = prow * 8192u + pch * 16u, vX0 = prow * (DM * 2u) + pchx0 * 16u, vX1 = prow * (DM * 2u) + pchx1 * 16u;
    auto fillK = [&](unsigned t) { const unsigned so = t * 16384u, ld = lds0 + ((SLOTK >> (4u * t)) & 15u) * 16384u;
      dma_nt(ld, vK0, sK, so); dma_nt(ld + 1024u, vK0, sK, so + 128u); dma_nt(ld + 2048u, vK1, sK, so + 2048u); dma_nt(ld + 3072u, vK1, sK, so + 2176u);
#pragma unroll
      for (int i = 1; i < 4; ++i) dma4_nt(ld + i * 4096u, vK0 + 1024u, vK0 + 128u, vK1 + 1024u, vK1 + 128u, sK, so + i * 4096u - 1024u); };
    auto fillV = [&](unsigned t) { const unsigned so = t * 16384u, ld = lds0 + ((SLOTV >> (4u * t)) & 15u) * 16384u;
      dma_nt(ld, vV, sV, so); dma_nt(ld + 1024u, vV, sV, so + 128u); dma_nt(ld + 2048u, vV, sV, so + 2048u); dma_nt(ld + 3072u, vV, sV, so + 2176u);
#pragma unroll
      for (int i = 1; i < 4; ++i) dma4_nt(ld + i * 4096u, vV + 1024u, vV + 128u, vV + 1024u, vV + 128u, sV, so + i * 4096u - 1024u); };
    auto fillQ = [&](unsigned c, unsigned stage) {
      const unsigned lw = lds0 + stage + STG_W, sw = c * 64u * 8192u + h * 256u, lx = lds0 + stage, sx = c * 128u;
      dma(lw, vW, sW, sw); dma(lw + 1024u, vW, sW, sw + 128u); dma(lw + 2048u, vW, sW, sw + 65536u); dma(lw + 3072u, vW, sW, sw + 65664u);
#pragma unroll
      for (int i = 1; i < 4; ++i) dma4(lw + i * 4096u, vW + 1024u, vW + 128u, vW + 65536u - 1024u, vW + 65664u - 2048u, sW, sw + i * 131072u - 1024u);
#pragma unroll
      for (int i = 0; i < 7; ++i) dma4(lx + i * 4096u, vX0, vX1 + 8192u, vX0 + 16384u, vX1 + 24576u, sX, sx + i * 32u * DM * 2u); };
    const srd_t sWo = mk_srd(wo, NH * DH * DM * 2);
    const unsigned vWo = prow * (DM * 2u) + pch * 16u;
    auto fillWo = [&](unsigned i) { const unsigned ld = lds0 + ((SLOTW >> (4u * i)) & 15u) * 16384u, so = (g4 * 8u + i) * 128u * (DM * 2u) + ct * 128u;
#pragma unroll
      for (int k = 0; k < 4; ++k) dma4(ld + k * 4096u, vWo, vWo + 8192u, vWo + 16384u, vWo + 24576u, sWo, so + k * 32u * (DM * 2u)); };
    const unsigned rot = QROT ? 3u * (((ix & 3u) + (ix >> 2)) % 3u) : 0u;
    auto chunk = [&](unsigned c) { const unsigned k = c + rot; return k >= 9u ? k - 9u : k; };
    fillQ(chunk(0), 0);
    { unsigned stage = STG;
#pragma nounroll
      for (unsigned c = 1; c < 8; ++c) { fillQ(chunk(c), stage); stage = stage == 2 * STG ? 0u : stage + STG; wait_vm<44>(); bar(); ST(1 + c); } }
    fillQ(chunk(8), 2 * STG); fillK(0); wait_vm<60>(); bar(); ST(9);
    fillK(1); fillV(0); wait_vm<48>(); bar(); ST(10);
    fillK(2); wait_vm<48>(); bar(); ST(11);
    fillV(1); wait_vm<32>(); bar(); ST(12);
#pragma nounroll
    for (unsigned t = 1; t < 5; ++t) { fillWo(t - 1u); fillK(t + 2u); fillV(t + 1u); wait_vm<48>(); bar(); ST(12 + t); }
    fillK(7); fillV(6); wait_vm<32>(); bar(); ST(17);
    fillV(7); fillWo(4); wait_vm<32>(); bar(); ST(18);
    fillWo(5); fillWo(6); wait_vm<48>(); bar(); ST(19);
    fillWo(7);
    tag = mix64(ld_agent(ws + EPOCH_OFF));
    ST(20); ST(21);
  } else {
  unsigned cdummy = 0;
  if (CODEPF) {
    const srd_t sC = mk_srd((const void*)(pc0 & ~255ull), CODEPF);
    touch(cdummy, lane * 64u, sC, wid * 4096u); }
  tag = mix64(ld_agent(ws + EPOCH_OFF));
  tag = ((unsigned long long)rfl((unsigned)(tag >> 32)) << 32) | rfl((unsigned)tag);
  const unsigned rsw0 = 64u * (r & 7u) + 16u * (hh ^ ((r >> 2) & 3u)), rsw1 = rsw0 ^ 32u;
  f32x16 qa[4] = {}, o[4] = {};
  asm volatile("" : "+v"(qa[0]), "+v"(qa[1]), "+v"(qa[2]), "+v"(qa[3]), "+v"(o[0]), "+v"(o[1]), "+v"(o[2]), "+v"(o[3]));
  __builtin_amdgcn_s_waitcnt(0x0070);
  wait_vm<0>(); pinu(cdummy);
  ST(1);
  bar();
  ST(2);
  { lds_cptr wp = (lds_cptr)lds + STG_W + 2048u * hh + 64u * q4 + trcol;
    lds_cptr xp0 = (lds_cptr)lds + wid * 4096u + 1024u * (r >> 3) + rsw0, xp1 = (lds_cptr)lds + wid * 4096u + 1024u * (r >> 3) + rsw1;
    bf16x8 xf[4], af[5];
    auto rdA = [&](int i) { return trd2(wp, 4096 * (i >> 2) + 512 * (i & 3), 4096 * (i >> 2) + 512 * (i & 3) + 256); };
    xf[0] = rd128(xp0); af[0] = rdA(0); af[1] = rdA(1); af[2] = rdA(2); af[3] = rdA(3);
    unsigned stage = 0;
#pragma nounroll
    for (unsigned c = 0; c < 9; ++c) {
      SBAR();
      static_for<12>([&](auto I) { constexpr int i = I.value;
        af[(i + 4) % 5] = rdA(i + 4);
        if constexpr (i == 0) xf[1] = rd128(xp1); if constexpr (i == 3) xf[2] = rd128(xp0 + 512); if constexpr (i == 7) xf[3] = rd128(xp1 + 512);
        qa[i & 3] = MFMA(af[i % 5], xf[i >> 2], qa[i & 3]); SBAR(); });
      qa[0] = MFMA(af[12 % 5], xf[3], qa[0]); SBAR();
      bar();
      const unsigned nstage = stage == 2 * STG ? 0u : stage + STG; const int dlt = (int)nstage - (int)stage; stage = nstage;
      wp += dlt; xp0 += dlt; xp1 += dlt;
      const bf16x8 xl = xf[3], a13 = af[13 % 5], a14 = af[14 % 5], a15 = af[15 % 5];
      xf[0] = rd128(xp0); af[0] = rdA(0); af[1] = rdA(1);
      SBAR();
      qa[1] = MFMA(a13, xl, qa[1]); SBAR();
      af[2] = rdA(2);
      qa[2] = MFMA(a14, xl, qa[2]); SBAR();
      af[3] = rdA(3);
      qa[3] = MFMA(a15, xl, qa[3]); SBAR();
      ST(3 + c);
    } }

  const lds_cptr kb0 = (lds_cptr)lds + 2048u * (r >> 3) + rsw0, kb1 = (lds_cptr)lds + 2048u * (r >> 3) + rsw1;
  const lds_cptr vb = (lds_cptr)lds + 64u * (4u * hh + q4) + trcol;
  bf16x8 qf[8], pre[RA];
  f32x16 negm, S0, S1; u32x4 P0[2], P1[2]; float l = 0.f;
  auto rdK = [&](lds_cptr k0, lds_cptr k1, int kb, int i) { return rd128(((i & 1) ? k1 : k0) + 8192 * kb + 512 * (i >> 1)); };
  auto rdV = [&](lds_cptr v, int kb, int j) { return trd2(v, 8192 * kb + 4096 * (j >> 2) + 512 * (j & 3), 8192 * kb + 4096 * (j >> 2) + 2048 + 512 * (j & 3)); };
  auto sm_fix = [&](float ps, f32x16& Sc, f32x16& Sn, u32x4 (&P)[2]) {
    if (__builtin_expect(__any(!(ps <= SUM_THR)), 0)) {
      const float dl = fmaxf(swap_max(max16(Sc)), 0.f), f = __builtin_amdgcn_exp2f(-dl);
#pragma unroll
      for (int i = 0; i < 16; ++i) { Sc[i] -= dl; Sn[i] -= dl; negm[i] -= dl; }
#pragma unroll
      for (int db = 0; db < 4; ++db)
#pragma unroll
        for (int i = 0; i < 16; ++i) o[db][i] *= f;
      l *= f; ps = expo(Sc, P); }
    l += ps; };
  {
    lds_cptr k0 = kb0 + 9 * 16384, k1 = kb1 + 9 * 16384; asm volatile("" : "+v"(k0), "+v"(k1));
    bf16x8 f[16];
#pragma unroll
    for (int i = 0; i < 4; ++i) f[i] = rdK(k0, k1, 0, i);
    auto cvq = [&](int g) { u32x4 w;
#pragma unroll
      for (int e = 0; e < 4; ++e) w[e] = cvtpk(qa[g >> 1][8 * (g & 1) + 2 * e] * QSCALE, qa[g >> 1][8 * (g & 1) + 2 * e + 1] * QSCALE);
      qf[g] = __builtin_bit_cast(bf16x8, w); pin(qf[g]); };
    cvq(0); SBAR();
    static_for<8>([&](auto G) { constexpr int g = G.value;
      f[g + 4] = rdK(k0, k1, (g + 4) >> 3, (g + 4) & 7);
      if constexpr (g == 0) { const f32x16 z = {}; S0 = MFMA(f[0], qf[0], z); } else S0 = MFMA(f[g], qf[g], S0);
      if constexpr (g < 7) cvq(g + 1);
      SBAR(); });
#pragma unroll
    for (int i = 12; i < 16; ++i) f[i] = rdK(k0, k1, 1, i & 7);
    const float m = swap_max(max16(S0));
#pragma unroll
    for (int i = 0; i < 16; ++i) negm[i] = -m;
    bar();
    { const unsigned ko = ((SLOTK >> 4) & 15u) * 16384u;
#pragma unroll
      for (int i = 0; i < RA; ++i) pre[i] = rdK(kb0 + ko, kb1 + ko, 0, i); }
    SBAR();
    float e[16]; float s0 = 0.f, s1 = 0.f;
    static_for<9>([&](auto G) { constexpr int g = G.value;
      if constexpr (g == 0) S1 = MFMA(f[8], qf[0], negm); else if constexpr (g < 8) S1 = MFMA(f[8 + g], qf[g], S1);
      if constexpr (g < 8) { e[2 * g] = __builtin_amdgcn_exp2f(S0[2 * g] - m); e[2 * g + 1] = __builtin_amdgcn_exp2f(S0[2 * g + 1] - m); pinf(e[2 * g]); pinf(e[2 * g + 1]); }
      if constexpr (g == 1) { s0 = e[0]; s1 = e[1]; } else if constexpr (g > 1) { s0 = fadd(s0, e[2 * g - 2]); s1 = fadd(s1, e[2 * g - 1]); pinf(s0); pinf(s1); }
      if constexpr (g >= 1) { unsigned w = cvtpk(e[2 * g - 2], e[2 * g - 1]); pinu(w); P0[(g - 1) >> 2][(g - 1) & 3] = w; }
      SBAR(); });
    l = s0 + s1; }
  ST(12);
  auto unit = [&](auto KB, auto LAST, lds_cptr k0, lds_cptr k1, lds_cptr v, lds_cptr nk0, lds_cptr nk1, f32x16& Sn, f32x16& Sc, const u32x4 (&Pp)[2], u32x4 (&Pc)[2]) {
    constexpr int kb = KB.value; constexpr bool last = LAST.value;
    float e[16]; float s0 = 0.f, s1 = 0.f; bf16x8 f[16 + RA];
#pragma unroll
    for (int i = 0; i < RA; ++i) f[i] = pre[i];
    static_for<16>([&](auto G) { constexpr int g = G.value; constexpr int n = g + RA; constexpr int gb = 16 - RA + 1;
      if constexpr (last && g == gb) { bar(); f[16] = rdK(nk0, nk1, 0, 0); }
      if constexpr (n < 8) f[n] = rdK(k0, k1, kb, n); else if constexpr (n < 16) f[n] = rdV(v, kb, n - 8);
      else if constexpr (!last) f[n] = rdK(k0, k1, kb + 1, n - 16);
      else if constexpr (n > 16) f[n] = rdK(nk0, nk1, 0, n - 16);
      if constexpr (g == 0) Sn = MFMA(f[0], qf[0], negm); else if constexpr (g < 8) Sn = MFMA(f[g], qf[g], Sn);
      else o[g & 3] = MFMA(f[g], __builtin_bit_cast(bf16x8, Pp[(g >> 2) & 1]), o[g & 3]);
      constexpr int e0 = g < 12 ? (g / 3) * 4 + (g % 3 == 0 ? 0 : g % 3 + 1) : 16, e1 = g + 1 < 12 ? ((g + 1) / 3) * 4 + ((g + 1) % 3 == 0 ? 0 : (g + 1) % 3 + 1) : 16;
      constexpr int p0 = g >= 1 ? (g - 1 < 12 ? ((g - 1) / 3) * 4 + ((g - 1) % 3 == 0 ? 0 : (g - 1) % 3 + 1) : 16) : 0, p1 = g >= 1 ? e0 : 0;
      static_for<e1 - e0>([&](auto J) { constexpr int i = e0 + J.value; e[i] = __builtin_amdgcn_exp2f(Sc[i]); pinf(e[i]); });
      static_for<p1 - p0>([&](auto J) { constexpr int i = p0 + J.value;
        if constexpr (i == 0) s0 = e[0]; else if constexpr (i == 1) s1 = e[1]; else if constexpr (i & 1) { s1 = fadd(s1, e[i]); pinf(s1); } else { s0 = fadd(s0, e[i]); pinf(s0); }
        if constexpr (i & 1) { unsigned w = cvtpk(e[i - 1], e[i]); pinu(w); Pc[i >> 3][(i >> 1) & 3] = w; } });
      SBAR(); });
#pragma unroll
    for (int i = 0; i < RA; ++i) pre[i] = f[16 + i];
    sm_fix(s0 + s1, Sc, Sn, Pc); };
#pragma nounroll
  for (unsigned t = 1; t < 8; ++t) {
    const unsigned ko = ((SLOTK >> (4u * t)) & 15u) * 16384u, vo = ((SLOTV >> (4u * (t - 1u))) & 15u) * 16384u, nko = ((SLOTK >> (4u * ((t + 1u) & 7u))) & 15u) * 16384u;
    SBAR();
    unit(std::integral_constant<int, 0>{}, std::false_type{}, kb0 + ko, kb1 + ko, vb + vo, kb0, kb1, S0, S1, P0, P1);
    unit(std::integral_constant<int, 1>{}, std::true_type{}, kb0 + ko, kb1 + ko, vb + vo, kb0 + nko, kb1 + nko, S1, S0, P1, P0);
    ST(12 + t);
  }
  {
    const lds_cptr v = vb + ((SLOTV >> 28) & 15u) * 16384u;
    bf16x8 f[16];
#pragma unroll
    for (int i = 0; i < 3; ++i) f[i] = rdV(v, 0, i);
    float e[16]; float s0 = 0.f, s1 = 0.f;
    SBAR();
    static_for<9>([&](auto G) { constexpr int g = G.value; constexpr int n = g + 3;
      if constexpr (n < 8) f[n] = rdV(v, 0, n); else if constexpr (g < 8) f[n] = rdV(v, 1, ((n - 8) >> 1) + 4 * ((n - 8) & 1));
      if constexpr (g < 8) { o[g & 3] = MFMA(f[g], __builtin_bit_cast(bf16x8, P0[g >> 2]), o[g & 3]);
        e[2 * g] = __builtin_amdgcn_exp2f(S1[2 * g]); e[2 * g + 1] = __builtin_amdgcn_exp2f(S1[2 * g + 1]); pinf(e[2 * g]); pinf(e[2 * g + 1]); }
      if constexpr (g == 1) { s0 = e[0]; s1 = e[1]; } else if constexpr (g > 1) { s0 = fadd(s0, e[2 * g - 2]); s1 = fadd(s1, e[2 * g - 1]); pinf(s0); pinf(s1); }
      if constexpr (g >= 1) { unsigned w = cvtpk(e[2 * g - 2], e[2 * g - 1]); pinu(w); P1[(g - 1) >> 2][(g - 1) & 3] = w; }
      SBAR(); });
    sm_fix(s0 + s1, S1, S0, P1);
    const float inv = __builtin_amdgcn_rcpf(swap_add(l));
    const unsigned so = ((b * 7u + wid) * NH + h) * 8192u, vo = lane * 16u;
    auto put = [&](int k8) { u32x4 w;
#pragma unroll
      for (int e2 = 0; e2 < 4; ++e2) w[e2] = cvtpk(o[k8 >> 1][8 * (k8 & 1) + 2 * e2] * inv, o[k8 >> 1][8 * (k8 & 1) + 2 * e2 + 1] * inv);
      bstore_sc1(w, vo, sO, so + k8 * 1024u); };
    static_for<8>([&](auto G) { constexpr int g = G.value; constexpr int n = g + 11;
      if constexpr (n < 16) f[n] = rdV(v, 1, ((n - 8) >> 1) + 4 * ((n - 8) & 1));
      o[g >> 1] = MFMA(f[8 + g], __builtin_bit_cast(bf16x8, P1[g & 1]), o[g >> 1]);
      if constexpr (g >= 4) put(g - 4);
      SBAR(); });
    ST(20);
    put(4); put(5); put(6); put(7); }
  ST(21);
  }
  wait_vm<0>();
  ST(22);
  bar();
  if (tid == 0) st_agent(ws + FLAG_OFF + (b * NH + h) * FLAG_STRIDE, tag);
  ST(23);

  unsigned dummy = 0;
  if (RETOUCH && wid < 7u) touch(dummy, lane * 128u, sO, ((b * 7u + wid) * NH + h) * 8192u);
  if (!has_tile) {
    wait_vm<0>(); pinu(dummy);
    if (bid == 249u && wid == 0u) {
#pragma unroll
      for (int i = 0; i < 4; ++i) poll_flags(ws + FLAG_OFF + (i * 64u + lane) * FLAG_STRIDE, tag);
      if (lane == 0) st_agent(ws + EPOCH_OFF, tag); }
    ST(24); ST(25); ST(26); ST(27); ST(28); ST(29); ST(30); ST(31); ST_OUT(ws, 0, 32); return; }
  const unsigned jq = (2u * g4 + 2u + wid) & 7u, G = 8u * rt + jq;
  poll_flags(ws + FLAG_OFF + (((G * 37u) >> 8) * NH + g4 * 8u + (lane & 7u)) * FLAG_STRIDE, tag);
  wait_vm<0>(); pinu(dummy);
  ST(24);
  const unsigned voffO = lane * 16u, soO = (G * NH + g4 * 8u) * 8192u, hr = HROT ? ct : 0u;
  const lds_cptr wb = (lds_cptr)lds + 1024u * hh + 64u * q4 + trcol;
  bf16x8 of[64], lf[64][2];
  auto issueO = [&](auto S) { constexpr int st = S.value; bload_sc1(of[st], voffO, sO, soO + (((st >> 3) + hr) & 7u) * 8192u + (st & 7) * 1024u); };
  auto rdW = [&](auto S) { constexpr int st = S.value; const lds_cptr p = wb + ((SLOTW >> (4u * (((st >> 3) + hr) & 7u))) & 15u) * 16384u;
    lf[st][0] = trd2(p, (st & 7) * 2048, (st & 7) * 2048 + 256); lf[st][1] = trd2(p, (st & 7) * 2048 + 512, (st & 7) * 2048 + 768); };
  static_for<WO>(issueO);
  rdW(std::integral_constant<int, 0>{}); rdW(std::integral_constant<int, 1>{});
  f32x16 acc[2] = {};
  static_for<64>([&](auto S) { constexpr int st = S.value;
    wait_vm1<(st + WO <= 64 ? WO - 1 : 63 - st)>(of[st]);
    if constexpr (st == 16) ST(25); if constexpr (st == 32) ST(26); if constexpr (st == 48) ST(27);
    if constexpr (st + 2 < 64) rdW(std::integral_constant<int, st + 2>{});
    acc[0] = MFMA(lf[st][0], of[st], acc[0]); acc[1] = MFMA(lf[st][1], of[st], acc[1]);
    SBAR();
    if constexpr (st + WO < 64) issueO(std::integral_constant<int, st + WO>{});
    SBAR(); });
  ST(28);
  const srd_t sX2 = mk_srd(ws + X_OFF, X_BYTES);
  if (wid < 6u) {
#pragma unroll
    for (int i = 0; i < 8; ++i) { const f32x4 v = {acc[i >> 2][4 * (i & 3)], acc[i >> 2][4 * (i & 3) + 1], acc[i >> 2][4 * (i & 3) + 2], acc[i >> 2][4 * (i & 3) + 3]};
      f32x4 v2 = v + 0.f; asm volatile("" : "+v"(v2));
      bstore_sc1(__builtin_bit_cast(u32x4, v2), voffO, sX2, ((u * 8u + jq) * 4u + g4) * 8192u + i * 1024u); }
    wait_vm<0>(); }
  bar();
  if (tid == 0) st_agent(ws + FLAG2_OFF + (u * 4u + g4) * FLAG_STRIDE, tag);
  ST(29);
  if (wid >= 6u) {
    poll_flags(ws + FLAG2_OFF + (u * 4u + (lane & 3u)) * FLAG_STRIDE, tag);
    ST(30);
    bf16x8 xp[3][8];
#pragma unroll
    for (int k = 0; k < 3; ++k)
#pragma unroll
      for (int i = 0; i < 8; ++i) bload_sc1(xp[k][i], voffO, sX2, ((u * 8u + jq) * 4u + ((g4 + 1u + k) & 3u)) * 8192u + i * 1024u);
    wait_vm<0>();
#pragma unroll
    for (int k = 0; k < 3; ++k)
#pragma unroll
      for (int i = 0; i < 8; ++i) { pin(xp[k][i]); const f32x4 v = __builtin_bit_cast(f32x4, xp[k][i]);
#pragma unroll
        for (int e = 0; e < 4; ++e) acc[i >> 2][4 * (i & 3) + e] += v[e]; }
    char* op = (char*)out + (size_t)(G * 32u + r) * (DM * 2u) + ct * 128u + hh * 16u;
#pragma unroll
    for (int i = 0; i < 4; ++i) { const u32x4 w4 = {cvtpk(acc[i >> 1][8 * (i & 1)], acc[i >> 1][8 * (i & 1) + 1]), cvtpk(acc[i >> 1][8 * (i & 1) + 2], acc[i >> 1][8 * (i & 1) + 3]),
                                                    cvtpk(acc[i >> 1][8 * (i & 1) + 4], acc[i >> 1][8 * (i & 1) + 5]), cvtpk(acc[i >> 1][8 * (i & 1) + 6], acc[i >> 1][8 * (i & 1) + 7])};
      *(u32x4*)(op + i * 32) = w4; }
  } else { ST(30); }
  ST(31); ST_OUT(ws, 0, 32);
}

extern "C" void kernel_launch(void* const* d_in, const int* in_sizes, int n_in, void* d_out, int out_size, void* d_ws, size_t ws_size, hipStream_t stream) {
  (void)in_sizes; (void)n_in; (void)out_size; (void)ws_size;
  (void)hipFuncSetAttribute((const void*)mha_kernel, hipFuncAttributeMaxDynamicSharedMemorySize, LDS_BYTES);
  const unsigned short* x = (const unsigned short*)d_in[0]; const unsigned short* kc = (const unsigned short*)d_in[1]; const unsigned short* vc = (const unsigned short*)d_in[2];
  const unsigned short* wq = (const unsigned short*)d_in[3]; const unsigned short* wo = (const unsigned short*)d_in[4];
  mha_kernel<<<256, 512, LDS_BYTES, stream>>>(x, kc, vc, wq, wo, (char*)d_ws, (unsigned short*)d_out);
}
```

```cpp
#define DIAG_A 0
#define DIAG_B 2
#include <hip/hip_runtime.h>
#include <stdint.h>
#include <utility>

typedef short    bf16x8 __attribute__((ext_vector_type(8)));
typedef short    s16x4  __attribute__((ext_vector_type(4)));
typedef float    f32x16 __attribute__((ext_vector_type(16)));
typedef float    f32x4  __attribute__((ext_vector_type(4)));
typedef float    f32x2  __attribute__((ext_vector_type(2)));
typedef __bf16   bf16x2 __attribute__((ext_vector_type(2)));
typedef unsigned u32x4  __attribute__((ext_vector_type(4)));
typedef __attribute__((address_space(3))) const char* lds_cptr;
typedef __attribute__((address_space(3))) char* lds_ptr;
typedef __amdgpu_buffer_rsrc_t srd_t;

#define KV_AUX "nt "
#define CODEPF 0
#define TOUCH_AUX ""
#define ST_AUX "sc1"
#define LD_AUX "sc1"
#define DI __device__ __forceinline__
#define MFMA(a, b, c) __builtin_amdgcn_mfma_f32_32x32x16_bf16((a), (b), (c), 0, 0, 0)
#define SBAR() __builtin_amdgcn_sched_barrier(0)

constexpr int NB = 8, SQ = 224, DM = 576, NH = 32, SK = 512, DH = 128;
constexpr int LDS_BYTES = 163840;
constexpr float QSCALE = 0.08838834764831845f * 1.4426950408889634f;
constexpr float SUM_THR = 4096.f;

template <int... Is, class F> DI void static_for_impl(std::integer_sequence<int, Is...>, F&& f) { (f(std::integral_constant<int, Is>{}), ...); }
template <int N, class F> DI void static_for(F&& f) { static_for_impl(std::make_integer_sequence<int, N>{}, f); }

DI unsigned rfl(unsigned x) { return __builtin_amdgcn_readfirstlane(x); }
DI unsigned cvtpk(float lo, float hi) { f32x2 v = {lo, hi}; bf16x2 b = __builtin_convertvector(v, bf16x2); return __builtin_bit_cast(unsigned, b); }
DI srd_t mk_srd(const void* p, unsigned bytes) { return __builtin_amdgcn_make_buffer_rsrc((void*)p, (short)0, (int)bytes, 0x00020000); }
DI void dma(unsigned lds, unsigned voff, srd_t srd, unsigned soff) {
  asm volatile("s_mov_b32 m0, %0\n\ts_nop 0\n\tbuffer_load_dwordx4 %1, %2, %3 offen lds" :: "s"(lds), "v"(voff), "s"(srd), "s"(soff) : "memory");
}
DI void dma_nt(unsigned lds, unsigned voff, srd_t srd, unsigned soff) {
  asm volatile("s_mov_b32 m0, %0\n\ts_nop 0\n\tbuffer_load_dwordx4 %1, %2, %3 offen " KV_AUX "lds" :: "s"(lds), "v"(voff), "s"(srd), "s"(soff) : "memory");
}
DI void dma4(unsigned lds, unsigned v0, unsigned v1, unsigned v2, unsigned v3, srd_t srd, unsigned soff) {
  asm volatile("s_mov_b32 m0, %0\n\ts_nop 0\n\tbuffer_load_dwordx4 %1, %5, %6 offen lds\n\tbuffer_load_dwordx4 %2, %5, %6 offen offset:1024 lds\n\t"
               "buffer_load_dwordx4 %3, %5, %6 offen offset:2048 lds\n\tbuffer_load_dwordx4 %4, %5, %6 offen offset:3072 lds"
               :: "s"(lds), "v"(v0), "v"(v1), "v"(v2), "v"(v3), "s"(srd), "s"(soff) : "memory");
}
DI void dma4_nt(unsigned lds, unsigned v0, unsigned v1, unsigned v2, unsigned v3, srd_t srd, unsigned soff) {
  asm volatile("s_mov_b32 m0, %0\n\ts_nop 0\n\tbuffer_load_dwordx4 %1, %5, %6 offen " KV_AUX "lds\n\tbuffer_load_dwordx4 %2, %5, %6 offen offset:1024 " KV_AUX "lds\n\t"
               "buffer_load_dwordx4 %3, %5, %6 offen offset:2048 " KV_AUX "lds\n\tbuffer_load_dwordx4 %4, %5, %6 offen offset:3072 " KV_AUX "lds"
               :: "s"(lds), "v"(v0), "v"(v1), "v"(v2), "v"(v3), "s"(srd), "s"(soff) : "memory");
}
DI void bstore_sc1(const u32x4& d, unsigned voff, srd_t srd, unsigned soff) { asm volatile("buffer_store_dwordx4 %0, %1, %2, %3 offen " ST_AUX "\n\ts_nop 1" :: "v"(d), "v"(voff), "s"(srd), "s"(soff) : "memory"); }
DI void bload_sc1(bf16x8& d, unsigned voff, srd_t srd, unsigned soff) { asm volatile("buffer_load_dwordx4 %0, %1, %2, %3 offen " LD_AUX : "=v"(d) : "v"(voff), "s"(srd), "s"(soff) : "memory"); }
DI void bload(bf16x8& d, unsigned voff, srd_t srd, unsigned soff) { asm volatile("buffer_load_dwordx4 %0, %1, %2, %3 offen" : "=v"(d) : "v"(voff), "s"(srd), "s"(soff) : "memory"); }
DI void bload_nt(bf16x8& d, unsigned voff, srd_t srd, unsigned soff) { asm volatile("buffer_load_dwordx4 %0, %1, %2, %3 offen nt" : "=v"(d) : "v"(voff), "s"(srd), "s"(soff) : "memory"); }
DI void touch(unsigned& d, unsigned voff, srd_t srd, unsigned soff) { asm volatile("buffer_load_dword %0, %1, %2, %3 offen " TOUCH_AUX : "+v"(d) : "v"(voff), "s"(srd), "s"(soff) : "memory"); }
DI void pin(bf16x8& a) { asm volatile("" : "+v"(a)); }
DI void pinf(float& a) { asm volatile("" : "+v"(a)); }
DI void pinu(unsigned& a) { asm volatile("" : "+v"(a)); }
template <int N> DI void wait_vm2(bf16x8& a, bf16x8& b) { asm volatile("s_waitcnt vmcnt(%2)" : "+v"(a), "+v"(b) : "n"(N) : "memory"); }
template <int N> DI void wait_vm1(bf16x8& a) { asm volatile("s_waitcnt vmcnt(%1)" : "+v"(a) : "n"(N) : "memory"); }
template <int N> DI void wait_bar() { asm volatile("s_waitcnt vmcnt(%0) lgkmcnt(0)\n\ts_barrier" :: "n"(N) : "memory"); }
template <int N> DI void wait_vm() { asm volatile("s_waitcnt vmcnt(%0)" :: "n"(N) : "memory"); }
DI void bar() { asm volatile("s_waitcnt lgkmcnt(0)\n\ts_barrier" ::: "memory"); }
typedef short v4i16_t __attribute__((ext_vector_type(4)));
DI s16x4 trd(lds_cptr p) { return __builtin_amdgcn_ds_read_tr16_b64_v4i16((__attribute__((address_space(3))) v4i16_t*)p); }
DI bf16x8 trd2(lds_cptr p, int o0, int o1) { s16x4 a = trd(p + o0), b = trd(p + o1); return __builtin_shufflevector(a, b, 0, 1, 2, 3, 4, 5, 6, 7); }
DI bf16x8 rd128(lds_cptr p) { return *(const __attribute__((address_space(3))) bf16x8*)p; }
DI float swap_max(float m) { auto rr = __builtin_amdgcn_permlane32_swap(__float_as_uint(m), __float_as_uint(m), false, false); return fmaxf(__uint_as_float(rr[0]), __uint_as_float(rr[1])); }
DI float swap_add(float m) { auto rr = __builtin_amdgcn_permlane32_swap(__float_as_uint(m), __float_as_uint(m), false, false); return __uint_as_float(rr[0]) + __uint_as_float(rr[1]); }
DI float max16(const f32x16& s) { float a = fmaxf(fmaxf(s[0], s[1]), s[2]), b = fmaxf(fmaxf(s[3], s[4]), s[5]);
  a = fmaxf(fmaxf(a, s[6]), s[7]); b = fmaxf(fmaxf(b, s[8]), s[9]); a = fmaxf(fmaxf(a, s[10]), s[11]); b = fmaxf(fmaxf(b, s[12]), s[13]); a = fmaxf(fmaxf(a, s[14]), s[15]); return fmaxf(a, b); }
DI float fadd(float a, float b) { float r; asm("v_add_f32_e32 %0, %1, %2" : "=v"(r) : "v"(a), "v"(b)); return r; }

DI float expo(const f32x16& s, u32x4 (&p)[2]) {
  float e[16]; float s0 = 0.f, s1 = 0.f;
#pragma unroll
  for (int i = 0; i < 16; ++i) e[i] = __builtin_amdgcn_exp2f(s[i]);
#pragma unroll
  for (int i = 0; i < 16; i += 2) { s0 += e[i]; s1 += e[i + 1]; }
#pragma unroll
  for (int i = 0; i < 8; ++i) p[i >> 2][i & 3] = cvtpk(e[2 * i], e[2 * i + 1]);
  return s0 + s1;
}

DI unsigned rt32_() { unsigned long long t_; asm volatile("s_memrealtime %0\n\ts_waitcnt lgkmcnt(0)" : "=s"(t_) :: "memory"); return (unsigned)t_; }
#define ST_DECL unsigned dga = 0, dgb = 0;
#define ST(k) do { if (wid == 7u) { if ((k) == DIAG_A) dga = rt32_(); if ((k) == DIAG_B) dgb = rt32_(); } } while (0)
#define ST_OUT(ws, lo, hi) do { if (wid == 7u) { const unsigned e_ = rt32_() + (dgb - dga); for (int i_ = 0; i_ < 100000 && (int)(rt32_() - e_) < 0; ++i_) ; } } while (0)

constexpr unsigned SLOTK = 0x04204209u;
constexpr unsigned SLOTV = 0x31531531u;
constexpr unsigned STG = 45056u, STG_W = 28672u;
constexpr unsigned O_BYTES = NB * SQ * NH * DH * 2;
constexpr unsigned X_OFF = O_BYTES, X_BYTES = 63u * 8u * 4u * 8192u;
constexpr unsigned FLAG_OFF = X_OFF + X_BYTES, FLAG_STRIDE = 128, FLAG2_OFF = FLAG_OFF + 256 * FLAG_STRIDE, EPOCH_OFF = FLAG2_OFF + 256 * FLAG_STRIDE;
constexpr unsigned SPIN_CAP = 1u << 20;
#define RA 3
constexpr unsigned SLOTW = 0x05429876u;
#define WO 6
#define QROT 1
#define HROT 1
#define RETOUCH 1
typedef __attribute__((address_space(1))) unsigned long long gu64;
DI unsigned long long mix64(unsigned long long z) { z += 0x9E3779B97F4A7C15ull; z = (z ^ (z >> 30)) * 0xBF58476D1CE4E5B9ull; z = (z ^ (z >> 27)) * 0x94D049BB133111EBull; return z ^ (z >> 31); }
DI unsigned long long ld_agent(const char* p) { return __hip_atomic_load((gu64*)p, __ATOMIC_RELAXED, __HIP_MEMORY_SCOPE_AGENT); }
DI void st_agent(char* p, unsigned long long v) { __hip_atomic_store((gu64*)p, v, __ATOMIC_RELAXED, __HIP_MEMORY_SCOPE_AGENT); }
DI void poll_flags(const char* p, unsigned long long tag) {
  for (unsigned spins = 0; spins < SPIN_CAP; ++spins) { if (__all(ld_agent(p) == tag)) break; __builtin_amdgcn_s_sleep(2); }
  asm volatile("" ::: "memory");
}

__global__ __launch_bounds__(512, 1) void mha_kernel(const unsigned short* __restrict__ x, const unsigned short* __restrict__ kc, const unsigned short* __restrict__ vc,
                                                     const unsigned short* __restrict__ wq, const unsigned short* __restrict__ wo, char* ws, unsigned short* __restrict__ out) {
  extern __shared__ __attribute__((aligned(16))) char lds[];
  unsigned long long pc0; asm volatile("s_getpc_b64 %0" : "=s"(pc0));
  const unsigned tid = threadIdx.x, lane = tid & 63u, r = lane & 31u, hh = lane >> 5;
  const unsigned wid = rfl(tid >> 6);
  ST_DECL ST(0);
  const unsigned bid = blockIdx.x, xcd = bid & 7u, ix = bid >> 3;
  const unsigned b = (xcd & 1u) * 4u + (ix & 3u), h = (xcd >> 1) * 8u + (ix >> 2);
  const unsigned lds0 = (unsigned)(uintptr_t)lds;
  const unsigned prow = (lane >> 2) & 7u, pch = 4u * hh + (lane & 3u);
  const unsigned q4 = (lane & 15u) >> 2, p4 = lane & 3u, blk = (lane >> 4) & 1u;
  const unsigned trcol = 32u * blk + 16u * (p4 & 1u) + 8u * (p4 >> 1);
  const srd_t sO = mk_srd(ws, O_BYTES);
  unsigned long long tag;
  const unsigned g4 = xcd >> 1, u0 = (xcd & 1u) * 32u + ix;
  const bool has_tile = u0 < 63u;
  const unsigned u = has_tile ? u0 : 62u, rt = u / 9u, ct = u - rt * 9u;

  if (wid == 7u) {
    const srd_t sK = mk_srd(kc + (size_t)(b * NH + h) * SK * DH, SK * DH * 2);
    const srd_t sV = mk_srd(vc + (size_t)(b * NH + h) * SK * DH, SK * DH * 2);
    const srd_t sW = mk_srd(wq, DM * NH * DH * 2);
    const srd_t sX = mk_srd(x + (size_t)b * SQ * DM, SQ * DM * 2);
    const unsigned pchx0 = 4u * hh + ((lane & 3u) ^ ((lane >> 4) & 1u)), pchx1 = pchx0 ^ 2u;
    const unsigned vV = prow * 256u + pch * 16u, vK0 = prow * 256u + pchx0 * 16u, vK1 = prow * 256u + pchx1 * 16u;
    const unsigned vW = prow * 8192u + pch * 16u, vX0 = prow * (DM * 2u) + pchx0 * 16u, vX1 = prow * (DM * 2u) + pchx1 * 16u;
    auto fillK = [&](unsigned t) { const unsigned so = t * 16384u, ld = lds0 + ((SLOTK >> (4u * t)) & 15u) * 16384u;
      dma_nt(ld, vK0, sK, so); dma_nt(ld + 1024u, vK0, sK, so + 128u); dma_nt(ld + 2048u, vK1, sK, so + 2048u); dma_nt(ld + 3072u, vK1, sK, so + 2176u);
#pragma unroll
      for (int i = 1; i < 4; ++i) dma4_nt(ld + i * 4096u, vK0 + 1024u, vK0 + 128u, vK1 + 1024u, vK1 + 128u, sK, so + i * 4096u - 1024u); };
    auto fillV = [&](unsigned t) { const unsigned so = t * 16384u, ld = lds0 + ((SLOTV >> (4u * t)) & 15u) * 16384u;
      dma_nt(ld, vV, sV, so); dma_nt(ld + 1024u, vV, sV, so + 128u); dma_nt(ld + 2048u, vV, sV, so + 2048u); dma_nt(ld + 3072u, vV, sV, so + 2176u);
#pragma unroll
      for (int i = 1; i < 4; ++i) dma4_nt(ld + i * 4096u, vV + 1024u, vV + 128u, vV + 1024u, vV + 128u, sV, so + i * 4096u - 1024u); };
    auto fillQ = [&](unsigned c, unsigned stage) {
      const unsigned lw = lds0 + stage + STG_W, sw = c * 64u * 8192u + h * 256u, lx = lds0 + stage, sx = c * 128u;
      dma(lw, vW, sW, sw); dma(lw + 1024u, vW, sW, sw + 128u); dma(lw + 2048u, vW, sW, sw + 65536u); dma(lw + 3072u, vW, sW, sw + 65664u);
#pragma unroll
      for (int i = 1; i < 4; ++i) dma4(lw + i * 4096u, vW + 1024u, vW + 128u, vW + 65536u - 1024u, vW + 65664u - 2048u, sW, sw + i * 131072u - 1024u);
#pragma unroll
      for (int i = 0; i < 7; ++i) dma4(lx + i * 4096u, vX0, vX1 + 8192u, vX0 + 16384u, vX1 + 24576u, sX, sx + i * 32u * DM * 2u); };
    const srd_t sWo = mk_srd(wo, NH * DH * DM * 2);
    const unsigned vWo = prow * (DM * 2u) + pch * 16u;
    auto fillWo = [&](unsigned i) { const unsigned ld = lds0 + ((SLOTW >> (4u * i)) & 15u) * 16384u, so = (g4 * 8u + i) * 128u * (DM * 2u) + ct * 128u;
#pragma unroll
      for (int k = 0; k < 4; ++k) dma4(ld + k * 4096u, vWo, vWo + 8192u, vWo + 16384u, vWo + 24576u, sWo, so + k * 32u * (DM * 2u)); };
    const unsigned rot = QROT ? 3u * (((ix & 3u) + (ix >> 2)) % 3u) : 0u;
    auto chunk = [&](unsigned c) { const unsigned k = c + rot; return k >= 9u ? k - 9u : k; };
    fillQ(chunk(0), 0);
    { unsigned stage = STG;
#pragma nounroll
      for (unsigned c = 1; c < 8; ++c) { fillQ(chunk(c), stage); stage = stage == 2 * STG ? 0u : stage + STG; wait_vm<44>(); bar(); ST(1 + c); } }
    fillQ(chunk(8), 2 * STG); fillK(0); wait_vm<60>(); bar(); ST(9);
    fillK(1); fillV(0); wait_vm<48>(); bar(); ST(10);
    fillK(2); wait_vm<48>(); bar(); ST(11);
    fillV(1); wait_vm<32>(); bar(); ST(12);
#pragma nounroll
    for (unsigned t = 1; t < 5; ++t) { fillWo(t - 1u); fillK(t + 2u); fillV(t + 1u); wait_vm<48>(); bar(); ST(12 + t); }
    fillK(7); fillV(6); wait_vm<32>(); bar(); ST(17);
    fillV(7); fillWo(4); wait_vm<32>(); bar(); ST(18);
    fillWo(5); fillWo(6); wait_vm<48>(); bar(); ST(19);
    fillWo(7);
    tag = mix64(ld_agent(ws + EPOCH_OFF));
    ST(20); ST(21);
  } else {
  unsigned cdummy = 0;
  if (CODEPF) {
    const srd_t sC = mk_srd((const void*)(pc0 & ~255ull), CODEPF);
    touch(cdummy, lane * 64u, sC, wid * 4096u); }
  tag = mix64(ld_agent(ws + EPOCH_OFF));
  tag = ((unsigned long long)rfl((unsigned)(tag >> 32)) << 32) | rfl((unsigned)tag);
  const unsigned rsw0 = 64u * (r & 7u) + 16u * (hh ^ ((r >> 2) & 3u)), rsw1 = rsw0 ^ 32u;
  f32x16 qa[4] = {}, o[4] = {};
  asm volatile("" : "+v"(qa[0]), "+v"(qa[1]), "+v"(qa[2]), "+v"(qa[3]), "+v"(o[0]), "+v"(o[1]), "+v"(o[2]), "+v"(o[3]));
  __builtin_amdgcn_s_waitcnt(0x0070);
  wait_vm<0>(); pinu(cdummy);
  ST(1);
  bar();
  ST(2);
  { lds_cptr wp = (lds_cptr)lds + STG_W + 2048u * hh + 64u * q4 + trcol;
    lds_cptr xp0 = (lds_cptr)lds + wid * 4096u + 1024u * (r >> 3) + rsw0, xp1 = (lds_cptr)lds + wid * 4096u + 1024u * (r >> 3) + rsw1;
    bf16x8 xf[4], af[5];
    auto rdA = [&](int i) { return trd2(wp, 4096 * (i >> 2) + 512 * (i & 3), 4096 * (i >> 2) + 512 * (i & 3) + 256); };
    xf[0] = rd128(xp0); af[0] = rdA(0); af[1] = rdA(1); af[2] = rdA(2); af[3] = rdA(3);
    unsigned stage = 0;
#pragma nounroll
    for (unsigned c = 0; c < 9; ++c) {
      SBAR();
      static_for<12>([&](auto I) { constexpr int i = I.value;
        af[(i + 4) % 5] = rdA(i + 4);
        if constexpr (i == 0) xf[1] = rd128(xp1); if constexpr (i == 3) xf[2] = rd128(xp0 + 512); if constexpr (i == 7) xf[3] = rd128(xp1 + 512);
        qa[i & 3] = MFMA(af[i % 5], xf[i >> 2], qa[i & 3]); SBAR(); });
      qa[0] = MFMA(af[12 % 5], xf[3], qa[0]); SBAR();
      bar();
      const unsigned nstage = stage == 2 * STG ? 0u : stage + STG; const int dlt = (int)nstage - (int)stage; stage = nstage;
      wp += dlt; xp0 += dlt; xp1 += dlt;
      const bf16x8 xl = xf[3], a13 = af[13 % 5], a14 = af[14 % 5], a15 = af[15 % 5];
      xf[0] = rd128(xp0); af[0] = rdA(0); af[1] = rdA(1);
      SBAR();
      qa[1] = MFMA(a13, xl, qa[1]); SBAR();
      af[2] = rdA(2);
      qa[2] = MFMA(a14, xl, qa[2]); SBAR();
      af[3] = rdA(3);
      qa[3] = MFMA(a15, xl, qa[3]); SBAR();
      ST(3 + c);
    } }

  const lds_cptr kb0 = (lds_cptr)lds + 2048u * (r >> 3) + rsw0, kb1 = (lds_cptr)lds + 2048u * (r >> 3) + rsw1;
  const lds_cptr vb = (lds_cptr)lds + 64u * (4u * hh + q4) + trcol;
  bf16x8 qf[8], pre[RA];
  f32x16 negm, S0, S1; u32x4 P0[2], P1[2]; float l = 0.f;
  auto rdK = [&](lds_cptr k0, lds_cptr k1, int kb, int i) { return rd128(((i & 1) ? k1 : k0) + 8192 * kb + 512 * (i >> 1)); };
  auto rdV = [&](lds_cptr v, int kb, int j) { return trd2(v, 8192 * kb + 4096 * (j >> 2) + 512 * (j & 3), 8192 * kb + 4096 * (j >> 2) + 2048 + 512 * (j & 3)); };
  auto sm_fix = [&](float ps, f32x16& Sc, f32x16& Sn, u32x4 (&P)[2]) {
    if (__builtin_expect(__any(!(ps <= SUM_THR)), 0)) {
      const float dl = fmaxf(swap_max(max16(Sc)), 0.f), f = __builtin_amdgcn_exp2f(-dl);
#pragma unroll
      for (int i = 0; i < 16; ++i) { Sc[i] -= dl; Sn[i] -= dl; negm[i] -= dl; }
#pragma unroll
      for (int db = 0; db < 4; ++db)
#pragma unroll
        for (int i = 0; i < 16; ++i) o[db][i] *= f;
      l *= f; ps = expo(Sc, P); }
    l += ps; };
  {
    lds_cptr k0 = kb0 + 9 * 16384, k1 = kb1 + 9 * 16384; asm volatile("" : "+v"(k0), "+v"(k1));
    bf16x8 f[16];
#pragma unroll
    for (int i = 0; i < 4; ++i) f[i] = rdK(k0, k1, 0, i);
    auto cvq = [&](int g) { u32x4 w;
#pragma unroll
      for (int e = 0; e < 4; ++e) w[e] = cvtpk(qa[g >> 1][8 * (g & 1) + 2 * e] * QSCALE, qa[g >> 1][8 * (g & 1) + 2 * e + 1] * QSCALE);
      qf[g] = __builtin_bit_cast(bf16x8, w); pin(qf[g]); };
    cvq(0); SBAR();
    static_for<8>([&](auto G) { constexpr int g = G.value;
      f[g + 4] = rdK(k0, k1, (g + 4) >> 3, (g + 4) & 7);
      if constexpr (g == 0) { const f32x16 z = {}; S0 = MFMA(f[0], qf[0], z); } else S0 = MFMA(f[g], qf[g], S0);
      if constexpr (g < 7) cvq(g + 1);
      SBAR(); });
#pragma unroll
    for (int i = 12; i < 16; ++i) f[i] = rdK(k0, k1, 1, i & 7);
    const float m = swap_max(max16(S0));
#pragma unroll
    for (int i = 0; i < 16; ++i) negm[i] = -m;
    bar();
    { const unsigned ko = ((SLOTK >> 4) & 15u) * 16384u;
#pragma unroll
      for (int i = 0; i < RA; ++i) pre[i] = rdK(kb0 + ko, kb1 + ko, 0, i); }
    SBAR();
    float e[16]; float s0 = 0.f, s1 = 0.f;
    static_for<9>([&](auto G) { constexpr int g = G.value;
      if constexpr (g == 0) S1 = MFMA(f[8], qf[0], negm); else if constexpr (g < 8) S1 = MFMA(f[8 + g], qf[g], S1);
      if constexpr (g < 8) { e[2 * g] = __builtin_amdgcn_exp2f(S0[2 * g] - m); e[2 * g + 1] = __builtin_amdgcn_exp2f(S0[2 * g + 1] - m); pinf(e[2 * g]); pinf(e[2 * g + 1]); }
      if constexpr (g == 1) { s0 = e[0]; s1 = e[1]; } else if constexpr (g > 1) { s0 = fadd(s0, e[2 * g - 2]); s1 = fadd(s1, e[2 * g - 1]); pinf(s0); pinf(s1); }
      if constexpr (g >= 1) { unsigned w = cvtpk(e[2 * g - 2], e[2 * g - 1]); pinu(w); P0[(g - 1) >> 2][(g - 1) & 3] = w; }
      SBAR(); });
    l = s0 + s1; }
  ST(12);
  auto unit = [&](auto KB, auto LAST, lds_cptr k0, lds_cptr k1, lds_cptr v, lds_cptr nk0, lds_cptr nk1, f32x16& Sn, f32x16& Sc, const u32x4 (&Pp)[2], u32x4 (&Pc)[2]) {
    constexpr int kb = KB.value; constexpr bool last = LAST.value;
    float e[16]; float s0 = 0.f, s1 = 0.f; bf16x8 f[16 + RA];
#pragma unroll
    for (int i = 0; i < RA; ++i) f[i] = pre[i];
    static_for<16>([&](auto G) { constexpr int g = G.value; constexpr int n = g + RA; constexpr int gb = 16 - RA + 1;
      if constexpr (last && g == gb) { bar(); f[16] = rdK(nk0, nk1, 0, 0); }
      if constexpr (n < 8) f[n] = rdK(k0, k1, kb, n); else if constexpr (n < 16) f[n] = rdV(v, kb, n - 8);
      else if constexpr (!last) f[n] = rdK(k0, k1, kb + 1, n - 16);
      else if constexpr (n > 16) f[n] = rdK(nk0, nk1, 0, n - 16);
      if constexpr (g == 0) Sn = MFMA(f[0], qf[0], negm); else if constexpr (g < 8) Sn = MFMA(f[g], qf[g], Sn);
      else o[g & 3] = MFMA(f[g], __builtin_bit_cast(bf16x8, Pp[(g >> 2) & 1]), o[g & 3]);
      constexpr int e0 = g < 12 ? (g / 3) * 4 + (g % 3 == 0 ? 0 : g % 3 + 1) : 16, e1 = g + 1 < 12 ? ((g + 1) / 3) * 4 + ((g + 1) % 3 == 0 ? 0 : (g + 1) % 3 + 1) : 16;
      constexpr int p0 = g >= 1 ? (g - 1 < 12 ? ((g - 1) / 3) * 4 + ((g - 1) % 3 == 0 ? 0 : (g - 1) % 3 + 1) : 16) : 0, p1 = g >= 1 ? e0 : 0;
      static_for<e1 - e0>([&](auto J) { constexpr int i = e0 + J.value; e[i] = __builtin_amdgcn_exp2f(Sc[i]); pinf(e[i]); });
      static_for<p1 - p0>([&](auto J) { constexpr int i = p0 + J.value;
        if constexpr (i == 0) s0 = e[0]; else if constexpr (i == 1) s1 = e[1]; else if constexpr (i & 1) { s1 = fadd(s1, e[i]); pinf(s1); } else { s0 = fadd(s0, e[i]); pinf(s0); }
        if constexpr (i & 1) { unsigned w = cvtpk(e[i - 1], e[i]); pinu(w); Pc[i >> 3][(i >> 1) & 3] = w; } });
      SBAR(); });
#pragma unroll
    for (int i = 0; i < RA; ++i) pre[i] = f[16 + i];
    sm_fix(s0 + s1, Sc, Sn, Pc); };
#pragma nounroll
  for (unsigned t = 1; t < 8; ++t) {
    const unsigned ko = ((SLOTK >> (4u * t)) & 15u) * 16384u, vo = ((SLOTV >> (4u * (t - 1u))) & 15u) * 16384u, nko = ((SLOTK >> (4u * ((t + 1u) & 7u))) & 15u) * 16384u;
    SBAR();
    unit(std::integral_constant<int, 0>{}, std::false_type{}, kb0 + ko, kb1 + ko, vb + vo, kb0, kb1, S0, S1, P0, P1);
    unit(std::integral_constant<int, 1>{}, std::true_type{}, kb0 + ko, kb1 + ko, vb + vo, kb0 + nko, kb1 + nko, S1, S0, P1, P0);
    ST(12 + t);
  }
  {
    const lds_cptr v = vb + ((SLOTV >> 28) & 15u) * 16384u;
    bf16x8 f[16];
#pragma unroll
    for (int i = 0; i < 3; ++i) f[i] = rdV(v, 0, i);
    float e[16]; float s0 = 0.f, s1 = 0.f;
    SBAR();
    static_for<9>([&](auto G) { constexpr int g = G.value; constexpr int n = g + 3;
      if constexpr (n < 8) f[n] = rdV(v, 0, n); else if constexpr (g < 8) f[n] = rdV(v, 1, ((n - 8) >> 1) + 4 * ((n - 8) & 1));
      if constexpr (g < 8) { o[g & 3] = MFMA(f[g], __builtin_bit_cast(bf16x8, P0[g >> 2]), o[g & 3]);
        e[2 * g] = __builtin_amdgcn_exp2f(S1[2 * g]); e[2 * g + 1] = __builtin_amdgcn_exp2f(S1[2 * g + 1]); pinf(e[2 * g]); pinf(e[2 * g + 1]); }
      if constexpr (g == 1) { s0 = e[0]; s1 = e[1]; } else if constexpr (g > 1) { s0 = fadd(s0, e[2 * g - 2]); s1 = fadd(s1, e[2 * g - 1]); pinf(s0); pinf(s1); }
      if constexpr (g >= 1) { unsigned w = cvtpk(e[2 * g - 2], e[2 * g - 1]); pinu(w); P1[(g - 1) >> 2][(g - 1) & 3] = w; }
      SBAR(); });
    sm_fix(s0 + s1, S1, S0, P1);
    const float inv = __builtin_amdgcn_rcpf(swap_add(l));
    const unsigned so = ((b * 7u + wid) * NH + h) * 8192u, vo = lane * 16u;
    auto put = [&](int k8) { u32x4 w;
#pragma unroll
      for (int e2 = 0; e2 < 4; ++e2) w[e2] = cvtpk(o[k8 >> 1][8 * (k8 & 1) + 2 * e2] * inv, o[k8 >> 1][8 * (k8 & 1) + 2 * e2 + 1] * inv);
      bstore_sc1(w, vo, sO, so + k8 * 1024u); };
    static_for<8>([&](auto G) { constexpr int g = G.value; constexpr int n = g + 11;
      if constexpr (n < 16) f[n] = rdV(v, 1, ((n - 8) >> 1) + 4 * ((n - 8) & 1));
      o[g >> 1] = MFMA(f[8 + g], __builtin_bit_cast(bf16x8, P1[g & 1]), o[g >> 1]);
      if constexpr (g >= 4) put(g - 4);
      SBAR(); });
    ST(20);
    put(4); put(5); put(6); put(7); }
  ST(21);
  }
  wait_vm<0>();
  ST(22);
  bar();
  if (tid == 0) st_agent(ws + FLAG_OFF + (b * NH + h) * FLAG_STRIDE, tag);
  ST(23);

  unsigned dummy = 0;
  if (RETOUCH && wid < 7u) touch(dummy, lane * 128u, sO, ((b * 7u + wid) * NH + h) * 8192u);
  if (!has_tile) {
    wait_vm<0>(); pinu(dummy);
    if (bid == 249u && wid == 0u) {
#pragma unroll
      for (int i = 0; i < 4; ++i) poll_flags(ws + FLAG_OFF + (i * 64u + lane) * FLAG_STRIDE, tag);
      if (lane == 0) st_agent(ws + EPOCH_OFF, tag); }
    ST(24); ST(25); ST(26); ST(27); ST(28); ST(29); ST(30); ST(31); ST_OUT(ws, 0, 32); return; }
  const unsigned jq = (2u * g4 + 2u + wid) & 7u, G = 8u * rt + jq;
  poll_flags(ws + FLAG_OFF + (((G * 37u) >> 8) * NH + g4 * 8u + (lane & 7u)) * FLAG_STRIDE, tag);
  wait_vm<0>(); pinu(dummy);
  ST(24);
  const unsigned voffO = lane * 16u, soO = (G * NH + g4 * 8u) * 8192u, hr = HROT ? ct : 0u;
  const lds_cptr wb = (lds_cptr)lds + 1024u * hh + 64u * q4 + trcol;
  bf16x8 of[64], lf[64][2];
  auto issueO = [&](auto S) { constexpr int st = S.value; bload_sc1(of[st], voffO, sO, soO + (((st >> 3) + hr) & 7u) * 8192u + (st & 7) * 1024u); };
  auto rdW = [&](auto S) { constexpr int st = S.value; const lds_cptr p = wb + ((SLOTW >> (4u * (((st >> 3) + hr) & 7u))) & 15u) * 16384u;
    lf[st][0] = trd2(p, (st & 7) * 2048, (st & 7) * 2048 + 256); lf[st][1] = trd2(p, (st & 7) * 2048 + 512, (st & 7) * 2048 + 768); };
  static_for<WO>(issueO);
  rdW(std::integral_constant<int, 0>{}); rdW(std::integral_constant<int, 1>{});
  f32x16 acc[2] = {};
  static_for<64>([&](auto S) { constexpr int st = S.value;
    wait_vm1<(st + WO <= 64 ? WO - 1 : 63 - st)>(of[st]);
    if constexpr (st == 16) ST(25); if constexpr (st == 32) ST(26); if constexpr (st == 48) ST(27);
    if constexpr (st + 2 < 64) rdW(std::integral_constant<int, st + 2>{});
    acc[0] = MFMA(lf[st][0], of[st], acc[0]); acc[1] = MFMA(lf[st][1], of[st], acc[1]);
    SBAR();
    if constexpr (st + WO < 64) issueO(std::integral_constant<int, st + WO>{});
    SBAR(); });
  ST(28);
  const srd_t sX2 = mk_srd(ws + X_OFF, X_BYTES);
  if (wid < 6u) {
#pragma unroll
    for (int i = 0; i < 8; ++i) { const f32x4 v = {acc[i >> 2][4 * (i & 3)], acc[i >> 2][4 * (i & 3) + 1], acc[i >> 2][4 * (i & 3) + 2], acc[i >> 2][4 * (i & 3) + 3]};
      f32x4 v2 = v + 0.f; asm volatile("" : "+v"(v2));
      bstore_sc1(__builtin_bit_cast(u32x4, v2), voffO, sX2, ((u * 8u + jq) * 4u + g4) * 8192u + i * 1024u); }
    wait_vm<0>(); }
  bar();
  if (tid == 0) st_agent(ws + FLAG2_OFF + (u * 4u + g4) * FLAG_STRIDE, tag);
  ST(29);
  if (wid >= 6u) {
    poll_flags(ws + FLAG2_OFF + (u * 4u + (lane & 3u)) * FLAG_STRIDE, tag);
    ST(30);
    bf16x8 xp[3][8];
#pragma unroll
    for (int k = 0; k < 3; ++k)
#pragma unroll
      for (int i = 0; i < 8; ++i) bload_sc1(xp[k][i], voffO, sX2, ((u * 8u + jq) * 4u + ((g4 + 1u + k) & 3u)) * 8192u + i * 1024u);
    wait_vm<0>();
#pragma unroll
    for (int k = 0; k < 3; ++k)
#pragma unroll
      for (int i = 0; i < 8; ++i) { pin(xp[k][i]); const f32x4 v = __builtin_bit_cast(f32x4, xp[k][i]);
#pragma unroll
        for (int e = 0; e < 4; ++e) acc[i >> 2][4 * (i & 3) + e] += v[e]; }
    char* op = (char*)out + (size_t)(G * 32u + r) * (DM * 2u) + ct * 128u + hh * 16u;
#pragma unroll
    for (int i = 0; i < 4; ++i) { const u32x4 w4 = {cvtpk(acc[i >> 1][8 * (i & 1)], acc[i >> 1][8 * (i & 1) + 1]), cvtpk(acc[i >> 1][8 * (i & 1) + 2], acc[i >> 1][8 * (i & 1) + 3]),
                                                    cvtpk(acc[i >> 1][8 * (i & 1) + 4], acc[i >> 1][8 * (i & 1) + 5]), cvtpk(acc[i >> 1][8 * (i & 1) + 6], acc[i >> 1][8 * (i & 1) + 7])};
      *(u32x4*)(op + i * 32) = w4; }
  } else { ST(30); }
  ST(31); ST_OUT(ws, 0, 32);
}

extern "C" void kernel_launch(void* const* d_in, const int* in_sizes, int n_in, void* d_out, int out_size, void* d_ws, size_t ws_size, hipStream_t stream) {
  (void)in_sizes; (void)n_in; (void)out_size; (void)ws_size;
  (void)hipFuncSetAttribute((const void*)mha_kernel, hipFuncAttributeMaxDynamicSharedMemorySize, LDS_BYTES);
  const unsigned short* x = (const unsigned short*)d_in[0]; const unsigned short* kc = (const unsigned short*)d_in[1]; const unsigned short* vc = (const unsigned short*)d_in[2];
  const unsigned short* wq = (const unsigned short*)d_in[3]; const unsigned short* wo = (const unsigned short*)d_in[4];
  mha_kernel<<<256, 512, LDS_BYTES, stream>>>(x, kc, vc, wq, wo, (char*)d_ws, (unsigned short*)d_out);
}
```

```cpp
#define TLBW 1
#include <hip/hip_runtime.h>
#include <stdint.h>
#include <utility>

typedef short    bf16x8 __attribute__((ext_vector_type(8)));
typedef short    s16x4  __attribute__((ext_vector_type(4)));
typedef float    f32x16 __attribute__((ext_vector_type(16)));
typedef float    f32x4  __attribute__((ext_vector_type(4)));
typedef float    f32x2  __attribute__((ext_vector_type(2)));
typedef __bf16   bf16x2 __attribute__((ext_vector_type(2)));
typedef unsigned u32x4  __attribute__((ext_vector_type(4)));
typedef __attribute__((address_space(3))) const char* lds_cptr;
typedef __attribute__((address_space(3))) char* lds_ptr;
typedef __amdgpu_buffer_rsrc_t srd_t;

#define KV_AUX "nt "
#define CODEPF 0
#define TOUCH_AUX ""
#define ST_AUX "sc1"
#define LD_AUX "sc1"
#define DI __device__ __forceinline__
#define MFMA(a, b, c) __builtin_amdgcn_mfma_f32_32x32x16_bf16((a), (b), (c), 0, 0, 0)
#define SBAR() __builtin_amdgcn_sched_barrier(0)

constexpr int NB = 8, SQ = 224, DM = 576, NH = 32, SK = 512, DH = 128;
constexpr int LDS_BYTES = 163840;
constexpr float QSCALE = 0.08838834764831845f * 1.4426950408889634f;
constexpr float SUM_THR = 4096.f;

template <int... Is, class F> DI void static_for_impl(std::integer_sequence<int, Is...>, F&& f) { (f(std::integral_constant<int, Is>{}), ...); }
template <int N, class F> DI void static_for(F&& f) { static_for_impl(std::make_integer_sequence<int, N>{}, f); }

DI unsigned rfl(unsigned x) { return __builtin_amdgcn_readfirstlane(x); }
DI unsigned cvtpk(float lo, float hi) { f32x2 v = {lo, hi}; bf16x2 b = __builtin_convertvector(v, bf16x2); return __builtin_bit_cast(unsigned, b); }
DI srd_t mk_srd(const void* p, unsigned bytes) { return __builtin_amdgcn_make_buffer_rsrc((void*)p, (short)0, (int)bytes, 0x00020000); }
DI void dma(unsigned lds, unsigned voff, srd_t srd, unsigned soff) {
  asm volatile("s_mov_b32 m0, %0\n\ts_nop 0\n\tbuffer_load_dwordx4 %1, %2, %3 offen lds" :: "s"(lds), "v"(voff), "s"(srd), "s"(soff) : "memory");
}
DI void dma_nt(unsigned lds, unsigned voff, srd_t srd, unsigned soff) {
  asm volatile("s_mov_b32 m0, %0\n\ts_nop 0\n\tbuffer_load_dwordx4 %1, %2, %3 offen " KV_AUX "lds" :: "s"(lds), "v"(voff), "s"(srd), "s"(soff) : "memory");
}
DI void dma4(unsigned lds, unsigned v0, unsigned v1, unsigned v2, unsigned v3, srd_t srd, unsigned soff) {
  asm volatile("s_mov_b32 m0, %0\n\ts_nop 0\n\tbuffer_load_dwordx4 %1, %5, %6 offen lds\n\tbuffer_load_dwordx4 %2, %5, %6 offen offset:1024 lds\n\t"
               "buffer_load_dwordx4 %3, %5, %6 offen offset:2048 lds\n\tbuffer_load_dwordx4 %4, %5, %6 offen offset:3072 lds"
               :: "s"(lds), "v"(v0), "v"(v1), "v"(v2), "v"(v3), "s"(srd), "s"(soff) : "memory");
}
DI void dma4_nt(unsigned lds, unsigned v0, unsigned v1, unsigned v2, unsigned v3, srd_t srd, unsigned soff) {
  asm volatile("s_mov_b32 m0, %0\n\ts_nop 0\n\tbuffer_load_dwordx4 %1, %5, %6 offen " KV_AUX "lds\n\tbuffer_load_dwordx4 %2, %5, %6 offen offset:1024 " KV_AUX "lds\n\t"
               "buffer_load_dwordx4 %3, %5, %6 offen offset:2048 " KV_AUX "lds\n\tbuffer_load_dwordx4 %4, %5, %6 offen offset:3072 " KV_AUX "lds"
               :: "s"(lds), "v"(v0), "v"(v1), "v"(v2), "v"(v3), "s"(srd), "s"(soff) : "memory");
}
DI void bstore_sc1(const u32x4& d, unsigned voff, srd_t srd, unsigned soff) { asm volatile("buffer_store_dwordx4 %0, %1, %2, %3 offen " ST_AUX "\n\ts_nop 1" :: "v"(d), "v"(voff), "s"(srd), "s"(soff) : "memory"); }
DI void bload_sc1(bf16x8& d, unsigned voff, srd_t srd, unsigned soff) { asm volatile("buffer_load_dwordx4 %0, %1, %2, %3 offen " LD_AUX : "=v"(d) : "v"(voff), "s"(srd), "s"(soff) : "memory"); }
DI void bload(bf16x8& d, unsigned voff, srd_t srd, unsigned soff) { asm volatile("buffer_load_dwordx4 %0, %1, %2, %3 offen" : "=v"(d) : "v"(voff), "s"(srd), "s"(soff) : "memory"); }
DI void bload_nt(bf16x8& d, unsigned voff, srd_t srd, unsigned soff) { asm volatile("buffer_load_dwordx4 %0, %1, %2, %3 offen nt" : "=v"(d) : "v"(voff), "s"(srd), "s"(soff) : "memory"); }
DI void touch(unsigned& d, unsigned voff, srd_t srd, unsigned soff) { asm volatile("buffer_load_dword %0, %1, %2, %3 offen " TOUCH_AUX : "+v"(d) : "v"(voff), "s"(srd), "s"(soff) : "memory"); }
DI void pin(bf16x8& a) { asm volatile("" : "+v"(a)); }
DI void pinf(float& a) { asm volatile("" : "+v"(a)); }
DI void pinu(unsigned& a) { asm volatile("" : "+v"(a)); }
template <int N> DI void wait_vm2(bf16x8& a, bf16x8& b) { asm volatile("s_waitcnt vmcnt(%2)" : "+v"(a), "+v"(b) : "n"(N) : "memory"); }
template <int N> DI void wait_vm1(bf16x8& a) { asm volatile("s_waitcnt vmcnt(%1)" : "+v"(a) : "n"(N) : "memory"); }
template <int N> DI void wait_bar() { asm volatile("s_waitcnt vmcnt(%0) lgkmcnt(0)\n\ts_barrier" :: "n"(N) : "memory"); }
template <int N> DI void wait_vm() { asm volatile("s_waitcnt vmcnt(%0)" :: "n"(N) : "memory"); }
DI void bar() { asm volatile("s_waitcnt lgkmcnt(0)\n\ts_barrier" ::: "memory"); }
typedef short v4i16_t __attribute__((ext_vector_type(4)));
DI s16x4 trd(lds_cptr p) { return __builtin_amdgcn_ds_read_tr16_b64_v4i16((__attribute__((address_space(3))) v4i16_t*)p); }
DI bf16x8 trd2(lds_cptr p, int o0, int o1) { s16x4 a = trd(p + o0), b = trd(p + o1); return __builtin_shufflevector(a, b, 0, 1, 2, 3, 4, 5, 6, 7); }
DI bf16x8 rd128(lds_cptr p) { return *(const __attribute__((address_space(3))) bf16x8*)p; }
DI float swap_max(float m) { auto rr = __builtin_amdgcn_permlane32_swap(__float_as_uint(m), __float_as_uint(m), false, false); return fmaxf(__uint_as_float(rr[0]), __uint_as_float(rr[1])); }
DI float swap_add(float m) { auto rr = __builtin_amdgcn_permlane32_swap(__float_as_uint(m), __float_as_uint(m), false, false); return __uint_as_float(rr[0]) + __uint_as_float(rr[1]); }
DI float max16(const f32x16& s) { float a = fmaxf(fmaxf(s[0], s[1]), s[2]), b = fmaxf(fmaxf(s[3], s[4]), s[5]);
  a = fmaxf(fmaxf(a, s[6]), s[7]); b = fmaxf(fmaxf(b, s[8]), s[9]); a = fmaxf(fmaxf(a, s[10]), s[11]); b = fmaxf(fmaxf(b, s[12]), s[13]); a = fmaxf(fmaxf(a, s[14]), s[15]); return fmaxf(a, b); }
DI float fadd(float a, float b) { float r; asm("v_add_f32_e32 %0, %1, %2" : "=v"(r) : "v"(a), "v"(b)); return r; }

DI float expo(const f32x16& s, u32x4 (&p)[2]) {
  float e[16]; float s0 = 0.f, s1 = 0.f;
#pragma unroll
  for (int i = 0; i < 16; ++i) e[i] = __builtin_amdgcn_exp2f(s[i]);
#pragma unroll
  for (int i = 0; i < 16; i += 2) { s0 += e[i]; s1 += e[i + 1]; }
#pragma unroll
  for (int i = 0; i < 8; ++i) p[i >> 2][i & 3] = cvtpk(e[2 * i], e[2 * i + 1]);
  return s0 + s1;
}

#define ST_DECL
#define ST(k)
#define ST_OUT(ws, lo, hi)

constexpr unsigned SLOTK = 0x04204209u;
constexpr unsigned SLOTV = 0x31531531u;
constexpr unsigned STG = 45056u, STG_W = 28672u;
constexpr unsigned O_BYTES = NB * SQ * NH * DH * 2;
constexpr unsigned X_OFF = O_BYTES, X_BYTES = 63u * 8u * 4u * 8192u;
constexpr unsigned FLAG_OFF = X_OFF + X_BYTES, FLAG_STRIDE = 128, FLAG2_OFF = FLAG_OFF + 256 * FLAG_STRIDE, EPOCH_OFF = FLAG2_OFF + 256 * FLAG_STRIDE;
constexpr unsigned SPIN_CAP = 1u << 20;
#define RA 3
constexpr unsigned SLOTW = 0x05429876u;
#define WO 6
#define QROT 1
#define HROT 1
#define RETOUCH 1
typedef __attribute__((address_space(1))) unsigned long long gu64;
DI unsigned long long mix64(unsigned long long z) { z += 0x9E3779B97F4A7C15ull; z = (z ^ (z >> 30)) * 0xBF58476D1CE4E5B9ull; z = (z ^ (z >> 27)) * 0x94D049BB133111EBull; return z ^ (z >> 31); }
DI unsigned long long ld_agent(const char* p) { return __hip_atomic_load((gu64*)p, __ATOMIC_RELAXED, __HIP_MEMORY_SCOPE_AGENT); }
DI void st_agent(char* p, unsigned long long v) { __hip_atomic_store((gu64*)p, v, __ATOMIC_RELAXED, __HIP_MEMORY_SCOPE_AGENT); }
DI void poll_flags(const char* p, unsigned long long tag) {
  for (unsigned spins = 0; spins < SPIN_CAP; ++spins) { if (__all(ld_agent(p) == tag)) break; __builtin_amdgcn_s_sleep(2); }
  asm volatile("" ::: "memory");
}

__global__ __launch_bounds__(512, 1) void mha_kernel(const unsigned short* __restrict__ x, const unsigned short* __restrict__ kc, const unsigned short* __restrict__ vc,
                                                     const unsigned short* __restrict__ wq, const unsigned short* __restrict__ wo, char* ws, unsigned short* __restrict__ out) {
  extern __shared__ __attribute__((aligned(16))) char lds[];
  unsigned long long pc0; asm volatile("s_getpc_b64 %0" : "=s"(pc0));
  const unsigned tid = threadIdx.x, lane = tid & 63u, r = lane & 31u, hh = lane >> 5;
  const unsigned wid = rfl(tid >> 6);
  ST_DECL ST(0);
  const unsigned bid = blockIdx.x, xcd = bid & 7u, ix = bid >> 3;
  const unsigned b = (xcd & 1u) * 4u + (ix & 3u), h = (xcd >> 1) * 8u + (ix >> 2);
  const unsigned lds0 = (unsigned)(uintptr_t)lds;
  const unsigned prow = (lane >> 2) & 7u, pch = 4u * hh + (lane & 3u);
  const unsigned q4 = (lane & 15u) >> 2, p4 = lane & 3u, blk = (lane >> 4) & 1u;
  const unsigned trcol = 32u * blk + 16u * (p4 & 1u) + 8u * (p4 >> 1);
  const srd_t sO = mk_srd(ws, O_BYTES);
  unsigned long long tag;
  const unsigned g4 = xcd >> 1, u0 = (xcd & 1u) * 32u + ix;
  const bool has_tile = u0 < 63u;
  const unsigned u = has_tile ? u0 : 62u, rt = u / 9u, ct = u - rt * 9u;

  if (wid == 7u) {
    const srd_t sK = mk_srd(kc + (size_t)(b * NH + h) * SK * DH, SK * DH * 2);
    const srd_t sV = mk_srd(vc + (size_t)(b * NH + h) * SK * DH, SK * DH * 2);
    const srd_t sW = mk_srd(wq, DM * NH * DH * 2);
    const srd_t sX = mk_srd(x + (size_t)b * SQ * DM, SQ * DM * 2);
    const unsigned pchx0 = 4u * hh + ((lane & 3u) ^ ((lane >> 4) & 1u)), pchx1 = pchx0 ^ 2u;
    const unsigned vV = prow * 256u + pch * 16u, vK0 = prow * 256u + pchx0 * 16u, vK1 = prow * 256u + pchx1 * 16u;
    const unsigned vW = prow * 8192u + pch * 16u, vX0 = prow * (DM * 2u) + pchx0 * 16u, vX1 = prow * (DM * 2u) + pchx1 * 16u;
    auto fillK = [&](unsigned t) { const unsigned so = t * 16384u, ld = lds0 + ((SLOTK >> (4u * t)) & 15u) * 16384u;
      dma_nt(ld, vK0, sK, so); dma_nt(ld + 1024u, vK0, sK, so + 128u); dma_nt(ld + 2048u, vK1, sK, so + 2048u); dma_nt(ld + 3072u, vK1, sK, so + 2176u);
#pragma unroll
      for (int i = 1; i < 4; ++i) dma4_nt(ld + i * 4096u, vK0 + 1024u, vK0 + 128u, vK1 + 1024u, vK1 + 128u, sK, so + i * 4096u - 1024u); };
    auto fillV = [&](unsigned t) { const unsigned so = t * 16384u, ld = lds0 + ((SLOTV >> (4u * t)) & 15u) * 16384u;
      dma_nt(ld, vV, sV, so); dma_nt(ld + 1024u, vV, sV, so + 128u); dma_nt(ld + 2048u, vV, sV, so + 2048u); dma_nt(ld + 3072u, vV, sV, so + 2176u);
#pragma unroll
      for (int i = 1; i < 4; ++i) dma4_nt(ld + i * 4096u, vV + 1024u, vV + 128u, vV + 1024u, vV + 128u, sV, so + i * 4096u - 1024u); };
    auto fillQ = [&](unsigned c, unsigned stage) {
      const unsigned lw = lds0 + stage + STG_W, sw = c * 64u * 8192u + h * 256u, lx = lds0 + stage, sx = c * 128u;
      dma(lw, vW, sW, sw); dma(lw + 1024u, vW, sW, sw + 128u); dma(lw + 2048u, vW, sW, sw + 65536u); dma(lw + 3072u, vW, sW, sw + 65664u);
#pragma unroll
      for (int i = 1; i < 4; ++i) dma4(lw + i * 4096u, vW + 1024u, vW + 128u, vW + 65536u - 1024u, vW + 65664u - 2048u, sW, sw + i * 131072u - 1024u);
#pragma unroll
      for (int i = 0; i < 7; ++i) dma4(lx + i * 4096u, vX0, vX1 + 8192u, vX0 + 16384u, vX1 + 24576u, sX, sx + i * 32u * DM * 2u); };
    const srd_t sWo = mk_srd(wo, NH * DH * DM * 2);
    const unsigned vWo = prow * (DM * 2u) + pch * 16u;
    auto fillWo = [&](unsigned i) { const unsigned ld = lds0 + ((SLOTW >> (4u * i)) & 15u) * 16384u, so = (g4 * 8u + i) * 128u * (DM * 2u) + ct * 128u;
#pragma unroll
      for (int k = 0; k < 4; ++k) dma4(ld + k * 4096u, vWo, vWo + 8192u, vWo + 16384u, vWo + 24576u, sWo, so + k * 32u * (DM * 2u)); };
    const unsigned rot = QROT ? 3u * (((ix & 3u) + (ix >> 2)) % 3u) : 0u;
    auto chunk = [&](unsigned c) { const unsigned k = c + rot; return k >= 9u ? k - 9u : k; };
    fillQ(chunk(0), 0);
    { unsigned stage = STG;
#pragma nounroll
      for (unsigned c = 1; c < 8; ++c) { fillQ(chunk(c), stage); stage = stage == 2 * STG ? 0u : stage + STG; wait_vm<44>(); bar(); ST(1 + c); } }
    fillQ(chunk(8), 2 * STG); fillK(0); wait_vm<60>(); bar(); ST(9);
    fillK(1); fillV(0); wait_vm<48>(); bar(); ST(10);
    fillK(2); wait_vm<48>(); bar(); ST(11);
    fillV(1); wait_vm<32>(); bar(); ST(12);
#pragma nounroll
    for (unsigned t = 1; t < 5; ++t) { fillWo(t - 1u); fillK(t + 2u); fillV(t + 1u); wait_vm<48>(); bar(); ST(12 + t); }
    fillK(7); fillV(6); wait_vm<32>(); bar(); ST(17);
    fillV(7); fillWo(4); wait_vm<32>(); bar(); ST(18);
    fillWo(5); fillWo(6); wait_vm<48>(); bar(); ST(19);
    fillWo(7);
    tag = mix64(ld_agent(ws + EPOCH_OFF));
    ST(20); ST(21);
  } else {
  unsigned cdummy = 0;
  if (TLBW && wid < 6u) {
    const char* p;
    if (wid == 0u) p = (const char*)(lane & 1u ? vc : kc) + (size_t)(b * NH + h) * (SK * DH * 2) + (lane >> 1) * 4096u;
    else if (wid == 1u) p = (const char*)wo + (size_t)(g4 * 1024u + lane * 16u) * (DM * 2u) + ct * 128u;
    else if (wid == 2u) p = ws + ((b * 7u + (lane & 7u) % 7u) * NH + h) * 8192u + (lane >> 5) * 4096u;
    else if (wid == 3u) p = ws + ((8u * rt + (lane & 7u)) * NH + g4 * 8u + (lane >> 3)) * 8192u;
    else if (wid == 4u) p = ws + X_OFF + u * 262144u + lane * 4096u;
    else p = lane < 32u ? (const char*)out + (size_t)(rt * 256u + lane * 8u) * (DM * 2u) : ws + FLAG_OFF + (lane - 32u) * 2048u;
    asm volatile("global_load_dword %0, %1, off" : "+v"(cdummy) : "v"(p) : "memory"); }
  if (CODEPF) {
    const srd_t sC = mk_srd((const void*)(pc0 & ~255ull), CODEPF);
    touch(cdummy, lane * 64u, sC, wid * 4096u); }
  tag = mix64(ld_agent(ws + EPOCH_OFF));
  tag = ((unsigned long long)rfl((unsigned)(tag >> 32)) << 32) | rfl((unsigned)tag);
  const unsigned rsw0 = 64u * (r & 7u) + 16u * (hh ^ ((r >> 2) & 3u)), rsw1 = rsw0 ^ 32u;
  f32x16 qa[4] = {}, o[4] = {};
  asm volatile("" : "+v"(qa[0]), "+v"(qa[1]), "+v"(qa[2]), "+v"(qa[3]), "+v"(o[0]), "+v"(o[1]), "+v"(o[2]), "+v"(o[3]));
  __builtin_amdgcn_s_waitcnt(0x0070);
  wait_vm<0>(); pinu(cdummy);
  ST(1);
  bar();
  ST(2);
  { lds_cptr wp = (lds_cptr)lds + STG_W + 2048u * hh + 64u * q4 + trcol;
    lds_cptr xp0 = (lds_cptr)lds + wid * 4096u + 1024u * (r >> 3) + rsw0, xp1 = (lds_cptr)lds + wid * 4096u + 1024u * (r >> 3) + rsw1;
    bf16x8 xf[4], af[5];
    auto rdA = [&](int i) { return trd2(wp, 4096 * (i >> 2) + 512 * (i & 3), 4096 * (i >> 2) + 512 * (i & 3) + 256); };
    xf[0] = rd128(xp0); af[0] = rdA(0); af[1] = rdA(1); af[2] = rdA(2); af[3] = rdA(3);
    unsigned stage = 0;
#pragma nounroll
    for (unsigned c = 0; c < 9; ++c) {
      SBAR();
      static_for<12>([&](auto I) { constexpr int i = I.value;
        af[(i + 4) % 5] = rdA(i + 4);
        if constexpr (i == 0) xf[1] = rd128(xp1); if constexpr (i == 3) xf[2] = rd128(xp0 + 512); if constexpr (i == 7) xf[3] = rd128(xp1 + 512);
        qa[i & 3] = MFMA(af[i % 5], xf[i >> 2], qa[i & 3]); SBAR(); });
      qa[0] = MFMA(af[12 % 5], xf[3], qa[0]); SBAR();
      bar();
      const unsigned nstage = stage == 2 * STG ? 0u : stage + STG; const int dlt = (int)nstage - (int)stage; stage = nstage;
      wp += dlt; xp0 += dlt; xp1 += dlt;
      const bf16x8 xl = xf[3], a13 = af[13 % 5], a14 = af[14 % 5], a15 = af[15 % 5];
      xf[0] = rd128(xp0); af[0] = rdA(0); af[1] = rdA(1);
      SBAR();
      qa[1] = MFMA(a13, xl, qa[1]); SBAR();
      af[2] = rdA(2);
      qa[2] = MFMA(a14, xl, qa[2]); SBAR();
      af[3] = rdA(3);
      qa[3] = MFMA(a15, xl, qa[3]); SBAR();
      ST(3 + c);
    } }

  const lds_cptr kb0 = (lds_cptr)lds + 2048u * (r >> 3) + rsw0, kb1 = (lds_cptr)lds + 2048u * (r >> 3) + rsw1;
  const lds_cptr vb = (lds_cptr)lds + 64u * (4u * hh + q4) + trcol;
  bf16x8 qf[8], pre[RA];
  f32x16 negm, S0, S1; u32x4 P0[2], P1[2]; float l = 0.f;
  auto rdK = [&](lds_cptr k0, lds_cptr k1, int kb, int i) { return rd128(((i & 1) ? k1 : k0) + 8192 * kb + 512 * (i >> 1)); };
  auto rdV = [&](lds_cptr v, int kb, int j) { return trd2(v, 8192 * kb + 4096 * (j >> 2) + 512 * (j & 3), 8192 * kb + 4096 * (j >> 2) + 2048 + 512 * (j & 3)); };
  auto sm_fix = [&](float ps, f32x16& Sc, f32x16& Sn, u32x4 (&P)[2]) {
    if (__builtin_expect(__any(!(ps <= SUM_THR)), 0)) {
      const float dl = fmaxf(swap_max(max16(Sc)), 0.f), f = __builtin_amdgcn_exp2f(-dl);
#pragma unroll
      for (int i = 0; i < 16; ++i) { Sc[i] -= dl; Sn[i] -= dl; negm[i] -= dl; }
#pragma unroll
      for (int db = 0; db < 4; ++db)
#pragma unroll
        for (int i = 0; i < 16; ++i) o[db][i] *= f;
      l *= f; ps = expo(Sc, P); }
    l += ps; };
  {
    lds_cptr k0 = kb0 + 9 * 16384, k1 = kb1 + 9 * 16384; asm volatile("" : "+v"(k0), "+v"(k1));
    bf16x8 f[16];
#pragma unroll
    for (int i = 0; i < 4; ++i) f[i] = rdK(k0, k1, 0, i);
    auto cvq = [&](int g) { u32x4 w;
#pragma unroll
      for (int e = 0; e < 4; ++e) w[e] = cvtpk(qa[g >> 1][8 * (g & 1) + 2 * e] * QSCALE, qa[g >> 1][8 * (g & 1) + 2 * e + 1] * QSCALE);
      qf[g] = __builtin_bit_cast(bf16x8, w); pin(qf[g]); };
    cvq(0); SBAR();
    static_for<8>([&](auto G) { constexpr int g = G.value;
      f[g + 4] = rdK(k0, k1, (g + 4) >> 3, (g + 4) & 7);
      if constexpr (g == 0) { const f32x16 z = {}; S0 = MFMA(f[0], qf[0], z); } else S0 = MFMA(f[g], qf[g], S0);
      if constexpr (g < 7) cvq(g + 1);
      SBAR(); });
#pragma unroll
    for (int i = 12; i < 16; ++i) f[i] = rdK(k0, k1, 1, i & 7);
    const float m = swap_max(max16(S0));
#pragma unroll
    for (int i = 0; i < 16; ++i) negm[i] = -m;
    bar();
    { const unsigned ko = ((SLOTK >> 4) & 15u) * 16384u;
#pragma unroll
      for (int i = 0; i < RA; ++i) pre[i] = rdK(kb0 + ko, kb1 + ko, 0, i); }
    SBAR();
    float e[16]; float s0 = 0.f, s1 = 0.f;
    static_for<9>([&](auto G) { constexpr int g = G.value;
      if constexpr (g == 0) S1 = MFMA(f[8], qf[0], negm); else if constexpr (g < 8) S1 = MFMA(f[8 + g], qf[g], S1);
      if constexpr (g < 8) { e[2 * g] = __builtin_amdgcn_exp2f(S0[2 * g] - m); e[2 * g + 1] = __builtin_amdgcn_exp2f(S0[2 * g + 1] - m); pinf(e[2 * g]); pinf(e[2 * g + 1]); }
      if constexpr (g == 1) { s0 = e[0]; s1 = e[1]; } else if constexpr (g > 1) { s0 = fadd(s0, e[2 * g - 2]); s1 = fadd(s1, e[2 * g - 1]); pinf(s0); pinf(s1); }
      if constexpr (g >= 1) { unsigned w = cvtpk(e[2 * g - 2], e[2 * g - 1]); pinu(w); P0[(g - 1) >> 2][(g - 1) & 3] = w; }
      SBAR(); });
    l = s0 + s1; }
  ST(12);
  auto unit = [&](auto KB, auto LAST, lds_cptr k0, lds_cptr k1, lds_cptr v, lds_cptr nk0, lds_cptr nk1, f32x16& Sn, f32x16& Sc, const u32x4 (&Pp)[2], u32x4 (&Pc)[2]) {
    constexpr int kb = KB.value; constexpr bool last = LAST.value;
    float e[16]; float s0 = 0.f, s1 = 0.f; bf16x8 f[16 + RA];
#pragma unroll
    for (int i = 0; i < RA; ++i) f[i] = pre[i];
    static_for<16>([&](auto G) { constexpr int g = G.value; constexpr int n = g + RA; constexpr int gb = 16 - RA + 1;
      if constexpr (last && g == gb) { bar(); f[16] = rdK(nk0, nk1, 0, 0); }
      if constexpr (n < 8) f[n] = rdK(k0, k1, kb, n); else if constexpr (n < 16) f[n] = rdV(v, kb, n - 8);
      else if constexpr (!last) f[n] = rdK(k0, k1, kb + 1, n - 16);
      else if constexpr (n > 16) f[n] = rdK(nk0, nk1, 0, n - 16);
      if constexpr (g == 0) Sn = MFMA(f[0], qf[0], negm); else if constexpr (g < 8) Sn = MFMA(f[g], qf[g], Sn);
      else o[g & 3] = MFMA(f[g], __builtin_bit_cast(bf16x8, Pp[(g >> 2) & 1]), o[g & 3]);
      constexpr int e0 = g < 12 ? (g / 3) * 4 + (g % 3 == 0 ? 0 : g % 3 + 1) : 16, e1 = g + 1 < 12 ? ((g + 1) / 3) * 4 + ((g + 1) % 3 == 0 ? 0 : (g + 1) % 3 + 1) : 16;
      constexpr int p0 = g >= 1 ? (g - 1 < 12 ? ((g - 1) / 3) * 4 + ((g - 1) % 3 == 0 ? 0 : (g - 1) % 3 + 1) : 16) : 0, p1 = g >= 1 ? e0 : 0;
      static_for<e1 - e0>([&](auto J) { constexpr int i = e0 + J.value; e[i] = __builtin_amdgcn_exp2f(Sc[i]); pinf(e[i]); });
      static_for<p1 - p0>([&](auto J) { constexpr int i = p0 + J.value;
        if constexpr (i == 0) s0 = e[0]; else if constexpr (i == 1) s1 = e[1]; else if constexpr (i & 1) { s1 = fadd(s1, e[i]); pinf(s1); } else { s0 = fadd(s0, e[i]); pinf(s0); }
        if constexpr (i & 1) { unsigned w = cvtpk(e[i - 1], e[i]); pinu(w); Pc[i >> 3][(i >> 1) & 3] = w; } });
      SBAR(); });
#pragma unroll
    for (int i = 0; i < RA; ++i) pre[i] = f[16 + i];
    sm_fix(s0 + s1, Sc, Sn, Pc); };
#pragma nounroll
  for (unsigned t = 1; t < 8; ++t) {
    const unsigned ko = ((SLOTK >> (4u * t)) & 15u) * 16384u, vo = ((SLOTV >> (4u * (t - 1u))) & 15u) * 16384u, nko = ((SLOTK >> (4u * ((t + 1u) & 7u))) & 15u) * 16384u;
    SBAR();
    unit(std::integral_constant<int, 0>{}, std::false_type{}, kb0 + ko, kb1 + ko, vb + vo, kb0, kb1, S0, S1, P0, P1);
    unit(std::integral_constant<int, 1>{}, std::true_type{}, kb0 + ko, kb1 + ko, vb + vo, kb0 + nko, kb1 + nko, S1, S0, P1, P0);
    ST(12 + t);
  }
  {
    const lds_cptr v = vb + ((SLOTV >> 28) & 15u) * 16384u;
    bf16x8 f[16];
#pragma unroll
    for (int i = 0; i < 3; ++i) f[i] = rdV(v, 0, i);
    float e[16]; float s0 = 0.f, s1 = 0.f;
    SBAR();
    static_for<9>([&](auto G) { constexpr int g = G.value; constexpr int n = g + 3;
      if constexpr (n < 8) f[n] = rdV(v, 0, n); else if constexpr (g < 8) f[n] = rdV(v, 1, ((n - 8) >> 1) + 4 * ((n - 8) & 1));
      if constexpr (g < 8) { o[g & 3] = MFMA(f[g], __builtin_bit_cast(bf16x8, P0[g >> 2]), o[g & 3]);
        e[2 * g] = __builtin_amdgcn_exp2f(S1[2 * g]); e[2 * g + 1] = __builtin_amdgcn_exp2f(S1[2 * g + 1]); pinf(e[2 * g]); pinf(e[2 * g + 1]); }
      if constexpr (g == 1) { s0 = e[0]; s1 = e[1]; } else if constexpr (g > 1) { s0 = fadd(s0, e[2 * g - 2]); s1 = fadd(s1, e[2 * g - 1]); pinf(s0); pinf(s1); }
      if constexpr (g >= 1) { unsigned w = cvtpk(e[2 * g - 2], e[2 * g - 1]); pinu(w); P1[(g - 1) >> 2][(g - 1) & 3] = w; }
      SBAR(); });
    sm_fix(s0 + s1, S1, S0, P1);
    const float inv = __builtin_amdgcn_rcpf(swap_add(l));
    const unsigned so = ((b * 7u + wid) * NH + h) * 8192u, vo = lane * 16u;
    auto put = [&](int k8) { u32x4 w;
#pragma unroll
      for (int e2 = 0; e2 < 4; ++e2) w[e2] = cvtpk(o[k8 >> 1][8 * (k8 & 1) + 2 * e2] * inv, o[k8 >> 1][8 * (k8 & 1) + 2 * e2 + 1] * inv);
      bstore_sc1(w, vo, sO, so + k8 * 1024u); };
    static_for<8>([&](auto G) { constexpr int g = G.value; constexpr int n = g + 11;
      if constexpr (n < 16) f[n] = rdV(v, 1, ((n - 8) >> 1) + 4 * ((n - 8) & 1));
      o[g >> 1] = MFMA(f[8 + g], __builtin_bit_cast(bf16x8, P1[g & 1]), o[g >> 1]);
      if constexpr (g >= 4) put(g - 4);
      SBAR(); });
    ST(20);
    put(4); put(5); put(6); put(7); }
  ST(21);
  }
  wait_vm<0>();
  ST(22);
  bar();
  if (tid == 0) st_agent(ws + FLAG_OFF + (b * NH + h) * FLAG_STRIDE, tag);
  ST(23);

  unsigned dummy = 0;
  if (RETOUCH && wid < 7u) touch(dummy, lane * 128u, sO, ((b * 7u + wid) * NH + h) * 8192u);
  if (!has_tile) {
    wait_vm<0>(); pinu(dummy);
    if (bid == 249u && wid == 0u) {
#pragma unroll
      for (int i = 0; i < 4; ++i) poll_flags(ws + FLAG_OFF + (i * 64u + lane) * FLAG_STRIDE, tag);
      if (lane == 0) st_agent(ws + EPOCH_OFF, tag); }
    ST(24); ST(25); ST(26); ST(27); ST(28); ST(29); ST(30); ST(31); ST_OUT(ws, 0, 32); return; }
  const unsigned jq = (2u * g4 + 2u + wid) & 7u, G = 8u * rt + jq;
  poll_flags(ws + FLAG_OFF + (((G * 37u) >> 8) * NH + g4 * 8u + (lane & 7u)) * FLAG_STRIDE, tag);
  wait_vm<0>(); pinu(dummy);
  ST(24);
  const unsigned voffO = lane * 16u, soO = (G * NH + g4 * 8u) * 8192u, hr = HROT ? ct : 0u;
  const lds_cptr wb = (lds_cptr)lds + 1024u * hh + 64u * q4 + trcol;
  bf16x8 of[64], lf[64][2];
  auto issueO = [&](auto S) { constexpr int st = S.value; bload_sc1(of[st], voffO, sO, soO + (((st >> 3) + hr) & 7u) * 8192u + (st & 7) * 1024u); };
  auto rdW = [&](auto S) { constexpr int st = S.value; const lds_cptr p = wb + ((SLOTW >> (4u * (((st >> 3) + hr) & 7u))) & 15u) * 16384u;
    lf[st][0] = trd2(p, (st & 7) * 2048, (st & 7) * 2048 + 256); lf[st][1] = trd2(p, (st & 7) * 2048 + 512, (st & 7) * 2048 + 768); };
  static_for<WO>(issueO);
  rdW(std::integral_constant<int, 0>{}); rdW(std::integral_constant<int, 1>{});
  f32x16 acc[2] = {};
  static_for<64>([&](auto S) { constexpr int st = S.value;
    wait_vm1<(st + WO <= 64 ? WO - 1 : 63 - st)>(of[st]);
    if constexpr (st == 16) ST(25); if constexpr (st == 32) ST(26); if constexpr (st == 48) ST(27);
    if constexpr (st + 2 < 64) rdW(std::integral_constant<int, st + 2>{});
    acc[0] = MFMA(lf[st][0], of[st], acc[0]); acc[1] = MFMA(lf[st][1], of[st], acc[1]);
    SBAR();
    if constexpr (st + WO < 64) issueO(std::integral_constant<int, st + WO>{});
    SBAR(); });
  ST(28);
  const srd_t sX2 = mk_srd(ws + X_OFF, X_BYTES);
  if (wid < 6u) {
#pragma unroll
    for (int i = 0; i < 8; ++i) { const f32x4 v = {acc[i >> 2][4 * (i & 3)], acc[i >> 2][4 * (i & 3) + 1], acc[i >> 2][4 * (i & 3) + 2], acc[i >> 2][4 * (i & 3) + 3]};
      f32x4 v2 = v + 0.f; asm volatile("" : "+v"(v2));
      bstore_sc1(__builtin_bit_cast(u32x4, v2), voffO, sX2, ((u * 8u + jq) * 4u + g4) * 8192u + i * 1024u); }
    wait_vm<0>(); }
  bar();
  if (tid == 0) st_agent(ws + FLAG2_OFF + (u * 4u + g4) * FLAG_STRIDE, tag);
  ST(29);
  if (wid >= 6u) {
    poll_flags(ws + FLAG2_OFF + (u * 4u + (lane & 3u)) * FLAG_STRIDE, tag);
    ST(30);
    bf16x8 xp[3][8];
#pragma unroll
    for (int k = 0; k < 3; ++k)
#pragma unroll
      for (int i = 0; i < 8; ++i) bload_sc1(xp[k][i], voffO, sX2, ((u * 8u + jq) * 4u + ((g4 + 1u + k) & 3u)) * 8192u + i * 1024u);
    wait_vm<0>();
#pragma unroll
    for (int k = 0; k < 3; ++k)
#pragma unroll
      for (int i = 0; i < 8; ++i) { pin(xp[k][i]); const f32x4 v = __builtin_bit_cast(f32x4, xp[k][i]);
#pragma unroll
        for (int e = 0; e < 4; ++e) acc[i >> 2][4 * (i & 3) + e] += v[e]; }
    char* op = (char*)out + (size_t)(G * 32u + r) * (DM * 2u) + ct * 128u + hh * 16u;
#pragma unroll
    for (int i = 0; i < 4; ++i) { const u32x4 w4 = {cvtpk(acc[i >> 1][8 * (i & 1)], acc[i >> 1][8 * (i & 1) + 1]), cvtpk(acc[i >> 1][8 * (i & 1) + 2], acc[i >> 1][8 * (i & 1) + 3]),
                                                    cvtpk(acc[i >> 1][8 * (i & 1) + 4], acc[i >> 1][8 * (i & 1) + 5]), cvtpk(acc[i >> 1][8 * (i & 1) + 6], acc[i >> 1][8 * (i & 1) + 7])};
      *(u32x4*)(op + i * 32) = w4; }
  } else { ST(30); }
  ST(31); ST_OUT(ws, 0, 32);
}

extern "C" void kernel_launch(void* const* d_in, const int* in_sizes, int n_in, void* d_out, int out_size, void* d_ws, size_t ws_size, hipStream_t stream) {
  (void)in_sizes; (void)n_in; (void)out_size; (void)ws_size;
  (void)hipFuncSetAttribute((const void*)mha_kernel, hipFuncAttributeMaxDynamicSharedMemorySize, LDS_BYTES);
  const unsigned short* x = (const unsigned short*)d_in[0]; const unsigned short* kc = (const unsigned short*)d_in[1]; const unsigned short* vc = (const unsigned short*)d_in[2];
  const unsigned short* wq = (const unsigned short*)d_in[3]; const unsigned short* wo = (const unsigned short*)d_in[4];
  mha_kernel<<<256, 512, LDS_BYTES, stream>>>(x, kc, vc, wq, wo, (char*)d_ws, (unsigned short*)d_out);
}
```

```cpp
#include <hip/hip_runtime.h>
#include <stdint.h>
#include <utility>

typedef short    bf16x8 __attribute__((ext_vector_type(8)));
typedef short    s16x4  __attribute__((ext_vector_type(4)));
typedef float    f32x16 __attribute__((ext_vector_type(16)));
typedef float    f32x4  __attribute__((ext_vector_type(4)));
typedef float    f32x2  __attribute__((ext_vector_type(2)));
typedef __bf16   bf16x2 __attribute__((ext_vector_type(2)));
typedef unsigned u32x4  __attribute__((ext_vector_type(4)));
typedef __attribute__((address_space(3))) const char* lds_cptr;
typedef __attribute__((address_space(3))) char* lds_ptr;
typedef __amdgpu_buffer_rsrc_t srd_t;

#define KV_AUX "nt "
#define TLBW 1
#define CODEPF 16384
#define TOUCH_AUX ""
#define ST_AUX "sc1"
#define LD_AUX "sc1"
#define DI __device__ __forceinline__
#define MFMA(a, b, c) __builtin_amdgcn_mfma_f32_32x32x16_bf16((a), (b), (c), 0, 0, 0)
#define SBAR() __builtin_amdgcn_sched_barrier(0)

constexpr int NB = 8, SQ = 224, DM = 576, NH = 32, SK = 512, DH = 128;
constexpr int LDS_BYTES = 163840;
constexpr float QSCALE = 0.08838834764831845f * 1.4426950408889634f;
constexpr float SUM_THR = 4096.f;

template <int... Is, class F> DI void static_for_impl(std::integer_sequence<int, Is...>, F&& f) { (f(std::integral_constant<int, Is>{}), ...); }
template <int N, class F> DI void static_for(F&& f) { static_for_impl(std::make_integer_sequence<int, N>{}, f); }

DI unsigned rfl(unsigned x) { return __builtin_amdgcn_readfirstlane(x); }
DI unsigned cvtpk(float lo, float hi) { f32x2 v = {lo, hi}; bf16x2 b = __builtin_convertvector(v, bf16x2); return __builtin_bit_cast(unsigned, b); }
DI srd_t mk_srd(const void* p, unsigned bytes) { return __builtin_amdgcn_make_buffer_rsrc((void*)p, (short)0, (int)bytes, 0x00020000); }
DI void dma(unsigned lds, unsigned voff, srd_t srd, unsigned soff) {
  asm volatile("s_mov_b32 m0, %0\n\ts_nop 0\n\tbuffer_load_dwordx4 %1, %2, %3 offen lds" :: "s"(lds), "v"(voff), "s"(srd), "s"(soff) : "memory");
}
DI void dma_nt(unsigned lds, unsigned voff, srd_t srd, unsigned soff) {
  asm volatile("s_mov_b32 m0, %0\n\ts_nop 0\n\tbuffer_load_dwordx4 %1, %2, %3 offen " KV_AUX "lds" :: "s"(lds), "v"(voff), "s"(srd), "s"(soff) : "memory");
}
DI void dma4(unsigned lds, unsigned v0, unsigned v1, unsigned v2, unsigned v3, srd_t srd, unsigned soff) {
  asm volatile("s_mov_b32 m0, %0\n\ts_nop 0\n\tbuffer_load_dwordx4 %1, %5, %6 offen lds\n\tbuffer_load_dwordx4 %2, %5, %6 offen offset:1024 lds\n\t"
               "buffer_load_dwordx4 %3, %5, %6 offen offset:2048 lds\n\tbuffer_load_dwordx4 %4, %5, %6 offen offset:3072 lds"
               :: "s"(lds), "v"(v0), "v"(v1), "v"(v2), "v"(v3), "s"(srd), "s"(soff) : "memory");
}
DI void dma4_nt(unsigned lds, unsigned v0, unsigned v1, unsigned v2, unsigned v3, srd_t srd, unsigned soff) {
  asm volatile("s_mov_b32 m0, %0\n\ts_nop 0\n\tbuffer_load_dwordx4 %1, %5, %6 offen " KV_AUX "lds\n\tbuffer_load_dwordx4 %2, %5, %6 offen offset:1024 " KV_AUX "lds\n\t"
               "buffer_load_dwordx4 %3, %5, %6 offen offset:2048 " KV_AUX "lds\n\tbuffer_load_dwordx4 %4, %5, %6 offen offset:3072 " KV_AUX "lds"
               :: "s"(lds), "v"(v0), "v"(v1), "v"(v2), "v"(v3), "s"(srd), "s"(soff) : "memory");
}
DI void bstore_sc1(const u32x4& d, unsigned voff, srd_t srd, unsigned soff) { asm volatile("buffer_store_dwordx4 %0, %1, %2, %3 offen " ST_AUX "\n\ts_nop 1" :: "v"(d), "v"(voff), "s"(srd), "s"(soff) : "memory"); }
DI void breload_sc1(bf16x8& d, unsigned voff, srd_t srd, unsigned soff) { asm volatile("buffer_load_dwordx4 %0, %1, %2, %3 offen " LD_AUX : "+v"(d) : "v"(voff), "s"(srd), "s"(soff) : "memory"); }
DI void bload_sc1(bf16x8& d, unsigned voff, srd_t srd, unsigned soff) { asm volatile("buffer_load_dwordx4 %0, %1, %2, %3 offen " LD_AUX : "=v"(d) : "v"(voff), "s"(srd), "s"(soff) : "memory"); }
DI void bload(bf16x8& d, unsigned voff, srd_t srd, unsigned soff) { asm volatile("buffer_load_dwordx4 %0, %1, %2, %3 offen" : "=v"(d) : "v"(voff), "s"(srd), "s"(soff) : "memory"); }
DI void bload_nt(bf16x8& d, unsigned voff, srd_t srd, unsigned soff) { asm volatile("buffer_load_dwordx4 %0, %1, %2, %3 offen nt" : "=v"(d) : "v"(voff), "s"(srd), "s"(soff) : "memory"); }
DI void touch(unsigned& d, unsigned voff, srd_t srd, unsigned soff) { asm volatile("buffer_load_dword %0, %1, %2, %3 offen " TOUCH_AUX : "+v"(d) : "v"(voff), "s"(srd), "s"(soff) : "memory"); }
DI void pin(bf16x8& a) { asm volatile("" : "+v"(a)); }
DI void pinf(float& a) { asm volatile("" : "+v"(a)); }
DI void pinu(unsigned& a) { asm volatile("" : "+v"(a)); }
template <int N> DI void wait_vm2(bf16x8& a, bf16x8& b) { asm volatile("s_waitcnt vmcnt(%2)" : "+v"(a), "+v"(b) : "n"(N) : "memory"); }
template <int N> DI void wait_vm1(bf16x8& a) { asm volatile("s_waitcnt vmcnt(%1)" : "+v"(a) : "n"(N) : "memory"); }
template <int N> DI void wait_bar() { asm volatile("s_waitcnt vmcnt(%0) lgkmcnt(0)\n\ts_barrier" :: "n"(N) : "memory"); }
template <int N> DI void wait_vm() { asm volatile("s_waitcnt vmcnt(%0)" :: "n"(N) : "memory"); }
DI void bar() { asm volatile("s_waitcnt lgkmcnt(0)\n\ts_barrier" ::: "memory"); }
typedef short v4i16_t __attribute__((ext_vector_type(4)));
DI s16x4 trd(lds_cptr p) { return __builtin_amdgcn_ds_read_tr16_b64_v4i16((__attribute__((address_space(3))) v4i16_t*)p); }
DI bf16x8 trd2(lds_cptr p, int o0, int o1) { s16x4 a = trd(p + o0), b = trd(p + o1); return __builtin_shufflevector(a, b, 0, 1, 2, 3, 4, 5, 6, 7); }
DI bf16x8 rd128(lds_cptr p) { return *(const __attribute__((address_space(3))) bf16x8*)p; }
DI float swap_max(float m) { auto rr = __builtin_amdgcn_permlane32_swap(__float_as_uint(m), __float_as_uint(m), false, false); return fmaxf(__uint_as_float(rr[0]), __uint_as_float(rr[1])); }
DI float swap_add(float m) { auto rr = __builtin_amdgcn_permlane32_swap(__float_as_uint(m), __float_as_uint(m), false, false); return __uint_as_float(rr[0]) + __uint_as_float(rr[1]); }
DI float max16(const f32x16& s) { float a = fmaxf(fmaxf(s[0], s[1]), s[2]), b = fmaxf(fmaxf(s[3], s[4]), s[5]);
  a = fmaxf(fmaxf(a, s[6]), s[7]); b = fmaxf(fmaxf(b, s[8]), s[9]); a = fmaxf(fmaxf(a, s[10]), s[11]); b = fmaxf(fmaxf(b, s[12]), s[13]); a = fmaxf(fmaxf(a, s[14]), s[15]); return fmaxf(a, b); }
DI float fadd(float a, float b) { float r; asm("v_add_f32_e32 %0, %1, %2" : "=v"(r) : "v"(a), "v"(b)); return r; }

DI float expo(const f32x16& s, u32x4 (&p)[2]) {
  float e[16]; float s0 = 0.f, s1 = 0.f;
#pragma unroll
  for (int i = 0; i < 16; ++i) e[i] = __builtin_amdgcn_exp2f(s[i]);
#pragma unroll
  for (int i = 0; i < 16; i += 2) { s0 += e[i]; s1 += e[i + 1]; }
#pragma unroll
  for (int i = 0; i < 8; ++i) p[i >> 2][i & 3] = cvtpk(e[2 * i], e[2 * i + 1]);
  return s0 + s1;
}

#define ST_DECL
#define ST(k)
#define ST_OUT(ws, lo, hi)

constexpr unsigned SLOTK = 0x04204209u;
constexpr unsigned SLOTV = 0x31531531u;
constexpr unsigned STG = 45056u, STG_W = 28672u;
constexpr unsigned O_BYTES = NB * SQ * NH * DH * 2;
constexpr unsigned X_OFF = O_BYTES, X_BYTES = 63u * 8u * 4u * 4096u;
constexpr unsigned FLAG_OFF = X_OFF + X_BYTES, FLAG_STRIDE = 128, FLAG2_OFF = FLAG_OFF + 256 * FLAG_STRIDE, EPOCH_OFF = FLAG2_OFF + 63 * 32 * FLAG_STRIDE;
#define PSLEEP 4
#define OPRIO 0
constexpr unsigned SPIN_CAP = 1u << 20;
#define RA 3
constexpr unsigned SLOTW = 0x05429876u;
#define WO 6
#define QROT 1
#define HROT 1
#define RETOUCH 1
typedef __attribute__((address_space(1))) unsigned long long gu64;
DI unsigned long long mix64(unsigned long long z) { z += 0x9E3779B97F4A7C15ull; z = (z ^ (z >> 30)) * 0xBF58476D1CE4E5B9ull; z = (z ^ (z >> 27)) * 0x94D049BB133111EBull; return z ^ (z >> 31); }
DI unsigned long long ld_agent(const char* p) { return __hip_atomic_load((gu64*)p, __ATOMIC_RELAXED, __HIP_MEMORY_SCOPE_AGENT); }
DI void st_agent(char* p, unsigned long long v) { __hip_atomic_store((gu64*)p, v, __ATOMIC_RELAXED, __HIP_MEMORY_SCOPE_AGENT); }
DI void poll_flags(const char* p, unsigned long long tag) {
  for (unsigned spins = 0; spins < SPIN_CAP; ++spins) { if (__all(ld_agent(p) == tag)) break; __builtin_amdgcn_s_sleep(PSLEEP); }
  asm volatile("" ::: "memory");
}

__global__ __launch_bounds__(512, 1) void mha_kernel(const unsigned short* __restrict__ x, const unsigned short* __restrict__ kc, const unsigned short* __restrict__ vc,
                                                     const unsigned short* __restrict__ wq, const unsigned short* __restrict__ wo, char* ws, unsigned short* __restrict__ out) {
  extern __shared__ __attribute__((aligned(16))) char lds[];
  unsigned long long pc0; asm volatile("s_getpc_b64 %0" : "=s"(pc0));
  const unsigned tid = threadIdx.x, lane = tid & 63u, r = lane & 31u, hh = lane >> 5;
  const unsigned wid = rfl(tid >> 6);
  ST_DECL ST(0);
  const unsigned bid = blockIdx.x, xcd = bid & 7u, ix = bid >> 3;
  const unsigned b = (xcd & 1u) * 4u + (ix & 3u), h = (xcd >> 1) * 8u + (ix >> 2);
  const unsigned lds0 = (unsigned)(uintptr_t)lds;
  const unsigned prow = (lane >> 2) & 7u, pch = 4u * hh + (lane & 3u);
  const unsigned q4 = (lane & 15u) >> 2, p4 = lane & 3u, blk = (lane >> 4) & 1u;
  const unsigned trcol = 32u * blk + 16u * (p4 & 1u) + 8u * (p4 >> 1);
  const srd_t sO = mk_srd(ws, O_BYTES);
  unsigned long long tag;
  const unsigned g4 = xcd >> 1, u0 = (xcd & 1u) * 32u + ix;
  const bool has_tile = u0 < 63u;
  const unsigned u = has_tile ? u0 : 62u, rt = u / 9u, ct = u - rt * 9u;

  if (wid == 7u) {
    const srd_t sK = mk_srd(kc + (size_t)(b * NH + h) * SK * DH, SK * DH * 2);
    const srd_t sV = mk_srd(vc + (size_t)(b * NH + h) * SK * DH, SK * DH * 2);
    const srd_t sW = mk_srd(wq, DM * NH * DH * 2);
    const srd_t sX = mk_srd(x + (size_t)b * SQ * DM, SQ * DM * 2);
    const unsigned pchx0 = 4u * hh + ((lane & 3u) ^ ((lane >> 4) & 1u)), pchx1 = pchx0 ^ 2u;
    const unsigned vV = prow * 256u + pch * 16u, vK0 = prow * 256u + pchx0 * 16u, vK1 = prow * 256u + pchx1 * 16u;
    const unsigned vW = prow * 8192u + pch * 16u, vX0 = prow * (DM * 2u) + pchx0 * 16u, vX1 = prow * (DM * 2u) + pchx1 * 16u;
    auto fillK = [&](unsigned t) { const unsigned so = t * 16384u, ld = lds0 + ((SLOTK >> (4u * t)) & 15u) * 16384u;
      dma_nt(ld, vK0, sK, so); dma_nt(ld + 1024u, vK0, sK, so + 128u); dma_nt(ld + 2048u, vK1, sK, so + 2048u); dma_nt(ld + 3072u, vK1, sK, so + 2176u);
#pragma unroll
      for (int i = 1; i < 4; ++i) dma4_nt(ld + i * 4096u, vK0 + 1024u, vK0 + 128u, vK1 + 1024u, vK1 + 128u, sK, so + i * 4096u - 1024u); };
    auto fillV = [&](unsigned t) { const unsigned so = t * 16384u, ld = lds0 + ((SLOTV >> (4u * t)) & 15u) * 16384u;
      dma_nt(ld, vV, sV, so); dma_nt(ld + 1024u, vV, sV, so + 128u); dma_nt(ld + 2048u, vV, sV, so + 2048u); dma_nt(ld + 3072u, vV, sV, so + 2176u);
#pragma unroll
      for (int i = 1; i < 4; ++i) dma4_nt(ld + i * 4096u, vV + 1024u, vV + 128u, vV + 1024u, vV + 128u, sV, so + i * 4096u - 1024u); };
    auto fillQ = [&](unsigned c, unsigned stage) {
      const unsigned lw = lds0 + stage + STG_W, sw = c * 64u * 8192u + h * 256u, lx = lds0 + stage, sx = c * 128u;
      dma(lw, vW, sW, sw); dma(lw + 1024u, vW, sW, sw + 128u); dma(lw + 2048u, vW, sW, sw + 65536u); dma(lw + 3072u, vW, sW, sw + 65664u);
#pragma unroll
      for (int i = 1; i < 4; ++i) dma4(lw + i * 4096u, vW + 1024u, vW + 128u, vW + 65536u - 1024u, vW + 65664u - 2048u, sW, sw + i * 131072u - 1024u);
#pragma unroll
      for (int i = 0; i < 7; ++i) dma4(lx + i * 4096u, vX0, vX1 + 8192u, vX0 + 16384u, vX1 + 24576u, sX, sx + i * 32u * DM * 2u); };
    const srd_t sWo = mk_srd(wo, NH * DH * DM * 2);
    const unsigned vWo = prow * (DM * 2u) + pch * 16u;
    auto fillWo = [&](unsigned i) { const unsigned ld = lds0 + ((SLOTW >> (4u * i)) & 15u) * 16384u, so = (g4 * 8u + i) * 128u * (DM * 2u) + ct * 128u;
#pragma unroll
      for (int k = 0; k < 4; ++k) dma4(ld + k * 4096u, vWo, vWo + 8192u, vWo + 16384u, vWo + 24576u, sWo, so + k * 32u * (DM * 2u)); };
    const unsigned rot = QROT ? 3u * (((ix & 3u) + (ix >> 2)) % 3u) : 0u;
    auto chunk = [&](unsigned c) { const unsigned k = c + rot; return k >= 9u ? k - 9u : k; };
    unsigned long long ep; asm volatile("global_load_dwordx2 %0, %1, %2 sc1" : "=v"(ep) : "v"(0u), "s"(ws + EPOCH_OFF) : "memory");
    fillQ(chunk(0), 0);
    { unsigned stage = STG;
#pragma nounroll
      for (unsigned c = 1; c < 8; ++c) { fillQ(chunk(c), stage); stage = stage == 2 * STG ? 0u : stage + STG; wait_vm<44>(); bar(); ST(1 + c); } }
    fillQ(chunk(8), 2 * STG); fillK(0); wait_vm<60>(); bar(); ST(9);
    fillK(1); fillV(0); wait_vm<48>(); bar(); ST(10);
    fillK(2); wait_vm<48>(); bar(); ST(11);
    fillV(1); wait_vm<32>(); bar(); ST(12);
#pragma nounroll
    for (unsigned t = 1; t < 5; ++t) { fillWo(t - 1u); fillK(t + 2u); fillV(t + 1u); wait_vm<48>(); bar(); ST(12 + t); }
    fillK(7); fillV(6); wait_vm<32>(); bar(); ST(17);
    fillV(7); fillWo(4); wait_vm<32>(); bar(); ST(18);
    fillWo(5); fillWo(6); wait_vm<48>(); bar(); ST(19);
    fillWo(7);
    asm volatile("" : "+v"(ep)); tag = mix64(ep);
    tag = ((unsigned long long)rfl((unsigned)(tag >> 32)) << 32) | rfl((unsigned)tag);
    wait_vm<0>();
    ST(20); ST(21);
  } else {
  unsigned cdummy = 0;
  if (TLBW && wid < 6u) {
    const char* p;
    if (wid == 0u) p = (const char*)(lane & 1u ? vc : kc) + (size_t)(b * NH + h) * (SK * DH * 2) + (lane >> 1) * 4096u;
    else if (wid == 1u) p = (const char*)wo + (size_t)(g4 * 1024u + lane * 16u) * (DM * 2u) + ct * 128u;
    else if (wid == 2u) p = ws + ((b * 7u + (lane & 7u) % 7u) * NH + h) * 8192u + (lane >> 5) * 4096u;
    else if (wid == 3u) p = ws + ((8u * rt + (lane & 7u)) * NH + g4 * 8u + (lane >> 3)) * 8192u;
    else if (wid == 4u) p = ws + X_OFF + u * 131072u + (lane & 31u) * 4096u;
    else p = lane < 32u ? (const char*)out + (size_t)(rt * 256u + lane * 8u) * (DM * 2u) : ws + FLAG_OFF + (lane - 32u) * 8192u + 64u;
    asm volatile("global_load_dword %0, %1, off" : "+v"(cdummy) : "v"(p) : "memory"); }
  if (CODEPF) {
    const srd_t sC = mk_srd((const void*)(pc0 & ~255ull), CODEPF);
    touch(cdummy, lane * 64u, sC, wid * 4096u); }
  tag = mix64(ld_agent(ws + EPOCH_OFF));
  tag = ((unsigned long long)rfl((unsigned)(tag >> 32)) << 32) | rfl((unsigned)tag);
  const unsigned rsw0 = 64u * (r & 7u) + 16u * (hh ^ ((r >> 2) & 3u)), rsw1 = rsw0 ^ 32u;
  f32x16 qa[4] = {}, o[4] = {};
  asm volatile("" : "+v"(qa[0]), "+v"(qa[1]), "+v"(qa[2]), "+v"(qa[3]), "+v"(o[0]), "+v"(o[1]), "+v"(o[2]), "+v"(o[3]));
  __builtin_amdgcn_s_waitcnt(0x0070);
  wait_vm<0>(); pinu(cdummy);
  ST(1);
  bar();
  ST(2);
  { lds_cptr wp = (lds_cptr)lds + STG_W + 2048u * hh + 64u * q4 + trcol;
    lds_cptr xp0 = (lds_cptr)lds + wid * 4096u + 1024u * (r >> 3) + rsw0, xp1 = (lds_cptr)lds + wid * 4096u + 1024u * (r >> 3) + rsw1;
    bf16x8 xf[4], af[5];
    auto rdA = [&](int i) { return trd2(wp, 4096 * (i >> 2) + 512 * (i & 3), 4096 * (i >> 2) + 512 * (i & 3) + 256); };
    xf[0] = rd128(xp0); af[0] = rdA(0); af[1] = rdA(1); af[2] = rdA(2); af[3] = rdA(3);
    unsigned stage = 0;
#pragma nounroll
    for (unsigned c = 0; c < 9; ++c) {
      SBAR();
      static_for<12>([&](auto I) { constexpr int i = I.value;
        af[(i + 4) % 5] = rdA(i + 4);
        if constexpr (i == 0) xf[1] = rd128(xp1); if constexpr (i == 3) xf[2] = rd128(xp0 + 512); if constexpr (i == 7) xf[3] = rd128(xp1 + 512);
        qa[i & 3] = MFMA(af[i % 5], xf[i >> 2], qa[i & 3]); SBAR(); });
      qa[0] = MFMA(af[12 % 5], xf[3], qa[0]); SBAR();
      bar();
      const unsigned nstage = stage == 2 * STG ? 0u : stage + STG; const int dlt = (int)nstage - (int)stage; stage = nstage;
      wp += dlt; xp0 += dlt; xp1 += dlt;
      const bf16x8 xl = xf[3], a13 = af[13 % 5], a14 = af[14 % 5], a15 = af[15 % 5];
      xf[0] = rd128(xp0); af[0] = rdA(0); af[1] = rdA(1);
      SBAR();
      qa[1] = MFMA(a13, xl, qa[1]); SBAR();
      af[2] = rdA(2);
      qa[2] = MFMA(a14, xl, qa[2]); SBAR();
      af[3] = rdA(3);
      qa[3] = MFMA(a15, xl, qa[3]); SBAR();
      ST(3 + c);
    } }

  const lds_cptr kb0 = (lds_cptr)lds + 2048u * (r >> 3) + rsw0, kb1 = (lds_cptr)lds + 2048u * (r >> 3) + rsw1;
  const lds_cptr vb = (lds_cptr)lds + 64u * (4u * hh + q4) + trcol;
  bf16x8 qf[8], pre[RA];
  f32x16 negm, S0, S1; u32x4 P0[2], P1[2]; float l = 0.f;
  auto rdK = [&](lds_cptr k0, lds_cptr k1, int kb, int i) { return rd128(((i & 1) ? k1 : k0) + 8192 * kb + 512 * (i >> 1)); };
  auto rdV = [&](lds_cptr v, int kb, int j) { return trd2(v, 8192 * kb + 4096 * (j >> 2) + 512 * (j & 3), 8192 * kb + 4096 * (j >> 2) + 2048 + 512 * (j & 3)); };
  auto sm_fix = [&](float ps, f32x16& Sc, f32x16& Sn, u32x4 (&P)[2]) {
    if (__builtin_expect(__any(!(ps <= SUM_THR)), 0)) {
      const float dl = fmaxf(swap_max(max16(Sc)), 0.f), f = __builtin_amdgcn_exp2f(-dl);
#pragma unroll
      for (int i = 0; i < 16; ++i) { Sc[i] -= dl; Sn[i] -= dl; negm[i] -= dl; }
#pragma unroll
      for (int db = 0; db < 4; ++db)
#pragma unroll
        for (int i = 0; i < 16; ++i) o[db][i] *= f;
      l *= f; ps = expo(Sc, P); }
    l += ps; };
  {
    lds_cptr k0 = kb0 + 9 * 16384, k1 = kb1 + 9 * 16384; asm volatile("" : "+v"(k0), "+v"(k1));
    bf16x8 f[16];
#pragma unroll
    for (int i = 0; i < 4; ++i) f[i] = rdK(k0, k1, 0, i);
    auto cvq = [&](int g) { u32x4 w;
#pragma unroll
      for (int e = 0; e < 4; ++e) w[e] = cvtpk(qa[g >> 1][8 * (g & 1) + 2 * e] * QSCALE, qa[g >> 1][8 * (g & 1) + 2 * e + 1] * QSCALE);
      qf[g] = __builtin_bit_cast(bf16x8, w); pin(qf[g]); };
    cvq(0); SBAR();
    static_for<8>([&](auto G) { constexpr int g = G.value;
      f[g + 4] = rdK(k0, k1, (g + 4) >> 3, (g + 4) & 7);
      if constexpr (g == 0) { const f32x16 z = {}; S0 = MFMA(f[0], qf[0], z); } else S0 = MFMA(f[g], qf[g], S0);
      if constexpr (g < 7) cvq(g + 1);
      SBAR(); });
#pragma unroll
    for (int i = 12; i < 16; ++i) f[i] = rdK(k0, k1, 1, i & 7);
    const float m = swap_max(max16(S0));
#pragma unroll
    for (int i = 0; i < 16; ++i) negm[i] = -m;
    bar();
    { const unsigned ko = ((SLOTK >> 4) & 15u) * 16384u;
#pragma unroll
      for (int i = 0; i < RA; ++i) pre[i] = rdK(kb0 + ko, kb1 + ko, 0, i); }
    SBAR();
    float e[16]; float s0 = 0.f, s1 = 0.f;
    static_for<9>([&](auto G) { constexpr int g = G.value;
      if constexpr (g == 0) S1 = MFMA(f[8], qf[0], negm); else if constexpr (g < 8) S1 = MFMA(f[8 + g], qf[g], S1);
      if constexpr (g < 8) { e[2 * g] = __builtin_amdgcn_exp2f(S0[2 * g] - m); e[2 * g + 1] = __builtin_amdgcn_exp2f(S0[2 * g + 1] - m); pinf(e[2 * g]); pinf(e[2 * g + 1]); }
      if constexpr (g == 1) { s0 = e[0]; s1 = e[1]; } else if constexpr (g > 1) { s0 = fadd(s0, e[2 * g - 2]); s1 = fadd(s1, e[2 * g - 1]); pinf(s0); pinf(s1); }
      if constexpr (g >= 1) { unsigned w = cvtpk(e[2 * g - 2], e[2 * g - 1]); pinu(w); P0[(g - 1) >> 2][(g - 1) & 3] = w; }
      SBAR(); });
    l = s0 + s1; }
  ST(12);
  auto unit = [&](auto KB, auto LAST, lds_cptr k0, lds_cptr k1, lds_cptr v, lds_cptr nk0, lds_cptr nk1, f32x16& Sn, f32x16& Sc, const u32x4 (&Pp)[2], u32x4 (&Pc)[2]) {
    constexpr int kb = KB.value; constexpr bool last = LAST.value;
    float e[16]; float s0 = 0.f, s1 = 0.f; bf16x8 f[16 + RA];
#pragma unroll
    for (int i = 0; i < RA; ++i) f[i] = pre[i];
    static_for<16>([&](auto G) { constexpr int g = G.value; constexpr int n = g + RA; constexpr int gb = 16 - RA + 1;
      if constexpr (last && g == gb) { bar(); f[16] = rdK(nk0, nk1, 0, 0); }
      if constexpr (n < 8) f[n] = rdK(k0, k1, kb, n); else if constexpr (n < 16) f[n] = rdV(v, kb, n - 8);
      else if constexpr (!last) f[n] = rdK(k0, k1, kb + 1, n - 16);
      else if constexpr (n > 16) f[n] = rdK(nk0, nk1, 0, n - 16);
      if constexpr (g == 0) Sn = MFMA(f[0], qf[0], negm); else if constexpr (g < 8) Sn = MFMA(f[g], qf[g], Sn);
      else o[g & 3] = MFMA(f[g], __builtin_bit_cast(bf16x8, Pp[(g >> 2) & 1]), o[g & 3]);
      constexpr int e0 = g < 12 ? (g / 3) * 4 + (g % 3 == 0 ? 0 : g % 3 + 1) : 16, e1 = g + 1 < 12 ? ((g + 1) / 3) * 4 + ((g + 1) % 3 == 0 ? 0 : (g + 1) % 3 + 1) : 16;
      constexpr int p0 = g >= 1 ? (g - 1 < 12 ? ((g - 1) / 3) * 4 + ((g - 1) % 3 == 0 ? 0 : (g - 1) % 3 + 1) : 16) : 0, p1 = g >= 1 ? e0 : 0;
      static_for<e1 - e0>([&](auto J) { constexpr int i = e0 + J.value; e[i] = __builtin_amdgcn_exp2f(Sc[i]); pinf(e[i]); });
      static_for<p1 - p0>([&](auto J) { constexpr int i = p0 + J.value;
        if constexpr (i == 0) s0 = e[0]; else if constexpr (i == 1) s1 = e[1]; else if constexpr (i & 1) { s1 = fadd(s1, e[i]); pinf(s1); } else { s0 = fadd(s0, e[i]); pinf(s0); }
        if constexpr (i & 1) { unsigned w = cvtpk(e[i - 1], e[i]); pinu(w); Pc[i >> 3][(i >> 1) & 3] = w; } });
      SBAR(); });
#pragma unroll
    for (int i = 0; i < RA; ++i) pre[i] = f[16 + i];
    sm_fix(s0 + s1, Sc, Sn, Pc); };
#pragma nounroll
  for (unsigned t = 1; t < 8; ++t) {
    const unsigned ko = ((SLOTK >> (4u * t)) & 15u) * 16384u, vo = ((SLOTV >> (4u * (t - 1u))) & 15u) * 16384u, nko = ((SLOTK >> (4u * ((t + 1u) & 7u))) & 15u) * 16384u;
    SBAR();
    unit(std::integral_constant<int, 0>{}, std::false_type{}, kb0 + ko, kb1 + ko, vb + vo, kb0, kb1, S0, S1, P0, P1);
    unit(std::integral_constant<int, 1>{}, std::true_type{}, kb0 + ko, kb1 + ko, vb + vo, kb0 + nko, kb1 + nko, S1, S0, P1, P0);
    ST(12 + t);
  }
  {
    const lds_cptr v = vb + ((SLOTV >> 28) & 15u) * 16384u;
    bf16x8 f[16];
#pragma unroll
    for (int i = 0; i < 3; ++i) f[i] = rdV(v, 0, i);
    float e[16]; float s0 = 0.f, s1 = 0.f;
    SBAR();
    static_for<9>([&](auto G) { constexpr int g = G.value; constexpr int n = g + 3;
      if constexpr (n < 8) f[n] = rdV(v, 0, n); else if constexpr (g < 8) f[n] = rdV(v, 1, ((n - 8) >> 1) + 4 * ((n - 8) & 1));
      if constexpr (g < 8) { o[g & 3] = MFMA(f[g], __builtin_bit_cast(bf16x8, P0[g >> 2]), o[g & 3]);
        e[2 * g] = __builtin_amdgcn_exp2f(S1[2 * g]); e[2 * g + 1] = __builtin_amdgcn_exp2f(S1[2 * g + 1]); pinf(e[2 * g]); pinf(e[2 * g + 1]); }
      if constexpr (g == 1) { s0 = e[0]; s1 = e[1]; } else if constexpr (g > 1) { s0 = fadd(s0, e[2 * g - 2]); s1 = fadd(s1, e[2 * g - 1]); pinf(s0); pinf(s1); }
      if constexpr (g >= 1) { unsigned w = cvtpk(e[2 * g - 2], e[2 * g - 1]); pinu(w); P1[(g - 1) >> 2][(g - 1) & 3] = w; }
      SBAR(); });
    sm_fix(s0 + s1, S1, S0, P1);
    const float inv = __builtin_amdgcn_rcpf(swap_add(l));
    const unsigned so = ((b * 7u + wid) * NH + h) * 8192u, vo = lane * 16u;
    auto put = [&](int k8) { u32x4 w;
#pragma unroll
      for (int e2 = 0; e2 < 4; ++e2) w[e2] = cvtpk(o[k8 >> 1][8 * (k8 & 1) + 2 * e2] * inv, o[k8 >> 1][8 * (k8 & 1) + 2 * e2 + 1] * inv);
      bstore_sc1(w, vo, sO, so + k8 * 1024u); };
    static_for<8>([&](auto G) { constexpr int g = G.value; constexpr int n = g + 11;
      if constexpr (n < 16) f[n] = rdV(v, 1, ((n - 8) >> 1) + 4 * ((n - 8) & 1));
      o[g >> 1] = MFMA(f[8 + g], __builtin_bit_cast(bf16x8, P1[g & 1]), o[g >> 1]);
      if constexpr (g >= 4) put(g - 4);
      SBAR(); });
    ST(20);
    put(4); put(5); put(6); put(7); }
  ST(21);
  }
  wait_vm<0>();
  ST(22);
  bar();
  if (tid == 0) st_agent(ws + FLAG_OFF + (b * NH + h) * FLAG_STRIDE, tag);
  unsigned dummy = 0;
  if (RETOUCH && wid < 7u) touch(dummy, lane * 128u + ((b * 7u + wid) * NH + h) * 8192u, sO, 0u);
  ST(23);
  if (!has_tile) {
    wait_vm<0>(); pinu(dummy);
    if (bid == 249u && wid == 0u) {
#pragma nounroll
      for (unsigned i = 0; i < 4; ++i) poll_flags(ws + FLAG_OFF + (i * 64u + lane) * FLAG_STRIDE, tag);
      if (lane == 0) st_agent(ws + EPOCH_OFF, tag); }
    ST(24); ST(25); ST(26); ST(27); ST(28); ST(29); ST(30); ST(31); ST_OUT(ws, 0, 32); return; }
  const unsigned jq = (2u * g4 + 2u + wid) & 7u, G = 8u * rt + jq;
  const unsigned voffO = lane * 16u, soO = (G * NH + g4 * 8u) * 8192u, hr = HROT ? (ct & 7u) : 0u;
  const char* fp = ws + FLAG_OFF + (((G * 37u) >> 8) * NH + g4 * 8u + (((lane & 7u) + hr) & 7u)) * FLAG_STRIDE;
  unsigned avail = 0, todo = 0xFFu;
  auto next_head = [&]() {
    for (unsigned spins = 0; avail == 0u; ++spins) {
      avail = (unsigned)__ballot(ld_agent(fp) == tag) & todo;
      if (avail == 0u) { if (spins >= SPIN_CAP) avail = todo; else __builtin_amdgcn_s_sleep(PSLEEP); } }
    asm volatile("" ::: "memory");
    const unsigned i = __builtin_ctz(avail); avail &= avail - 1u; todo &= ~(1u << i); return (i + hr) & 7u; };
  const lds_cptr wb = (lds_cptr)lds + 1024u * hh + 64u * q4 + trcol;
  f32x16 acc[2] = {};
  if (OPRIO && wid < 6u) __builtin_amdgcn_s_setprio(1);
  { unsigned hd = next_head();
    pinu(dummy);
    ST(24);
    unsigned so_c = soO + hd * 8192u, so_n = 0; lds_cptr wp_c = wb + ((SLOTW >> (4u * hd)) & 15u) * 16384u, wp_n = wp_c;
    bf16x8 of[8], lf[4][2];
    auto rdW = [&](lds_cptr p, int s2) { lf[s2 & 3][0] = trd2(p, (s2 & 7) * 2048, (s2 & 7) * 2048 + 256); lf[s2 & 3][1] = trd2(p, (s2 & 7) * 2048 + 512, (s2 & 7) * 2048 + 768); };
#pragma unroll
    for (int i = 0; i < 8; ++i) { if (i < WO) bload_sc1(of[i], voffO, sO, so_c + i * 1024u); else of[i] = bf16x8{}; }
    rdW(wp_c, 0); rdW(wp_c, 1);
#pragma nounroll
    for (unsigned p = 0; p < 8; ++p) {
      static_for<8>([&](auto S) { constexpr int s2 = S.value;
        if constexpr (s2 == 8 - WO) { if (p < 7u) { hd = next_head(); so_n = soO + hd * 8192u; wp_n = wb + ((SLOTW >> (4u * hd)) & 15u) * 16384u; } else so_n = O_BYTES; }
        wait_vm1<WO - 1>(of[s2]);
        rdW(s2 + 2 < 8 ? wp_c : wp_n, s2 + 2);
        acc[0] = MFMA(lf[s2 & 3][0], of[s2], acc[0]); acc[1] = MFMA(lf[s2 & 3][1], of[s2], acc[1]);
        SBAR();
        breload_sc1(of[(s2 + WO) & 7], voffO, sO, (s2 + WO < 8 ? so_c : so_n) + ((s2 + WO) & 7) * 1024u);
        SBAR(); });
      so_c = so_n; wp_c = wp_n;
      if (p == 1u) ST(25); if (p == 3u) ST(26); if (p == 5u) ST(27);
    }
    wait_vm<0>();
#pragma unroll
    for (int i = 0; i < 8; ++i) pin(of[i]); }
  ST(28);
  const srd_t sX2 = mk_srd(ws + X_OFF, X_BYTES);
  const unsigned xb = (u * 8u + jq) * 4u;
  if (wid < 6u) {
#pragma unroll
    for (int i = 0; i < 4; ++i) { const u32x4 w4 = {cvtpk(acc[i >> 1][8 * (i & 1)], acc[i >> 1][8 * (i & 1) + 1]), cvtpk(acc[i >> 1][8 * (i & 1) + 2], acc[i >> 1][8 * (i & 1) + 3]),
                                                    cvtpk(acc[i >> 1][8 * (i & 1) + 4], acc[i >> 1][8 * (i & 1) + 5]), cvtpk(acc[i >> 1][8 * (i & 1) + 6], acc[i >> 1][8 * (i & 1) + 7])};
      bstore_sc1(w4, voffO, sX2, (xb + g4) * 4096u + i * 1024u); }
    wait_vm<0>();
    if (lane == 0) st_agent(ws + FLAG2_OFF + (xb + g4) * FLAG_STRIDE, tag);
    ST(29); ST(30);
  } else {
    poll_flags(ws + FLAG2_OFF + (xb + ((g4 + 1u + lane % 3u) & 3u)) * FLAG_STRIDE, tag);
    ST(29);
    bf16x8 xp[12];
#pragma unroll
    for (int i = 0; i < 12; ++i) bload_sc1(xp[i], voffO, sX2, (xb + ((g4 + 1u + (i >> 2)) & 3u)) * 4096u + (i & 3) * 1024u);
    wait_vm<0>();
#pragma unroll
    for (int j = 0; j < 12; ++j) { const int i = j & 3; pin(xp[j]); const u32x4 v = __builtin_bit_cast(u32x4, xp[j]);
#pragma unroll
      for (int e = 0; e < 4; ++e) { acc[i >> 1][8 * (i & 1) + 2 * e] += __uint_as_float(v[e] << 16); acc[i >> 1][8 * (i & 1) + 2 * e + 1] += __uint_as_float(v[e] & 0xFFFF0000u); } }
    ST(30);
    char* op = (char*)out + (size_t)(G * 32u + r) * (DM * 2u) + ct * 128u + hh * 16u;
#pragma unroll
    for (int i = 0; i < 4; ++i) { const u32x4 w4 = {cvtpk(acc[i >> 1][8 * (i & 1)], acc[i >> 1][8 * (i & 1) + 1]), cvtpk(acc[i >> 1][8 * (i & 1) + 2], acc[i >> 1][8 * (i & 1) + 3]),
                                                    cvtpk(acc[i >> 1][8 * (i & 1) + 4], acc[i >> 1][8 * (i & 1) + 5]), cvtpk(acc[i >> 1][8 * (i & 1) + 6], acc[i >> 1][8 * (i & 1) + 7])};
      *(u32x4*)(op + i * 32) = w4; }
  }
  ST(31); ST_OUT(ws, 0, 32);
}

#define WSOFF 0
extern "C" void kernel_launch(void* const* d_in, const int* in_sizes, int n_in, void* d_out, int out_size, void* d_ws, size_t ws_size, hipStream_t stream) {
  (void)in_sizes; (void)n_in; (void)out_size; (void)ws_size;
  (void)hipFuncSetAttribute((const void*)mha_kernel, hipFuncAttributeMaxDynamicSharedMemorySize, LDS_BYTES);
  const unsigned short* x = (const unsigned short*)d_in[0]; const unsigned short* kc = (const unsigned short*)d_in[1]; const unsigned short* vc = (const unsigned short*)d_in[2];
  const unsigned short* wq = (const unsigned short*)d_in[3]; const unsigned short* wo = (const unsigned short*)d_in[4];
  mha_kernel<<<256, 512, LDS_BYTES, stream>>>(x, kc, vc, wq, wo, (char*)d_ws + WSOFF, (unsigned short*)d_out);
}
```

```cpp
#define RET 0
#define HG 3
#include <hip/hip_runtime.h>
#include <stdint.h>
#include <utility>

typedef short    bf16x8 __attribute__((ext_vector_type(8)));
typedef short    s16x4  __attribute__((ext_vector_type(4)));
typedef float    f32x16 __attribute__((ext_vector_type(16)));
typedef float    f32x2  __attribute__((ext_vector_type(2)));
typedef __bf16   bf16x2 __attribute__((ext_vector_type(2)));
typedef unsigned u32x4  __attribute__((ext_vector_type(4)));
typedef __attribute__((address_space(3))) const char* lds_cptr;
typedef __attribute__((address_space(1))) unsigned long long gu64;
typedef __amdgpu_buffer_rsrc_t srd_t;

#define DI __device__ __forceinline__
#define MFMA(a, b, c) __builtin_amdgcn_mfma_f32_32x32x16_bf16((a), (b), (c), 0, 0, 0)
#define SBAR() __builtin_amdgcn_sched_barrier(0)

constexpr int NB = 8, SQ = 224, DM = 576, NH = 32, SK = 512, DH = 128;
constexpr int LDS_BYTES = 163840;
constexpr float QSCALE = 0.08838834764831845f * 1.4426950408889634f;
constexpr float SUM_THR = 4096.f;

template <int... Is, class F> DI void static_for_impl(std::integer_sequence<int, Is...>, F&& f) { (f(std::integral_constant<int, Is>{}), ...); }
template <int N, class F> DI void static_for(F&& f) { static_for_impl(std::make_integer_sequence<int, N>{}, f); }

DI unsigned rfl(unsigned x) { return __builtin_amdgcn_readfirstlane(x); }
DI unsigned cvtpk(float lo, float hi) { f32x2 v = {lo, hi}; bf16x2 b = __builtin_convertvector(v, bf16x2); return __builtin_bit_cast(unsigned, b); }
DI srd_t mk_srd(const void* p, unsigned bytes) { return __builtin_amdgcn_make_buffer_rsrc((void*)p, (short)0, (int)bytes, 0x00020000); }
DI void dma4(unsigned lds, unsigned v0, unsigned v1, unsigned v2, unsigned v3, srd_t srd, unsigned soff) {
  asm volatile("s_mov_b32 m0, %0\n\ts_nop 0\n\tbuffer_load_dwordx4 %1, %5, %6 offen lds\n\tbuffer_load_dwordx4 %2, %5, %6 offen offset:1024 lds\n\t"
               "buffer_load_dwordx4 %3, %5, %6 offen offset:2048 lds\n\tbuffer_load_dwordx4 %4, %5, %6 offen offset:3072 lds"
               :: "s"(lds), "v"(v0), "v"(v1), "v"(v2), "v"(v3), "s"(srd), "s"(soff) : "memory");
}
DI void dma4_nt(unsigned lds, unsigned v0, unsigned v1, unsigned v2, unsigned v3, srd_t srd, unsigned soff) {
  asm volatile("s_mov_b32 m0, %0\n\ts_nop 0\n\tbuffer_load_dwordx4 %1, %5, %6 offen nt lds\n\tbuffer_load_dwordx4 %2, %5, %6 offen offset:1024 nt lds\n\t"
               "buffer_load_dwordx4 %3, %5, %6 offen offset:2048 nt lds\n\tbuffer_load_dwordx4 %4, %5, %6 offen offset:3072 nt lds"
               :: "s"(lds), "v"(v0), "v"(v1), "v"(v2), "v"(v3), "s"(srd), "s"(soff) : "memory");
}
DI void bstore_sc1(const u32x4& d, unsigned voff, srd_t srd, unsigned soff) { asm volatile("buffer_store_dwordx4 %0, %1, %2, %3 offen sc1\n\ts_nop 1" :: "v"(d), "v"(voff), "s"(srd), "s"(soff) : "memory"); }
DI void bload_sc1(bf16x8& d, unsigned voff, srd_t srd, unsigned soff) { asm volatile("buffer_load_dwordx4 %0, %1, %2, %3 offen sc1" : "=v"(d) : "v"(voff), "s"(srd), "s"(soff) : "memory"); }
DI void breload_sc1(bf16x8& d, unsigned voff, srd_t srd, unsigned soff) { asm volatile("buffer_load_dwordx4 %0, %1, %2, %3 offen sc1" : "+v"(d) : "v"(voff), "s"(srd), "s"(soff) : "memory"); }
DI void touch(unsigned& d, unsigned voff, srd_t srd, unsigned soff) { asm volatile("buffer_load_dword %0, %1, %2, %3 offen" : "+v"(d) : "v"(voff), "s"(srd), "s"(soff) : "memory"); }
DI void touch1(unsigned& d, unsigned voff, srd_t srd, unsigned soff) { asm volatile("buffer_load_dword %0, %1, %2, %3 offen sc1" : "+v"(d) : "v"(voff), "s"(srd), "s"(soff) : "memory"); }
DI void pin(bf16x8& a) { asm volatile("" : "+v"(a)); }
DI void pinf(float& a) { asm volatile("" : "+v"(a)); }
DI void pinu(unsigned& a) { asm volatile("" : "+v"(a)); }
template <int N> DI void wait_vm1(bf16x8& a) { asm volatile("s_waitcnt vmcnt(%1)" : "+v"(a) : "n"(N) : "memory"); }
template <int N> DI void wait_vm() { asm volatile("s_waitcnt vmcnt(%0)" :: "n"(N) : "memory"); }
template <int N> DI void wait_lgkm() { __builtin_amdgcn_s_waitcnt(0xC07F | (N << 8)); SBAR(); }
DI void bar() { asm volatile("s_waitcnt lgkmcnt(0)\n\ts_barrier" ::: "memory"); }
typedef short v4i16_t __attribute__((ext_vector_type(4)));
DI s16x4 trd(lds_cptr p) { return __builtin_amdgcn_ds_read_tr16_b64_v4i16((__attribute__((address_space(3))) v4i16_t*)p); }
DI bf16x8 trd2(lds_cptr p, int o0, int o1) { s16x4 a = trd(p + o0), b = trd(p + o1); return __builtin_shufflevector(a, b, 0, 1, 2, 3, 4, 5, 6, 7); }
DI bf16x8 rd128(lds_cptr p) { return *(const __attribute__((address_space(3))) bf16x8*)p; }
DI float swap_max(float m) { auto rr = __builtin_amdgcn_permlane32_swap(__float_as_uint(m), __float_as_uint(m), false, false); return fmaxf(__uint_as_float(rr[0]), __uint_as_float(rr[1])); }
DI float swap_add(float m) { auto rr = __builtin_amdgcn_permlane32_swap(__float_as_uint(m), __float_as_uint(m), false, false); return __uint_as_float(rr[0]) + __uint_as_float(rr[1]); }
DI float max16(const f32x16& s) { float a = fmaxf(fmaxf(s[0], s[1]), s[2]), b = fmaxf(fmaxf(s[3], s[4]), s[5]);
  a = fmaxf(fmaxf(a, s[6]), s[7]); b = fmaxf(fmaxf(b, s[8]), s[9]); a = fmaxf(fmaxf(a, s[10]), s[11]); b = fmaxf(fmaxf(b, s[12]), s[13]); a = fmaxf(fmaxf(a, s[14]), s[15]); return fmaxf(a, b); }
DI float fadd(float a, float b) { float r; asm("v_add_f32_e32 %0, %1, %2" : "=v"(r) : "v"(a), "v"(b)); return r; }
DI u32x4 pack8(const f32x16& a, int s, float f) { return u32x4{cvtpk(a[8 * s] * f, a[8 * s + 1] * f), cvtpk(a[8 * s + 2] * f, a[8 * s + 3] * f), cvtpk(a[8 * s + 4] * f, a[8 * s + 5] * f), cvtpk(a[8 * s + 6] * f, a[8 * s + 7] * f)}; }
DI u32x4 pack8(const f32x16& a, int s) { return u32x4{cvtpk(a[8 * s], a[8 * s + 1]), cvtpk(a[8 * s + 2], a[8 * s + 3]), cvtpk(a[8 * s + 4], a[8 * s + 5]), cvtpk(a[8 * s + 6], a[8 * s + 7])}; }

DI float expo(const f32x16& s, u32x4 (&p)[2]) {
  float e[16]; float s0 = 0.f, s1 = 0.f;
#pragma unroll
  for (int i = 0; i < 16; ++i) e[i] = __builtin_amdgcn_exp2f(s[i]);
#pragma unroll
  for (int i = 0; i < 16; i += 2) { s0 += e[i]; s1 += e[i + 1]; }
#pragma unroll
  for (int i = 0; i < 8; ++i) p[i >> 2][i & 3] = cvtpk(e[2 * i], e[2 * i + 1]);
  return s0 + s1;
}

#define ST_DECL
#define ST(k)
#define ST_OUT(ws, lo, hi)

constexpr unsigned SLOTK = 0x04204209u;
constexpr unsigned SLOTV = 0x31531531u;
constexpr unsigned SLOTW = 0x05429876u;
constexpr unsigned STG = 45056u, STG_W = 28672u;
constexpr unsigned O_BYTES = NB * SQ * NH * DH * 2;
constexpr unsigned X_OFF = O_BYTES, X_BYTES = 63u * 8u * 4u * 8192u;
constexpr unsigned FLAG_OFF = X_OFF + X_BYTES, FLAG_STRIDE = 128, FLAG2_OFF = FLAG_OFF + 256 * FLAG_STRIDE, EPOCH_OFF = FLAG2_OFF + 63 * 32 * FLAG_STRIDE;
constexpr unsigned SPIN_CAP = 1u << 20;
#define RA_ 7
constexpr int RA = RA_;
#define WO 6
#define XMODE 0
#define A0BAR 6
#define PSLEEP 4
#define CODEPF 17920
constexpr int lds_ops(int a, int b, bool last) { int n = 0; for (int i = a; i < b; ++i) n += i >= 16 ? (last ? 0 : 1) : (i >= 8 ? 2 : 1); return n; }
constexpr int pv_order(int i) { return i < 4 ? i : i < 8 ? i + 4 : i < 12 ? i - 4 : i; }
DI unsigned long long mix64(unsigned long long z) { z += 0x9E3779B97F4A7C15ull; z = (z ^ (z >> 30)) * 0xBF58476D1CE4E5B9ull; z = (z ^ (z >> 27)) * 0x94D049BB133111EBull; return z ^ (z >> 31); }
DI unsigned long long rfl64(unsigned long long v) { return ((unsigned long long)rfl((unsigned)(v >> 32)) << 32) | rfl((unsigned)v); }
DI unsigned long long ld_agent(const char* p) { return __hip_atomic_load((gu64*)p, __ATOMIC_RELAXED, __HIP_MEMORY_SCOPE_AGENT); }
DI void st_agent(char* p, unsigned long long v) { __hip_atomic_store((gu64*)p, v, __ATOMIC_RELAXED, __HIP_MEMORY_SCOPE_AGENT); }
DI void poll_flags(const char* p, unsigned long long tag) {
  _Pragma("nounroll") for (unsigned spins = 0; spins < SPIN_CAP; ++spins) { if (__all(ld_agent(p) == tag)) break; __builtin_amdgcn_s_sleep(PSLEEP); }
  asm volatile("" ::: "memory");
}

__global__ __launch_bounds__(512, 1) void mha_kernel(const unsigned short* __restrict__ x, const unsigned short* __restrict__ kc, const unsigned short* __restrict__ vc,
                                                     const unsigned short* __restrict__ wq, const unsigned short* __restrict__ wo, char* ws, unsigned short* __restrict__ out) {
  extern __shared__ __attribute__((aligned(16))) char lds[];
  unsigned long long pc0; asm volatile("s_getpc_b64 %0" : "=s"(pc0));
  const unsigned tid = threadIdx.x, lane = tid & 63u, r = lane & 31u, hh = lane >> 5;
  const unsigned wid = rfl(tid >> 6);
  ST_DECL ST(0);
  const unsigned bid = blockIdx.x, xcd = bid & 7u, ix = bid >> 3;
  const unsigned b = (xcd & 1u) * 4u + (ix & 3u), h = (xcd >> 1) * 8u + (ix >> 2);
  const unsigned lds0 = (unsigned)(uintptr_t)lds;
  const srd_t sW = mk_srd((const char*)wq - 1024, DM * NH * DH * 2 + 1024);
  const srd_t sX = mk_srd(x + (size_t)b * SQ * DM, SQ * DM * 2);
  const unsigned prow = (lane >> 2) & 7u, pch = 4u * hh + (lane & 3u);
  const unsigned pchx0 = 4u * hh + ((lane & 3u) ^ ((lane >> 4) & 1u)), pchx1 = pchx0 ^ 2u;
  const unsigned vW = prow * 8192u + pch * 16u, vX0 = prow * (DM * 2u) + pchx0 * 16u, vX1 = prow * (DM * 2u) + pchx1 * 16u;
  const unsigned ck0 = 3u * (((ix & 3u) + (ix >> 2)) % 3u);
  if (wid >= 4u) dma4(lds0 + STG_W + (wid - 4u) * 4096u, vW + 1024u, vW + 128u, vW + 65536u - 1024u, vW + 65664u - 2048u, sW, ck0 * 64u * 8192u + h * 256u + (wid - 4u) * 131072u);
  if (wid < 7u) dma4(lds0 + wid * 4096u, vX0, vX1 + 8192u, vX0 + 16384u, vX1 + 24576u, sX, ck0 * 128u + wid * 32u * DM * 2u);
  const unsigned q4 = (lane & 15u) >> 2, p4 = lane & 3u, blk = (lane >> 4) & 1u;
  const unsigned trcol = 32u * blk + 16u * (p4 & 1u) + 8u * (p4 >> 1);
  const srd_t sO = mk_srd(ws, O_BYTES);
  unsigned long long tag;
  const unsigned g4 = xcd >> 1, u0 = (xcd & 1u) * 32u + ix;
  const bool has_tile = u0 < 63u;
  const unsigned u = has_tile ? u0 : 62u, rt = u / 9u, ct = u - rt * 9u;

  if (wid == 7u) {
    const srd_t sK = mk_srd((const char*)(kc + (size_t)(b * NH + h) * SK * DH) - 1024, SK * DH * 2 + 1024);
    const srd_t sV = mk_srd((const char*)(vc + (size_t)(b * NH + h) * SK * DH) - 1024, SK * DH * 2 + 1024);
    auto fillQ = [&](unsigned c, unsigned stage) {
      const unsigned lw = lds0 + stage + STG_W, sw = c * 64u * 8192u + h * 256u, lx = lds0 + stage, sx = c * 128u;
#pragma unroll
      for (int i = 0; i < 4; ++i) dma4(lw + i * 4096u, vW + 1024u, vW + 128u, vW + 65536u - 1024u, vW + 65664u - 2048u, sW, sw + i * 131072u);
#pragma unroll
      for (int i = 0; i < 7; ++i) dma4(lx + i * 4096u, vX0, vX1 + 8192u, vX0 + 16384u, vX1 + 24576u, sX, sx + i * 32u * DM * 2u); };
    unsigned long long ep; asm volatile("global_load_dwordx2 %0, %1, %2 sc1" : "=v"(ep) : "v"(0u), "s"(ws + EPOCH_OFF) : "memory");
    { unsigned stage = STG, ck = ck0 + 1u;
#pragma nounroll
      for (unsigned c = 1; c < 9; ++c) { fillQ(ck, stage); ck = ck == 8u ? 0u : ck + 1u; stage = stage == 2 * STG ? 0u : stage + STG;
        if (c < 8u) { wait_vm<44>(); bar(); ST(1 + c); } } }
    const unsigned vV = prow * 256u + pch * 16u, vK0 = prow * 256u + pchx0 * 16u, vK1 = prow * 256u + pchx1 * 16u;
    const srd_t sWo = mk_srd(wo, NH * DH * DM * 2);
    const unsigned vWo = prow * (DM * 2u) + pch * 16u;
    { unsigned j = 0, jw = 0;
#pragma nounroll
      for (unsigned n = 0; n < 12; ++n) {
        const unsigned we = (unsigned)(0x875443210000ull >> (4u * n)) & 15u;
#pragma nounroll
        for (; jw < we; ++jw) { const unsigned ld = lds0 + ((SLOTW >> (4u * jw)) & 15u) * 16384u, so = (g4 * 8u + jw) * 128u * (DM * 2u) + ct * 128u;
#pragma unroll
          for (int k = 0; k < 4; ++k) dma4(ld + k * 4096u, vWo, vWo + 8192u, vWo + 16384u, vWo + 24576u, sWo, so + k * 32u * (DM * 2u)); }
        const unsigned je = ((unsigned)(0xFFFECA864320ull >> (4u * n)) & 15u) + 1u;
#pragma nounroll
        for (; j < je; ++j) {
          if (j == 0u || ((j & 1u) && j != 15u)) { const unsigned t = (j + 1u) >> 1, ld = lds0 + ((SLOTK >> (4u * t)) & 15u) * 16384u;
#pragma unroll
            for (int i = 0; i < 4; ++i) dma4_nt(ld + i * 4096u, vK0 + 1024u, vK0 + 128u, vK1 + 1024u, vK1 + 128u, sK, t * 16384u + i * 4096u);
          } else { const unsigned t = (j - 1u) >> 1, ld = lds0 + ((SLOTV >> (4u * t)) & 15u) * 16384u;
#pragma unroll
            for (int i = 0; i < 4; ++i) dma4_nt(ld + i * 4096u, vV + 1024u, vV + 128u, vV + 1024u, vV + 128u, sV, t * 16384u + i * 4096u); } }
        if (n == 11u) break;
        if (n == 0u) wait_vm<60>(); else if (n == 3u || n >= 8u) wait_vm<32>(); else wait_vm<48>();
        bar(); ST(9 + n); } }
    asm volatile("" : "+v"(ep)); tag = mix64(rfl64(ep));
    ST(20); ST(21);
  } else {
  const unsigned rsw0 = 64u * (r & 7u) + 16u * (hh ^ ((r >> 2) & 3u)), rsw1 = rsw0 ^ 32u;
  f32x16 qa[4] = {}, o[4] = {};
  asm volatile("" : "+v"(qa[0]), "+v"(qa[1]), "+v"(qa[2]), "+v"(qa[3]), "+v"(o[0]), "+v"(o[1]), "+v"(o[2]), "+v"(o[3]));
  const char* wup;
  { const unsigned k = lane & 7u, sel = lane >> 3;
    wup = sel == 0u ? (const char*)(k & 1u ? vc : kc) + (size_t)(b * NH + h) * (SK * DH * 2) + (k >> 1) * 32768u
        : sel == 1u ? (const char*)wo + (size_t)(g4 * 1024u + k * 128u) * (DM * 2u) + ct * 128u
        : sel == 2u ? ws + ((b * 7u + k % 7u) * NH + h) * 8192u
        : sel == 3u ? ws + ((8u * rt + k) * NH + g4 * 8u) * 8192u
        : sel == 4u ? ws + X_OFF + u * (XMODE ? 262144u : 131072u) + (k & 1u) * 65536u
        : sel == 5u ? (const char*)out + (size_t)((8u * rt + k) * 32u) * (DM * 2u)
        : sel == 6u ? ws + FLAG_OFF + k * 32768u + 64u
        : (const char*)(pc0 & ~255ull) + min((ix * 8u + k) * 128u, (unsigned)CODEPF); }
  __builtin_amdgcn_s_waitcnt(0x0070);
  wait_vm<0>();
  ST(1);
  bar();
  unsigned cdummy = 0; unsigned long long ep;
  asm volatile("global_load_dwordx2 %0, %1, %2 sc1" : "=v"(ep) : "v"(0u), "s"(ws + EPOCH_OFF) : "memory");
  if (wid == 3u) asm volatile("global_load_dword %0, %1, off" : "+v"(cdummy) : "v"(wup) : "memory");
  ST(2);
  { lds_cptr wp = (lds_cptr)lds + STG_W + 2048u * hh + 64u * q4 + trcol;
    lds_cptr xp0 = (lds_cptr)lds + wid * 4096u + 1024u * (r >> 3) + rsw0, xp1 = (lds_cptr)lds + wid * 4096u + 1024u * (r >> 3) + rsw1;
    bf16x8 xf[4], af[5];
    auto rdA = [&](int i) { return trd2(wp, 4096 * (i >> 2) + 512 * (i & 3), 4096 * (i >> 2) + 512 * (i & 3) + 256); };
    xf[0] = rd128(xp0); af[0] = rdA(0); af[1] = rdA(1); af[2] = rdA(2); af[3] = rdA(3);
    unsigned stage = 0;
#pragma nounroll
    for (unsigned c = 0; c < 9; ++c) {
      SBAR();
      static_for<12>([&](auto I) { constexpr int i = I.value;
        af[(i + 4) % 5] = rdA(i + 4);
        if constexpr (i == 0) xf[1] = rd128(xp1); if constexpr (i == 3) xf[2] = rd128(xp0 + 512); if constexpr (i == 7) xf[3] = rd128(xp1 + 512);
        qa[i & 3] = MFMA(af[i % 5], xf[i >> 2], qa[i & 3]); SBAR(); });
      qa[0] = MFMA(af[12 % 5], xf[3], qa[0]); SBAR();
      bar();
      const unsigned nstage = stage == 2 * STG ? 0u : stage + STG; const int dlt = (int)nstage - (int)stage; stage = nstage;
      wp += dlt; xp0 += dlt; xp1 += dlt;
      const bf16x8 xl = xf[3], a13 = af[13 % 5], a14 = af[14 % 5], a15 = af[15 % 5];
      xf[0] = rd128(xp0); af[0] = rdA(0); af[1] = rdA(1);
      SBAR();
      qa[1] = MFMA(a13, xl, qa[1]); SBAR();
      af[2] = rdA(2);
      qa[2] = MFMA(a14, xl, qa[2]); SBAR();
      af[3] = rdA(3);
      qa[3] = MFMA(a15, xl, qa[3]); SBAR();
      ST(3 + c);
    } }

  const lds_cptr kb0 = (lds_cptr)lds + 2048u * (r >> 3) + rsw0, kb1 = (lds_cptr)lds + 2048u * (r >> 3) + rsw1;
  const lds_cptr vb = (lds_cptr)lds + 64u * (4u * hh + q4) + trcol;
  bf16x8 qf[8], pre[RA];
  f32x16 negm, S0, S1; u32x4 P0[2], P1[2]; float l = 0.f;
  auto rdK = [&](lds_cptr k0, lds_cptr k1, int kb, int i) { return rd128(((i & 1) ? k1 : k0) + 8192 * kb + 512 * (i >> 1)); };
  auto rdV = [&](lds_cptr v, int kb, int j) { return trd2(v, 8192 * kb + 4096 * (j >> 2) + 512 * (j & 3), 8192 * kb + 4096 * (j >> 2) + 2048 + 512 * (j & 3)); };
  auto sm_fix = [&](float ps, f32x16& Sc, f32x16& Sn, u32x4 (&P)[2]) {
    if (__builtin_expect(__any(!(ps <= SUM_THR)), 0)) {
      const float dl = fmaxf(swap_max(max16(Sc)), 0.f), f = __builtin_amdgcn_exp2f(-dl);
#pragma unroll
      for (int i = 0; i < 16; ++i) { Sc[i] -= dl; Sn[i] -= dl; negm[i] -= dl; }
#pragma unroll
      for (int db = 0; db < 4; ++db)
#pragma unroll
        for (int i = 0; i < 16; ++i) o[db][i] *= f;
      l *= f; ps = expo(Sc, P); }
    l += ps; };
  {
    lds_cptr k0 = kb0 + 9 * 16384, k1 = kb1 + 9 * 16384; asm volatile("" : "+v"(k0), "+v"(k1));
    bf16x8 f[16];
#pragma unroll
    for (int i = 0; i < 4; ++i) f[i] = rdK(k0, k1, 0, i);
    auto cvq = [&](int g) { qf[g] = __builtin_bit_cast(bf16x8, pack8(qa[g >> 1], g & 1, QSCALE)); pin(qf[g]); };
    cvq(0); SBAR();
    static_for<8>([&](auto G) { constexpr int g = G.value;
      f[g + 4] = rdK(k0, k1, (g + 4) >> 3, (g + 4) & 7);
      if constexpr (g == 0) { const f32x16 z = {}; S0 = MFMA(f[0], qf[0], z); } else S0 = MFMA(f[g], qf[g], S0);
      if constexpr (g < 7) cvq(g + 1);
      SBAR(); });
#pragma unroll
    for (int i = 12; i < 16; ++i) f[i] = rdK(k0, k1, 1, i & 7);
    const float m = swap_max(max16(S0));
#pragma unroll
    for (int i = 0; i < 16; ++i) negm[i] = -m;
    SBAR();
    float e[16]; float s0 = 0.f, s1 = 0.f;
    static_for<9>([&](auto G) { constexpr int g = G.value;
      if constexpr (g == A0BAR) { wait_vm<0>(); pinu(cdummy); asm volatile("" : "+v"(ep)); ep = rfl64(ep); bar();
        constexpr unsigned ko = ((SLOTK >> 4) & 15u) * 16384u;
#pragma unroll
        for (int i = 0; i < RA; ++i) pre[i] = rdK(kb0 + ko, kb1 + ko, 0, i);
        SBAR(); }
      if constexpr (g == 0) S1 = MFMA(f[8], qf[0], negm); else if constexpr (g < 8) S1 = MFMA(f[8 + g], qf[g], S1);
      if constexpr (g < 8) { e[2 * g] = __builtin_amdgcn_exp2f(S0[2 * g] - m); e[2 * g + 1] = __builtin_amdgcn_exp2f(S0[2 * g + 1] - m); pinf(e[2 * g]); pinf(e[2 * g + 1]); }
      if constexpr (g == 1) { s0 = e[0]; s1 = e[1]; } else if constexpr (g > 1) { s0 += e[2 * g - 2]; pinf(s0); s1 += e[2 * g - 1]; pinf(s1); }
      if constexpr (g >= 1) { unsigned w = cvtpk(e[2 * g - 2], e[2 * g - 1]); pinu(w); P0[(g - 1) >> 2][(g - 1) & 3] = w; }
      SBAR(); });
    l = s0 + s1; }
  ST(12);
  auto unit = [&](auto KB, auto LAST, lds_cptr k0, lds_cptr k1, lds_cptr v, lds_cptr nk0, lds_cptr nk1, f32x16& Sn, f32x16& Sc, const u32x4 (&Pp)[2], u32x4 (&Pc)[2]) {
    constexpr int kb = KB.value; constexpr bool last = LAST.value;
    float e[16]; float s0 = 0.f, s1 = 0.f; bf16x8 f[16 + RA];
#pragma unroll
    for (int i = 0; i < RA; ++i) f[i] = pre[i];
    static_for<16>([&](auto G) { constexpr int g = G.value; constexpr int n = g + RA; constexpr int gb = 12;
      if constexpr (last && g == gb) { wait_lgkm<0>(); bar(); SBAR(); }
      else if constexpr ((g & 3) == 0) wait_lgkm<lds_ops(g + 4, g + RA, last)>();
      if constexpr (g == 0) Sn = MFMA(f[0], qf[0], negm); else if constexpr (g < 8) Sn = MFMA(f[g], qf[g], Sn);
      else o[g & 3] = MFMA(f[g], __builtin_bit_cast(bf16x8, Pp[(g >> 2) & 1]), o[g & 3]);
      if constexpr (n < 8) f[n] = rdK(k0, k1, kb, n); else if constexpr (n < 16) f[n] = rdV(v, kb, n - 8);
      else if constexpr (!last) f[n] = rdK(k0, k1, kb + 1, n - 16);
      else if constexpr (g >= gb) { f[16 + 2 * (g - gb)] = rdK(nk0, nk1, 0, 2 * (g - gb)); if constexpr (2 * (g - gb) + 1 < RA) f[17 + 2 * (g - gb)] = rdK(nk0, nk1, 0, 2 * (g - gb) + 1); }
      if constexpr (g == 0) { e[0] = __builtin_amdgcn_exp2f(Sc[0]); pinf(e[0]); }
      if constexpr (g < 15) { e[g + 1] = __builtin_amdgcn_exp2f(Sc[g + 1]); pinf(e[g + 1]); }
      if constexpr (g == 1) { s0 = e[0]; s1 = e[1]; } else if constexpr (g > 1) { if constexpr (g & 1) { s1 += e[g]; pinf(s1); } else { s0 += e[g]; pinf(s0); } }
      if constexpr (g & 1) { unsigned w = cvtpk(e[g - 1], e[g]); pinu(w); Pc[g >> 3][(g >> 1) & 3] = w; }
      SBAR(); });
#pragma unroll
    for (int i = 0; i < RA; ++i) pre[i] = f[16 + i];
    sm_fix(s0 + s1, Sc, Sn, Pc); };
#pragma nounroll
  for (unsigned t = 1; t < 7; ++t) {
    const unsigned ko = ((SLOTK >> (4u * t)) & 15u) * 16384u, vo = ((SLOTV >> (4u * (t - 1u))) & 15u) * 16384u, nko = ((SLOTK >> (4u * ((t + 1u) & 7u))) & 15u) * 16384u;
    SBAR();
    unit(std::integral_constant<int, 0>{}, std::false_type{}, kb0 + ko, kb1 + ko, vb + vo, kb0, kb1, S0, S1, P0, P1);
    unit(std::integral_constant<int, 1>{}, std::true_type{}, kb0 + ko, kb1 + ko, vb + vo, kb0 + nko, kb1 + nko, S1, S0, P1, P0);
    if (t == 1u || t == 6u) ST(12 + t);
  }
  const unsigned me = (b * 7u + wid) * NH + h;
  {
    constexpr unsigned ko = ((SLOTK >> 28) & 15u) * 16384u;
    const lds_cptr k0 = kb0 + ko, k1 = kb1 + ko, v6 = vb + ((SLOTV >> 24) & 15u) * 16384u, v7 = vb + ((SLOTV >> 28) & 15u) * 16384u;
    u32x4 P2[2], P3[2];
    auto scaleP = [&](u32x4 (&P)[2], float f) {
#pragma unroll
      for (int i = 0; i < 8; ++i) { const unsigned w = P[i >> 2][i & 3]; P[i >> 2][i & 3] = cvtpk(__uint_as_float(w << 16) * f, __uint_as_float(w & 0xFFFF0000u) * f); } };
    auto fix = [&](auto NP, float ps, f32x16& Sc, f32x16& Sn, u32x4 (&P)[2]) {
      if (__builtin_expect(__any(!(ps <= SUM_THR)), 0)) {
        const float f = __builtin_amdgcn_exp2f(-fmaxf(swap_max(max16(Sc)), 0.f));
        scaleP(P0, f); if constexpr (NP.value > 1) scaleP(P1, f); if constexpr (NP.value > 2) scaleP(P2, f); }
      sm_fix(ps, Sc, Sn, P); };
    auto rdVt = [&](int i) { const int j = pv_order(i), db = j >> 3, un = (j >> 1) & 3, ks = j & 1; return rdV(un < 2 ? v6 : v7, un & 1, 4 * ks + db); };
    auto pvt = [&](auto I, const bf16x8& vf) { constexpr int j = pv_order(I.value), un = (j >> 1) & 3;
      const u32x4 pb = un == 0 ? P0[j & 1] : un == 1 ? P1[j & 1] : un == 2 ? P2[j & 1] : P3[j & 1];
      o[j >> 3] = MFMA(vf, __builtin_bit_cast(bf16x8, pb), o[j >> 3]); };
    bf16x8 f[16], w[32];
#pragma unroll
    for (int i = 0; i < RA; ++i) f[i] = pre[i];
    auto qsm = [&](auto H, f32x16& Sn, const f32x16& Sc, u32x4 (&Pc)[2]) { constexpr int hf = H.value;
      float e[16]; float s0 = 0.f, s1 = 0.f;
      static_for<9>([&](auto G) { constexpr int g = G.value, n = 8 * hf + g + RA;
        if constexpr (hf < 2 && g < 8) { if constexpr (g == 0) Sn = MFMA(f[8 * hf], qf[0], negm); else Sn = MFMA(f[8 * hf + g], qf[g], Sn);
          if constexpr (n < 16) f[n] = rdK(k0, k1, n >> 3, n & 7); }
        if constexpr (hf == 1 && g >= 2 && g < 8) w[g - 2] = rdVt(g - 2);
        if constexpr (hf == 2 && g < 8) { pvt(G, w[g]); w[g + 6] = rdVt(g + 6); }
        if constexpr (g < 8) { e[2 * g] = __builtin_amdgcn_exp2f(Sc[2 * g]); e[2 * g + 1] = __builtin_amdgcn_exp2f(Sc[2 * g + 1]); pinf(e[2 * g]); pinf(e[2 * g + 1]); }
        if constexpr (g == 1) { s0 = e[0]; s1 = e[1]; } else if constexpr (g > 1) { s0 += e[2 * g - 2]; pinf(s0); s1 += e[2 * g - 1]; pinf(s1); }
        if constexpr (g >= 1) { unsigned pw = cvtpk(e[2 * g - 2], e[2 * g - 1]); pinu(pw); Pc[(g - 1) >> 2][(g - 1) & 3] = pw; }
        SBAR(); });
      return s0 + s1; };
    SBAR();
    fix(std::integral_constant<int, 1>{}, qsm(std::integral_constant<int, 0>{}, S0, S1, P1), S1, S0, P1);
    fix(std::integral_constant<int, 2>{}, qsm(std::integral_constant<int, 1>{}, S1, S0, P2), S0, S1, P2);
    bar();
    ST(19);
    fix(std::integral_constant<int, 3>{}, qsm(std::integral_constant<int, 2>{}, S0, S1, P3), S1, S0, P3);
    const float inv = __builtin_amdgcn_rcpf(swap_add(l));
    auto put = [&](int k8) { bstore_sc1(pack8(o[k8 >> 1], k8 & 1, inv), lane * 16u, sO, me * 8192u + k8 * 1024u); };
    static_for<24>([&](auto G) { constexpr int g = G.value + 8;
      pvt(std::integral_constant<int, g>{}, w[g]);
      if constexpr (g + 6 < 32) w[g + 6] = rdVt(g + 6);
      if constexpr (g == 14) put(0); if constexpr (g == 16) put(1); if constexpr (g == 19) put(2); if constexpr (g == 22) put(3); if constexpr (g == 26) put(4); if constexpr (g == 29) put(5);
      SBAR(); });
    ST(20);
    put(6); put(7); }
  ST(21);
  tag = mix64(ep);
  }
  wait_vm<0>();
  ST(22);
  bar();
  if (tid == 0) st_agent(ws + FLAG_OFF + (b * NH + h) * FLAG_STRIDE, tag);
  if (RET && wid < 7u) { unsigned dummy = 0; touch1(dummy, lane * 128u + ((b * 7u + wid) * NH + h) * 8192u, sO, 0u); wait_vm<0>(); pinu(dummy); }
  ST(23);
  if (!has_tile) {
    if (bid == 249u && wid == 0u) {
#pragma nounroll
      for (unsigned i = 0; i < 4; ++i) poll_flags(ws + FLAG_OFF + (i * 64u + lane) * FLAG_STRIDE, tag);
      if (lane == 0) st_agent(ws + EPOCH_OFF, tag); }
    ST(24); ST(25); ST(26); ST(27); ST(28); ST(29); ST(30); ST(31); ST_OUT(ws, 0, 32); return; }
  const unsigned jq = (2u * g4 + 2u + wid) & 7u, G = 8u * rt + jq;
  const unsigned voffO = lane * 16u, soO = (G * NH + g4 * 8u) * 8192u;
  poll_flags(ws + FLAG_OFF + (((G * 37u) >> 8) * NH + g4 * 8u + (lane & 7u)) * FLAG_STRIDE, tag);
  ST(24);
  const lds_cptr wb = (lds_cptr)lds + 1024u * hh + 64u * q4 + trcol;
  f32x16 acc[2] = {};
  { unsigned hd = HG == 3 ? (ct % 3u) * 3u : (ct * (8u / HG)) & 7u, so_c = soO + hd * 8192u; lds_cptr wp = wb + ((SLOTW >> (4u * hd)) & 15u) * 16384u;
    bf16x8 of[8], lf[4][2];
    auto rdW = [&](int s2) { lf[s2 & 3][0] = trd2(wp, (s2 & 7) * 2048, (s2 & 7) * 2048 + 256); lf[s2 & 3][1] = trd2(wp, (s2 & 7) * 2048 + 512, (s2 & 7) * 2048 + 768); };
#pragma unroll
    for (int i = 0; i < 8; ++i) { if (i < WO) bload_sc1(of[i], voffO, sO, so_c + i * 1024u); else of[i] = bf16x8{}; }
    rdW(0); rdW(1);
#pragma nounroll
    for (unsigned p = 0; p < 8; ++p) {
      hd = (hd + 1u) & 7u;
      const unsigned so_n = p < 7u ? soO + hd * 8192u : O_BYTES;
      static_for<8>([&](auto S) { constexpr int s2 = S.value;
        if constexpr (s2 == 6) wp = wb + ((SLOTW >> (4u * hd)) & 15u) * 16384u;
        wait_vm1<WO - 1>(of[s2]);
        wait_lgkm<4>();
        acc[0] = MFMA(lf[s2 & 3][0], of[s2], acc[0]);
        rdW(s2 + 2);
        SBAR();
        acc[1] = MFMA(lf[s2 & 3][1], of[s2], acc[1]);
        SBAR();
        breload_sc1(of[(s2 + WO) & 7], voffO, sO, (s2 + WO < 8 ? so_c : so_n) + ((s2 + WO) & 7) * 1024u);
        SBAR(); });
      so_c = so_n;
    }
    wait_vm<0>();
#pragma unroll
    for (int i = 0; i < 8; ++i) pin(of[i]); }
  ST(28);
  const srd_t sX2 = mk_srd(ws + X_OFF, X_BYTES);
  const unsigned xb = (u * 8u + jq) * 4u;
  if (wid < 6u) {
#pragma unroll
    for (int i = 0; i < 4; ++i) bstore_sc1(pack8(acc[i >> 1], i & 1), voffO, sX2, (xb + g4) * 4096u + i * 1024u);
    wait_vm<0>();
    if (lane == 0) st_agent(ws + FLAG2_OFF + (xb + g4) * FLAG_STRIDE, tag);
    ST(29); ST(30);
  } else {
    poll_flags(ws + FLAG2_OFF + (xb + ((g4 + 1u + lane % 3u) & 3u)) * FLAG_STRIDE, tag);
    ST(29);
    bf16x8 xp[12];
#pragma unroll
    for (int i = 0; i < 12; ++i) bload_sc1(xp[i], voffO, sX2, (xb + ((g4 + 1u + (i >> 2)) & 3u)) * 4096u + (i & 3) * 1024u);
    wait_vm<0>();
#pragma unroll
    for (int j = 0; j < 12; ++j) { const int i = j & 3; pin(xp[j]); const u32x4 v = __builtin_bit_cast(u32x4, xp[j]);
#pragma unroll
      for (int e = 0; e < 4; ++e) { acc[i >> 1][8 * (i & 1) + 2 * e] += __uint_as_float(v[e] << 16); acc[i >> 1][8 * (i & 1) + 2 * e + 1] += __uint_as_float(v[e] & 0xFFFF0000u); } }
    ST(30);
    char* op = (char*)out + (size_t)(G * 32u + r) * (DM * 2u) + ct * 128u + hh * 16u;
#pragma unroll
    for (int i = 0; i < 4; ++i) *(u32x4*)(op + i * 32) = pack8(acc[i >> 1], i & 1);
  }
  ST(31); ST_OUT(ws, 0, 32);
}

extern "C" void kernel_launch(void* const* d_in, const int* in_sizes, int n_in, void* d_out, int out_size, void* d_ws, size_t ws_size, hipStream_t stream) {
  (void)in_sizes; (void)n_in; (void)out_size; (void)ws_size;
  (void)hipFuncSetAttribute((const void*)mha_kernel, hipFuncAttributeMaxDynamicSharedMemorySize, LDS_BYTES);
  const unsigned short* x = (const unsigned short*)d_in[0]; const unsigned short* kc = (const unsigned short*)d_in[1]; const unsigned short* vc = (const unsigned short*)d_in[2];
  const unsigned short* wq = (const unsigned short*)d_in[3]; const unsigned short* wo = (const unsigned short*)d_in[4];
  mha_kernel<<<256, 512, LDS_BYTES, stream>>>(x, kc, vc, wq, wo, (char*)d_ws, (unsigned short*)d_out);
}
```

```cpp
#include <hip/hip_runtime.h>
#include <stdint.h>
#include <utility>

typedef short    bf16x8 __attribute__((ext_vector_type(8)));
typedef short    s16x4  __attribute__((ext_vector_type(4)));
typedef float    f32x16 __attribute__((ext_vector_type(16)));
typedef float    f32x2  __attribute__((ext_vector_type(2)));
typedef __bf16   bf16x2 __attribute__((ext_vector_type(2)));
typedef unsigned u32x4  __attribute__((ext_vector_type(4)));
typedef __attribute__((address_space(3))) const char* lds_cptr;
typedef __attribute__((address_space(1))) unsigned long long gu64;
typedef __amdgpu_buffer_rsrc_t srd_t;

#define DI __device__ __forceinline__
#define MFMA(a, b, c) __builtin_amdgcn_mfma_f32_32x32x16_bf16((a), (b), (c), 0, 0, 0)
#define SBAR() __builtin_amdgcn_sched_barrier(0)

constexpr int NB = 8, SQ = 224, DM = 576, NH = 32, SK = 512, DH = 128;
constexpr int LDS_BYTES = 163840;
constexpr float QSCALE = 0.08838834764831845f * 1.4426950408889634f;
constexpr float SUM_THR = 4096.f;

template <int... Is, class F> DI void static_for_impl(std::integer_sequence<int, Is...>, F&& f) { (f(std::integral_constant<int, Is>{}), ...); }
template <int N, class F> DI void static_for(F&& f) { static_for_impl(std::make_integer_sequence<int, N>{}, f); }

DI unsigned rfl(unsigned x) { return __builtin_amdgcn_readfirstlane(x); }
DI unsigned cvtpk(float lo, float hi) { f32x2 v = {lo, hi}; bf16x2 b = __builtin_convertvector(v, bf16x2); return __builtin_bit_cast(unsigned, b); }
DI srd_t mk_srd(const void* p, unsigned bytes) { return __builtin_amdgcn_make_buffer_rsrc((void*)p, (short)0, (int)bytes, 0x00020000); }
DI void dma4(unsigned lds, unsigned v0, unsigned v1, unsigned v2, unsigned v3, srd_t srd, unsigned soff) {
  asm volatile("s_mov_b32 m0, %0\n\ts_nop 0\n\tbuffer_load_dwordx4 %1, %5, %6 offen lds\n\tbuffer_load_dwordx4 %2, %5, %6 offen offset:1024 lds\n\t"
               "buffer_load_dwordx4 %3, %5, %6 offen offset:2048 lds\n\tbuffer_load_dwordx4 %4, %5, %6 offen offset:3072 lds"
               :: "s"(lds), "v"(v0), "v"(v1), "v"(v2), "v"(v3), "s"(srd), "s"(soff) : "memory");
}
DI void dma4_nt(unsigned lds, unsigned v0, unsigned v1, unsigned v2, unsigned v3, srd_t srd, unsigned soff) {
  asm volatile("s_mov_b32 m0, %0\n\ts_nop 0\n\tbuffer_load_dwordx4 %1, %5, %6 offen nt lds\n\tbuffer_load_dwordx4 %2, %5, %6 offen offset:1024 nt lds\n\t"
               "buffer_load_dwordx4 %3, %5, %6 offen offset:2048 nt lds\n\tbuffer_load_dwordx4 %4, %5, %6 offen offset:3072 nt lds"
               :: "s"(lds), "v"(v0), "v"(v1), "v"(v2), "v"(v3), "s"(srd), "s"(soff) : "memory");
}
DI void bstore_sc1(const u32x4& d, unsigned voff, srd_t srd, unsigned soff) { asm volatile("buffer_store_dwordx4 %0, %1, %2, %3 offen sc1\n\ts_nop 1" :: "v"(d), "v"(voff), "s"(srd), "s"(soff) : "memory"); }
DI void bload_sc1(bf16x8& d, unsigned voff, srd_t srd, unsigned soff) { asm volatile("buffer_load_dwordx4 %0, %1, %2, %3 offen sc1" : "=v"(d) : "v"(voff), "s"(srd), "s"(soff) : "memory"); }
DI void breload_sc1(bf16x8& d, unsigned voff, srd_t srd, unsigned soff) { asm volatile("buffer_load_dwordx4 %0, %1, %2, %3 offen sc1" : "+v"(d) : "v"(voff), "s"(srd), "s"(soff) : "memory"); }
DI void touch(unsigned& d, unsigned voff, srd_t srd, unsigned soff) { asm volatile("buffer_load_dword %0, %1, %2, %3 offen" : "+v"(d) : "v"(voff), "s"(srd), "s"(soff) : "memory"); }
DI void touch1(unsigned& d, unsigned voff, srd_t srd, unsigned soff) { asm volatile("buffer_load_dword %0, %1, %2, %3 offen sc1" : "+v"(d) : "v"(voff), "s"(srd), "s"(soff) : "memory"); }
DI void pin(bf16x8& a) { asm volatile("" : "+v"(a)); }
DI void pin16(f32x16& a) { asm volatile("" : "+v"(a)); }
DI void pinf(float& a) { asm volatile("" : "+v"(a)); }
DI void pinu(unsigned& a) { asm volatile("" : "+v"(a)); }
template <int N> DI void wait_vm1(bf16x8& a) { asm volatile("s_waitcnt vmcnt(%1)" : "+v"(a) : "n"(N) : "memory"); }
template <int N> DI void wait_vm() { asm volatile("s_waitcnt vmcnt(%0)" :: "n"(N) : "memory"); }
template <int N> DI void wait_lgkm() { __builtin_amdgcn_s_waitcnt(0xC07F | (N << 8)); SBAR(); }
DI void bar_if(unsigned c) { asm volatile("s_cmp_eq_u32 %0, 0\n\ts_cbranch_scc1 1f\n\ts_waitcnt lgkmcnt(0)\n\ts_barrier\n1:" :: "s"(c) : "memory", "scc"); }
DI void bar() { asm volatile("s_waitcnt lgkmcnt(0)\n\ts_barrier" ::: "memory"); }
typedef short v4i16_t __attribute__((ext_vector_type(4)));
DI s16x4 trd(lds_cptr p) { return __builtin_amdgcn_ds_read_tr16_b64_v4i16((__attribute__((address_space(3))) v4i16_t*)p); }
DI bf16x8 trd2(lds_cptr p, int o0, int o1) { s16x4 a = trd(p + o0), b = trd(p + o1); return __builtin_shufflevector(a, b, 0, 1, 2, 3, 4, 5, 6, 7); }
DI bf16x8 rd128(lds_cptr p) { return *(const __attribute__((address_space(3))) bf16x8*)p; }
DI float swap_max(float m) { auto rr = __builtin_amdgcn_permlane32_swap(__float_as_uint(m), __float_as_uint(m), false, false); return fmaxf(__uint_as_float(rr[0]), __uint_as_float(rr[1])); }
DI float swap_add(float m) { auto rr = __builtin_amdgcn_permlane32_swap(__float_as_uint(m), __float_as_uint(m), false, false); return __uint_as_float(rr[0]) + __uint_as_float(rr[1]); }
DI float max16(const f32x16& s) { float a = fmaxf(fmaxf(s[0], s[1]), s[2]), b = fmaxf(fmaxf(s[3], s[4]), s[5]);
  a = fmaxf(fmaxf(a, s[6]), s[7]); b = fmaxf(fmaxf(b, s[8]), s[9]); a = fmaxf(fmaxf(a, s[10]), s[11]); b = fmaxf(fmaxf(b, s[12]), s[13]); a = fmaxf(fmaxf(a, s[14]), s[15]); return fmaxf(a, b); }
DI float fadd(float a, float b) { float r; asm("v_add_f32_e32 %0, %1, %2" : "=v"(r) : "v"(a), "v"(b)); return r; }
DI u32x4 pack8(const f32x16& a, int s, float f) { return u32x4{cvtpk(a[8 * s] * f, a[8 * s + 1] * f), cvtpk(a[8 * s + 2] * f, a[8 * s + 3] * f), cvtpk(a[8 * s + 4] * f, a[8 * s + 5] * f), cvtpk(a[8 * s + 6] * f, a[8 * s + 7] * f)}; }
DI u32x4 pack8(const f32x16& a, int s) { return u32x4{cvtpk(a[8 * s], a[8 * s + 1]), cvtpk(a[8 * s + 2], a[8 * s + 3]), cvtpk(a[8 * s + 4], a[8 * s + 5]), cvtpk(a[8 * s + 6], a[8 * s + 7])}; }

DI float expo(const f32x16& s, u32x4 (&p)[2]) {
  float e[16]; float s0 = 0.f, s1 = 0.f;
#pragma unroll
  for (int i = 0; i < 16; ++i) e[i] = __builtin_amdgcn_exp2f(s[i]);
#pragma unroll
  for (int i = 0; i < 16; i += 2) { s0 += e[i]; s1 += e[i + 1]; }
#pragma unroll
  for (int i = 0; i < 8; ++i) p[i >> 2][i & 3] = cvtpk(e[2 * i], e[2 * i + 1]);
  return s0 + s1;
}

#define ST_DECL
#define ST(k)
#define ST_OUT(ws, lo, hi)

constexpr unsigned SLOTK = 0x04204209u;
constexpr unsigned SLOTV = 0x31531531u;
constexpr unsigned SLOTW = 0x05429876u;
constexpr unsigned STG = 45056u, STG_W = 28672u;
constexpr unsigned O_BYTES = NB * SQ * NH * DH * 2;
constexpr unsigned X_OFF = O_BYTES, X_BYTES = 63u * 8u * 4u * 4096u;
constexpr unsigned FLAG_OFF = X_OFF + X_BYTES, FLAG_STRIDE = 128, FLAG2_OFF = FLAG_OFF + 256 * FLAG_STRIDE, EPOCH_OFF = FLAG2_OFF + 63 * 32 * FLAG_STRIDE;
constexpr unsigned SPIN_CAP = 1u << 20;
#define RA_ 7
constexpr int RA = RA_;
#define WO 6
#define HG 3
#define RET 0
#define YPRIO 0
#define P2PRIO 0
#define GB 12
#define GY GB
#define A0BAR 6
#define PSLEEP 4
#define CODEPF 18944
constexpr int lds_ops(int a, int b, bool last) { int n = 0; for (int i = a; i < b; ++i) n += i >= 16 ? (last ? 0 : 1) : (i >= 8 ? 2 : 1); return n; }
constexpr int pv_order(int i) { return i < 4 ? i : i < 8 ? i + 4 : i < 12 ? i - 4 : i; }
DI unsigned long long mix64(unsigned long long z) { z += 0x9E3779B97F4A7C15ull; z = (z ^ (z >> 30)) * 0xBF58476D1CE4E5B9ull; z = (z ^ (z >> 27)) * 0x94D049BB133111EBull; return z ^ (z >> 31); }
DI unsigned long long rfl64(unsigned long long v) { return ((unsigned long long)rfl((unsigned)(v >> 32)) << 32) | rfl((unsigned)v); }
DI unsigned long long ld_agent(const char* p) { return __hip_atomic_load((gu64*)p, __ATOMIC_RELAXED, __HIP_MEMORY_SCOPE_AGENT); }
DI void st_agent(char* p, unsigned long long v) { __hip_atomic_store((gu64*)p, v, __ATOMIC_RELAXED, __HIP_MEMORY_SCOPE_AGENT); }
DI void poll_flags(const char* p, unsigned long long tag) {
  _Pragma("nounroll") for (unsigned spins = 0; spins < SPIN_CAP; ++spins) { if (__all(ld_agent(p) == tag)) break; __builtin_amdgcn_s_sleep(PSLEEP); }
  asm volatile("" ::: "memory");
}

__global__ __launch_bounds__(512, 1) void mha_kernel(const unsigned short* __restrict__ x, const unsigned short* __restrict__ kc, const unsigned short* __restrict__ vc,
                                                     const unsigned short* __restrict__ wq, const unsigned short* __restrict__ wo, char* ws, unsigned short* __restrict__ out) {
  extern __shared__ __attribute__((aligned(16))) char lds[];
  unsigned long long pc0; asm volatile("s_getpc_b64 %0" : "=s"(pc0));
  const unsigned tid = threadIdx.x, lane = tid & 63u, r = lane & 31u, hh = lane >> 5;
  const unsigned wid = rfl(tid >> 6);
  ST_DECL ST(0);
  const unsigned bid = blockIdx.x, xcd = bid & 7u, ix = bid >> 3;
  const unsigned b = (xcd & 1u) * 4u + (ix & 3u), h = (xcd >> 1) * 8u + (ix >> 2);
  const unsigned lds0 = (unsigned)(uintptr_t)lds;
  const srd_t sW = mk_srd((const char*)wq - 1024, DM * NH * DH * 2 + 1024);
  const srd_t sX = mk_srd(x + (size_t)b * SQ * DM, SQ * DM * 2);
  const unsigned prow = (lane >> 2) & 7u, pch = 4u * hh + (lane & 3u);
  const unsigned pchx0 = 4u * hh + ((lane & 3u) ^ ((lane >> 4) & 1u)), pchx1 = pchx0 ^ 2u;
  const unsigned vW = prow * 8192u + pch * 16u, vX0 = prow * (DM * 2u) + pchx0 * 16u, vX1 = prow * (DM * 2u) + pchx1 * 16u;
  const unsigned ck0 = 3u * (((ix & 3u) + (ix >> 2)) % 3u);
#pragma unroll
  for (unsigned c = 0; c < 2; ++c) {
    if (wid >= 4u) dma4(lds0 + c * STG + STG_W + (wid - 4u) * 4096u, vW + 1024u, vW + 128u, vW + 65536u - 1024u, vW + 65664u - 2048u, sW, (ck0 + c) * 64u * 8192u + h * 256u + (wid - 4u) * 131072u);
    if (wid < 7u) dma4(lds0 + c * STG + wid * 4096u, vX0, vX1 + 8192u, vX0 + 16384u, vX1 + 24576u, sX, (ck0 + c) * 128u + wid * 32u * DM * 2u);
    if (c == 0) __builtin_amdgcn_s_barrier(); }
  const srd_t sK = mk_srd((const char*)(kc + (size_t)(b * NH + h) * SK * DH) - 1024, SK * DH * 2 + 1024);
  const unsigned vK0 = prow * 256u + pchx0 * 16u, vK1 = prow * 256u + pchx1 * 16u;
  const unsigned q4 = (lane & 15u) >> 2, p4 = lane & 3u, blk = (lane >> 4) & 1u;
  const unsigned trcol = 32u * blk + 16u * (p4 & 1u) + 8u * (p4 >> 1);
  const srd_t sO = mk_srd(ws, O_BYTES);
  unsigned long long tag;
  const unsigned g4 = xcd >> 1, u0 = (xcd & 1u) * 32u + ix;
  const bool has_tile = u0 < 63u;
  const unsigned u = has_tile ? u0 : 62u, rt = u / 9u, ct = u - rt * 9u;

  if (wid == 7u) {
    const srd_t sV = mk_srd((const char*)(vc + (size_t)(b * NH + h) * SK * DH) - 1024, SK * DH * 2 + 1024);
    auto fillQ = [&](unsigned c, unsigned stage) {
      const unsigned lw = lds0 + stage + STG_W, sw = c * 64u * 8192u + h * 256u, lx = lds0 + stage, sx = c * 128u;
#pragma unroll
      for (int i = 0; i < 4; ++i) dma4(lw + i * 4096u, vW + 1024u, vW + 128u, vW + 65536u - 1024u, vW + 65664u - 2048u, sW, sw + i * 131072u);
#pragma unroll
      for (int i = 0; i < 7; ++i) dma4(lx + i * 4096u, vX0, vX1 + 8192u, vX0 + 16384u, vX1 + 24576u, sX, sx + i * 32u * DM * 2u); };
    wait_vm<4>(); bar(); ST(2);
    unsigned long long ep; asm volatile("global_load_dwordx2 %0, %1, %2 sc1" : "=v"(ep) : "v"(0u), "s"(ws + EPOCH_OFF) : "memory");
    { unsigned stage = 2 * STG, ck = ck0 + 2u;
#pragma nounroll
      for (unsigned c = 2; c < 9; ++c) { fillQ(ck, stage); ck = ck == 8u ? 0u : ck + 1u; stage = stage == 2 * STG ? 0u : stage + STG;
        wait_vm<44>(); bar(); ST(1 + c); } }
    const unsigned vV = prow * 256u + pch * 16u;
    const srd_t sWo = mk_srd(wo, NH * DH * DM * 2);
    const unsigned vWo = prow * (DM * 2u) + pch * 16u;
    { unsigned j = 1, jw = 0;
#pragma nounroll
      for (unsigned n = 0; n < 10; ++n) {
        const unsigned we = (unsigned)(0x8754432100ull >> (4u * n)) & 15u;
#pragma nounroll
        for (; jw < we; ++jw) { const unsigned ld = lds0 + ((SLOTW >> (4u * jw)) & 15u) * 16384u, so = (g4 * 8u + jw) * 128u * (DM * 2u) + ct * 128u;
#pragma unroll
          for (int k = 0; k < 4; ++k) dma4(ld + k * 4096u, vWo, vWo + 8192u, vWo + 16384u, vWo + 24576u, sWo, so + k * 32u * (DM * 2u)); }
        const unsigned je = ((unsigned)(0xFFFECA8642ull >> (4u * n)) & 15u) + 1u;
#pragma nounroll
        for (; j < je; ++j) {
          if ((j & 1u) && j != 15u) { const unsigned t = (j + 1u) >> 1, ld = lds0 + ((SLOTK >> (4u * t)) & 15u) * 16384u;
#pragma unroll
            for (int i = 0; i < 4; ++i) dma4_nt(ld + i * 4096u, vK0 + 1024u, vK0 + 128u, vK1 + 1024u, vK1 + 128u, sK, t * 16384u + i * 4096u);
          } else { const unsigned t = (j - 1u) >> 1, ld = lds0 + ((SLOTV >> (4u * t)) & 15u) * 16384u;
#pragma unroll
            for (int i = 0; i < 4; ++i) dma4_nt(ld + i * 4096u, vV + 1024u, vV + 128u, vV + 1024u, vV + 128u, sV, t * 16384u + i * 4096u); } }
        if (n == 9u) break;
        if (n < 2u || n >= 6u) wait_vm<32>(); else wait_vm<48>();
        bar(); ST(n == 0u ? 10u : 11u + n); } }
    asm volatile("" : "+v"(ep)); tag = mix64(rfl64(ep));
    ST(20); ST(21);
  } else {
  const unsigned rsw0 = 64u * (r & 7u) + 16u * (hh ^ ((r >> 2) & 3u)), rsw1 = rsw0 ^ 32u;
  f32x16 qa[4] = {}, o[4] = {};
  asm volatile("" : "+v"(qa[0]), "+v"(qa[1]), "+v"(qa[2]), "+v"(qa[3]), "+v"(o[0]), "+v"(o[1]), "+v"(o[2]), "+v"(o[3]));
  const char* wup;
  { const unsigned k = lane & 7u, sel = lane >> 3;
    wup = sel == 0u ? (const char*)(k & 1u ? vc : kc) + (size_t)(b * NH + h) * (SK * DH * 2) + (k >> 1) * 32768u
        : sel == 1u ? (const char*)wo + (size_t)(g4 * 1024u + k * 128u) * (DM * 2u) + ct * 128u
        : sel == 2u ? ws + ((b * 7u + k % 7u) * NH + h) * 8192u
        : sel == 3u ? ws + ((8u * rt + k) * NH + g4 * 8u) * 8192u
        : sel == 4u ? ws + X_OFF + u * 131072u + (k & 1u) * 65536u
        : sel == 5u ? (const char*)out + (size_t)((8u * rt + k) * 32u) * (DM * 2u)
        : sel == 6u ? ws + FLAG_OFF + k * 32768u + 64u
        : (const char*)(pc0 & ~255ull) + min((ix * 8u + k) * 128u, (unsigned)CODEPF); }
  wait_lgkm<0>();
  if (wid >= 4u) wait_vm<8>(); else wait_vm<4>();
  ST(1);
  bar();
  unsigned cdummy = 0; unsigned long long ep = 0;
  ST(2);
  const lds_cptr kb0 = (lds_cptr)lds + 2048u * (r >> 3) + rsw0, kb1 = (lds_cptr)lds + 2048u * (r >> 3) + rsw1;
  const lds_cptr vb = (lds_cptr)lds + 64u * (4u * hh + q4) + trcol;
  bf16x8 qf[8], pre[RA];
  f32x16 negm, S0, S1; u32x4 P0[2], P1[2]; float l = 0.f;
  auto rdK = [&](lds_cptr k0, lds_cptr k1, int kb, int i) { return rd128(((i & 1) ? k1 : k0) + 8192 * kb + 512 * (i >> 1)); };
  auto rdV = [&](lds_cptr v, int kb, int j) { return trd2(v, 8192 * kb + 4096 * (j >> 2) + 512 * (j & 3), 8192 * kb + 4096 * (j >> 2) + 2048 + 512 * (j & 3)); };
  auto sm_fix = [&](float ps, f32x16& Sc, f32x16& Sn, u32x4 (&P)[2]) {
    if (__builtin_expect(__any(!(ps <= SUM_THR)), 0)) {
      const float dl = fmaxf(swap_max(max16(Sc)), 0.f), f = __builtin_amdgcn_exp2f(-dl);
#pragma unroll
      for (int i = 0; i < 16; ++i) { Sc[i] -= dl; Sn[i] -= dl; negm[i] -= dl; }
#pragma unroll
      for (int db = 0; db < 4; ++db)
#pragma unroll
        for (int i = 0; i < 16; ++i) o[db][i] *= f;
      l *= f; ps = expo(Sc, P); }
    l += ps; };
  { lds_cptr wp = (lds_cptr)lds + STG_W + 2048u * hh + 64u * q4 + trcol;
    lds_cptr xp0 = (lds_cptr)lds + wid * 4096u + 1024u * (r >> 3) + rsw0, xp1 = (lds_cptr)lds + wid * 4096u + 1024u * (r >> 3) + rsw1;
    bf16x8 xf[4], af[5];
    auto rdA = [&](int i) { return trd2(wp, 4096 * (i >> 2) + 512 * (i & 3), 4096 * (i >> 2) + 512 * (i & 3) + 256); };
    xf[0] = rd128(xp0); af[0] = rdA(0); af[1] = rdA(1); af[2] = rdA(2); af[3] = rdA(3);
    unsigned stage = 0;
#pragma nounroll
    for (unsigned c = 0; c < 8; ++c) {
      if (__builtin_expect(c == 3u, 0)) {
        asm volatile("global_load_dwordx2 %0, %1, %2 sc1" : "+v"(ep) : "v"(0u), "s"(ws + EPOCH_OFF) : "memory");
        if (wid == 3u) { asm volatile("global_load_dword %0, %1, off" : "+v"(cdummy) : "v"(wup) : "memory");
#pragma unroll
          for (int i = 0; i < 4; ++i) dma4_nt(lds0 + 9u * 16384u + i * 4096u, vK0 + 1024u, vK0 + 128u, vK1 + 1024u, vK1 + 128u, sK, i * 4096u); } }
      const unsigned nstage = stage == 2 * STG ? 0u : stage + STG; const int dlt = (int)nstage - (int)stage; stage = nstage;
      SBAR();
      static_for<12>([&](auto I) { constexpr int i = I.value;
        qa[i & 3] = MFMA(af[i % 5], xf[i >> 2], qa[i & 3]);
        af[(i + 4) % 5] = rdA(i + 4);
        if constexpr (i == 0) xf[1] = rd128(xp1); if constexpr (i == 3) xf[2] = rd128(xp0 + 512); if constexpr (i == 7) xf[3] = rd128(xp1 + 512);
        SBAR(); });
      qa[0] = MFMA(af[12 % 5], xf[3], qa[0]);
      wp += dlt; xp0 += dlt; xp1 += dlt; asm volatile("" : "+v"(wp), "+v"(xp0), "+v"(xp1));
      SBAR();
      asm volatile("s_bitcmp1_b32 %0, %1\n\ts_cbranch_scc1 1f\n\ts_waitcnt vmcnt(0)\n1:\n\ts_waitcnt lgkmcnt(0)\n\ts_barrier" :: "s"(0x78u), "s"(c) : "memory", "scc");
      const bf16x8 xl = xf[3], a13 = af[13 % 5], a14 = af[14 % 5], a15 = af[15 % 5];
      xf[0] = rd128(xp0); af[0] = rdA(0); af[1] = rdA(1);
      SBAR();
      qa[1] = MFMA(a13, xl, qa[1]); SBAR();
      af[2] = rdA(2);
      qa[2] = MFMA(a14, xl, qa[2]); SBAR();
      af[3] = rdA(3);
      qa[3] = MFMA(a15, xl, qa[3]); SBAR();
      ST(3 + c);
    }
    pinu(cdummy); asm volatile("" : "+v"(ep)); ep = rfl64(ep);
    lds_cptr k0 = kb0 + 9 * 16384, k1 = kb1 + 9 * 16384; asm volatile("" : "+v"(k0), "+v"(k1));
    bf16x8 ag[16], f[16]; u32x4 qw[8]; float qs = QSCALE; asm volatile("" : "+s"(qs));
#pragma unroll
    for (int i = 0; i < 4; ++i) ag[i] = af[i];
    auto cvq = [&](int j) { float lo = qa[j >> 3][2 * (j & 7)] * qs; pinf(lo); float hi = qa[j >> 3][2 * (j & 7) + 1] * qs; unsigned w = cvtpk(lo, hi); pinu(w); qw[j >> 2][j & 3] = w; };
    SBAR();
    static_for<24>([&](auto G) { constexpr int g = G.value, n = g + 4;
      if constexpr (g < 4) qa[g] = MFMA(ag[g], xf[0], qa[g]);
      else if constexpr (g < 16) qa[(g - 4) / 3] = MFMA(ag[g], xf[1 + (g - 4) % 3], qa[(g - 4) / 3]);
      else { qf[g - 16] = __builtin_bit_cast(bf16x8, qw[g - 16]);
        if constexpr (g == 16) { const f32x16 z = {}; S0 = MFMA(f[0], qf[0], z); } else S0 = MFMA(f[g - 16], qf[g - 16], S0); pin16(S0); }
      if constexpr (g == 0) { xf[1] = rd128(xp1); xf[2] = rd128(xp0 + 512); } if constexpr (g == 1) xf[3] = rd128(xp1 + 512);
      if constexpr (n < 16) ag[n] = rdA(4 * (1 + (n - 4) % 3) + (n - 4) / 3); else f[n - 16] = rdK(k0, k1, (n - 16) >> 3, (n - 16) & 7);
      if constexpr (g >= 9 && g < 21) { constexpr int j0 = (g - 9) * 8 / 3, j1 = (g - 8) * 8 / 3; static_for<j1 - j0>([&](auto J) { cvq(j0 + J.value); }); }
      SBAR(); });
    ST(11);
#pragma unroll
    for (int i = 12; i < 16; ++i) f[i] = rdK(k0, k1, 1, i & 7);
    const float m = swap_max(max16(S0));
#pragma unroll
    for (int i = 0; i < 16; ++i) negm[i] = -m;
    SBAR();
    float e[16]; float s0 = 0.f, s1 = 0.f;
    static_for<9>([&](auto G) { constexpr int g = G.value;
      if constexpr (g == 0) { if (wid >= 4u) bar(); }
      if constexpr (g == A0BAR) { if (wid < 4u) bar();
        constexpr unsigned ko = ((SLOTK >> 4) & 15u) * 16384u;
#pragma unroll
        for (int i = 0; i < RA; ++i) pre[i] = rdK(kb0 + ko, kb1 + ko, 0, i);
        SBAR(); }
      if constexpr (g == 0) S1 = MFMA(f[8], qf[0], negm); else if constexpr (g < 8) S1 = MFMA(f[8 + g], qf[g], S1);
      if constexpr (g < 8) pin16(S1);
      if constexpr (g < 8) { e[2 * g] = __builtin_amdgcn_exp2f(S0[2 * g] - m); e[2 * g + 1] = __builtin_amdgcn_exp2f(S0[2 * g + 1] - m); pinf(e[2 * g]); pinf(e[2 * g + 1]); }
      if constexpr (g == 1) { s0 = e[0]; s1 = e[1]; } else if constexpr (g > 1) { s0 += e[2 * g - 2]; pinf(s0); s1 += e[2 * g - 1]; pinf(s1); }
      if constexpr (g >= 1) { unsigned w = cvtpk(e[2 * g - 2], e[2 * g - 1]); pinu(w); P0[(g - 1) >> 2][(g - 1) & 3] = w; }
      SBAR(); });
    l = s0 + s1; }
  ST(12);
  auto unit = [&](auto KB, auto LAST, lds_cptr k0, lds_cptr k1, lds_cptr v, lds_cptr nk0, lds_cptr nk1, f32x16& Sn, f32x16& Sc, const u32x4 (&Pp)[2], u32x4 (&Pc)[2]) {
    constexpr int kb = KB.value; constexpr bool last = LAST.value;
    float e[16]; float s0 = 0.f, s1 = 0.f; bf16x8 f[16 + RA];
#pragma unroll
    for (int i = 0; i < RA; ++i) f[i] = pre[i];
    static_for<16>([&](auto G) { constexpr int g = G.value; constexpr int n = g + RA; constexpr int gb = GB;
      if constexpr (last && GY != GB && g == GY) { bar_if(wid >> 2); SBAR(); }
      if constexpr (last && g == gb) { if constexpr (GY != GB) bar_if((wid >> 2) ^ 1u); else bar(); SBAR(); }
      if constexpr (last && g == 12) wait_lgkm<0>();
      else if constexpr ((g & 3) == 0) wait_lgkm<lds_ops(g + 4, g + RA, last)>();
      if constexpr (g == 0) Sn = MFMA(f[0], qf[0], negm); else if constexpr (g < 8) Sn = MFMA(f[g], qf[g], Sn);
      else o[g & 3] = MFMA(f[g], __builtin_bit_cast(bf16x8, Pp[(g >> 2) & 1]), o[g & 3]);
      if constexpr (n < 8) f[n] = rdK(k0, k1, kb, n); else if constexpr (n < 16) f[n] = rdV(v, kb, n - 8);
      else if constexpr (!last) f[n] = rdK(k0, k1, kb + 1, n - 16);
      else if constexpr (g >= gb) { constexpr int per = (RA + 15 - gb) / (16 - gb); static_for<per>([&](auto J) { constexpr int i = per * (g - gb) + J.value; if constexpr (i < RA) f[16 + i] = rdK(nk0, nk1, 0, i); }); }
      if constexpr (g == 0) { e[0] = __builtin_amdgcn_exp2f(Sc[0]); pinf(e[0]); }
      if constexpr (g < 15) { e[g + 1] = __builtin_amdgcn_exp2f(Sc[g + 1]); pinf(e[g + 1]); }
      if constexpr (g == 1) { s0 = e[0]; s1 = e[1]; } else if constexpr (g > 1) { if constexpr (g & 1) { s1 += e[g]; pinf(s1); } else { s0 += e[g]; pinf(s0); } }
      if constexpr (g & 1) { unsigned w = cvtpk(e[g - 1], e[g]); pinu(w); Pc[g >> 3][(g >> 1) & 3] = w; }
      SBAR(); });
#pragma unroll
    for (int i = 0; i < RA; ++i) pre[i] = f[16 + i];
    sm_fix(s0 + s1, Sc, Sn, Pc); };
  if (YPRIO && wid >= 4u) __builtin_amdgcn_s_setprio(1);
#pragma nounroll
  for (unsigned t = 1; t < 7; ++t) {
    const unsigned ko = ((SLOTK >> (4u * t)) & 15u) * 16384u, vo = ((SLOTV >> (4u * (t - 1u))) & 15u) * 16384u, nko = ((SLOTK >> (4u * ((t + 1u) & 7u))) & 15u) * 16384u;
    SBAR();
    unit(std::integral_constant<int, 0>{}, std::false_type{}, kb0 + ko, kb1 + ko, vb + vo, kb0, kb1, S0, S1, P0, P1);
    unit(std::integral_constant<int, 1>{}, std::true_type{}, kb0 + ko, kb1 + ko, vb + vo, kb0 + nko, kb1 + nko, S1, S0, P1, P0);
    if (t == 1u || t == 6u) ST(12 + t);
  }
  const unsigned me = (b * 7u + wid) * NH + h;
  {
    constexpr unsigned ko = ((SLOTK >> 28) & 15u) * 16384u;
    const lds_cptr k0 = kb0 + ko, k1 = kb1 + ko, v6 = vb + ((SLOTV >> 24) & 15u) * 16384u, v7 = vb + ((SLOTV >> 28) & 15u) * 16384u;
    u32x4 P2[2], P3[2];
    auto scaleP = [&](u32x4 (&P)[2], float f) {
#pragma unroll
      for (int i = 0; i < 8; ++i) { const unsigned w = P[i >> 2][i & 3]; P[i >> 2][i & 3] = cvtpk(__uint_as_float(w << 16) * f, __uint_as_float(w & 0xFFFF0000u) * f); } };
    auto fix = [&](auto NP, float ps, f32x16& Sc, f32x16& Sn, u32x4 (&P)[2]) {
      if (__builtin_expect(__any(!(ps <= SUM_THR)), 0)) {
        const float f = __builtin_amdgcn_exp2f(-fmaxf(swap_max(max16(Sc)), 0.f));
        scaleP(P0, f); if constexpr (NP.value > 1) scaleP(P1, f); if constexpr (NP.value > 2) scaleP(P2, f); }
      sm_fix(ps, Sc, Sn, P); };
    auto rdVt = [&](int i) { const int j = pv_order(i), db = j >> 3, un = (j >> 1) & 3, ks = j & 1; return rdV(un < 2 ? v6 : v7, un & 1, 4 * ks + db); };
    auto pvt = [&](auto I, const bf16x8& vf) { constexpr int j = pv_order(I.value), un = (j >> 1) & 3;
      const u32x4 pb = un == 0 ? P0[j & 1] : un == 1 ? P1[j & 1] : un == 2 ? P2[j & 1] : P3[j & 1];
      o[j >> 3] = MFMA(vf, __builtin_bit_cast(bf16x8, pb), o[j >> 3]); pin16(o[j >> 3]); };
    bf16x8 f[16], w[32];
#pragma unroll
    for (int i = 0; i < RA; ++i) f[i] = pre[i];
    auto qsm = [&](auto H, f32x16& Sn, const f32x16& Sc, u32x4 (&Pc)[2]) { constexpr int hf = H.value;
      float e[16]; float s0 = 0.f, s1 = 0.f;
      static_for<9>([&](auto G) { constexpr int g = G.value, n = 8 * hf + g + RA;
        if constexpr (hf < 2 && g < 8) { if constexpr (g == 0) Sn = MFMA(f[8 * hf], qf[0], negm); else Sn = MFMA(f[8 * hf + g], qf[g], Sn);
          pin16(Sn);
          if constexpr (n < 16) f[n] = rdK(k0, k1, n >> 3, n & 7); }
        if constexpr (hf == 1 && g >= 2 && g < 8) w[g - 2] = rdVt(g - 2);
        if constexpr (hf == 2 && g < 8) { pvt(G, w[g]); w[g + 6] = rdVt(g + 6); }
        if constexpr (g < 8) { e[2 * g] = __builtin_amdgcn_exp2f(Sc[2 * g]); e[2 * g + 1] = __builtin_amdgcn_exp2f(Sc[2 * g + 1]); pinf(e[2 * g]); pinf(e[2 * g + 1]); }
        if constexpr (g == 1) { s0 = e[0]; s1 = e[1]; } else if constexpr (g > 1) { s0 += e[2 * g - 2]; pinf(s0); s1 += e[2 * g - 1]; pinf(s1); }
        if constexpr (g >= 1) { unsigned pw = cvtpk(e[2 * g - 2], e[2 * g - 1]); pinu(pw); Pc[(g - 1) >> 2][(g - 1) & 3] = pw; }
        SBAR(); });
      return s0 + s1; };
    SBAR();
    fix(std::integral_constant<int, 1>{}, qsm(std::integral_constant<int, 0>{}, S0, S1, P1), S1, S0, P1);
    fix(std::integral_constant<int, 2>{}, qsm(std::integral_constant<int, 1>{}, S1, S0, P2), S0, S1, P2);
    bar();
    ST(19);
    fix(std::integral_constant<int, 3>{}, qsm(std::integral_constant<int, 2>{}, S0, S1, P3), S1, S0, P3);
    const float inv = __builtin_amdgcn_rcpf(swap_add(l));
    auto put = [&](int k8) { bstore_sc1(pack8(o[k8 >> 1], k8 & 1, inv), lane * 16u, sO, me * 8192u + k8 * 1024u); };
    static_for<24>([&](auto G) { constexpr int g = G.value + 8;
      pvt(std::integral_constant<int, g>{}, w[g]);
      if constexpr (g + 6 < 32) w[g + 6] = rdVt(g + 6);
      if constexpr (g == 14) put(0); if constexpr (g == 16) put(1); if constexpr (g == 19) put(2); if constexpr (g == 22) put(3); if constexpr (g == 26) put(4); if constexpr (g == 29) put(5);
      SBAR(); });
    ST(20);
    put(6); put(7); }
  ST(21);
  tag = mix64(ep);
  }
  wait_vm<0>();
  ST(22);
  bar();
  if (tid == 0) st_agent(ws + FLAG_OFF + (b * NH + h) * FLAG_STRIDE, tag);
  if (RET && wid < 7u) { unsigned dummy = 0; touch1(dummy, lane * 128u + ((b * 7u + wid) * NH + h) * 8192u, sO, 0u); wait_vm<0>(); pinu(dummy); }
  ST(23);
  if (!has_tile) {
    if (bid == 249u && wid == 0u) {
#pragma nounroll
      for (unsigned i = 0; i < 4; ++i) poll_flags(ws + FLAG_OFF + (i * 64u + lane) * FLAG_STRIDE, tag);
      if (lane == 0) st_agent(ws + EPOCH_OFF, tag); }
    ST(24); ST(25); ST(26); ST(27); ST(28); ST(29); ST(30); ST(31); ST_OUT(ws, 0, 32); return; }
  const unsigned jq = (2u * g4 + 2u + wid) & 7u, G = 8u * rt + jq;
  const unsigned voffO = lane * 16u, soO = (G * NH + g4 * 8u) * 8192u;
  poll_flags(ws + FLAG_OFF + (((G * 37u) >> 8) * NH + g4 * 8u + (lane & 7u)) * FLAG_STRIDE, tag);
  ST(24);
  const lds_cptr wb = (lds_cptr)lds + 1024u * hh + 64u * q4 + trcol;
  if (P2PRIO) { if ((wid & 2u) == 0u) __builtin_amdgcn_s_setprio(2); else if (wid < 4u) __builtin_amdgcn_s_setprio(1); else __builtin_amdgcn_s_setprio(0); } else if (YPRIO) __builtin_amdgcn_s_setprio(0);
  f32x16 acc[2] = {};
  { unsigned hd = HG == 3 ? (ct % 3u) * 3u : (ct * (8u / HG)) & 7u, so_c = soO + hd * 8192u; lds_cptr wp = wb + ((SLOTW >> (4u * hd)) & 15u) * 16384u;
    bf16x8 of[8], lf[4][2];
    auto rdW = [&](int s2) { lf[s2 & 3][0] = trd2(wp, (s2 & 7) * 2048, (s2 & 7) * 2048 + 256); lf[s2 & 3][1] = trd2(wp, (s2 & 7) * 2048 + 512, (s2 & 7) * 2048 + 768); };
#pragma unroll
    for (int i = 0; i < 8; ++i) { if (i < WO) bload_sc1(of[i], voffO, sO, so_c + i * 1024u); else of[i] = bf16x8{}; }
    rdW(0); rdW(1);
#pragma nounroll
    for (unsigned p = 0; p < 8; ++p) {
      hd = (hd + 1u) & 7u;
      const unsigned so_n = p < 7u ? soO + hd * 8192u : O_BYTES;
      static_for<8>([&](auto S) { constexpr int s2 = S.value;
        if constexpr (s2 == 6) wp = wb + ((SLOTW >> (4u * hd)) & 15u) * 16384u;
        wait_vm1<WO - 1>(of[s2]);
        wait_lgkm<4>();
        acc[0] = MFMA(lf[s2 & 3][0], of[s2], acc[0]);
        rdW(s2 + 2);
        SBAR();
        acc[1] = MFMA(lf[s2 & 3][1], of[s2], acc[1]);
        SBAR();
        breload_sc1(of[(s2 + WO) & 7], voffO, sO, (s2 + WO < 8 ? so_c : so_n) + ((s2 + WO) & 7) * 1024u);
        SBAR(); });
      so_c = so_n;
    }
    wait_vm<0>();
#pragma unroll
    for (int i = 0; i < 8; ++i) pin(of[i]); }
  ST(28);
  const srd_t sX2 = mk_srd(ws + X_OFF, X_BYTES);
  const unsigned xb = (u * 8u + jq) * 4u;
  if (wid < 6u) {
#pragma unroll
    for (int i = 0; i < 4; ++i) bstore_sc1(pack8(acc[i >> 1], i & 1), voffO, sX2, (xb + g4) * 4096u + i * 1024u);
    wait_vm<0>();
    if (lane == 0) st_agent(ws + FLAG2_OFF + (xb + g4) * FLAG_STRIDE, tag);
    ST(29); ST(30);
  } else {
    poll_flags(ws + FLAG2_OFF + (xb + ((g4 + 1u + lane % 3u) & 3u)) * FLAG_STRIDE, tag);
    ST(29);
    bf16x8 xp[12];
#pragma unroll
    for (int i = 0; i < 12; ++i) bload_sc1(xp[i], voffO, sX2, (xb + ((g4 + 1u + (i >> 2)) & 3u)) * 4096u + (i & 3) * 1024u);
    wait_vm<0>();
#pragma unroll
    for (int j = 0; j < 12; ++j) { const int i = j & 3; pin(xp[j]); const u32x4 v = __builtin_bit_cast(u32x4, xp[j]);
#pragma unroll
      for (int e = 0; e < 4; ++e) { acc[i >> 1][8 * (i & 1) + 2 * e] += __uint_as_float(v[e] << 16); acc[i >> 1][8 * (i & 1) + 2 * e + 1] += __uint_as_float(v[e] & 0xFFFF0000u); } }
    ST(30);
    char* op = (char*)out + (size_t)(G * 32u + r) * (DM * 2u) + ct * 128u + hh * 16u;
#pragma unroll
    for (int i = 0; i < 4; ++i) *(u32x4*)(op + i * 32) = pack8(acc[i >> 1], i & 1);
  }
  ST(31); ST_OUT(ws, 0, 32);
}

extern "C" void kernel_launch(void* const* d_in, const int* in_sizes, int n_in, void* d_out, int out_size, void* d_ws, size_t ws_size, hipStream_t stream) {
  (void)in_sizes; (void)n_in; (void)out_size; (void)ws_size;
  (void)hipFuncSetAttribute((const void*)mha_kernel, hipFuncAttributeMaxDynamicSharedMemorySize, LDS_BYTES);
  const unsigned short* x = (const unsigned short*)d_in[0]; const unsigned short* kc = (const unsigned short*)d_in[1]; const unsigned short* vc = (const unsigned short*)d_in[2];
  const unsigned short* wq = (const unsigned short*)d_in[3]; const unsigned short* wo = (const unsigned short*)d_in[4];
  mha_kernel<<<256, 512, LDS_BYTES, stream>>>(x, kc, vc, wq, wo, (char*)d_ws, (unsigned short*)d_out);
}
```

```cpp
#include <hip/hip_runtime.h>
#include <stdint.h>
#include <utility>

typedef short    bf16x8 __attribute__((ext_vector_type(8)));
typedef short    s16x4  __attribute__((ext_vector_type(4)));
typedef float    f32x16 __attribute__((ext_vector_type(16)));
typedef float    f32x2  __attribute__((ext_vector_type(2)));
typedef __bf16   bf16x2 __attribute__((ext_vector_type(2)));
typedef unsigned u32x4  __attribute__((ext_vector_type(4)));
typedef __attribute__((address_space(3))) const char* lds_cptr;
typedef __attribute__((address_space(1))) unsigned long long gu64;
typedef __amdgpu_buffer_rsrc_t srd_t;

#define DI __device__ __forceinline__
#define MFMA(a, b, c) __builtin_amdgcn_mfma_f32_32x32x16_bf16((a), (b), (c), 0, 0, 0)
#define SBAR() __builtin_amdgcn_sched_barrier(0)

constexpr int NB = 8, SQ = 224, DM = 576, NH = 32, SK = 512, DH = 128;
constexpr int LDS_BYTES = 163840;
constexpr float QSCALE = 0.08838834764831845f * 1.4426950408889634f;
constexpr float SUM_THR = 4096.f;

template <int... Is, class F> DI void static_for_impl(std::integer_sequence<int, Is...>, F&& f) { (f(std::integral_constant<int, Is>{}), ...); }
template <int N, class F> DI void static_for(F&& f) { static_for_impl(std::make_integer_sequence<int, N>{}, f); }

DI unsigned rfl(unsigned x) { return __builtin_amdgcn_readfirstlane(x); }
DI unsigned cvtpk(float lo, float hi) { f32x2 v = {lo, hi}; bf16x2 b = __builtin_convertvector(v, bf16x2); return __builtin_bit_cast(unsigned, b); }
DI srd_t mk_srd(const void* p, unsigned bytes) { return __builtin_amdgcn_make_buffer_rsrc((void*)p, (short)0, (int)bytes, 0x00020000); }
DI void dma4(unsigned lds, unsigned v0, unsigned v1, unsigned v2, unsigned v3, srd_t srd, unsigned soff) {
  asm volatile("s_mov_b32 m0, %0\n\ts_nop 0\n\tbuffer_load_dwordx4 %1, %5, %6 offen lds\n\tbuffer_load_dwordx4 %2, %5, %6 offen offset:1024 lds\n\t"
               "buffer_load_dwordx4 %3, %5, %6 offen offset:2048 lds\n\tbuffer_load_dwordx4 %4, %5, %6 offen offset:3072 lds"
               :: "s"(lds), "v"(v0), "v"(v1), "v"(v2), "v"(v3), "s"(srd), "s"(soff) : "memory");
}
DI void dma4_nt(unsigned lds, unsigned v0, unsigned v1, unsigned v2, unsigned v3, srd_t srd, unsigned soff) {
  asm volatile("s_mov_b32 m0, %0\n\ts_nop 0\n\tbuffer_load_dwordx4 %1, %5, %6 offen nt lds\n\tbuffer_load_dwordx4 %2, %5, %6 offen offset:1024 nt lds\n\t"
               "buffer_load_dwordx4 %3, %5, %6 offen offset:2048 nt lds\n\tbuffer_load_dwordx4 %4, %5, %6 offen offset:3072 nt lds"
               :: "s"(lds), "v"(v0), "v"(v1), "v"(v2), "v"(v3), "s"(srd), "s"(soff) : "memory");
}
DI void bstore_sc1(const u32x4& d, unsigned voff, srd_t srd, unsigned soff) { asm volatile("buffer_store_dwordx4 %0, %1, %2, %3 offen sc1\n\ts_nop 1" :: "v"(d), "v"(voff), "s"(srd), "s"(soff) : "memory"); }
DI void bload_sc1(bf16x8& d, unsigned voff, srd_t srd, unsigned soff) { asm volatile("buffer_load_dwordx4 %0, %1, %2, %3 offen sc1" : "=v"(d) : "v"(voff), "s"(srd), "s"(soff) : "memory"); }
DI void breload_sc1(bf16x8& d, unsigned voff, srd_t srd, unsigned soff) { asm volatile("buffer_load_dwordx4 %0, %1, %2, %3 offen sc1" : "+v"(d) : "v"(voff), "s"(srd), "s"(soff) : "memory"); }
DI void touch(unsigned& d, unsigned voff, srd_t srd, unsigned soff) { asm volatile("buffer_load_dword %0, %1, %2, %3 offen" : "+v"(d) : "v"(voff), "s"(srd), "s"(soff) : "memory"); }
DI void touch1(unsigned& d, unsigned voff, srd_t srd, unsigned soff) { asm volatile("buffer_load_dword %0, %1, %2, %3 offen sc1" : "+v"(d) : "v"(voff), "s"(srd), "s"(soff) : "memory"); }
DI void pin(bf16x8& a) { asm volatile("" : "+v"(a)); }
DI void pin16(f32x16& a) { asm volatile("" : "+v"(a)); }
DI void pinf(float& a) { asm volatile("" : "+v"(a)); }
DI void pinu(unsigned& a) { asm volatile("" : "+v"(a)); }
template <int N> DI void wait_vm1(bf16x8& a) { asm volatile("s_waitcnt vmcnt(%1)" : "+v"(a) : "n"(N) : "memory"); }
template <int N> DI void wait_vm() { asm volatile("s_waitcnt vmcnt(%0)" :: "n"(N) : "memory"); }
template <int N> DI void wait_lgkm() { __builtin_amdgcn_s_waitcnt(0xC07F | (N << 8)); SBAR(); }
DI void bar_if(unsigned c) { asm volatile("s_cmp_eq_u32 %0, 0\n\ts_cbranch_scc1 1f\n\ts_waitcnt lgkmcnt(0)\n\ts_barrier\n1:" :: "s"(c) : "memory", "scc"); }
DI void bar() { asm volatile("s_waitcnt lgkmcnt(0)\n\ts_barrier" ::: "memory"); }
typedef short v4i16_t __attribute__((ext_vector_type(4)));
DI s16x4 trd(lds_cptr p) { return __builtin_amdgcn_ds_read_tr16_b64_v4i16((__attribute__((address_space(3))) v4i16_t*)p); }
DI bf16x8 trd2(lds_cptr p, int o0, int o1) { s16x4 a = trd(p + o0), b = trd(p + o1); return __builtin_shufflevector(a, b, 0, 1, 2, 3, 4, 5, 6, 7); }
DI bf16x8 rd128(lds_cptr p) { return *(const __attribute__((address_space(3))) bf16x8*)p; }
DI float swap_max(float m) { auto rr = __builtin_amdgcn_permlane32_swap(__float_as_uint(m), __float_as_uint(m), false, false); return fmaxf(__uint_as_float(rr[0]), __uint_as_float(rr[1])); }
DI float swap_add(float m) { auto rr = __builtin_amdgcn_permlane32_swap(__float_as_uint(m), __float_as_uint(m), false, false); return __uint_as_float(rr[0]) + __uint_as_float(rr[1]); }
DI float max16(const f32x16& s) { float a = fmaxf(fmaxf(s[0], s[1]), s[2]), b = fmaxf(fmaxf(s[3], s[4]), s[5]);
  a = fmaxf(fmaxf(a, s[6]), s[7]); b = fmaxf(fmaxf(b, s[8]), s[9]); a = fmaxf(fmaxf(a, s[10]), s[11]); b = fmaxf(fmaxf(b, s[12]), s[13]); a = fmaxf(fmaxf(a, s[14]), s[15]); return fmaxf(a, b); }
DI float fadd(float a, float b) { float r; asm("v_add_f32_e32 %0, %1, %2" : "=v"(r) : "v"(a), "v"(b)); return r; }
DI u32x4 pack8(const f32x16& a, int s, float f) { return u32x4{cvtpk(a[8 * s] * f, a[8 * s + 1] * f), cvtpk(a[8 * s + 2] * f, a[8 * s + 3] * f), cvtpk(a[8 * s + 4] * f, a[8 * s + 5] * f), cvtpk(a[8 * s + 6] * f, a[8 * s + 7] * f)}; }
DI u32x4 pack8(const f32x16& a, int s) { return u32x4{cvtpk(a[8 * s], a[8 * s + 1]), cvtpk(a[8 * s + 2], a[8 * s + 3]), cvtpk(a[8 * s + 4], a[8 * s + 5]), cvtpk(a[8 * s + 6], a[8 * s + 7])}; }

DI float expo(const f32x16& s, u32x4 (&p)[2]) {
  float e[16]; float s0 = 0.f, s1 = 0.f;
#pragma unroll
  for (int i = 0; i < 16; ++i) e[i] = __builtin_amdgcn_exp2f(s[i]);
#pragma unroll
  for (int i = 0; i < 16; i += 2) { s0 += e[i]; s1 += e[i + 1]; }
#pragma unroll
  for (int i = 0; i < 8; ++i) p[i >> 2][i & 3] = cvtpk(e[2 * i], e[2 * i + 1]);
  return s0 + s1;
}

#define ST_DECL
#define ST(k)
#define ST_OUT(ws, lo, hi)

constexpr unsigned SLOTK = 0x04204209u;
constexpr unsigned SLOTV = 0x31531531u;
constexpr unsigned SLOTW = 0x05429876u;
constexpr unsigned STG = 45056u, STG_W = 28672u;
constexpr unsigned O_BYTES = NB * SQ * NH * DH * 2;
constexpr unsigned X_OFF = O_BYTES, X_BYTES = 63u * 8u * 4u * 4096u;
constexpr unsigned FLAG_OFF = X_OFF + X_BYTES, FLAG_STRIDE = 128, FLAG2_OFF = FLAG_OFF + 256 * FLAG_STRIDE, EPOCH_OFF = FLAG2_OFF + 63 * 32 * FLAG_STRIDE;
constexpr unsigned SPIN_CAP = 1u << 20;
#define RA_ 7
constexpr int RA = RA_;
#define WO 6
#define HG 3
#define RET 0
#define YPRIO 0
#define P2PRIO 0
#define GB 12
#define GY GB
#define DENSE 0
#define NOWUP 0
#define A0BAR 6
#define PSLEEP 4
#define CODEPF 18944
constexpr int lds_ops(int a, int b, bool last) { int n = 0; for (int i = a; i < b; ++i) n += i >= 16 ? (last ? 0 : 1) : (i >= 8 ? 2 : 1); return n; }
constexpr int pv_order(int i) { return i < 4 ? i : i < 8 ? i + 4 : i < 12 ? i - 4 : i; }
DI unsigned long long mix64(unsigned long long z) { z += 0x9E3779B97F4A7C15ull; z = (z ^ (z >> 30)) * 0xBF58476D1CE4E5B9ull; z = (z ^ (z >> 27)) * 0x94D049BB133111EBull; return z ^ (z >> 31); }
DI unsigned long long rfl64(unsigned long long v) { return ((unsigned long long)rfl((unsigned)(v >> 32)) << 32) | rfl((unsigned)v); }
DI unsigned long long ld_agent(const char* p) { return __hip_atomic_load((gu64*)p, __ATOMIC_RELAXED, __HIP_MEMORY_SCOPE_AGENT); }
DI void st_agent(char* p, unsigned long long v) { __hip_atomic_store((gu64*)p, v, __ATOMIC_RELAXED, __HIP_MEMORY_SCOPE_AGENT); }
DI void poll_flags(const char* p, unsigned long long tag) {
  _Pragma("nounroll") for (unsigned spins = 0; spins < SPIN_CAP; ++spins) { if (__all(ld_agent(p) == tag)) break; __builtin_amdgcn_s_sleep(PSLEEP); }
  asm volatile("" ::: "memory");
}

__global__ __launch_bounds__(512, 1) void mha_kernel(const unsigned short* __restrict__ x, const unsigned short* __restrict__ kc, const unsigned short* __restrict__ vc,
                                                     const unsigned short* __restrict__ wq, const unsigned short* __restrict__ wo, char* ws, unsigned short* __restrict__ out) {
  extern __shared__ __attribute__((aligned(16))) char lds[];
  unsigned long long pc0; asm volatile("s_getpc_b64 %0" : "=s"(pc0));
  const unsigned tid = threadIdx.x, lane = tid & 63u, r = lane & 31u, hh = lane >> 5;
  const unsigned wid = rfl(tid >> 6);
  ST_DECL ST(0);
  const unsigned bid = blockIdx.x, xcd = bid & 7u, ix = bid >> 3;
  const unsigned b = (xcd & 1u) * 4u + (ix & 3u), h = (xcd >> 1) * 8u + (ix >> 2);
  const unsigned lds0 = (unsigned)(uintptr_t)lds;
  const srd_t sW = mk_srd((const char*)wq - 1024, DM * NH * DH * 2 + 1024);
  const srd_t sX = mk_srd(x + (size_t)b * SQ * DM, SQ * DM * 2);
  const unsigned prow = (lane >> 2) & 7u, pch = 4u * hh + (lane & 3u);
  const unsigned pchx0 = 4u * hh + ((lane & 3u) ^ ((lane >> 4) & 1u)), pchx1 = pchx0 ^ 2u;
  const unsigned vW = prow * 8192u + pch * 16u, vX0 = prow * (DM * 2u) + pchx0 * 16u, vX1 = prow * (DM * 2u) + pchx1 * 16u;
  const unsigned ck0 = 3u * (((ix & 3u) + (ix >> 2)) % 3u);
#pragma unroll
  for (unsigned c = 0; c < 2; ++c) {
    if (wid >= 4u) dma4(lds0 + c * STG + STG_W + (wid - 4u) * 4096u, vW + 1024u, vW + 128u, vW + 65536u - 1024u, vW + 65664u - 2048u, sW, (ck0 + c) * 64u * 8192u + h * 256u + (wid - 4u) * 131072u);
    if (wid < 7u) dma4(lds0 + c * STG + wid * 4096u, vX0, vX1 + 8192u, vX0 + 16384u, vX1 + 24576u, sX, (ck0 + c) * 128u + wid * 32u * DM * 2u);
    if (c == 0) __builtin_amdgcn_s_barrier(); }
  const srd_t sK = mk_srd((const char*)(kc + (size_t)(b * NH + h) * SK * DH) - 1024, SK * DH * 2 + 1024);
  const unsigned vK0 = prow * 256u + pchx0 * 16u, vK1 = prow * 256u + pchx1 * 16u;
  const unsigned q4 = (lane & 15u) >> 2, p4 = lane & 3u, blk = (lane >> 4) & 1u;
  const unsigned trcol = 32u * blk + 16u * (p4 & 1u) + 8u * (p4 >> 1);
  const srd_t sO = mk_srd(ws, O_BYTES);
  unsigned long long tag;
  const unsigned g4 = xcd >> 1, u0 = (xcd & 1u) * 32u + ix;
  const bool has_tile = u0 < 63u;
  const unsigned u = has_tile ? u0 : 62u, rt = u / 9u, ct = u - rt * 9u;

  if (wid == 7u) {
    const srd_t sV = mk_srd((const char*)(vc + (size_t)(b * NH + h) * SK * DH) - 1024, SK * DH * 2 + 1024);
    auto fillQ = [&](unsigned c, unsigned stage) {
      const unsigned lw = lds0 + stage + STG_W, sw = c * 64u * 8192u + h * 256u, lx = lds0 + stage, sx = c * 128u;
#pragma unroll
      for (int i = 0; i < 4; ++i) dma4(lw + i * 4096u, vW + 1024u, vW + 128u, vW + 65536u - 1024u, vW + 65664u - 2048u, sW, sw + i * 131072u);
#pragma unroll
      for (int i = 0; i < 7; ++i) dma4(lx + i * 4096u, vX0, vX1 + 8192u, vX0 + 16384u, vX1 + 24576u, sX, sx + i * 32u * DM * 2u); };
    wait_vm<4>(); bar(); ST(2);
    unsigned long long ep; asm volatile("global_load_dwordx2 %0, %1, %2 sc1" : "=v"(ep) : "v"(0u), "s"(ws + EPOCH_OFF) : "memory");
    { unsigned stage = 2 * STG, ck = ck0 + 2u;
#pragma nounroll
      for (unsigned c = 2; c < 9; ++c) { fillQ(ck, stage); ck = ck == 8u ? 0u : ck + 1u; stage = stage == 2 * STG ? 0u : stage + STG;
        wait_vm<44>(); bar(); ST(1 + c); } }
    const unsigned vV = prow * 256u + pch * 16u;
    const srd_t sWo = mk_srd(wo, NH * DH * DM * 2);
    const unsigned vWo = prow * (DM * 2u) + pch * 16u;
    { unsigned j = 1, jw = 0;
#pragma nounroll
      for (unsigned n = 0; n < 10; ++n) {
        const unsigned we = (unsigned)(0x8754432100ull >> (4u * n)) & 15u;
#pragma nounroll
        for (; jw < we; ++jw) { const unsigned ld = lds0 + ((SLOTW >> (4u * jw)) & 15u) * 16384u, so = (g4 * 8u + jw) * 128u * (DM * 2u) + ct * 128u;
#pragma unroll
          for (int k = 0; k < 4; ++k) dma4(ld + k * 4096u, vWo, vWo + 8192u, vWo + 16384u, vWo + 24576u, sWo, so + k * 32u * (DM * 2u)); }
        const unsigned je = ((unsigned)(0xFFFECA8642ull >> (4u * n)) & 15u) + 1u;
#pragma nounroll
        for (; j < je; ++j) {
          if ((j & 1u) && j != 15u) { const unsigned t = (j + 1u) >> 1, ld = lds0 + ((SLOTK >> (4u * t)) & 15u) * 16384u;
#pragma unroll
            for (int i = 0; i < 4; ++i) dma4_nt(ld + i * 4096u, vK0 + 1024u, vK0 + 128u, vK1 + 1024u, vK1 + 128u, sK, t * 16384u + i * 4096u);
          } else { const unsigned t = (j - 1u) >> 1, ld = lds0 + ((SLOTV >> (4u * t)) & 15u) * 16384u;
#pragma unroll
            for (int i = 0; i < 4; ++i) dma4_nt(ld + i * 4096u, vV + 1024u, vV + 128u, vV + 1024u, vV + 128u, sV, t * 16384u + i * 4096u); } }
        if (n == 9u) break;
        if (n < 2u || n >= 6u) wait_vm<32>(); else wait_vm<48>();
        bar(); ST(n == 0u ? 10u : 11u + n); } }
    asm volatile("" : "+v"(ep)); tag = mix64(rfl64(ep));
    ST(20); ST(21);
  } else {
  const unsigned rsw0 = 64u * (r & 7u) + 16u * (hh ^ ((r >> 2) & 3u)), rsw1 = rsw0 ^ 32u;
  f32x16 qa[4] = {}, o[4] = {};
  asm volatile("" : "+v"(qa[0]), "+v"(qa[1]), "+v"(qa[2]), "+v"(qa[3]), "+v"(o[0]), "+v"(o[1]), "+v"(o[2]), "+v"(o[3]));
  const char* wup;
  { const unsigned k = lane & 7u, sel = lane >> 3;
    wup = sel == 0u ? (const char*)(k & 1u ? vc : kc) + (size_t)(b * NH + h) * (SK * DH * 2) + (k >> 1) * 32768u
        : sel == 1u ? (const char*)wo + (size_t)(g4 * 1024u + k * 128u) * (DM * 2u) + ct * 128u
        : sel == 2u ? ws + ((b * 7u + k % 7u) * NH + h) * 8192u
        : sel == 3u ? ws + ((8u * rt + k) * NH + g4 * 8u) * 8192u
        : sel == 4u ? ws + X_OFF + u * 131072u + (k & 1u) * 65536u
        : sel == 5u ? (const char*)out + (size_t)((8u * rt + k) * 32u) * (DM * 2u)
        : sel == 6u ? ws + FLAG_OFF + k * 32768u + 64u
        : (const char*)(pc0 & ~255ull) + min((ix * 8u + k) * 128u, (unsigned)CODEPF); }
  wait_lgkm<0>();
  if (wid >= 4u) wait_vm<8>(); else wait_vm<4>();
  ST(1);
  bar();
  unsigned cdummy = 0; unsigned long long ep = 0;
  ST(2);
  const lds_cptr kb0 = (lds_cptr)lds + 2048u * (r >> 3) + rsw0, kb1 = (lds_cptr)lds + 2048u * (r >> 3) + rsw1;
  const lds_cptr vb = (lds_cptr)lds + 64u * (4u * hh + q4) + trcol;
  bf16x8 qf[8], pre[RA];
  f32x16 negm, S0, S1; u32x4 P0[2], P1[2]; float l = 0.f;
  auto rdK = [&](lds_cptr k0, lds_cptr k1, int kb, int i) { return rd128(((i & 1) ? k1 : k0) + 8192 * kb + 512 * (i >> 1)); };
  auto rdV = [&](lds_cptr v, int kb, int j) { return trd2(v, 8192 * kb + 4096 * (j >> 2) + 512 * (j & 3), 8192 * kb + 4096 * (j >> 2) + 2048 + 512 * (j & 3)); };
  auto sm_fix = [&](float ps, f32x16& Sc, f32x16& Sn, u32x4 (&P)[2]) {
    if (__builtin_expect(__any(!(ps <= SUM_THR)), 0)) {
      const float dl = fmaxf(swap_max(max16(Sc)), 0.f), f = __builtin_amdgcn_exp2f(-dl);
#pragma unroll
      for (int i = 0; i < 16; ++i) { Sc[i] -= dl; Sn[i] -= dl; negm[i] -= dl; }
#pragma unroll
      for (int db = 0; db < 4; ++db)
#pragma unroll
        for (int i = 0; i < 16; ++i) o[db][i] *= f;
      l *= f; ps = expo(Sc, P); }
    l += ps; };
  { lds_cptr wp = (lds_cptr)lds + STG_W + 2048u * hh + 64u * q4 + trcol;
    lds_cptr xp0 = (lds_cptr)lds + wid * 4096u + 1024u * (r >> 3) + rsw0, xp1 = (lds_cptr)lds + wid * 4096u + 1024u * (r >> 3) + rsw1;
    bf16x8 xf[4], af[5];
    auto rdA = [&](int i) { return trd2(wp, 4096 * (i >> 2) + 512 * (i & 3), 4096 * (i >> 2) + 512 * (i & 3) + 256); };
    xf[0] = rd128(xp0); af[0] = rdA(0); af[1] = rdA(1); af[2] = rdA(2); af[3] = rdA(3);
    unsigned stage = 0;
#pragma nounroll
    for (unsigned c = 0; c < 8; ++c) {
      if (__builtin_expect(c == 3u, 0)) {
        asm volatile("global_load_dwordx2 %0, %1, %2 sc1" : "+v"(ep) : "v"(0u), "s"(ws + EPOCH_OFF) : "memory");
        if (DENSE && wid == 2u) { const char* p2 = lane < 16u ? ws + ((b * 7u + (lane >> 1) % 7u) * NH + h) * 8192u + (lane & 1u) * 4096u : lane < 48u ? ws + X_OFF + u * 131072u + (lane - 16u) * 4096u
            : lane < 56u ? ws + FLAG_OFF + (lane - 48u) * 4096u + 64u : lane == 56u ? ws + FLAG2_OFF + u * 4096u + 64u : (const char*)out + (size_t)((8u * rt + 2u * g4) * 32u + (lane - 57u) * 9u) * (DM * 2u);
          asm volatile("global_load_dword %0, %1, off" : "+v"(cdummy) : "v"(p2) : "memory"); }
        if (DENSE > 1 && wid == 1u) { const char* p2 = (const char*)(lane & 1u ? vc : kc) + (size_t)(b * NH + h) * (SK * DH * 2) + (lane >> 1) * 4096u;
          asm volatile("global_load_dword %0, %1, off" : "+v"(cdummy) : "v"(p2) : "memory"); }
        if (wid == 3u) { if (!NOWUP) asm volatile("global_load_dword %0, %1, off" : "+v"(cdummy) : "v"(wup) : "memory");
#pragma unroll
          for (int i = 0; i < 4; ++i) dma4_nt(lds0 + 9u * 16384u + i * 4096u, vK0 + 1024u, vK0 + 128u, vK1 + 1024u, vK1 + 128u, sK, i * 4096u); } }
      const unsigned nstage = stage == 2 * STG ? 0u : stage + STG; const int dlt = (int)nstage - (int)stage; stage = nstage;
      SBAR();
      static_for<12>([&](auto I) { constexpr int i = I.value;
        qa[i & 3] = MFMA(af[i % 5], xf[i >> 2], qa[i & 3]);
        af[(i + 4) % 5] = rdA(i + 4);
        if constexpr (i == 0) xf[1] = rd128(xp1); if constexpr (i == 3) xf[2] = rd128(xp0 + 512); if constexpr (i == 7) xf[3] = rd128(xp1 + 512);
        SBAR(); });
      qa[0] = MFMA(af[12 % 5], xf[3], qa[0]);
      wp += dlt; xp0 += dlt; xp1 += dlt; asm volatile("" : "+v"(wp), "+v"(xp0), "+v"(xp1));
      SBAR();
      asm volatile("s_bitcmp1_b32 %0, %1\n\ts_cbranch_scc1 1f\n\ts_waitcnt vmcnt(0)\n1:\n\ts_waitcnt lgkmcnt(0)\n\ts_barrier" :: "s"(0x78u), "s"(c) : "memory", "scc");
      const bf16x8 xl = xf[3], a13 = af[13 % 5], a14 = af[14 % 5], a15 = af[15 % 5];
      xf[0] = rd128(xp0); af[0] = rdA(0); af[1] = rdA(1);
      SBAR();
      qa[1] = MFMA(a13, xl, qa[1]); SBAR();
      af[2] = rdA(2);
      qa[2] = MFMA(a14, xl, qa[2]); SBAR();
      af[3] = rdA(3);
      qa[3] = MFMA(a15, xl, qa[3]); SBAR();
      ST(3 + c);
    }
    pinu(cdummy); asm volatile("" : "+v"(ep)); ep = rfl64(ep);
    lds_cptr k0 = kb0 + 9 * 16384, k1 = kb1 + 9 * 16384; asm volatile("" : "+v"(k0), "+v"(k1));
    bf16x8 ag[16], f[16]; u32x4 qw[8]; float qs = QSCALE; asm volatile("" : "+s"(qs));
#pragma unroll
    for (int i = 0; i < 4; ++i) ag[i] = af[i];
    auto cvq = [&](int j) { float lo = qa[j >> 3][2 * (j & 7)] * qs; pinf(lo); float hi = qa[j >> 3][2 * (j & 7) + 1] * qs; unsigned w = cvtpk(lo, hi); pinu(w); qw[j >> 2][j & 3] = w; };
    SBAR();
    static_for<24>([&](auto G) { constexpr int g = G.value, n = g + 4;
      if constexpr (g < 4) qa[g] = MFMA(ag[g], xf[0], qa[g]);
      else if constexpr (g < 16) qa[(g - 4) / 3] = MFMA(ag[g], xf[1 + (g - 4) % 3], qa[(g - 4) / 3]);
      else { qf[g - 16] = __builtin_bit_cast(bf16x8, qw[g - 16]);
        if constexpr (g == 16) { const f32x16 z = {}; S0 = MFMA(f[0], qf[0], z); } else S0 = MFMA(f[g - 16], qf[g - 16], S0); pin16(S0); }
      if constexpr (g == 0) { xf[1] = rd128(xp1); xf[2] = rd128(xp0 + 512); } if constexpr (g == 1) xf[3] = rd128(xp1 + 512);
      if constexpr (n < 16) ag[n] = rdA(4 * (1 + (n - 4) % 3) + (n - 4) / 3); else f[n - 16] = rdK(k0, k1, (n - 16) >> 3, (n - 16) & 7);
      if constexpr (g >= 9 && g < 21) { constexpr int j0 = (g - 9) * 8 / 3, j1 = (g - 8) * 8 / 3; static_for<j1 - j0>([&](auto J) { cvq(j0 + J.value); }); }
      SBAR(); });
    ST(11);
#pragma unroll
    for (int i = 12; i < 16; ++i) f[i] = rdK(k0, k1, 1, i & 7);
    const float m = swap_max(max16(S0));
#pragma unroll
    for (int i = 0; i < 16; ++i) negm[i] = -m;
    SBAR();
    float e[16]; float s0 = 0.f, s1 = 0.f;
    static_for<9>([&](auto G) { constexpr int g = G.value;
      if constexpr (g == 0) { if (wid >= 4u) bar(); }
      if constexpr (g == A0BAR) { if (wid < 4u) bar();
        constexpr unsigned ko = ((SLOTK >> 4) & 15u) * 16384u;
#pragma unroll
        for (int i = 0; i < RA; ++i) pre[i] = rdK(kb0 + ko, kb1 + ko, 0, i);
        SBAR(); }
      if constexpr (g == 0) S1 = MFMA(f[8], qf[0], negm); else if constexpr (g < 8) S1 = MFMA(f[8 + g], qf[g], S1);
      if constexpr (g < 8) pin16(S1);
      if constexpr (g < 8) { e[2 * g] = __builtin_amdgcn_exp2f(S0[2 * g] - m); e[2 * g + 1] = __builtin_amdgcn_exp2f(S0[2 * g + 1] - m); pinf(e[2 * g]); pinf(e[2 * g + 1]); }
      if constexpr (g == 1) { s0 = e[0]; s1 = e[1]; } else if constexpr (g > 1) { s0 += e[2 * g - 2]; pinf(s0); s1 += e[2 * g - 1]; pinf(s1); }
      if constexpr (g >= 1) { unsigned w = cvtpk(e[2 * g - 2], e[2 * g - 1]); pinu(w); P0[(g - 1) >> 2][(g - 1) & 3] = w; }
      SBAR(); });
    l = s0 + s1; }
  ST(12);
  auto unit = [&](auto KB, auto LAST, lds_cptr k0, lds_cptr k1, lds_cptr v, lds_cptr nk0, lds_cptr nk1, f32x16& Sn, f32x16& Sc, const u32x4 (&Pp)[2], u32x4 (&Pc)[2]) {
    constexpr int kb = KB.value; constexpr bool last = LAST.value;
    float e[16]; float s0 = 0.f, s1 = 0.f; bf16x8 f[16 + RA];
#pragma unroll
    for (int i = 0; i < RA; ++i) f[i] = pre[i];
    static_for<16>([&](auto G) { constexpr int g = G.value; constexpr int n = g + RA; constexpr int gb = GB;
      if constexpr (last && GY != GB && g == GY) { bar_if(wid >> 2); SBAR(); }
      if constexpr (last && g == gb) { if constexpr (GY != GB) bar_if((wid >> 2) ^ 1u); else bar(); SBAR(); }
      if constexpr (last && g == 12) wait_lgkm<0>();
      else if constexpr ((g & 3) == 0) wait_lgkm<lds_ops(g + 4, g + RA, last)>();
      if constexpr (g == 0) Sn = MFMA(f[0], qf[0], negm); else if constexpr (g < 8) Sn = MFMA(f[g], qf[g], Sn);
      else o[g & 3] = MFMA(f[g], __builtin_bit_cast(bf16x8, Pp[(g >> 2) & 1]), o[g & 3]);
      if constexpr (n < 8) f[n] = rdK(k0, k1, kb, n); else if constexpr (n < 16) f[n] = rdV(v, kb, n - 8);
      else if constexpr (!last) f[n] = rdK(k0, k1, kb + 1, n - 16);
      else if constexpr (g >= gb) { constexpr int per = (RA + 15 - gb) / (16 - gb); static_for<per>([&](auto J) { constexpr int i = per * (g - gb) + J.value; if constexpr (i < RA) f[16 + i] = rdK(nk0, nk1, 0, i); }); }
      if constexpr (g == 0) { e[0] = __builtin_amdgcn_exp2f(Sc[0]); pinf(e[0]); }
      if constexpr (g < 15) { e[g + 1] = __builtin_amdgcn_exp2f(Sc[g + 1]); pinf(e[g + 1]); }
      if constexpr (g == 1) { s0 = e[0]; s1 = e[1]; } else if constexpr (g > 1) { if constexpr (g & 1) { s1 += e[g]; pinf(s1); } else { s0 += e[g]; pinf(s0); } }
      if constexpr (g & 1) { unsigned w = cvtpk(e[g - 1], e[g]); pinu(w); Pc[g >> 3][(g >> 1) & 3] = w; }
      SBAR(); });
#pragma unroll
    for (int i = 0; i < RA; ++i) pre[i] = f[16 + i];
    sm_fix(s0 + s1, Sc, Sn, Pc); };
  if (YPRIO && wid >= 4u) __builtin_amdgcn_s_setprio(1);
#pragma nounroll
  for (unsigned t = 1; t < 7; ++t) {
    const unsigned ko = ((SLOTK >> (4u * t)) & 15u) * 16384u, vo = ((SLOTV >> (4u * (t - 1u))) & 15u) * 16384u, nko = ((SLOTK >> (4u * ((t + 1u) & 7u))) & 15u) * 16384u;
    SBAR();
    unit(std::integral_constant<int, 0>{}, std::false_type{}, kb0 + ko, kb1 + ko, vb + vo, kb0, kb1, S0, S1, P0, P1);
    unit(std::integral_constant<int, 1>{}, std::true_type{}, kb0 + ko, kb1 + ko, vb + vo, kb0 + nko, kb1 + nko, S1, S0, P1, P0);
    if (t == 1u || t == 6u) ST(12 + t);
  }
  const unsigned me = (b * 7u + wid) * NH + h;
  {
    constexpr unsigned ko = ((SLOTK >> 28) & 15u) * 16384u;
    const lds_cptr k0 = kb0 + ko, k1 = kb1 + ko, v6 = vb + ((SLOTV >> 24) & 15u) * 16384u, v7 = vb + ((SLOTV >> 28) & 15u) * 16384u;
    u32x4 P2[2], P3[2];
    auto scaleP = [&](u32x4 (&P)[2], float f) {
#pragma unroll
      for (int i = 0; i < 8; ++i) { const unsigned w = P[i >> 2][i & 3]; P[i >> 2][i & 3] = cvtpk(__uint_as_float(w << 16) * f, __uint_as_float(w & 0xFFFF0000u) * f); } };
    auto fix = [&](auto NP, float ps, f32x16& Sc, f32x16& Sn, u32x4 (&P)[2]) {
      if (__builtin_expect(__any(!(ps <= SUM_THR)), 0)) {
        const float f = __builtin_amdgcn_exp2f(-fmaxf(swap_max(max16(Sc)), 0.f));
        scaleP(P0, f); if constexpr (NP.value > 1) scaleP(P1, f); if constexpr (NP.value > 2) scaleP(P2, f); }
      sm_fix(ps, Sc, Sn, P); };
    auto rdVt = [&](int i) { const int j = pv_order(i), db = j >> 3, un = (j >> 1) & 3, ks = j & 1; return rdV(un < 2 ? v6 : v7, un & 1, 4 * ks + db); };
    auto pvt = [&](auto I, const bf16x8& vf) { constexpr int j = pv_order(I.value), un = (j >> 1) & 3;
      const u32x4 pb = un == 0 ? P0[j & 1] : un == 1 ? P1[j & 1] : un == 2 ? P2[j & 1] : P3[j & 1];
      o[j >> 3] = MFMA(vf, __builtin_bit_cast(bf16x8, pb), o[j >> 3]); pin16(o[j >> 3]); };
    bf16x8 f[16], w[32];
#pragma unroll
    for (int i = 0; i < RA; ++i) f[i] = pre[i];
    auto qsm = [&](auto H, f32x16& Sn, const f32x16& Sc, u32x4 (&Pc)[2]) { constexpr int hf = H.value;
      float e[16]; float s0 = 0.f, s1 = 0.f;
      static_for<9>([&](auto G) { constexpr int g = G.value, n = 8 * hf + g + RA;
        if constexpr (hf < 2 && g < 8) { if constexpr (g == 0) Sn = MFMA(f[8 * hf], qf[0], negm); else Sn = MFMA(f[8 * hf + g], qf[g], Sn);
          pin16(Sn);
          if constexpr (n < 16) f[n] = rdK(k0, k1, n >> 3, n & 7); }
        if constexpr (hf == 1 && g >= 2 && g < 8) w[g - 2] = rdVt(g - 2);
        if constexpr (hf == 2 && g < 8) { pvt(G, w[g]); w[g + 6] = rdVt(g + 6); }
        if constexpr (g < 8) { e[2 * g] = __builtin_amdgcn_exp2f(Sc[2 * g]); e[2 * g + 1] = __builtin_amdgcn_exp2f(Sc[2 * g + 1]); pinf(e[2 * g]); pinf(e[2 * g + 1]); }
        if constexpr (g == 1) { s0 = e[0]; s1 = e[1]; } else if constexpr (g > 1) { s0 += e[2 * g - 2]; pinf(s0); s1 += e[2 * g - 1]; pinf(s1); }
        if constexpr (g >= 1) { unsigned pw = cvtpk(e[2 * g - 2], e[2 * g - 1]); pinu(pw); Pc[(g - 1) >> 2][(g - 1) & 3] = pw; }
        SBAR(); });
      return s0 + s1; };
    SBAR();
    fix(std::integral_constant<int, 1>{}, qsm(std::integral_constant<int, 0>{}, S0, S1, P1), S1, S0, P1);
    fix(std::integral_constant<int, 2>{}, qsm(std::integral_constant<int, 1>{}, S1, S0, P2), S0, S1, P2);
    bar();
    ST(19);
    fix(std::integral_constant<int, 3>{}, qsm(std::integral_constant<int, 2>{}, S0, S1, P3), S1, S0, P3);
    const float inv = __builtin_amdgcn_rcpf(swap_add(l));
    auto put = [&](int k8) { bstore_sc1(pack8(o[k8 >> 1], k8 & 1, inv), lane * 16u, sO, me * 8192u + k8 * 1024u); };
    static_for<24>([&](auto G) { constexpr int g = G.value + 8;
      pvt(std::integral_constant<int, g>{}, w[g]);
      if constexpr (g + 6 < 32) w[g + 6] = rdVt(g + 6);
      if constexpr (g == 14) put(0); if constexpr (g == 16) put(1); if constexpr (g == 19) put(2); if constexpr (g == 22) put(3); if constexpr (g == 26) put(4); if constexpr (g == 29) put(5);
      SBAR(); });
    ST(20);
    put(6); put(7); }
  ST(21);
  tag = mix64(ep);
  }
  wait_vm<0>();
  ST(22);
  bar();
  if (tid == 0) st_agent(ws + FLAG_OFF + (b * NH + h) * FLAG_STRIDE, tag);
  if (RET && wid < 7u) { unsigned dummy = 0; touch1(dummy, lane * 128u + ((b * 7u + wid) * NH + h) * 8192u, sO, 0u); wait_vm<0>(); pinu(dummy); }
  ST(23);
  if (!has_tile) {
    if (bid == 249u && wid == 0u) {
#pragma nounroll
      for (unsigned i = 0; i < 4; ++i) poll_flags(ws + FLAG_OFF + (i * 64u + lane) * FLAG_STRIDE, tag);
      if (lane == 0) st_agent(ws + EPOCH_OFF, tag); }
    ST(24); ST(25); ST(26); ST(27); ST(28); ST(29); ST(30); ST(31); ST_OUT(ws, 0, 32); return; }
  const unsigned jq = (2u * g4 + 2u + wid) & 7u, G = 8u * rt + jq;
  const unsigned voffO = lane * 16u, soO = (G * NH + g4 * 8u) * 8192u;
  poll_flags(ws + FLAG_OFF + (((G * 37u) >> 8) * NH + g4 * 8u + (lane & 7u)) * FLAG_STRIDE, tag);
  ST(24);
  const lds_cptr wb = (lds_cptr)lds + 1024u * hh + 64u * q4 + trcol;
  if (P2PRIO) { if ((wid & 2u) == 0u) __builtin_amdgcn_s_setprio(2); else if (wid < 4u) __builtin_amdgcn_s_setprio(1); else __builtin_amdgcn_s_setprio(0); } else if (YPRIO) __builtin_amdgcn_s_setprio(0);
  f32x16 acc[2] = {};
  { unsigned hd = HG == 3 ? (ct % 3u) * 3u : (ct * (8u / HG)) & 7u, so_c = soO + hd * 8192u; lds_cptr wp = wb + ((SLOTW >> (4u * hd)) & 15u) * 16384u;
    bf16x8 of[8], lf[4][2];
    auto rdW = [&](int s2) { lf[s2 & 3][0] = trd2(wp, (s2 & 7) * 2048, (s2 & 7) * 2048 + 256); lf[s2 & 3][1] = trd2(wp, (s2 & 7) * 2048 + 512, (s2 & 7) * 2048 + 768); };
#pragma unroll
    for (int i = 0; i < 8; ++i) { if (i < WO) bload_sc1(of[i], voffO, sO, so_c + i * 1024u); else of[i] = bf16x8{}; }
    rdW(0); rdW(1);
#pragma nounroll
    for (unsigned p = 0; p < 8; ++p) {
      hd = (hd + 1u) & 7u;
      const unsigned so_n = p < 7u ? soO + hd * 8192u : O_BYTES;
      static_for<8>([&](auto S) { constexpr int s2 = S.value;
        if constexpr (s2 == 6) wp = wb + ((SLOTW >> (4u * hd)) & 15u) * 16384u;
        wait_vm1<WO - 1>(of[s2]);
        wait_lgkm<4>();
        acc[0] = MFMA(lf[s2 & 3][0], of[s2], acc[0]);
        rdW(s2 + 2);
        SBAR();
        acc[1] = MFMA(lf[s2 & 3][1], of[s2], acc[1]);
        SBAR();
        breload_sc1(of[(s2 + WO) & 7], voffO, sO, (s2 + WO < 8 ? so_c : so_n) + ((s2 + WO) & 7) * 1024u);
        SBAR(); });
      so_c = so_n;
    }
    wait_vm<0>();
#pragma unroll
    for (int i = 0; i < 8; ++i) pin(of[i]); }
  ST(28);
  const srd_t sX2 = mk_srd(ws + X_OFF, X_BYTES);
  const unsigned xb = (u * 8u + jq) * 4u;
  if (wid < 6u) {
#pragma unroll
    for (int i = 0; i < 4; ++i) bstore_sc1(pack8(acc[i >> 1], i & 1), voffO, sX2, (xb + g4) * 4096u + i * 1024u);
    wait_vm<0>();
    if (lane == 0) st_agent(ws + FLAG2_OFF + (xb + g4) * FLAG_STRIDE, tag);
    ST(29); ST(30);
  } else {
    poll_flags(ws + FLAG2_OFF + (xb + ((g4 + 1u + lane % 3u) & 3u)) * FLAG_STRIDE, tag);
    ST(29);
    bf16x8 xp[12];
#pragma unroll
    for (int i = 0; i < 12; ++i) bload_sc1(xp[i], voffO, sX2, (xb + ((g4 + 1u + (i >> 2)) & 3u)) * 4096u + (i & 3) * 1024u);
    wait_vm<0>();
#pragma unroll
    for (int j = 0; j < 12; ++j) { const int i = j & 3; pin(xp[j]); const u32x4 v = __builtin_bit_cast(u32x4, xp[j]);
#pragma unroll
      for (int e = 0; e < 4; ++e) { acc[i >> 1][8 * (i & 1) + 2 * e] += __uint_as_float(v[e] << 16); acc[i >> 1][8 * (i & 1) + 2 * e + 1] += __uint_as_float(v[e] & 0xFFFF0000u); } }
    ST(30);
    char* op = (char*)out + (size_t)(G * 32u + r) * (DM * 2u) + ct * 128u + hh * 16u;
#pragma unroll
    for (int i = 0; i < 4; ++i) *(u32x4*)(op + i * 32) = pack8(acc[i >> 1], i & 1);
  }
  ST(31); ST_OUT(ws, 0, 32);
}

extern "C" void kernel_launch(void* const* d_in, const int* in_sizes, int n_in, void* d_out, int out_size, void* d_ws, size_t ws_size, hipStream_t stream) {
  (void)in_sizes; (void)n_in; (void)out_size; (void)ws_size;
  (void)hipFuncSetAttribute((const void*)mha_kernel, hipFuncAttributeMaxDynamicSharedMemorySize, LDS_BYTES);
  const unsigned short* x = (const unsigned short*)d_in[0]; const unsigned short* kc = (const unsigned short*)d_in[1]; const unsigned short* vc = (const unsigned short*)d_in[2];
  const unsigned short* wq = (const unsigned short*)d_in[3]; const unsigned short* wo = (const unsigned short*)d_in[4];
  mha_kernel<<<256, 512, LDS_BYTES, stream>>>(x, kc, vc, wq, wo, (char*)d_ws, (unsigned short*)d_out);
}
```

```cpp
#define PDELAY 30
#define PDELAY2 40
#include <hip/hip_runtime.h>
#include <stdint.h>
#include <utility>

typedef short    bf16x8 __attribute__((ext_vector_type(8)));
typedef short    s16x4  __attribute__((ext_vector_type(4)));
typedef float    f32x16 __attribute__((ext_vector_type(16)));
typedef float    f32x2  __attribute__((ext_vector_type(2)));
typedef __bf16   bf16x2 __attribute__((ext_vector_type(2)));
typedef unsigned u32x4  __attribute__((ext_vector_type(4)));
typedef __attribute__((address_space(3))) const char* lds_cptr;
typedef __attribute__((address_space(1))) unsigned long long gu64;
typedef __amdgpu_buffer_rsrc_t srd_t;

#define DI __device__ __forceinline__
#define MFMA(a, b, c) __builtin_amdgcn_mfma_f32_32x32x16_bf16((a), (b), (c), 0, 0, 0)
#define SBAR() __builtin_amdgcn_sched_barrier(0)

constexpr int NB = 8, SQ = 224, DM = 576, NH = 32, SK = 512, DH = 128;
constexpr int LDS_BYTES = 163840;
constexpr float QSCALE = 0.08838834764831845f * 1.4426950408889634f;
constexpr float SUM_THR = 4096.f;

template <int... Is, class F> DI void static_for_impl(std::integer_sequence<int, Is...>, F&& f) { (f(std::integral_constant<int, Is>{}), ...); }
template <int N, class F> DI void static_for(F&& f) { static_for_impl(std::make_integer_sequence<int, N>{}, f); }

DI unsigned rfl(unsigned x) { return __builtin_amdgcn_readfirstlane(x); }
DI unsigned cvtpk(float lo, float hi) { f32x2 v = {lo, hi}; bf16x2 b = __builtin_convertvector(v, bf16x2); return __builtin_bit_cast(unsigned, b); }
DI srd_t mk_srd(const void* p, unsigned bytes) { return __builtin_amdgcn_make_buffer_rsrc((void*)p, (short)0, (int)bytes, 0x00020000); }
DI void dma4(unsigned lds, unsigned v0, unsigned v1, unsigned v2, unsigned v3, srd_t srd, unsigned soff) {
  asm volatile("s_mov_b32 m0, %0\n\ts_nop 0\n\tbuffer_load_dwordx4 %1, %5, %6 offen lds\n\tbuffer_load_dwordx4 %2, %5, %6 offen offset:1024 lds\n\t"
               "buffer_load_dwordx4 %3, %5, %6 offen offset:2048 lds\n\tbuffer_load_dwordx4 %4, %5, %6 offen offset:3072 lds"
               :: "s"(lds), "v"(v0), "v"(v1), "v"(v2), "v"(v3), "s"(srd), "s"(soff) : "memory");
}
DI void dma4_nt(unsigned lds, unsigned v0, unsigned v1, unsigned v2, unsigned v3, srd_t srd, unsigned soff) {
  asm volatile("s_mov_b32 m0, %0\n\ts_nop 0\n\tbuffer_load_dwordx4 %1, %5, %6 offen nt lds\n\tbuffer_load_dwordx4 %2, %5, %6 offen offset:1024 nt lds\n\t"
               "buffer_load_dwordx4 %3, %5, %6 offen offset:2048 nt lds\n\tbuffer_load_dwordx4 %4, %5, %6 offen offset:3072 nt lds"
               :: "s"(lds), "v"(v0), "v"(v1), "v"(v2), "v"(v3), "s"(srd), "s"(soff) : "memory");
}
DI void bstore_sc1(const u32x4& d, unsigned voff, srd_t srd, unsigned soff) { asm volatile("buffer_store_dwordx4 %0, %1, %2, %3 offen sc1\n\ts_nop 1" :: "v"(d), "v"(voff), "s"(srd), "s"(soff) : "memory"); }
DI void bload_sc1(bf16x8& d, unsigned voff, srd_t srd, unsigned soff) { asm volatile("buffer_load_dwordx4 %0, %1, %2, %3 offen sc1" : "=v"(d) : "v"(voff), "s"(srd), "s"(soff) : "memory"); }
DI void breload_sc1(bf16x8& d, unsigned voff, srd_t srd, unsigned soff) { asm volatile("buffer_load_dwordx4 %0, %1, %2, %3 offen sc1" : "+v"(d) : "v"(voff), "s"(srd), "s"(soff) : "memory"); }
DI void touch(unsigned& d, unsigned voff, srd_t srd, unsigned soff) { asm volatile("buffer_load_dword %0, %1, %2, %3 offen" : "+v"(d) : "v"(voff), "s"(srd), "s"(soff) : "memory"); }
DI void touch_nt(unsigned& d, unsigned voff, srd_t srd, unsigned soff) { asm volatile("buffer_load_dword %0, %1, %2, %3 offen nt" : "+v"(d) : "v"(voff), "s"(srd), "s"(soff) : "memory"); }
DI void touch1(unsigned& d, unsigned voff, srd_t srd, unsigned soff) { asm volatile("buffer_load_dword %0, %1, %2, %3 offen sc1" : "+v"(d) : "v"(voff), "s"(srd), "s"(soff) : "memory"); }
DI void pin(bf16x8& a) { asm volatile("" : "+v"(a)); }
DI void pin16(f32x16& a) { asm volatile("" : "+v"(a)); }
DI void pinf(float& a) { asm volatile("" : "+v"(a)); }
DI void pinu(unsigned& a) { asm volatile("" : "+v"(a)); }
template <int N> DI void wait_vm1(bf16x8& a) { asm volatile("s_waitcnt vmcnt(%1)" : "+v"(a) : "n"(N) : "memory"); }
template <int N> DI void wait_vm() { asm volatile("s_waitcnt vmcnt(%0)" :: "n"(N) : "memory"); }
template <int N> DI void wait_lgkm() { __builtin_amdgcn_s_waitcnt(0xC07F | (N << 8)); SBAR(); }
DI void bar_if(unsigned c) { asm volatile("s_cmp_eq_u32 %0, 0\n\ts_cbranch_scc1 1f\n\ts_waitcnt lgkmcnt(0)\n\ts_barrier\n1:" :: "s"(c) : "memory", "scc"); }
DI void bar() { asm volatile("s_waitcnt lgkmcnt(0)\n\ts_barrier" ::: "memory"); }
typedef short v4i16_t __attribute__((ext_vector_type(4)));
DI s16x4 trd(lds_cptr p) { return __builtin_amdgcn_ds_read_tr16_b64_v4i16((__attribute__((address_space(3))) v4i16_t*)p); }
DI bf16x8 trd2(lds_cptr p, int o0, int o1) { s16x4 a = trd(p + o0), b = trd(p + o1); return __builtin_shufflevector(a, b, 0, 1, 2, 3, 4, 5, 6, 7); }
DI bf16x8 rd128(lds_cptr p) { return *(const __attribute__((address_space(3))) bf16x8*)p; }
DI float swap_max(float m) { auto rr = __builtin_amdgcn_permlane32_swap(__float_as_uint(m), __float_as_uint(m), false, false); return fmaxf(__uint_as_float(rr[0]), __uint_as_float(rr[1])); }
DI float swap_add(float m) { auto rr = __builtin_amdgcn_permlane32_swap(__float_as_uint(m), __float_as_uint(m), false, false); return __uint_as_float(rr[0]) + __uint_as_float(rr[1]); }
DI float max16(const f32x16& s) { float a = fmaxf(fmaxf(s[0], s[1]), s[2]), b = fmaxf(fmaxf(s[3], s[4]), s[5]);
  a = fmaxf(fmaxf(a, s[6]), s[7]); b = fmaxf(fmaxf(b, s[8]), s[9]); a = fmaxf(fmaxf(a, s[10]), s[11]); b = fmaxf(fmaxf(b, s[12]), s[13]); a = fmaxf(fmaxf(a, s[14]), s[15]); return fmaxf(a, b); }
DI float fadd(float a, float b) { float r; asm("v_add_f32_e32 %0, %1, %2" : "=v"(r) : "v"(a), "v"(b)); return r; }
DI u32x4 pack8(const f32x16& a, int s, float f) { return u32x4{cvtpk(a[8 * s] * f, a[8 * s + 1] * f), cvtpk(a[8 * s + 2] * f, a[8 * s + 3] * f), cvtpk(a[8 * s + 4] * f, a[8 * s + 5] * f), cvtpk(a[8 * s + 6] * f, a[8 * s + 7] * f)}; }
DI u32x4 pack8(const f32x16& a, int s) { return u32x4{cvtpk(a[8 * s], a[8 * s + 1]), cvtpk(a[8 * s + 2], a[8 * s + 3]), cvtpk(a[8 * s + 4], a[8 * s + 5]), cvtpk(a[8 * s + 6], a[8 * s + 7])}; }

DI float expo(const f32x16& s, u32x4 (&p)[2]) {
  float e[16]; float s0 = 0.f, s1 = 0.f;
#pragma unroll
  for (int i = 0; i < 16; ++i) e[i] = __builtin_amdgcn_exp2f(s[i]);
#pragma unroll
  for (int i = 0; i < 16; i += 2) { s0 += e[i]; s1 += e[i + 1]; }
#pragma unroll
  for (int i = 0; i < 8; ++i) p[i >> 2][i & 3] = cvtpk(e[2 * i], e[2 * i + 1]);
  return s0 + s1;
}

#define ST_DECL
#define ST(k)
#define ST_OUT(ws, lo, hi)

constexpr unsigned SLOTK = 0x04204209u;
constexpr unsigned SLOTV = 0x31531531u;
constexpr unsigned SLOTW = 0x05429876u;
constexpr unsigned STG = 45056u, STG_W = 28672u;
constexpr unsigned O_BYTES = NB * SQ * NH * DH * 2;
constexpr unsigned X_OFF = O_BYTES, X_BYTES = 63u * 8u * 4u * 4096u;
constexpr unsigned FLAG_OFF = X_OFF + X_BYTES, FLAG_STRIDE = 128, FLAG2_OFF = FLAG_OFF + 256 * FLAG_STRIDE, EPOCH_OFF = FLAG2_OFF + 63 * 32 * FLAG_STRIDE;
constexpr unsigned SPIN_CAP = 1u << 20;
#define RA_ 7
constexpr int RA = RA_;
#define WO 6
#define HG 1
#define RET 0
#define YPRIO 0
#define P2PRIO 0
#define GB 12
#define GY GB
#define NOWUP 0
#define PF 0
#define PF0 4
#define KVT 0xFFFECA8642ull
#define WOT 0x8754432100ull
#define WLATE 0
#define LAZY 0
#define TSTAG 1
#define FMODE 0
#define ONEP 0
#define A0BAR 6
#define PSLEEP 4
#define CODEPF 18944
constexpr int lds_ops(int a, int b, bool last) { int n = 0; for (int i = a; i < b; ++i) n += i >= 16 ? (last ? 0 : 1) : (i >= 8 ? 2 : 1); return n; }
constexpr int pv_order(int i) { return i < 4 ? i : i < 8 ? i + 4 : i < 12 ? i - 4 : i; }
DI unsigned long long mix64(unsigned long long z) { z += 0x9E3779B97F4A7C15ull; z = (z ^ (z >> 30)) * 0xBF58476D1CE4E5B9ull; z = (z ^ (z >> 27)) * 0x94D049BB133111EBull; return z ^ (z >> 31); }
DI unsigned long long rfl64(unsigned long long v) { return ((unsigned long long)rfl((unsigned)(v >> 32)) << 32) | rfl((unsigned)v); }
#define PSYS 0
DI unsigned long long ld_agent(const char* p) {
  if (PSYS == 1) return __hip_atomic_load((gu64*)p, __ATOMIC_RELAXED, __HIP_MEMORY_SCOPE_SYSTEM);
  if (PSYS == 2) return __hip_atomic_fetch_add((gu64*)p, 0ull, __ATOMIC_RELAXED, __HIP_MEMORY_SCOPE_AGENT);
  return __hip_atomic_load((gu64*)p, __ATOMIC_RELAXED, __HIP_MEMORY_SCOPE_AGENT); }
DI void st_agent(char* p, unsigned long long v) { __hip_atomic_store((gu64*)p, v, __ATOMIC_RELAXED, __HIP_MEMORY_SCOPE_AGENT); }
DI void poll_flags(const char* p, unsigned long long tag) {
  _Pragma("nounroll") for (unsigned spins = 0; spins < SPIN_CAP; ++spins) { if (__all(ld_agent(p) == tag)) break; __builtin_amdgcn_s_sleep(PSLEEP); }
  asm volatile("" ::: "memory");
}

__global__ __launch_bounds__(512, 1) void mha_kernel(const unsigned short* __restrict__ x, const unsigned short* __restrict__ kc, const unsigned short* __restrict__ vc,
                                                     const unsigned short* __restrict__ wq, const unsigned short* __restrict__ wo, char* ws, unsigned short* __restrict__ out) {
  extern __shared__ __attribute__((aligned(16))) char lds[];
  unsigned long long pc0; asm volatile("s_getpc_b64 %0" : "=s"(pc0));
  const unsigned tid = threadIdx.x, lane = tid & 63u, r = lane & 31u, hh = lane >> 5;
  const unsigned wid = rfl(tid >> 6);
  ST_DECL ST(0);
  const unsigned bid = blockIdx.x, xcd = bid & 7u, ix = bid >> 3;
  const unsigned b = (xcd & 1u) * 4u + (ix & 3u), h = (xcd >> 1) * 8u + (ix >> 2);
  const unsigned lds0 = (unsigned)(uintptr_t)lds;
  const srd_t sW = mk_srd((const char*)wq - 1024, DM * NH * DH * 2 + 1024);
  const srd_t sX = mk_srd(x + (size_t)b * SQ * DM, SQ * DM * 2);
  const unsigned prow = (lane >> 2) & 7u, pch = 4u * hh + (lane & 3u);
  const unsigned pchx0 = 4u * hh + ((lane & 3u) ^ ((lane >> 4) & 1u)), pchx1 = pchx0 ^ 2u;
  const unsigned vW = prow * 8192u + pch * 16u, vX0 = prow * (DM * 2u) + pchx0 * 16u, vX1 = prow * (DM * 2u) + pchx1 * 16u;
  const unsigned ck0 = 3u * (((ix & 3u) + (ix >> 2)) % 3u);
#pragma unroll
  for (unsigned c = 0; c < 2; ++c) {
    if (wid >= 4u) dma4(lds0 + c * STG + STG_W + (wid - 4u) * 4096u, vW + 1024u, vW + 128u, vW + 65536u - 1024u, vW + 65664u - 2048u, sW, (ck0 + c) * 64u * 8192u + h * 256u + (wid - 4u) * 131072u);
    if (wid < 7u) dma4(lds0 + c * STG + wid * 4096u, vX0, vX1 + 8192u, vX0 + 16384u, vX1 + 24576u, sX, (ck0 + c) * 128u + wid * 32u * DM * 2u);
    if (c == 0) __builtin_amdgcn_s_barrier(); }
  const srd_t sK = mk_srd((const char*)(kc + (size_t)(b * NH + h) * SK * DH) - 1024, SK * DH * 2 + 1024);
  const srd_t sV = mk_srd((const char*)(vc + (size_t)(b * NH + h) * SK * DH) - 1024, SK * DH * 2 + 1024);
  const unsigned vK0 = prow * 256u + pchx0 * 16u, vK1 = prow * 256u + pchx1 * 16u;
  const unsigned q4 = (lane & 15u) >> 2, p4 = lane & 3u, blk = (lane >> 4) & 1u;
  const unsigned trcol = 32u * blk + 16u * (p4 & 1u) + 8u * (p4 >> 1);
  const srd_t sO = mk_srd(ws, O_BYTES);
  unsigned long long tag;
  const unsigned g4 = xcd >> 1, u0 = (xcd & 1u) * 32u + ix;
  const bool has_tile = u0 < 63u;
  const unsigned u = has_tile ? u0 : 62u, rt = u / 9u, ct = u - rt * 9u;

  if (wid == 7u) {
    auto fillQ = [&](unsigned c, unsigned stage) {
      const unsigned lw = lds0 + stage + STG_W, sw = c * 64u * 8192u + h * 256u, lx = lds0 + stage, sx = c * 128u;
#pragma unroll
      for (int i = 0; i < 4; ++i) dma4(lw + i * 4096u, vW + 1024u, vW + 128u, vW + 65536u - 1024u, vW + 65664u - 2048u, sW, sw + i * 131072u);
#pragma unroll
      for (int i = 0; i < 7; ++i) dma4(lx + i * 4096u, vX0, vX1 + 8192u, vX0 + 16384u, vX1 + 24576u, sX, sx + i * 32u * DM * 2u); };
    wait_vm<4>(); bar(); ST(2);
    unsigned long long ep; asm volatile("global_load_dwordx2 %0, %1, %2 sc1" : "=v"(ep) : "v"(0u), "s"(ws + EPOCH_OFF) : "memory");
    { unsigned stage = 2 * STG, ck = ck0 + 2u;
#pragma nounroll
      for (unsigned c = 2; c < 9; ++c) { fillQ(ck, stage); ck = ck == 8u ? 0u : ck + 1u; stage = stage == 2 * STG ? 0u : stage + STG;
        wait_vm<44>(); bar(); ST(1 + c); } }
    const unsigned vV = prow * 256u + pch * 16u;
    const srd_t sWo = mk_srd(wo, NH * DH * DM * 2);
    const unsigned vWo = prow * (DM * 2u) + pch * 16u;
    constexpr unsigned last_n = 9u;
    { unsigned j = 1, jw = 0;
#pragma nounroll
      for (unsigned n = 0; n < 10; ++n) {
        if (TSTAG && n == 9u) { wait_vm<0>(); bar(); }
        const unsigned we = (unsigned)(WOT >> (4u * n)) & 15u;
#pragma nounroll
        for (; jw < we; ++jw) { const unsigned ld = lds0 + ((SLOTW >> (4u * jw)) & 15u) * 16384u, so = (g4 * 8u + jw) * 128u * (DM * 2u) + ct * 128u;
#pragma unroll
          for (int k = 0; k < 4; ++k) dma4(ld + k * 4096u, vWo, vWo + 8192u, vWo + 16384u, vWo + 24576u, sWo, so + k * 32u * (DM * 2u)); }
        const unsigned je = ((unsigned)(KVT >> (4u * n)) & 15u) + 1u;
#pragma nounroll
        for (; j < je; ++j) {
          if ((j & 1u) && j != 15u) { const unsigned t = (j + 1u) >> 1, ld = lds0 + ((SLOTK >> (4u * t)) & 15u) * 16384u;
#pragma unroll
            for (int i = 0; i < 4; ++i) dma4_nt(ld + i * 4096u, vK0 + 1024u, vK0 + 128u, vK1 + 1024u, vK1 + 128u, sK, t * 16384u + i * 4096u);
          } else { const unsigned t = (j - 1u) >> 1, ld = lds0 + ((SLOTV >> (4u * t)) & 15u) * 16384u;
#pragma unroll
            for (int i = 0; i < 4; ++i) dma4_nt(ld + i * 4096u, vV + 1024u, vV + 128u, vV + 1024u, vV + 128u, sV, t * 16384u + i * 4096u); } }
        if (n == last_n) break;
        if (n < 2u || n >= 6u) wait_vm<32>(); else wait_vm<48>();
        bar(); ST(n == 0u ? 10u : 11u + n); } }
    asm volatile("" : "+v"(ep)); tag = mix64(rfl64(ep));
    ST(20); ST(21);
  } else {
  const unsigned rsw0 = 64u * (r & 7u) + 16u * (hh ^ ((r >> 2) & 3u)), rsw1 = rsw0 ^ 32u;
  f32x16 qa[4] = {}, o[4] = {};
  asm volatile("" : "+v"(qa[0]), "+v"(qa[1]), "+v"(qa[2]), "+v"(qa[3]), "+v"(o[0]), "+v"(o[1]), "+v"(o[2]), "+v"(o[3]));
  const char* wup;
  { const unsigned k = lane & 7u, sel = lane >> 3;
    wup = sel == 0u ? (const char*)(k & 1u ? vc : kc) + (size_t)(b * NH + h) * (SK * DH * 2) + (k >> 1) * 32768u
        : sel == 1u ? (const char*)wo + (size_t)(g4 * 1024u + k * 128u) * (DM * 2u) + ct * 128u
        : sel == 2u ? ws + ((b * 7u + k % 7u) * NH + h) * 8192u
        : sel == 3u ? ws + ((8u * rt + k) * NH + g4 * 8u) * 8192u
        : sel == 4u ? ws + X_OFF + u * 131072u + (k & 1u) * 65536u
        : sel == 5u ? (const char*)out + (size_t)((8u * rt + k) * 32u) * (DM * 2u)
        : sel == 6u ? ws + FLAG_OFF + k * 32768u + 64u
        : (const char*)(pc0 & ~255ull) + min((ix * 8u + k) * 128u, (unsigned)CODEPF); }
  wait_lgkm<0>();
  if (wid >= 4u) wait_vm<8>(); else wait_vm<4>();
  ST(1);
  bar();
  unsigned cdummy = 0; unsigned long long ep = 0;
  ST(2);
  const lds_cptr kb0 = (lds_cptr)lds + 2048u * (r >> 3) + rsw0, kb1 = (lds_cptr)lds + 2048u * (r >> 3) + rsw1;
  const lds_cptr vb = (lds_cptr)lds + 64u * (4u * hh + q4) + trcol;
  bf16x8 qf[8], pre[RA];
  f32x16 negm, S0, S1; u32x4 P0[2], P1[2]; float l = 0.f;
  auto rdK = [&](lds_cptr k0, lds_cptr k1, int kb, int i) { return rd128(((i & 1) ? k1 : k0) + 8192 * kb + 512 * (i >> 1)); };
  auto rdV = [&](lds_cptr v, int kb, int j) { return trd2(v, 8192 * kb + 4096 * (j >> 2) + 512 * (j & 3), 8192 * kb + 4096 * (j >> 2) + 2048 + 512 * (j & 3)); };
  auto sm_fix = [&](float ps, f32x16& Sc, f32x16& Sn, u32x4 (&P)[2]) {
    if (__builtin_expect(__any(!(ps <= SUM_THR)), 0)) {
      const float dl = fmaxf(swap_max(max16(Sc)), 0.f), f = __builtin_amdgcn_exp2f(-dl);
#pragma unroll
      for (int i = 0; i < 16; ++i) { Sc[i] -= dl; Sn[i] -= dl; negm[i] -= dl; }
#pragma unroll
      for (int db = 0; db < 4; ++db)
#pragma unroll
        for (int i = 0; i < 16; ++i) o[db][i] *= f;
      l *= f; ps = expo(Sc, P); }
    l += ps; };
  { lds_cptr wp = (lds_cptr)lds + STG_W + 2048u * hh + 64u * q4 + trcol;
    lds_cptr xp0 = (lds_cptr)lds + wid * 4096u + 1024u * (r >> 3) + rsw0, xp1 = (lds_cptr)lds + wid * 4096u + 1024u * (r >> 3) + rsw1;
    bf16x8 xf[4], af[5];
    auto rdA = [&](int i) { return trd2(wp, 4096 * (i >> 2) + 512 * (i & 3), 4096 * (i >> 2) + 512 * (i & 3) + 256); };
    xf[0] = rd128(xp0); af[0] = rdA(0); af[1] = rdA(1); af[2] = rdA(2); af[3] = rdA(3);
    unsigned stage = 0;
#pragma nounroll
    for (unsigned c = 0; c < 8; ++c) {
      if (__builtin_expect(c == 3u, 0)) {
        asm volatile("global_load_dwordx2 %0, %1, %2 sc1" : "+v"(ep) : "v"(0u), "s"(ws + EPOCH_OFF) : "memory");
        if (wid == 3u) { if (!NOWUP) asm volatile("global_load_dword %0, %1, off" : "+v"(cdummy) : "v"(wup) : "memory");
#pragma unroll
          for (int i = 0; i < 4; ++i) dma4_nt(lds0 + 9u * 16384u + i * 4096u, vK0 + 1024u, vK0 + 128u, vK1 + 1024u, vK1 + 128u, sK, i * 4096u); } }
      if (PF && __builtin_expect(wid == 3u && c >= PF0 && c < PF0 + PF, 0)) {
        const unsigned so = (c - PF0) * 16384u + 1024u;
        touch_nt(cdummy, lane * 128u, sK, so + 16384u); touch_nt(cdummy, lane * 128u, sK, so + 24576u); touch_nt(cdummy, lane * 128u, sV, so); touch_nt(cdummy, lane * 128u, sV, so + 8192u); }
      const unsigned nstage = stage == 2 * STG ? 0u : stage + STG; const int dlt = (int)nstage - (int)stage; stage = nstage;
      SBAR();
      static_for<12>([&](auto I) { constexpr int i = I.value;
        qa[i & 3] = MFMA(af[i % 5], xf[i >> 2], qa[i & 3]);
        af[(i + 4) % 5] = rdA(i + 4);
        if constexpr (i == 0) xf[1] = rd128(xp1); if constexpr (i == 3) xf[2] = rd128(xp0 + 512); if constexpr (i == 7) xf[3] = rd128(xp1 + 512);
        SBAR(); });
      qa[0] = MFMA(af[12 % 5], xf[3], qa[0]);
      wp += dlt; xp0 += dlt; xp1 += dlt; asm volatile("" : "+v"(wp), "+v"(xp0), "+v"(xp1));
      SBAR();
      asm volatile("s_bitcmp1_b32 %0, %1\n\ts_cbranch_scc1 1f\n\ts_waitcnt vmcnt(0)\n1:\n\ts_waitcnt lgkmcnt(0)\n\ts_barrier" :: "s"(0x78u), "s"(c) : "memory", "scc");
      const bf16x8 xl = xf[3], a13 = af[13 % 5], a14 = af[14 % 5], a15 = af[15 % 5];
      xf[0] = rd128(xp0); af[0] = rdA(0); af[1] = rdA(1);
      SBAR();
      qa[1] = MFMA(a13, xl, qa[1]); SBAR();
      af[2] = rdA(2);
      qa[2] = MFMA(a14, xl, qa[2]); SBAR();
      af[3] = rdA(3);
      qa[3] = MFMA(a15, xl, qa[3]); SBAR();
      ST(3 + c);
    }
    pinu(cdummy); asm volatile("" : "+v"(ep)); ep = rfl64(ep);
    lds_cptr k0 = kb0 + 9 * 16384, k1 = kb1 + 9 * 16384; asm volatile("" : "+v"(k0), "+v"(k1));
    bf16x8 ag[16], f[16]; u32x4 qw[8]; float qs = QSCALE; asm volatile("" : "+s"(qs));
#pragma unroll
    for (int i = 0; i < 4; ++i) ag[i] = af[i];
    auto cvq = [&](int j) { float lo = qa[j >> 3][2 * (j & 7)] * qs; pinf(lo); float hi = qa[j >> 3][2 * (j & 7) + 1] * qs; unsigned w = cvtpk(lo, hi); pinu(w); qw[j >> 2][j & 3] = w; };
    SBAR();
    static_for<24>([&](auto G) { constexpr int g = G.value, n = g + 4;
      if constexpr (g < 4) qa[g] = MFMA(ag[g], xf[0], qa[g]);
      else if constexpr (g < 16) qa[(g - 4) / 3] = MFMA(ag[g], xf[1 + (g - 4) % 3], qa[(g - 4) / 3]);
      else { qf[g - 16] = __builtin_bit_cast(bf16x8, qw[g - 16]);
        if constexpr (g == 16) { const f32x16 z = {}; S0 = MFMA(f[0], qf[0], z); } else S0 = MFMA(f[g - 16], qf[g - 16], S0); pin16(S0); }
      if constexpr (g == 0) { xf[1] = rd128(xp1); xf[2] = rd128(xp0 + 512); } if constexpr (g == 1) xf[3] = rd128(xp1 + 512);
      if constexpr (n < 16) ag[n] = rdA(4 * (1 + (n - 4) % 3) + (n - 4) / 3); else f[n - 16] = rdK(k0, k1, (n - 16) >> 3, (n - 16) & 7);
      if constexpr (g >= 9 && g < 21) { constexpr int j0 = (g - 9) * 8 / 3, j1 = (g - 8) * 8 / 3; static_for<j1 - j0>([&](auto J) { cvq(j0 + J.value); }); }
      SBAR(); });
    ST(11);
#pragma unroll
    for (int i = 12; i < 16; ++i) f[i] = rdK(k0, k1, 1, i & 7);
    const float m = swap_max(max16(S0));
#pragma unroll
    for (int i = 0; i < 16; ++i) negm[i] = -m;
    SBAR();
    float e[16]; float s0 = 0.f, s1 = 0.f;
    static_for<9>([&](auto G) { constexpr int g = G.value;
      if constexpr (g == 0) { if (wid >= 4u) bar(); }
      if constexpr (g == A0BAR) { if (wid < 4u) bar();
        constexpr unsigned ko = ((SLOTK >> 4) & 15u) * 16384u;
#pragma unroll
        for (int i = 0; i < RA; ++i) pre[i] = rdK(kb0 + ko, kb1 + ko, 0, i);
        SBAR(); }
      if constexpr (g == 0) S1 = MFMA(f[8], qf[0], negm); else if constexpr (g < 8) S1 = MFMA(f[8 + g], qf[g], S1);
      if constexpr (g < 8) pin16(S1);
      if constexpr (g < 8) { e[2 * g] = __builtin_amdgcn_exp2f(S0[2 * g] - m); e[2 * g + 1] = __builtin_amdgcn_exp2f(S0[2 * g + 1] - m); pinf(e[2 * g]); pinf(e[2 * g + 1]); }
      if constexpr (g == 1) { s0 = e[0]; s1 = e[1]; } else if constexpr (g > 1) { s0 += e[2 * g - 2]; pinf(s0); s1 += e[2 * g - 1]; pinf(s1); }
      if constexpr (g >= 1) { unsigned w = cvtpk(e[2 * g - 2], e[2 * g - 1]); pinu(w); P0[(g - 1) >> 2][(g - 1) & 3] = w; }
      SBAR(); });
    l = s0 + s1; }
  ST(12);
  auto unit = [&](auto KB, auto LAST, lds_cptr k0, lds_cptr k1, lds_cptr v, lds_cptr nk0, lds_cptr nk1, f32x16& Sn, f32x16& Sc, const u32x4 (&Pp)[2], u32x4 (&Pc)[2]) {
    constexpr int kb = KB.value; constexpr bool last = LAST.value;
    float e[16]; float s0 = 0.f, s1 = 0.f; bf16x8 f[16 + RA];
#pragma unroll
    for (int i = 0; i < RA; ++i) f[i] = pre[i];
    static_for<16>([&](auto G) { constexpr int g = G.value; constexpr int n = g + RA; constexpr int gb = GB;
      if constexpr (last && GY != GB && g == GY) { bar_if(wid >> 2); SBAR(); }
      if constexpr (last && g == gb) { if constexpr (GY != GB) bar_if((wid >> 2) ^ 1u); else bar(); SBAR(); }
      if constexpr (last && g == 12) wait_lgkm<0>();
      else if constexpr ((g & 3) == 0) wait_lgkm<lds_ops(g + 4, g + RA, last)>();
      if constexpr (g == 0) Sn = MFMA(f[0], qf[0], negm); else if constexpr (g < 8) Sn = MFMA(f[g], qf[g], Sn);
      else o[g & 3] = MFMA(f[g], __builtin_bit_cast(bf16x8, Pp[(g >> 2) & 1]), o[g & 3]);
      if constexpr (n < 8) f[n] = rdK(k0, k1, kb, n); else if constexpr (n < 16) f[n] = rdV(v, kb, n - 8);
      else if constexpr (!last) f[n] = rdK(k0, k1, kb + 1, n - 16);
      else if constexpr (g >= gb) { constexpr int per = (RA + 15 - gb) / (16 - gb); static_for<per>([&](auto J) { constexpr int i = per * (g - gb) + J.value; if constexpr (i < RA) f[16 + i] = rdK(nk0, nk1, 0, i); }); }
      if constexpr (g == 0) { e[0] = __builtin_amdgcn_exp2f(Sc[0]); pinf(e[0]); }
      if constexpr (g < 15) { e[g + 1] = __builtin_amdgcn_exp2f(Sc[g + 1]); pinf(e[g + 1]); }
      if constexpr (g == 1) { s0 = e[0]; s1 = e[1]; } else if constexpr (g > 1) { if constexpr (g & 1) { s1 += e[g]; pinf(s1); } else { s0 += e[g]; pinf(s0); } }
      if constexpr (g & 1) { unsigned w = cvtpk(e[g - 1], e[g]); pinu(w); Pc[g >> 3][(g >> 1) & 3] = w; }
      SBAR(); });
#pragma unroll
    for (int i = 0; i < RA; ++i) pre[i] = f[16 + i];
    sm_fix(s0 + s1, Sc, Sn, Pc); };
  if (YPRIO && wid >= 4u) __builtin_amdgcn_s_setprio(1);
#pragma nounroll
  for (unsigned t = 1; t < 7; ++t) {
    const unsigned ko = ((SLOTK >> (4u * t)) & 15u) * 16384u, vo = ((SLOTV >> (4u * (t - 1u))) & 15u) * 16384u, nko = ((SLOTK >> (4u * ((t + 1u) & 7u))) & 15u) * 16384u;
    SBAR();
    unit(std::integral_constant<int, 0>{}, std::false_type{}, kb0 + ko, kb1 + ko, vb + vo, kb0, kb1, S0, S1, P0, P1);
    unit(std::integral_constant<int, 1>{}, std::true_type{}, kb0 + ko, kb1 + ko, vb + vo, kb0 + nko, kb1 + nko, S1, S0, P1, P0);
    if (t == 1u || t == 6u) ST(12 + t);
  }
  const unsigned me = (b * 7u + wid) * NH + h;
  {
    constexpr unsigned ko = ((SLOTK >> 28) & 15u) * 16384u;
    const lds_cptr k0 = kb0 + ko, k1 = kb1 + ko, v6 = vb + ((SLOTV >> 24) & 15u) * 16384u, v7 = vb + ((SLOTV >> 28) & 15u) * 16384u;
    u32x4 P2[2], P3[2];
    auto scaleP = [&](u32x4 (&P)[2], float f) {
#pragma unroll
      for (int i = 0; i < 8; ++i) { const unsigned w = P[i >> 2][i & 3]; P[i >> 2][i & 3] = cvtpk(__uint_as_float(w << 16) * f, __uint_as_float(w & 0xFFFF0000u) * f); } };
    auto fix = [&](auto NP, float ps, f32x16& Sc, f32x16& Sn, u32x4 (&P)[2]) {
      if (__builtin_expect(__any(!(ps <= SUM_THR)), 0)) {
        const float f = __builtin_amdgcn_exp2f(-fmaxf(swap_max(max16(Sc)), 0.f));
        scaleP(P0, f); if constexpr (NP.value > 1) scaleP(P1, f); if constexpr (NP.value > 2) scaleP(P2, f); }
      sm_fix(ps, Sc, Sn, P); };
    auto rdVt = [&](int i) { const int j = pv_order(i), db = j >> 3, un = (j >> 1) & 3, ks = j & 1; return rdV(un < 2 ? v6 : v7, un & 1, 4 * ks + db); };
    auto pvt = [&](auto I, const bf16x8& vf) { constexpr int j = pv_order(I.value), un = (j >> 1) & 3;
      const u32x4 pb = un == 0 ? P0[j & 1] : un == 1 ? P1[j & 1] : un == 2 ? P2[j & 1] : P3[j & 1];
      o[j >> 3] = MFMA(vf, __builtin_bit_cast(bf16x8, pb), o[j >> 3]); pin16(o[j >> 3]); };
    bf16x8 f[16], w[32];
#pragma unroll
    for (int i = 0; i < RA; ++i) f[i] = pre[i];
    auto qsm = [&](auto H, f32x16& Sn, const f32x16& Sc, u32x4 (&Pc)[2]) { constexpr int hf = H.value;
      float e[16]; float s0 = 0.f, s1 = 0.f;
      static_for<9>([&](auto G) { constexpr int g = G.value, n = 8 * hf + g + RA;
        if constexpr (hf < 2 && g < 8) { if constexpr (g == 0) Sn = MFMA(f[8 * hf], qf[0], negm); else Sn = MFMA(f[8 * hf + g], qf[g], Sn);
          pin16(Sn);
          if constexpr (n < 16) f[n] = rdK(k0, k1, n >> 3, n & 7); }
        if constexpr (hf == 1 && g >= 2 && g < 8) w[g - 2] = rdVt(g - 2);
        if constexpr (hf == 2 && g < 8) { pvt(G, w[g]); w[g + 6] = rdVt(g + 6); }
        if constexpr (g < 8) { e[2 * g] = __builtin_amdgcn_exp2f(Sc[2 * g]); e[2 * g + 1] = __builtin_amdgcn_exp2f(Sc[2 * g + 1]); pinf(e[2 * g]); pinf(e[2 * g + 1]); }
        if constexpr (g == 1) { s0 = e[0]; s1 = e[1]; } else if constexpr (g > 1) { s0 += e[2 * g - 2]; pinf(s0); s1 += e[2 * g - 1]; pinf(s1); }
        if constexpr (g >= 1) { unsigned pw = cvtpk(e[2 * g - 2], e[2 * g - 1]); pinu(pw); Pc[(g - 1) >> 2][(g - 1) & 3] = pw; }
        SBAR(); });
      return s0 + s1; };
    if (TSTAG && wid < 4u) __builtin_amdgcn_s_setprio(1);
    SBAR();
    fix(std::integral_constant<int, 1>{}, qsm(std::integral_constant<int, 0>{}, S0, S1, P1), S1, S0, P1);
    if (TSTAG) bar_if(wid >> 2);
    fix(std::integral_constant<int, 2>{}, qsm(std::integral_constant<int, 1>{}, S1, S0, P2), S0, S1, P2);
    if (TSTAG) bar_if((wid >> 2) ^ 1u); else bar();
    ST(19);
    fix(std::integral_constant<int, 3>{}, qsm(std::integral_constant<int, 2>{}, S0, S1, P3), S1, S0, P3);
    const float inv = __builtin_amdgcn_rcpf(swap_add(l));
    auto put = [&](int k8) { bstore_sc1(pack8(o[k8 >> 1], k8 & 1, inv), lane * 16u, sO, me * 8192u + k8 * 1024u); };
    static_for<24>([&](auto G) { constexpr int g = G.value + 8;
      pvt(std::integral_constant<int, g>{}, w[g]);
      if constexpr (g + 6 < 32) w[g + 6] = rdVt(g + 6);
      if constexpr (g == 14) put(0); if constexpr (g == 16) put(1); if constexpr (g == 19) put(2); if constexpr (g == 22) put(3); if constexpr (g == 26) put(4); if constexpr (g == 29) put(5);
      SBAR(); });
    ST(20);
    put(6); put(7); }
  ST(21);
  tag = mix64(ep);
  if (TSTAG) __builtin_amdgcn_s_setprio(0);
  }
  if (!TSTAG || wid < 7u) { wait_vm<0>();
  ST(22);
  bar(); }
  if (FMODE == 1) { if (tid < 16u) st_agent(ws + FLAG_OFF + (b * NH + h) * FLAG_STRIDE + tid * 8u, tag); }
  else if (FMODE == 2) { if (tid == 0) __hip_atomic_exchange((gu64*)(ws + FLAG_OFF + (b * NH + h) * FLAG_STRIDE), tag, __ATOMIC_RELAXED, __HIP_MEMORY_SCOPE_AGENT); }
  else if (tid == 0) st_agent(ws + FLAG_OFF + (b * NH + h) * FLAG_STRIDE, tag);
  if (RET && wid < 7u) { unsigned dummy = 0; touch1(dummy, lane * 128u + ((b * 7u + wid) * NH + h) * 8192u, sO, 0u); wait_vm<0>(); pinu(dummy); }
  ST(23);
  if (!has_tile) {
    if (bid == 249u && wid == 0u) {
#pragma nounroll
      for (unsigned i = 0; i < 4; ++i) poll_flags(ws + FLAG_OFF + (i * 64u + lane) * FLAG_STRIDE, tag);
      if (lane == 0) st_agent(ws + EPOCH_OFF, tag); }
    ST(24); ST(25); ST(26); ST(27); ST(28); ST(29); ST(30); ST(31); ST_OUT(ws, 0, 32); return; }
  const unsigned jq = (2u * g4 + 2u + wid) & 7u, G = 8u * rt + jq;
  const unsigned voffO = lane * 16u, soO = (G * NH + g4 * 8u) * 8192u;
  const char* fp = ws + FLAG_OFF + (((G * 37u) >> 8) * NH + g4 * 8u + (lane & 7u)) * FLAG_STRIDE;
  unsigned ready = 0;
  auto need = [&](unsigned m) {
    _Pragma("nounroll") for (unsigned spins = 0; (ready & m) != m && spins < SPIN_CAP; ++spins) { ready = (unsigned)__ballot(ld_agent(fp) == tag); if ((ready & m) != m) __builtin_amdgcn_s_sleep(PSLEEP); }
    asm volatile("" ::: "memory"); };
  const unsigned hd0 = HG == 3 ? (ct % 3u) * 3u : (ct * (8u / HG)) & 7u;
  if (ONEP) { if (wid == 0u) poll_flags(ws + FLAG_OFF + ((((8u * rt + (lane & 8u ? 7u : 0u)) * 37u) >> 8) * NH + g4 * 8u + (lane & 7u)) * FLAG_STRIDE, tag); bar(); }
  else { if (PDELAY) __builtin_amdgcn_s_sleep(PDELAY); need(LAZY ? 1u << hd0 : 0xFFu); }
  ST(24);
  const lds_cptr wb = (lds_cptr)lds + 1024u * hh + 64u * q4 + trcol;
  if (P2PRIO) { if ((wid & 2u) == 0u) __builtin_amdgcn_s_setprio(2); else if (wid < 4u) __builtin_amdgcn_s_setprio(1); else __builtin_amdgcn_s_setprio(0); } else if (YPRIO) __builtin_amdgcn_s_setprio(0);
  f32x16 acc[2] = {};
  { unsigned hd = hd0, so_c = soO + hd * 8192u; lds_cptr wp = wb + ((SLOTW >> (4u * hd)) & 15u) * 16384u;
    bf16x8 of[8], lf[4][2];
    auto rdW = [&](int s2) { lf[s2 & 3][0] = trd2(wp, (s2 & 7) * 2048, (s2 & 7) * 2048 + 256); lf[s2 & 3][1] = trd2(wp, (s2 & 7) * 2048 + 512, (s2 & 7) * 2048 + 768); };
#pragma unroll
    for (int i = 0; i < 8; ++i) { if (i < WO) bload_sc1(of[i], voffO, sO, so_c + i * 1024u); else of[i] = bf16x8{}; }
    rdW(0); rdW(1);
#pragma nounroll
    for (unsigned p = 0; p < 8; ++p) {
      hd = (hd + 1u) & 7u;
      if (TSTAG && p == 5u) __builtin_amdgcn_s_barrier();
      const unsigned so_n = p < 7u ? soO + hd * 8192u : O_BYTES;
      static_for<8>([&](auto S) { constexpr int s2 = S.value;
        if constexpr (LAZY && s2 == 8 - WO) need(1u << hd);
        if constexpr (s2 == 6) wp = wb + ((SLOTW >> (4u * hd)) & 15u) * 16384u;
        wait_vm1<WO - 1>(of[s2]);
        wait_lgkm<4>();
        acc[0] = MFMA(lf[s2 & 3][0], of[s2], acc[0]);
        rdW(s2 + 2);
        SBAR();
        acc[1] = MFMA(lf[s2 & 3][1], of[s2], acc[1]);
        SBAR();
        breload_sc1(of[(s2 + WO) & 7], voffO, sO, (s2 + WO < 8 ? so_c : so_n) + ((s2 + WO) & 7) * 1024u);
        SBAR(); });
      so_c = so_n;
    }
    wait_vm<0>();
#pragma unroll
    for (int i = 0; i < 8; ++i) pin(of[i]); }
  ST(28);
  const srd_t sX2 = mk_srd(ws + X_OFF, X_BYTES);
  const unsigned xb = (u * 8u + jq) * 4u;
  if (wid < 6u) {
#pragma unroll
    for (int i = 0; i < 4; ++i) bstore_sc1(pack8(acc[i >> 1], i & 1), voffO, sX2, (xb + g4) * 4096u + i * 1024u);
    wait_vm<0>();
    if (FMODE == 1) { if (lane < 16u) st_agent(ws + FLAG2_OFF + (xb + g4) * FLAG_STRIDE + lane * 8u, tag); }
    else if (FMODE == 2) { if (lane == 0) __hip_atomic_exchange((gu64*)(ws + FLAG2_OFF + (xb + g4) * FLAG_STRIDE), tag, __ATOMIC_RELAXED, __HIP_MEMORY_SCOPE_AGENT); }
    else if (lane == 0) st_agent(ws + FLAG2_OFF + (xb + g4) * FLAG_STRIDE, tag);
    ST(29); ST(30);
  } else {
    if (PDELAY2) __builtin_amdgcn_s_sleep(PDELAY2);
    poll_flags(ws + FLAG2_OFF + (xb + ((g4 + 1u + lane % 3u) & 3u)) * FLAG_STRIDE, tag);
    ST(29);
    bf16x8 xp[12];
#pragma unroll
    for (int i = 0; i < 12; ++i) bload_sc1(xp[i], voffO, sX2, (xb + ((g4 + 1u + (i >> 2)) & 3u)) * 4096u + (i & 3) * 1024u);
    wait_vm<0>();
#pragma unroll
    for (int j = 0; j < 12; ++j) { const int i = j & 3; pin(xp[j]); const u32x4 v = __builtin_bit_cast(u32x4, xp[j]);
#pragma unroll
      for (int e = 0; e < 4; ++e) { acc[i >> 1][8 * (i & 1) + 2 * e] += __uint_as_float(v[e] << 16); acc[i >> 1][8 * (i & 1) + 2 * e + 1] += __uint_as_float(v[e] & 0xFFFF0000u); } }
    ST(30);
    char* op = (char*)out + (size_t)(G * 32u + r) * (DM * 2u) + ct * 128u + hh * 16u;
#pragma unroll
    for (int i = 0; i < 4; ++i) *(u32x4*)(op + i * 32) = pack8(acc[i >> 1], i & 1);
  }
  ST(31); ST_OUT(ws, 0, 32);
}

extern "C" void kernel_launch(void* const* d_in, const int* in_sizes, int n_in, void* d_out, int out_size, void* d_ws, size_t ws_size, hipStream_t stream) {
  (void)in_sizes; (void)n_in; (void)out_size; (void)ws_size;
  (void)hipFuncSetAttribute((const void*)mha_kernel, hipFuncAttributeMaxDynamicSharedMemorySize, LDS_BYTES);
  const unsigned short* x = (const unsigned short*)d_in[0]; const unsigned short* kc = (const unsigned short*)d_in[1]; const unsigned short* vc = (const unsigned short*)d_in[2];
  const unsigned short* wq = (const unsigned short*)d_in[3]; const unsigned short* wo = (const unsigned short*)d_in[4];
  mha_kernel<<<256, 512, LDS_BYTES, stream>>>(x, kc, vc, wq, wo, (char*)d_ws, (unsigned short*)d_out);
}
```

```cpp
#define NOWUP 0
#include <hip/hip_runtime.h>
#include <stdint.h>
#include <utility>

typedef short    bf16x8 __attribute__((ext_vector_type(8)));
typedef short    s16x4  __attribute__((ext_vector_type(4)));
typedef float    f32x16 __attribute__((ext_vector_type(16)));
typedef float    f32x2  __attribute__((ext_vector_type(2)));
typedef __bf16   bf16x2 __attribute__((ext_vector_type(2)));
typedef unsigned u32x4  __attribute__((ext_vector_type(4)));
typedef __attribute__((address_space(3))) const char* lds_cptr;
typedef __attribute__((address_space(1))) unsigned long long gu64;
typedef __amdgpu_buffer_rsrc_t srd_t;

#define KVP "nt "
#define DI __device__ __forceinline__
#define MFMA(a, b, c) __builtin_amdgcn_mfma_f32_32x32x16_bf16((a), (b), (c), 0, 0, 0)
#define SBAR() __builtin_amdgcn_sched_barrier(0)

constexpr int NB = 8, SQ = 224, DM = 576, NH = 32, SK = 512, DH = 128;
constexpr int LDS_BYTES = 163840;
constexpr float QSCALE = 0.08838834764831845f * 1.4426950408889634f;
constexpr float SUM_THR = 4096.f;

template <int... Is, class F> DI void static_for_impl(std::integer_sequence<int, Is...>, F&& f) { (f(std::integral_constant<int, Is>{}), ...); }
template <int N, class F> DI void static_for(F&& f) { static_for_impl(std::make_integer_sequence<int, N>{}, f); }

DI unsigned rfl(unsigned x) { return __builtin_amdgcn_readfirstlane(x); }
DI unsigned cvtpk(float lo, float hi) { f32x2 v = {lo, hi}; bf16x2 b = __builtin_convertvector(v, bf16x2); return __builtin_bit_cast(unsigned, b); }
DI srd_t mk_srd(const void* p, unsigned bytes) { return __builtin_amdgcn_make_buffer_rsrc((void*)p, (short)0, (int)bytes, 0x00020000); }
DI void dma4(unsigned lds, unsigned v0, unsigned v1, unsigned v2, unsigned v3, srd_t srd, unsigned soff) {
  asm volatile("s_mov_b32 m0, %0\n\ts_nop 0\n\tbuffer_load_dwordx4 %1, %5, %6 offen lds\n\tbuffer_load_dwordx4 %2, %5, %6 offen offset:1024 lds\n\t"
               "buffer_load_dwordx4 %3, %5, %6 offen offset:2048 lds\n\tbuffer_load_dwordx4 %4, %5, %6 offen offset:3072 lds"
               :: "s"(lds), "v"(v0), "v"(v1), "v"(v2), "v"(v3), "s"(srd), "s"(soff) : "memory");
}
DI void dma4_nt(unsigned lds, unsigned v0, unsigned v1, unsigned v2, unsigned v3, srd_t srd, unsigned soff) {
  asm volatile("s_mov_b32 m0, %0\n\ts_nop 0\n\tbuffer_load_dwordx4 %1, %5, %6 offen " KVP "lds\n\tbuffer_load_dwordx4 %2, %5, %6 offen offset:1024 " KVP "lds\n\t"
               "buffer_load_dwordx4 %3, %5, %6 offen offset:2048 " KVP "lds\n\tbuffer_load_dwordx4 %4, %5, %6 offen offset:3072 " KVP "lds"
               :: "s"(lds), "v"(v0), "v"(v1), "v"(v2), "v"(v3), "s"(srd), "s"(soff) : "memory");
}
DI void bstore_sc1(const u32x4& d, unsigned voff, srd_t srd, unsigned soff) { asm volatile("buffer_store_dwordx4 %0, %1, %2, %3 offen sc1\n\ts_nop 1" :: "v"(d), "v"(voff), "s"(srd), "s"(soff) : "memory"); }
DI void bload_sc1(bf16x8& d, unsigned voff, srd_t srd, unsigned soff) { asm volatile("buffer_load_dwordx4 %0, %1, %2, %3 offen sc1" : "=v"(d) : "v"(voff), "s"(srd), "s"(soff) : "memory"); }
DI void breload_sc1(bf16x8& d, unsigned voff, srd_t srd, unsigned soff) { asm volatile("buffer_load_dwordx4 %0, %1, %2, %3 offen sc1" : "+v"(d) : "v"(voff), "s"(srd), "s"(soff) : "memory"); }
DI void touch(unsigned& d, unsigned voff, srd_t srd, unsigned soff) { asm volatile("buffer_load_dword %0, %1, %2, %3 offen" : "+v"(d) : "v"(voff), "s"(srd), "s"(soff) : "memory"); }
DI void touch2(unsigned lds, unsigned voff, srd_t srd, unsigned s0, unsigned s1) { asm volatile("s_mov_b32 m0, %0\n\ts_nop 0\n\tbuffer_load_dword %1, %2, %3 offen " KVP "lds\n\tbuffer_load_dword %1, %2, %4 offen " KVP "lds" :: "s"(lds), "v"(voff), "s"(srd), "s"(s0), "s"(s1) : "memory"); }
DI void touch1(unsigned& d, unsigned voff, srd_t srd, unsigned soff) { asm volatile("buffer_load_dword %0, %1, %2, %3 offen sc1" : "+v"(d) : "v"(voff), "s"(srd), "s"(soff) : "memory"); }
DI void pin(bf16x8& a) { asm volatile("" : "+v"(a)); }
DI void pin16(f32x16& a) { asm volatile("" : "+v"(a)); }
DI void pinf(float& a) { asm volatile("" : "+v"(a)); }
DI void pinu(unsigned& a) { asm volatile("" : "+v"(a)); }
template <int N> DI void wait_vm1(bf16x8& a) { asm volatile("s_waitcnt vmcnt(%1)" : "+v"(a) : "n"(N) : "memory"); }
template <int N> DI void wait_vm() { asm volatile("s_waitcnt vmcnt(%0)" :: "n"(N) : "memory"); }
template <int N> DI void wait_lgkm() { __builtin_amdgcn_s_waitcnt(0xC07F | (N << 8)); SBAR(); }
DI void bar_if(unsigned c) { asm volatile("s_cmp_eq_u32 %0, 0\n\ts_cbranch_scc1 1f\n\ts_waitcnt lgkmcnt(0)\n\ts_barrier\n1:" :: "s"(c) : "memory", "scc"); }
DI void bar() { asm volatile("s_waitcnt lgkmcnt(0)\n\ts_barrier" ::: "memory"); }
typedef short v4i16_t __attribute__((ext_vector_type(4)));
DI s16x4 trd(lds_cptr p) { return __builtin_amdgcn_ds_read_tr16_b64_v4i16((__attribute__((address_space(3))) v4i16_t*)p); }
DI bf16x8 trd2(lds_cptr p, int o0, int o1) { s16x4 a = trd(p + o0), b = trd(p + o1); return __builtin_shufflevector(a, b, 0, 1, 2, 3, 4, 5, 6, 7); }
DI bf16x8 rd128(lds_cptr p) { return *(const __attribute__((address_space(3))) bf16x8*)p; }
DI float swap_max(float m) { auto rr = __builtin_amdgcn_permlane32_swap(__float_as_uint(m), __float_as_uint(m), false, false); return fmaxf(__uint_as_float(rr[0]), __uint_as_float(rr[1])); }
DI float swap_add(float m) { auto rr = __builtin_amdgcn_permlane32_swap(__float_as_uint(m), __float_as_uint(m), false, false); return __uint_as_float(rr[0]) + __uint_as_float(rr[1]); }
DI float max16(const f32x16& s) { float a = fmaxf(fmaxf(s[0], s[1]), s[2]), b = fmaxf(fmaxf(s[3], s[4]), s[5]);
  a = fmaxf(fmaxf(a, s[6]), s[7]); b = fmaxf(fmaxf(b, s[8]), s[9]); a = fmaxf(fmaxf(a, s[10]), s[11]); b = fmaxf(fmaxf(b, s[12]), s[13]); a = fmaxf(fmaxf(a, s[14]), s[15]); return fmaxf(a, b); }
DI float fadd(float a, float b) { float r; asm("v_add_f32_e32 %0, %1, %2" : "=v"(r) : "v"(a), "v"(b)); return r; }
DI u32x4 pack8(const f32x16& a, int s, float f) { return u32x4{cvtpk(a[8 * s] * f, a[8 * s + 1] * f), cvtpk(a[8 * s + 2] * f, a[8 * s + 3] * f), cvtpk(a[8 * s + 4] * f, a[8 * s + 5] * f), cvtpk(a[8 * s + 6] * f, a[8 * s + 7] * f)}; }
DI u32x4 pack8(const f32x16& a, int s) { return u32x4{cvtpk(a[8 * s], a[8 * s + 1]), cvtpk(a[8 * s + 2], a[8 * s + 3]), cvtpk(a[8 * s + 4], a[8 * s + 5]), cvtpk(a[8 * s + 6], a[8 * s + 7])}; }

DI float expo(const f32x16& s, u32x4 (&p)[2]) {
  float e[16]; float s0 = 0.f, s1 = 0.f;
#pragma unroll
  for (int i = 0; i < 16; ++i) e[i] = __builtin_amdgcn_exp2f(s[i]);
#pragma unroll
  for (int i = 0; i < 16; i += 2) { s0 += e[i]; s1 += e[i + 1]; }
#pragma unroll
  for (int i = 0; i < 8; ++i) p[i >> 2][i & 3] = cvtpk(e[2 * i], e[2 * i + 1]);
  return s0 + s1;
}

#define ST_DECL
#define ST(k)
#define ST_OUT(ws, lo, hi)

constexpr unsigned SLOTK = 0x04204209u;
constexpr unsigned SLOTV = 0x31531531u;
constexpr unsigned SLOTW = 0x05429876u;
constexpr unsigned STG = 45056u, STG_W = 28672u;
constexpr unsigned O_BYTES = NB * SQ * NH * DH * 2;
constexpr unsigned X_OFF = O_BYTES, X_BYTES = 63u * 8u * 4u * 4096u;
constexpr unsigned FLAG_OFF = X_OFF + X_BYTES, FLAG_STRIDE = 128, FLAG2_OFF = FLAG_OFF + 256 * FLAG_STRIDE, EPOCH_OFF = FLAG2_OFF + 63 * 32 * FLAG_STRIDE;
constexpr unsigned SPIN_CAP = 1u << 20;
#define RA_ 7
constexpr int RA = RA_;
#define WO 6
#define HG 1
#define RET 0
#define YPRIO 0
#define P2PRIO 0
#define GB 12
#define GY GB
#define PF 0
#define PF0 4
#define KVT 0xFFFECA8642ull
#define WOT 0x8754432100ull
#define WLATE 1
#define LAZY 0
#define TSTAG 1
#define FMODE 0
#define ONEP 0
#define PDELAY 20
#define PDELAY2 25
#define K0C 3
#define S1MODE 2
#define A0BAR 6
#define PSLEEP 4
#define CODEPF 18944
constexpr int lds_ops(int a, int b, bool last) { int n = 0; for (int i = a; i < b; ++i) n += i >= 16 ? (last ? 0 : 1) : (i >= 8 ? 2 : 1); return n; }
constexpr int pv_order(int i) { return i < 4 ? i : i < 8 ? i + 4 : i < 12 ? i - 4 : i; }
DI unsigned long long mix64(unsigned long long z) { z += 0x9E3779B97F4A7C15ull; z = (z ^ (z >> 30)) * 0xBF58476D1CE4E5B9ull; z = (z ^ (z >> 27)) * 0x94D049BB133111EBull; return z ^ (z >> 31); }
DI unsigned long long rfl64(unsigned long long v) { return ((unsigned long long)rfl((unsigned)(v >> 32)) << 32) | rfl((unsigned)v); }
#define PSYS 0
DI unsigned long long ld_agent(const char* p) {
  if (PSYS == 1) return __hip_atomic_load((gu64*)p, __ATOMIC_RELAXED, __HIP_MEMORY_SCOPE_SYSTEM);
  if (PSYS == 2) return __hip_atomic_fetch_add((gu64*)p, 0ull, __ATOMIC_RELAXED, __HIP_MEMORY_SCOPE_AGENT);
  return __hip_atomic_load((gu64*)p, __ATOMIC_RELAXED, __HIP_MEMORY_SCOPE_AGENT); }
DI void st_agent(char* p, unsigned long long v) { __hip_atomic_store((gu64*)p, v, __ATOMIC_RELAXED, __HIP_MEMORY_SCOPE_AGENT); }
DI void poll_flags(const char* p, unsigned long long tag) {
  _Pragma("nounroll") for (unsigned spins = 0; spins < SPIN_CAP; ++spins) { if (__all(ld_agent(p) == tag)) break; __builtin_amdgcn_s_sleep(PSLEEP); }
  asm volatile("" ::: "memory");
}

__global__ __launch_bounds__(512, 1) void mha_kernel(const unsigned short* __restrict__ x, const unsigned short* __restrict__ kc, const unsigned short* __restrict__ vc,
                                                     const unsigned short* __restrict__ wq, const unsigned short* __restrict__ wo, char* ws, unsigned short* __restrict__ out) {
  extern __shared__ __attribute__((aligned(16))) char lds[];
  unsigned long long pc0; asm volatile("s_getpc_b64 %0" : "=s"(pc0));
  const unsigned tid = threadIdx.x, lane = tid & 63u, r = lane & 31u, hh = lane >> 5;
  const unsigned wid = rfl(tid >> 6);
  ST_DECL ST(0);
  const unsigned bid = blockIdx.x, xcd = bid & 7u, ix = bid >> 3;
  const unsigned b = (xcd & 1u) * 4u + (ix & 3u), h = (xcd >> 1) * 8u + (ix >> 2);
  const unsigned lds0 = (unsigned)(uintptr_t)lds;
  const srd_t sW = mk_srd((const char*)wq - 1024, DM * NH * DH * 2 + 1024);
  const srd_t sX = mk_srd(x + (size_t)b * SQ * DM, SQ * DM * 2);
  const unsigned prow = (lane >> 2) & 7u, pch = 4u * hh + (lane & 3u);
  const unsigned pchx0 = 4u * hh + ((lane & 3u) ^ ((lane >> 4) & 1u)), pchx1 = pchx0 ^ 2u;
  const unsigned vW = prow * 8192u + pch * 16u, vX0 = prow * (DM * 2u) + pchx0 * 16u, vX1 = prow * (DM * 2u) + pchx1 * 16u;
  const unsigned ck0 = 3u * (((ix & 3u) + (ix >> 2)) % 3u);
  unsigned long long ep; asm volatile("global_load_dwordx2 %0, %1, %2 sc1" : "=v"(ep) : "v"(0u), "s"(ws + EPOCH_OFF) : "memory");
  if (wid >= 4u) dma4(lds0 + STG_W + (wid - 4u) * 4096u, vW + 1024u, vW + 128u, vW + 65536u - 1024u, vW + 65664u - 2048u, sW, ck0 * 64u * 8192u + h * 256u + (wid - 4u) * 131072u);
  if (wid < 7u) dma4(lds0 + wid * 4096u, vX0, vX1 + 8192u, vX0 + 16384u, vX1 + 24576u, sX, ck0 * 128u + wid * 32u * DM * 2u);
  else {
#pragma unroll
    for (int i = 0; i < 4; ++i) dma4(lds0 + STG + STG_W + i * 4096u, vW + 1024u, vW + 128u, vW + 65536u - 1024u, vW + 65664u - 2048u, sW, (ck0 + 1u) * 64u * 8192u + h * 256u + i * 131072u); }
  const srd_t sK = mk_srd((const char*)(kc + (size_t)(b * NH + h) * SK * DH) - 1024, SK * DH * 2 + 1024);
  const srd_t sV = mk_srd((const char*)(vc + (size_t)(b * NH + h) * SK * DH) - 1024, SK * DH * 2 + 1024);
  const unsigned vK0 = prow * 256u + pchx0 * 16u, vK1 = prow * 256u + pchx1 * 16u;
  const unsigned q4 = (lane & 15u) >> 2, p4 = lane & 3u, blk = (lane >> 4) & 1u;
  const unsigned trcol = 32u * blk + 16u * (p4 & 1u) + 8u * (p4 >> 1);
  const srd_t sO = mk_srd(ws, O_BYTES);
  unsigned long long tag;
  const unsigned g4 = xcd >> 1, u0 = (xcd & 1u) * 32u + ix;
  const bool has_tile = u0 < 63u;
  const unsigned u = has_tile ? u0 : 62u, rt = u / 9u, ct = u - rt * 9u;

  if (wid == 7u) {
    auto fillQ = [&](unsigned c, unsigned stage) {
      const unsigned lw = lds0 + stage + STG_W, sw = c * 64u * 8192u + h * 256u, lx = lds0 + stage, sx = c * 128u;
#pragma unroll
      for (int i = 0; i < 4; ++i) dma4(lw + i * 4096u, vW + 1024u, vW + 128u, vW + 65536u - 1024u, vW + 65664u - 2048u, sW, sw + i * 131072u);
#pragma unroll
      for (int i = 0; i < 7; ++i) dma4(lx + i * 4096u, vX0, vX1 + 8192u, vX0 + 16384u, vX1 + 24576u, sX, sx + i * 32u * DM * 2u); };
    if (S1MODE) wait_vm<16>(); else wait_vm<4>();
    bar(); ST(2);
    { unsigned stage = 2 * STG, ck = ck0 + 2u;
#pragma nounroll
      for (unsigned c = 2; c < 9; ++c) { fillQ(ck, stage); ck = ck == 8u ? 0u : ck + 1u; stage = stage == 2 * STG ? 0u : stage + STG;
        wait_vm<44>(); bar(); ST(1 + c); } }
    const unsigned vV = prow * 256u + pch * 16u;
    const srd_t sWo = mk_srd(wo, NH * DH * DM * 2);
    const unsigned vWo = prow * (DM * 2u) + pch * 16u;
    constexpr unsigned last_n = 9u;
    { unsigned j = 1, jw = 0;
#pragma nounroll
      for (unsigned n = 0; n < 10; ++n) {
        if (TSTAG && n == 9u) { wait_vm<0>(); bar(); }
        const unsigned je = ((unsigned)(KVT >> (4u * n)) & 15u) + 1u;
#pragma nounroll
        for (; j < je; ++j) {
          if ((j & 1u) && j != 15u) { const unsigned t = (j + 1u) >> 1, ld = lds0 + ((SLOTK >> (4u * t)) & 15u) * 16384u;
#pragma unroll
            for (int i = 0; i < 4; ++i) dma4_nt(ld + i * 4096u, vK0 + 1024u, vK0 + 128u, vK1 + 1024u, vK1 + 128u, sK, t * 16384u + i * 4096u);
          } else { const unsigned t = (j - 1u) >> 1, ld = lds0 + ((SLOTV >> (4u * t)) & 15u) * 16384u;
#pragma unroll
            for (int i = 0; i < 4; ++i) dma4_nt(ld + i * 4096u, vV + 1024u, vV + 128u, vV + 1024u, vV + 128u, sV, t * 16384u + i * 4096u); } }
        const unsigned we = (unsigned)(0x8741000000ull >> (4u * n)) & 15u;
#pragma nounroll
        for (; jw < we; ++jw) { const unsigned ld = lds0 + ((SLOTW >> (4u * jw)) & 15u) * 16384u, so = (g4 * 8u + jw) * 128u * (DM * 2u) + ct * 128u;
#pragma unroll
          for (int k = 0; k < 4; ++k) dma4(ld + k * 4096u, vWo, vWo + 8192u, vWo + 16384u, vWo + 24576u, sWo, so + k * 32u * (DM * 2u)); }
        if (n == last_n) break;
        if (n < 6u) wait_vm<32>(); else if (n == 6u) wait_vm<48>(); else wait_vm<62>();
        bar(); ST(n == 0u ? 10u : 11u + n); } }
    asm volatile("" : "+v"(ep)); tag = mix64(rfl64(ep));
    ST(20); ST(21);
  } else {
  const unsigned rsw0 = 64u * (r & 7u) + 16u * (hh ^ ((r >> 2) & 3u)), rsw1 = rsw0 ^ 32u;
  f32x16 qa[4] = {}, o[4] = {};
  asm volatile("" : "+v"(qa[0]), "+v"(qa[1]), "+v"(qa[2]), "+v"(qa[3]), "+v"(o[0]), "+v"(o[1]), "+v"(o[2]), "+v"(o[3]));
  const char* wup;
  { const unsigned k = lane & 7u, sel = lane >> 3;
    wup = sel == 0u ? (const char*)(k & 1u ? vc : kc) + (size_t)(b * NH + h) * (SK * DH * 2) + (k >> 1) * 32768u
        : sel == 1u ? (const char*)wo + (size_t)(g4 * 1024u + k * 128u) * (DM * 2u) + ct * 128u
        : sel == 2u ? ws + ((b * 7u + k % 7u) * NH + h) * 8192u
        : sel == 3u ? ws + ((8u * rt + k) * NH + g4 * 8u) * 8192u
        : sel == 4u ? ws + X_OFF + u * 131072u + (k & 1u) * 65536u
        : sel == 5u ? (const char*)out + (size_t)((8u * rt + k) * 32u) * (DM * 2u)
        : sel == 6u ? ws + FLAG_OFF + k * 32768u + 64u
        : (const char*)(pc0 & ~255ull) + min((ix * 8u + k) * 128u, (unsigned)CODEPF); }
  wait_lgkm<0>();
  if (S1MODE) wait_vm<0>(); else if (wid >= 4u) wait_vm<8>(); else wait_vm<4>();
  ST(1);
  if (S1MODE == 2) dma4(lds0 + STG + wid * 4096u, vX0, vX1 + 8192u, vX0 + 16384u, vX1 + 24576u, sX, (ck0 + 1u) * 128u + wid * 32u * DM * 2u);
  bar();
  if (S1MODE == 1) dma4(lds0 + STG + wid * 4096u, vX0, vX1 + 8192u, vX0 + 16384u, vX1 + 24576u, sX, (ck0 + 1u) * 128u + wid * 32u * DM * 2u);
  asm volatile("" : "+v"(ep)); ep = rfl64(ep);
  ST(2);
  const lds_cptr kb0 = (lds_cptr)lds + 2048u * (r >> 3) + rsw0, kb1 = (lds_cptr)lds + 2048u * (r >> 3) + rsw1;
  const lds_cptr vb = (lds_cptr)lds + 64u * (4u * hh + q4) + trcol;
  bf16x8 qf[8], pre[RA];
  f32x16 negm, S0, S1; u32x4 P0[2], P1[2]; float l = 0.f;
  auto rdK = [&](lds_cptr k0, lds_cptr k1, int kb, int i) { return rd128(((i & 1) ? k1 : k0) + 8192 * kb + 512 * (i >> 1)); };
  auto rdV = [&](lds_cptr v, int kb, int j) { return trd2(v, 8192 * kb + 4096 * (j >> 2) + 512 * (j & 3), 8192 * kb + 4096 * (j >> 2) + 2048 + 512 * (j & 3)); };
  auto sm_fix = [&](float ps, f32x16& Sc, f32x16& Sn, u32x4 (&P)[2]) {
    if (__builtin_expect(__any(!(ps <= SUM_THR)), 0)) {
      const float dl = fmaxf(swap_max(max16(Sc)), 0.f), f = __builtin_amdgcn_exp2f(-dl);
#pragma unroll
      for (int i = 0; i < 16; ++i) { Sc[i] -= dl; Sn[i] -= dl; negm[i] -= dl; }
#pragma unroll
      for (int db = 0; db < 4; ++db)
#pragma unroll
        for (int i = 0; i < 16; ++i) o[db][i] *= f;
      l *= f; ps = expo(Sc, P); }
    l += ps; };
  { lds_cptr wp = (lds_cptr)lds + STG_W + 2048u * hh + 64u * q4 + trcol;
    lds_cptr xp0 = (lds_cptr)lds + wid * 4096u + 1024u * (r >> 3) + rsw0, xp1 = (lds_cptr)lds + wid * 4096u + 1024u * (r >> 3) + rsw1;
    bf16x8 xf[4], af[5];
    auto rdA = [&](int i) { return trd2(wp, 4096 * (i >> 2) + 512 * (i & 3), 4096 * (i >> 2) + 512 * (i & 3) + 256); };
    xf[0] = rd128(xp0); af[0] = rdA(0); af[1] = rdA(1); af[2] = rdA(2); af[3] = rdA(3);
    unsigned stage = 0;
#pragma nounroll
    for (unsigned c = 0; c < 8; ++c) {
      if (__builtin_expect(c == K0C && wid == 3u, 0)) {
        { if (!NOWUP) asm volatile("s_mov_b32 m0, %0\n\ts_nop 0\n\tglobal_load_lds_dword %1, off" :: "s"(lds0 + 3u * STG), "v"(wup) : "memory");
#pragma unroll
          for (int i = 0; i < 4; ++i) dma4_nt(lds0 + 9u * 16384u + i * 4096u, vK0 + 1024u, vK0 + 128u, vK1 + 1024u, vK1 + 128u, sK, i * 4096u); } }
      if (PF && __builtin_expect(wid == 3u && c >= PF0 && c < PF0 + PF, 0)) {
        const unsigned i = c - PF0, so = ((i + 2u) >> 1) * 16384u + 1024u;
        if (i & 1u) touch2(lds0 + 3u * STG, lane * 128u, sV, so - 16384u, so - 8192u); else touch2(lds0 + 3u * STG, lane * 128u, sK, so, so + 8192u); }
      const unsigned nstage = stage == 2 * STG ? 0u : stage + STG; const int dlt = (int)nstage - (int)stage; stage = nstage;
      SBAR();
      static_for<12>([&](auto I) { constexpr int i = I.value;
        qa[i & 3] = MFMA(af[i % 5], xf[i >> 2], qa[i & 3]);
        af[(i + 4) % 5] = rdA(i + 4);
        if constexpr (i == 0) xf[1] = rd128(xp1); if constexpr (i == 3) xf[2] = rd128(xp0 + 512); if constexpr (i == 7) xf[3] = rd128(xp1 + 512);
        SBAR(); });
      qa[0] = MFMA(af[12 % 5], xf[3], qa[0]);
      wp += dlt; xp0 += dlt; xp1 += dlt; asm volatile("" : "+v"(wp), "+v"(xp0), "+v"(xp1));
      SBAR();
      asm volatile("s_bitcmp1_b32 %0, %1\n\ts_cbranch_scc1 1f\n\ts_waitcnt vmcnt(0)\n1:\n\ts_waitcnt lgkmcnt(0)\n\ts_barrier" :: "s"(0x78u), "s"(c) : "memory", "scc");
      const bf16x8 xl = xf[3], a13 = af[13 % 5], a14 = af[14 % 5], a15 = af[15 % 5];
      xf[0] = rd128(xp0); af[0] = rdA(0); af[1] = rdA(1);
      SBAR();
      qa[1] = MFMA(a13, xl, qa[1]); SBAR();
      af[2] = rdA(2);
      qa[2] = MFMA(a14, xl, qa[2]); SBAR();
      af[3] = rdA(3);
      qa[3] = MFMA(a15, xl, qa[3]); SBAR();
      ST(3 + c);
    }
    lds_cptr k0 = kb0 + 9 * 16384, k1 = kb1 + 9 * 16384; asm volatile("" : "+v"(k0), "+v"(k1));
    bf16x8 ag[16], f[16]; u32x4 qw[8]; float qs = QSCALE; asm volatile("" : "+s"(qs));
#pragma unroll
    for (int i = 0; i < 4; ++i) ag[i] = af[i];
    auto cvq = [&](int j) { float lo = qa[j >> 3][2 * (j & 7)] * qs; pinf(lo); float hi = qa[j >> 3][2 * (j & 7) + 1] * qs; unsigned w = cvtpk(lo, hi); pinu(w); qw[j >> 2][j & 3] = w; };
    SBAR();
    static_for<24>([&](auto G) { constexpr int g = G.value, n = g + 4;
      if constexpr (g < 4) qa[g] = MFMA(ag[g], xf[0], qa[g]);
      else if constexpr (g < 16) qa[(g - 4) / 3] = MFMA(ag[g], xf[1 + (g - 4) % 3], qa[(g - 4) / 3]);
      else { qf[g - 16] = __builtin_bit_cast(bf16x8, qw[g - 16]);
        if constexpr (g == 16) { const f32x16 z = {}; S0 = MFMA(f[0], qf[0], z); } else S0 = MFMA(f[g - 16], qf[g - 16], S0); pin16(S0); }
      if constexpr (g == 0) { xf[1] = rd128(xp1); xf[2] = rd128(xp0 + 512); } if constexpr (g == 1) xf[3] = rd128(xp1 + 512);
      if constexpr (n < 16) ag[n] = rdA(4 * (1 + (n - 4) % 3) + (n - 4) / 3); else f[n - 16] = rdK(k0, k1, (n - 16) >> 3, (n - 16) & 7);
      if constexpr (g >= 9 && g < 21) { constexpr int j0 = (g - 9) * 8 / 3, j1 = (g - 8) * 8 / 3; static_for<j1 - j0>([&](auto J) { cvq(j0 + J.value); }); }
      SBAR(); });
    ST(11);
#pragma unroll
    for (int i = 12; i < 16; ++i) f[i] = rdK(k0, k1, 1, i & 7);
    const float m = swap_max(max16(S0));
#pragma unroll
    for (int i = 0; i < 16; ++i) negm[i] = -m;
    SBAR();
    float e[16]; float s0 = 0.f, s1 = 0.f;
    static_for<9>([&](auto G) { constexpr int g = G.value;
      if constexpr (g == 0) { if (wid >= 4u) bar(); }
      if constexpr (g == A0BAR) { if (wid < 4u) bar();
        constexpr unsigned ko = ((SLOTK >> 4) & 15u) * 16384u;
#pragma unroll
        for (int i = 0; i < RA; ++i) pre[i] = rdK(kb0 + ko, kb1 + ko, 0, i);
        SBAR(); }
      if constexpr (g == 0) S1 = MFMA(f[8], qf[0], negm); else if constexpr (g < 8) S1 = MFMA(f[8 + g], qf[g], S1);
      if constexpr (g < 8) pin16(S1);
      if constexpr (g < 8) { e[2 * g] = __builtin_amdgcn_exp2f(S0[2 * g] - m); e[2 * g + 1] = __builtin_amdgcn_exp2f(S0[2 * g + 1] - m); pinf(e[2 * g]); pinf(e[2 * g + 1]); }
      if constexpr (g == 1) { s0 = e[0]; s1 = e[1]; } else if constexpr (g > 1) { s0 += e[2 * g - 2]; pinf(s0); s1 += e[2 * g - 1]; pinf(s1); }
      if constexpr (g >= 1) { unsigned w = cvtpk(e[2 * g - 2], e[2 * g - 1]); pinu(w); P0[(g - 1) >> 2][(g - 1) & 3] = w; }
      SBAR(); });
    l = s0 + s1; }
  ST(12);
  auto unit = [&](auto KB, auto LAST, lds_cptr k0, lds_cptr k1, lds_cptr v, lds_cptr nk0, lds_cptr nk1, f32x16& Sn, f32x16& Sc, const u32x4 (&Pp)[2], u32x4 (&Pc)[2]) {
    constexpr int kb = KB.value; constexpr bool last = LAST.value;
    float e[16]; float s0 = 0.f, s1 = 0.f; bf16x8 f[16 + RA];
#pragma unroll
    for (int i = 0; i < RA; ++i) f[i] = pre[i];
    static_for<16>([&](auto G) { constexpr int g = G.value; constexpr int n = g + RA; constexpr int gb = GB;
      if constexpr (last && GY != GB && g == GY) { bar_if(wid >> 2); SBAR(); }
      if constexpr (last && g == gb) { if constexpr (GY != GB) bar_if((wid >> 2) ^ 1u); else bar(); SBAR(); }
      if constexpr (last && g == 12) wait_lgkm<0>();
      else if constexpr ((g & 3) == 0) wait_lgkm<lds_ops(g + 4, g + RA, last)>();
      if constexpr (g == 0) Sn = MFMA(f[0], qf[0], negm); else if constexpr (g < 8) Sn = MFMA(f[g], qf[g], Sn);
      else o[g & 3] = MFMA(f[g], __builtin_bit_cast(bf16x8, Pp[(g >> 2) & 1]), o[g & 3]);
      if constexpr (n < 8) f[n] = rdK(k0, k1, kb, n); else if constexpr (n < 16) f[n] = rdV(v, kb, n - 8);
      else if constexpr (!last) f[n] = rdK(k0, k1, kb + 1, n - 16);
      else if constexpr (g >= gb) { constexpr int per = (RA + 15 - gb) / (16 - gb); static_for<per>([&](auto J) { constexpr int i = per * (g - gb) + J.value; if constexpr (i < RA) f[16 + i] = rdK(nk0, nk1, 0, i); }); }
      if constexpr (g == 0) { e[0] = __builtin_amdgcn_exp2f(Sc[0]); pinf(e[0]); }
      if constexpr (g < 15) { e[g + 1] = __builtin_amdgcn_exp2f(Sc[g + 1]); pinf(e[g + 1]); }
      if constexpr (g == 1) { s0 = e[0]; s1 = e[1]; } else if constexpr (g > 1) { if constexpr (g & 1) { s1 += e[g]; pinf(s1); } else { s0 += e[g]; pinf(s0); } }
      if constexpr (g & 1) { unsigned w = cvtpk(e[g - 1], e[g]); pinu(w); Pc[g >> 3][(g >> 1) & 3] = w; }
      SBAR(); });
#pragma unroll
    for (int i = 0; i < RA; ++i) pre[i] = f[16 + i];
    sm_fix(s0 + s1, Sc, Sn, Pc); };
  if (YPRIO && wid >= 4u) __builtin_amdgcn_s_setprio(1);
#pragma nounroll
  for (unsigned t = 1; t < 7; ++t) {
    const unsigned ko = ((SLOTK >> (4u * t)) & 15u) * 16384u, vo = ((SLOTV >> (4u * (t - 1u))) & 15u) * 16384u, nko = ((SLOTK >> (4u * ((t + 1u) & 7u))) & 15u) * 16384u;
    SBAR();
    unit(std::integral_constant<int, 0>{}, std::false_type{}, kb0 + ko, kb1 + ko, vb + vo, kb0, kb1, S0, S1, P0, P1);
    unit(std::integral_constant<int, 1>{}, std::true_type{}, kb0 + ko, kb1 + ko, vb + vo, kb0 + nko, kb1 + nko, S1, S0, P1, P0);
    if (t == 1u || t == 6u) ST(12 + t);
  }
  const unsigned me = (b * 7u + wid) * NH + h;
  {
    constexpr unsigned ko = ((SLOTK >> 28) & 15u) * 16384u;
    const lds_cptr k0 = kb0 + ko, k1 = kb1 + ko, v6 = vb + ((SLOTV >> 24) & 15u) * 16384u, v7 = vb + ((SLOTV >> 28) & 15u) * 16384u;
    u32x4 P2[2], P3[2];
    auto scaleP = [&](u32x4 (&P)[2], float f) {
#pragma unroll
      for (int i = 0; i < 8; ++i) { const unsigned w = P[i >> 2][i & 3]; P[i >> 2][i & 3] = cvtpk(__uint_as_float(w << 16) * f, __uint_as_float(w & 0xFFFF0000u) * f); } };
    auto fix = [&](auto NP, float ps, f32x16& Sc, f32x16& Sn, u32x4 (&P)[2]) {
      if (__builtin_expect(__any(!(ps <= SUM_THR)), 0)) {
        const float f = __builtin_amdgcn_exp2f(-fmaxf(swap_max(max16(Sc)), 0.f));
        scaleP(P0, f); if constexpr (NP.value > 1) scaleP(P1, f); if constexpr (NP.value > 2) scaleP(P2, f); }
      sm_fix(ps, Sc, Sn, P); };
    auto rdVt = [&](int i) { const int j = pv_order(i), db = j >> 3, un = (j >> 1) & 3, ks = j & 1; return rdV(un < 2 ? v6 : v7, un & 1, 4 * ks + db); };
    auto pvt = [&](auto I, const bf16x8& vf) { constexpr int j = pv_order(I.value), un = (j >> 1) & 3;
      const u32x4 pb = un == 0 ? P0[j & 1] : un == 1 ? P1[j & 1] : un == 2 ? P2[j & 1] : P3[j & 1];
      o[j >> 3] = MFMA(vf, __builtin_bit_cast(bf16x8, pb), o[j >> 3]); pin16(o[j >> 3]); };
    bf16x8 f[16], w[32];
#pragma unroll
    for (int i = 0; i < RA; ++i) f[i] = pre[i];
    auto qsm = [&](auto H, f32x16& Sn, const f32x16& Sc, u32x4 (&Pc)[2]) { constexpr int hf = H.value;
      float e[16]; float s0 = 0.f, s1 = 0.f;
      static_for<9>([&](auto G) { constexpr int g = G.value, n = 8 * hf + g + RA;
        if constexpr (hf < 2 && g < 8) { if constexpr (g == 0) Sn = MFMA(f[8 * hf], qf[0], negm); else Sn = MFMA(f[8 * hf + g], qf[g], Sn);
          pin16(Sn);
          if constexpr (n < 16) f[n] = rdK(k0, k1, n >> 3, n & 7); }
        if constexpr (hf == 1 && g >= 2 && g < 8) w[g - 2] = rdVt(g - 2);
        if constexpr (hf == 2 && g < 8) { pvt(G, w[g]); w[g + 6] = rdVt(g + 6); }
        if constexpr (g < 8) { e[2 * g] = __builtin_amdgcn_exp2f(Sc[2 * g]); e[2 * g + 1] = __builtin_amdgcn_exp2f(Sc[2 * g + 1]); pinf(e[2 * g]); pinf(e[2 * g + 1]); }
        if constexpr (g == 1) { s0 = e[0]; s1 = e[1]; } else if constexpr (g > 1) { s0 += e[2 * g - 2]; pinf(s0); s1 += e[2 * g - 1]; pinf(s1); }
        if constexpr (g >= 1) { unsigned pw = cvtpk(e[2 * g - 2], e[2 * g - 1]); pinu(pw); Pc[(g - 1) >> 2][(g - 1) & 3] = pw; }
        SBAR(); });
      return s0 + s1; };
    if (TSTAG && wid < 4u) __builtin_amdgcn_s_setprio(1);
    SBAR();
    fix(std::integral_constant<int, 1>{}, qsm(std::integral_constant<int, 0>{}, S0, S1, P1), S1, S0, P1);
    if (TSTAG) bar_if(wid >> 2);
    fix(std::integral_constant<int, 2>{}, qsm(std::integral_constant<int, 1>{}, S1, S0, P2), S0, S1, P2);
    if (TSTAG) bar_if((wid >> 2) ^ 1u); else bar();
    ST(19);
    fix(std::integral_constant<int, 3>{}, qsm(std::integral_constant<int, 2>{}, S0, S1, P3), S1, S0, P3);
    const float inv = __builtin_amdgcn_rcpf(swap_add(l));
    auto put = [&](int k8) { bstore_sc1(pack8(o[k8 >> 1], k8 & 1, inv), lane * 16u, sO, me * 8192u + k8 * 1024u); };
    static_for<24>([&](auto G) { constexpr int g = G.value + 8;
      pvt(std::integral_constant<int, g>{}, w[g]);
      if constexpr (g + 6 < 32) w[g + 6] = rdVt(g + 6);
      if constexpr (g == 14) put(0); if constexpr (g == 16) put(1); if constexpr (g == 19) put(2); if constexpr (g == 22) put(3); if constexpr (g == 26) put(4); if constexpr (g == 29) put(5);
      SBAR(); });
    ST(20);
    put(6); put(7); }
  ST(21);
  tag = mix64(ep);
  if (TSTAG) __builtin_amdgcn_s_setprio(0);
  }
  if (!TSTAG || wid < 7u) { wait_vm<0>();
  ST(22);
  bar(); }
  if (FMODE == 1) { if (tid < 16u) st_agent(ws + FLAG_OFF + (b * NH + h) * FLAG_STRIDE + tid * 8u, tag); }
  else if (FMODE == 2) { if (tid == 0) __hip_atomic_exchange((gu64*)(ws + FLAG_OFF + (b * NH + h) * FLAG_STRIDE), tag, __ATOMIC_RELAXED, __HIP_MEMORY_SCOPE_AGENT); }
  else if (tid == 0) st_agent(ws + FLAG_OFF + (b * NH + h) * FLAG_STRIDE, tag);
  if (RET && wid < 7u) { unsigned dummy = 0; touch1(dummy, lane * 128u + ((b * 7u + wid) * NH + h) * 8192u, sO, 0u); wait_vm<0>(); pinu(dummy); }
  ST(23);
  if (!has_tile) {
    if (bid == 249u && wid == 0u) {
#pragma nounroll
      for (unsigned i = 0; i < 4; ++i) poll_flags(ws + FLAG_OFF + (i * 64u + lane) * FLAG_STRIDE, tag);
      if (lane == 0) st_agent(ws + EPOCH_OFF, tag); }
    ST(24); ST(25); ST(26); ST(27); ST(28); ST(29); ST(30); ST(31); ST_OUT(ws, 0, 32); return; }
  const unsigned jq = (2u * g4 + 2u + wid) & 7u, G = 8u * rt + jq;
  const unsigned voffO = lane * 16u, soO = (G * NH + g4 * 8u) * 8192u;
  const char* fp = ws + FLAG_OFF + (((G * 37u) >> 8) * NH + g4 * 8u + (lane & 7u)) * FLAG_STRIDE;
  unsigned ready = 0;
  auto need = [&](unsigned m) {
    _Pragma("nounroll") for (unsigned spins = 0; (ready & m) != m && spins < SPIN_CAP; ++spins) { ready = (unsigned)__ballot(ld_agent(fp) == tag); if ((ready & m) != m) __builtin_amdgcn_s_sleep(PSLEEP); }
    asm volatile("" ::: "memory"); };
  const unsigned hd0 = HG == 3 ? (ct % 3u) * 3u : (ct * (8u / HG)) & 7u;
  if (ONEP) { if (wid == 0u) poll_flags(ws + FLAG_OFF + ((((8u * rt + (lane & 8u ? 7u : 0u)) * 37u) >> 8) * NH + g4 * 8u + (lane & 7u)) * FLAG_STRIDE, tag); bar(); }
  else { if (PDELAY) __builtin_amdgcn_s_sleep(PDELAY); need(LAZY ? 1u << hd0 : 0xFFu); }
  ST(24);
  const lds_cptr wb = (lds_cptr)lds + 1024u * hh + 64u * q4 + trcol;
  if (P2PRIO) { if ((wid & 2u) == 0u) __builtin_amdgcn_s_setprio(2); else if (wid < 4u) __builtin_amdgcn_s_setprio(1); else __builtin_amdgcn_s_setprio(0); } else if (YPRIO) __builtin_amdgcn_s_setprio(0);
  f32x16 acc[2] = {};
  { unsigned hd = hd0, so_c = soO + hd * 8192u; lds_cptr wp = wb + ((SLOTW >> (4u * hd)) & 15u) * 16384u;
    bf16x8 of[8], lf[4][2];
    auto rdW = [&](int s2) { lf[s2 & 3][0] = trd2(wp, (s2 & 7) * 2048, (s2 & 7) * 2048 + 256); lf[s2 & 3][1] = trd2(wp, (s2 & 7) * 2048 + 512, (s2 & 7) * 2048 + 768); };
#pragma unroll
    for (int i = 0; i < 8; ++i) { if (i < WO) bload_sc1(of[i], voffO, sO, so_c + i * 1024u); else of[i] = bf16x8{}; }
    rdW(0); rdW(1);
#pragma nounroll
    for (unsigned p = 0; p < 8; ++p) {
      hd = (hd + 1u) & 7u;
      if (TSTAG && p == 5u) __builtin_amdgcn_s_barrier();
      const unsigned so_n = p < 7u ? soO + hd * 8192u : O_BYTES;
      static_for<8>([&](auto S) { constexpr int s2 = S.value;
        if constexpr (LAZY && s2 == 8 - WO) need(1u << hd);
        if constexpr (s2 == 6) wp = wb + ((SLOTW >> (4u * hd)) & 15u) * 16384u;
        wait_vm1<WO - 1>(of[s2]);
        wait_lgkm<4>();
        acc[0] = MFMA(lf[s2 & 3][0], of[s2], acc[0]);
        rdW(s2 + 2);
        SBAR();
        acc[1] = MFMA(lf[s2 & 3][1], of[s2], acc[1]);
        SBAR();
        breload_sc1(of[(s2 + WO) & 7], voffO, sO, (s2 + WO < 8 ? so_c : so_n) + ((s2 + WO) & 7) * 1024u);
        SBAR(); });
      so_c = so_n;
    }
    wait_vm<0>();
#pragma unroll
    for (int i = 0; i < 8; ++i) pin(of[i]); }
  ST(28);
  const srd_t sX2 = mk_srd(ws + X_OFF, X_BYTES);
  const unsigned xb = (u * 8u + jq) * 4u;
  if (wid < 6u) {
#pragma unroll
    for (int i = 0; i < 4; ++i) bstore_sc1(pack8(acc[i >> 1], i & 1), voffO, sX2, (xb + g4) * 4096u + i * 1024u);
    wait_vm<0>();
    if (FMODE == 1) { if (lane < 16u) st_agent(ws + FLAG2_OFF + (xb + g4) * FLAG_STRIDE + lane * 8u, tag); }
    else if (FMODE == 2) { if (lane == 0) __hip_atomic_exchange((gu64*)(ws + FLAG2_OFF + (xb + g4) * FLAG_STRIDE), tag, __ATOMIC_RELAXED, __HIP_MEMORY_SCOPE_AGENT); }
    else if (lane == 0) st_agent(ws + FLAG2_OFF + (xb + g4) * FLAG_STRIDE, tag);
    ST(29); ST(30);
  } else {
    if (PDELAY2) __builtin_amdgcn_s_sleep(PDELAY2);
    poll_flags(ws + FLAG2_OFF + (xb + ((g4 + 1u + lane % 3u) & 3u)) * FLAG_STRIDE, tag);
    ST(29);
    bf16x8 xp[12];
#pragma unroll
    for (int i = 0; i < 12; ++i) bload_sc1(xp[i], voffO, sX2, (xb + ((g4 + 1u + (i >> 2)) & 3u)) * 4096u + (i & 3) * 1024u);
    wait_vm<0>();
#pragma unroll
    for (int j = 0; j < 12; ++j) { const int i = j & 3; pin(xp[j]); const u32x4 v = __builtin_bit_cast(u32x4, xp[j]);
#pragma unroll
      for (int e = 0; e < 4; ++e) { acc[i >> 1][8 * (i & 1) + 2 * e] += __uint_as_float(v[e] << 16); acc[i >> 1][8 * (i & 1) + 2 * e + 1] += __uint_as_float(v[e] & 0xFFFF0000u); } }
    ST(30);
    char* op = (char*)out + (size_t)(G * 32u + r) * (DM * 2u) + ct * 128u + hh * 16u;
#pragma unroll
    for (int i = 0; i < 4; ++i) *(u32x4*)(op + i * 32) = pack8(acc[i >> 1], i & 1);
  }
  ST(31); ST_OUT(ws, 0, 32);
}

extern "C" void kernel_launch(void* const* d_in, const int* in_sizes, int n_in, void* d_out, int out_size, void* d_ws, size_t ws_size, hipStream_t stream) {
  (void)in_sizes; (void)n_in; (void)out_size; (void)ws_size;
  (void)hipFuncSetAttribute((const void*)mha_kernel, hipFuncAttributeMaxDynamicSharedMemorySize, LDS_BYTES);
  const unsigned short* x = (const unsigned short*)d_in[0]; const unsigned short* kc = (const unsigned short*)d_in[1]; const unsigned short* vc = (const unsigned short*)d_in[2];
  const unsigned short* wq = (const unsigned short*)d_in[3]; const unsigned short* wo = (const unsigned short*)d_in[4];
  mha_kernel<<<256, 512, LDS_BYTES, stream>>>(x, kc, vc, wq, wo, (char*)d_ws, (unsigned short*)d_out);
}
```

```cpp
#define CTOP 1
#include <hip/hip_runtime.h>
#include <stdint.h>
#include <utility>

typedef short    bf16x8 __attribute__((ext_vector_type(8)));
typedef short    s16x4  __attribute__((ext_vector_type(4)));
typedef float    f32x16 __attribute__((ext_vector_type(16)));
typedef float    f32x2  __attribute__((ext_vector_type(2)));
typedef __bf16   bf16x2 __attribute__((ext_vector_type(2)));
typedef unsigned u32x4  __attribute__((ext_vector_type(4)));
typedef __attribute__((address_space(3))) const char* lds_cptr;
typedef __attribute__((address_space(1))) unsigned long long gu64;
typedef __amdgpu_buffer_rsrc_t srd_t;

#define KVP "nt "
#define DI __device__ __forceinline__
#define MFMA(a, b, c) __builtin_amdgcn_mfma_f32_32x32x16_bf16((a), (b), (c), 0, 0, 0)
#define SBAR() __builtin_amdgcn_sched_barrier(0)

constexpr int NB = 8, SQ = 224, DM = 576, NH = 32, SK = 512, DH = 128;
constexpr int LDS_BYTES = 163840;
constexpr float QSCALE = 0.08838834764831845f * 1.4426950408889634f;
constexpr float SUM_THR = 4096.f;

template <int... Is, class F> DI void static_for_impl(std::integer_sequence<int, Is...>, F&& f) { (f(std::integral_constant<int, Is>{}), ...); }
template <int N, class F> DI void static_for(F&& f) { static_for_impl(std::make_integer_sequence<int, N>{}, f); }

DI unsigned rfl(unsigned x) { return __builtin_amdgcn_readfirstlane(x); }
DI unsigned cvtpk(float lo, float hi) { f32x2 v = {lo, hi}; bf16x2 b = __builtin_convertvector(v, bf16x2); return __builtin_bit_cast(unsigned, b); }
DI srd_t mk_srd(const void* p, unsigned bytes) { return __builtin_amdgcn_make_buffer_rsrc((void*)p, (short)0, (int)bytes, 0x00020000); }
DI void dma4(unsigned lds, unsigned v0, unsigned v1, unsigned v2, unsigned v3, srd_t srd, unsigned soff) {
  asm volatile("s_mov_b32 m0, %0\n\ts_nop 0\n\tbuffer_load_dwordx4 %1, %5, %6 offen lds\n\tbuffer_load_dwordx4 %2, %5, %6 offen offset:1024 lds\n\t"
               "buffer_load_dwordx4 %3, %5, %6 offen offset:2048 lds\n\tbuffer_load_dwordx4 %4, %5, %6 offen offset:3072 lds"
               :: "s"(lds), "v"(v0), "v"(v1), "v"(v2), "v"(v3), "s"(srd), "s"(soff) : "memory");
}
DI void dma4_nt(unsigned lds, unsigned v0, unsigned v1, unsigned v2, unsigned v3, srd_t srd, unsigned soff) {
  asm volatile("s_mov_b32 m0, %0\n\ts_nop 0\n\tbuffer_load_dwordx4 %1, %5, %6 offen " KVP "lds\n\tbuffer_load_dwordx4 %2, %5, %6 offen offset:1024 " KVP "lds\n\t"
               "buffer_load_dwordx4 %3, %5, %6 offen offset:2048 " KVP "lds\n\tbuffer_load_dwordx4 %4, %5, %6 offen offset:3072 " KVP "lds"
               :: "s"(lds), "v"(v0), "v"(v1), "v"(v2), "v"(v3), "s"(srd), "s"(soff) : "memory");
}
DI void bstore_sc1(const u32x4& d, unsigned voff, srd_t srd, unsigned soff) { asm volatile("buffer_store_dwordx4 %0, %1, %2, %3 offen sc1\n\ts_nop 1" :: "v"(d), "v"(voff), "s"(srd), "s"(soff) : "memory"); }
DI void bload_sc1(bf16x8& d, unsigned voff, srd_t srd, unsigned soff) { asm volatile("buffer_load_dwordx4 %0, %1, %2, %3 offen sc1" : "=v"(d) : "v"(voff), "s"(srd), "s"(soff) : "memory"); }
DI void breload_sc1(bf16x8& d, unsigned voff, srd_t srd, unsigned soff) { asm volatile("buffer_load_dwordx4 %0, %1, %2, %3 offen sc1" : "+v"(d) : "v"(voff), "s"(srd), "s"(soff) : "memory"); }
DI void touch(unsigned& d, unsigned voff, srd_t srd, unsigned soff) { asm volatile("buffer_load_dword %0, %1, %2, %3 offen" : "+v"(d) : "v"(voff), "s"(srd), "s"(soff) : "memory"); }
DI void touch2(unsigned lds, unsigned voff, srd_t srd, unsigned s0, unsigned s1) { asm volatile("s_mov_b32 m0, %0\n\ts_nop 0\n\tbuffer_load_dword %1, %2, %3 offen " KVP "lds\n\tbuffer_load_dword %1, %2, %4 offen " KVP "lds" :: "s"(lds), "v"(voff), "s"(srd), "s"(s0), "s"(s1) : "memory"); }
DI void touch1(unsigned& d, unsigned voff, srd_t srd, unsigned soff) { asm volatile("buffer_load_dword %0, %1, %2, %3 offen sc1" : "+v"(d) : "v"(voff), "s"(srd), "s"(soff) : "memory"); }
DI void pin(bf16x8& a) { asm volatile("" : "+v"(a)); }
DI void pin16(f32x16& a) { asm volatile("" : "+v"(a)); }
DI void pinf(float& a) { asm volatile("" : "+v"(a)); }
DI void pinu(unsigned& a) { asm volatile("" : "+v"(a)); }
template <int N> DI void wait_vm1(bf16x8& a) { asm volatile("s_waitcnt vmcnt(%1)" : "+v"(a) : "n"(N) : "memory"); }
template <int N> DI void wait_vm() { asm volatile("s_waitcnt vmcnt(%0)" :: "n"(N) : "memory"); }
template <int N> DI void wait_lgkm() { __builtin_amdgcn_s_waitcnt(0xC07F | (N << 8)); SBAR(); }
DI void bar_if(unsigned c) { asm volatile("s_cmp_eq_u32 %0, 0\n\ts_cbranch_scc1 1f\n\ts_waitcnt lgkmcnt(0)\n\ts_barrier\n1:" :: "s"(c) : "memory", "scc"); }
DI void bar() { asm volatile("s_waitcnt lgkmcnt(0)\n\ts_barrier" ::: "memory"); }
typedef short v4i16_t __attribute__((ext_vector_type(4)));
DI s16x4 trd(lds_cptr p) { return __builtin_amdgcn_ds_read_tr16_b64_v4i16((__attribute__((address_space(3))) v4i16_t*)p); }
DI bf16x8 trd2(lds_cptr p, int o0, int o1) { s16x4 a = trd(p + o0), b = trd(p + o1); return __builtin_shufflevector(a, b, 0, 1, 2, 3, 4, 5, 6, 7); }
DI bf16x8 rd128(lds_cptr p) { return *(const __attribute__((address_space(3))) bf16x8*)p; }
DI float swap_max(float m) { auto rr = __builtin_amdgcn_permlane32_swap(__float_as_uint(m), __float_as_uint(m), false, false); return fmaxf(__uint_as_float(rr[0]), __uint_as_float(rr[1])); }
DI float swap_add(float m) { auto rr = __builtin_amdgcn_permlane32_swap(__float_as_uint(m), __float_as_uint(m), false, false); return __uint_as_float(rr[0]) + __uint_as_float(rr[1]); }
DI float max16(const f32x16& s) { float a = fmaxf(fmaxf(s[0], s[1]), s[2]), b = fmaxf(fmaxf(s[3], s[4]), s[5]);
  a = fmaxf(fmaxf(a, s[6]), s[7]); b = fmaxf(fmaxf(b, s[8]), s[9]); a = fmaxf(fmaxf(a, s[10]), s[11]); b = fmaxf(fmaxf(b, s[12]), s[13]); a = fmaxf(fmaxf(a, s[14]), s[15]); return fmaxf(a, b); }
DI float fadd(float a, float b) { float r; asm("v_add_f32_e32 %0, %1, %2" : "=v"(r) : "v"(a), "v"(b)); return r; }
DI u32x4 pack8(const f32x16& a, int s, float f) { return u32x4{cvtpk(a[8 * s] * f, a[8 * s + 1] * f), cvtpk(a[8 * s + 2] * f, a[8 * s + 3] * f), cvtpk(a[8 * s + 4] * f, a[8 * s + 5] * f), cvtpk(a[8 * s + 6] * f, a[8 * s + 7] * f)}; }
DI u32x4 pack8(const f32x16& a, int s) { return u32x4{cvtpk(a[8 * s], a[8 * s + 1]), cvtpk(a[8 * s + 2], a[8 * s + 3]), cvtpk(a[8 * s + 4], a[8 * s + 5]), cvtpk(a[8 * s + 6], a[8 * s + 7])}; }

DI float expo(const f32x16& s, u32x4 (&p)[2]) {
  float e[16]; float s0 = 0.f, s1 = 0.f;
#pragma unroll
  for (int i = 0; i < 16; ++i) e[i] = __builtin_amdgcn_exp2f(s[i]);
#pragma unroll
  for (int i = 0; i < 16; i += 2) { s0 += e[i]; s1 += e[i + 1]; }
#pragma unroll
  for (int i = 0; i < 8; ++i) p[i >> 2][i & 3] = cvtpk(e[2 * i], e[2 * i + 1]);
  return s0 + s1;
}

#define ST_DECL
#define ST(k)
#define ST_OUT(ws, lo, hi)

constexpr unsigned SLOTK = 0x04204209u;
constexpr unsigned SLOTV = 0x31531531u;
constexpr unsigned SLOTW = 0x05429876u;
constexpr unsigned STG = 45056u, STG_W = 28672u;
constexpr unsigned O_BYTES = NB * SQ * NH * DH * 2;
constexpr unsigned X_OFF = O_BYTES, X_BYTES = 63u * 8u * 4u * 4096u;
constexpr unsigned FLAG_OFF = X_OFF + X_BYTES, FLAG_STRIDE = 128, FLAG2_OFF = FLAG_OFF + 256 * FLAG_STRIDE, EPOCH_OFF = FLAG2_OFF + 63 * 32 * FLAG_STRIDE;
constexpr unsigned SPIN_CAP = 1u << 20;
#define RA_ 7
constexpr int RA = RA_;
#define WO 6
#define HG 1
#define RET 0
#define YPRIO 0
#define P2PRIO 0
#define GB 12
#define GY GB
#define NOWUP 0
#define PF 0
#define PF0 4
#define KVT 0xFFFECA8642ull
#define WOT 0x8754432100ull
#define WLATE 1
#define LAZY 0
#define TSTAG 1
#define FMODE 0
#define ONEP 0
#define PDELAY 20
#define PDELAY2 25
#define K0C 3
#define S1MODE 2
#define A0BAR 6
#define PSLEEP 4
#define CODEPF 19456
constexpr int lds_ops(int a, int b, bool last) { int n = 0; for (int i = a; i < b; ++i) n += i >= 16 ? (last ? 0 : 1) : (i >= 8 ? 2 : 1); return n; }
constexpr int pv_order(int i) { return i < 4 ? i : i < 8 ? i + 4 : i < 12 ? i - 4 : i; }
DI unsigned long long mix64(unsigned long long z) { z += 0x9E3779B97F4A7C15ull; z = (z ^ (z >> 30)) * 0xBF58476D1CE4E5B9ull; z = (z ^ (z >> 27)) * 0x94D049BB133111EBull; return z ^ (z >> 31); }
DI unsigned long long rfl64(unsigned long long v) { return ((unsigned long long)rfl((unsigned)(v >> 32)) << 32) | rfl((unsigned)v); }
#define PSYS 0
DI unsigned long long ld_agent(const char* p) {
  if (PSYS == 1) return __hip_atomic_load((gu64*)p, __ATOMIC_RELAXED, __HIP_MEMORY_SCOPE_SYSTEM);
  if (PSYS == 2) return __hip_atomic_fetch_add((gu64*)p, 0ull, __ATOMIC_RELAXED, __HIP_MEMORY_SCOPE_AGENT);
  return __hip_atomic_load((gu64*)p, __ATOMIC_RELAXED, __HIP_MEMORY_SCOPE_AGENT); }
DI void st_agent(char* p, unsigned long long v) { __hip_atomic_store((gu64*)p, v, __ATOMIC_RELAXED, __HIP_MEMORY_SCOPE_AGENT); }
DI void poll_flags(const char* p, unsigned long long tag) {
  _Pragma("nounroll") for (unsigned spins = 0; spins < SPIN_CAP; ++spins) { if (__all(ld_agent(p) == tag)) break; __builtin_amdgcn_s_sleep(PSLEEP); }
  asm volatile("" ::: "memory");
}

__global__ __launch_bounds__(512, 1) void mha_kernel(const unsigned short* __restrict__ x, const unsigned short* __restrict__ kc, const unsigned short* __restrict__ vc,
                                                     const unsigned short* __restrict__ wq, const unsigned short* __restrict__ wo, char* ws, unsigned short* __restrict__ out) {
  extern __shared__ __attribute__((aligned(16))) char lds[];
  unsigned long long pc0; asm volatile("s_getpc_b64 %0" : "=s"(pc0));
  const unsigned tid = threadIdx.x, lane = tid & 63u, r = lane & 31u, hh = lane >> 5;
  const unsigned wid = rfl(tid >> 6);
  ST_DECL ST(0);
  const unsigned bid = blockIdx.x, xcd = bid & 7u, ix = bid >> 3;
  const unsigned b = (xcd & 1u) * 4u + (ix & 3u), h = (xcd >> 1) * 8u + (ix >> 2);
  const unsigned lds0 = (unsigned)(uintptr_t)lds;
  const srd_t sW = mk_srd((const char*)wq - 1024, DM * NH * DH * 2 + 1024);
  const srd_t sX = mk_srd(x + (size_t)b * SQ * DM, SQ * DM * 2);
  const unsigned prow = (lane >> 2) & 7u, pch = 4u * hh + (lane & 3u);
  const unsigned pchx0 = 4u * hh + ((lane & 3u) ^ ((lane >> 4) & 1u)), pchx1 = pchx0 ^ 2u;
  const unsigned vW = prow * 8192u + pch * 16u, vX0 = prow * (DM * 2u) + pchx0 * 16u, vX1 = prow * (DM * 2u) + pchx1 * 16u;
  const unsigned ck0 = 3u * (((ix & 3u) + (ix >> 2)) % 3u);
  unsigned long long ep; asm volatile("global_load_dwordx2 %0, %1, %2 sc1" : "=v"(ep) : "v"(0u), "s"(ws + EPOCH_OFF) : "memory");
  if (wid >= 4u) dma4(lds0 + STG_W + (wid - 4u) * 4096u, vW + 1024u, vW + 128u, vW + 65536u - 1024u, vW + 65664u - 2048u, sW, ck0 * 64u * 8192u + h * 256u + (wid - 4u) * 131072u);
  if (wid < 7u) dma4(lds0 + wid * 4096u, vX0, vX1 + 8192u, vX0 + 16384u, vX1 + 24576u, sX, ck0 * 128u + wid * 32u * DM * 2u);
  else {
#pragma unroll
    for (int i = 0; i < 4; ++i) dma4(lds0 + STG + STG_W + i * 4096u, vW + 1024u, vW + 128u, vW + 65536u - 1024u, vW + 65664u - 2048u, sW, (ck0 + 1u) * 64u * 8192u + h * 256u + i * 131072u); }
  if (CTOP && wid == CTOP - 1u) {
    const char* cp = (const char*)(pc0 & ~255ull) + min((ix * 8u + (lane & 7u)) * 128u, (unsigned)CODEPF);
    asm volatile("s_mov_b32 m0, %0\n\ts_nop 0\n\tglobal_load_lds_dword %1, off" :: "s"(lds0 + 3u * STG), "v"(cp) : "memory"); }
  const srd_t sK = mk_srd((const char*)(kc + (size_t)(b * NH + h) * SK * DH) - 1024, SK * DH * 2 + 1024);
  const srd_t sV = mk_srd((const char*)(vc + (size_t)(b * NH + h) * SK * DH) - 1024, SK * DH * 2 + 1024);
  const unsigned vK0 = prow * 256u + pchx0 * 16u, vK1 = prow * 256u + pchx1 * 16u;
  const unsigned q4 = (lane & 15u) >> 2, p4 = lane & 3u, blk = (lane >> 4) & 1u;
  const unsigned trcol = 32u * blk + 16u * (p4 & 1u) + 8u * (p4 >> 1);
  const srd_t sO = mk_srd(ws, O_BYTES);
  unsigned long long tag;
  const unsigned g4 = xcd >> 1, u0 = (xcd & 1u) * 32u + ix;
  const bool has_tile = u0 < 63u;
  const unsigned u = has_tile ? u0 : 62u, rt = u / 9u, ct = u - rt * 9u;

  if (wid == 7u) {
    auto fillQ = [&](unsigned c, unsigned stage) {
      const unsigned lw = lds0 + stage + STG_W, sw = c * 64u * 8192u + h * 256u, lx = lds0 + stage, sx = c * 128u;
#pragma unroll
      for (int i = 0; i < 4; ++i) dma4(lw + i * 4096u, vW + 1024u, vW + 128u, vW + 65536u - 1024u, vW + 65664u - 2048u, sW, sw + i * 131072u);
#pragma unroll
      for (int i = 0; i < 7; ++i) dma4(lx + i * 4096u, vX0, vX1 + 8192u, vX0 + 16384u, vX1 + 24576u, sX, sx + i * 32u * DM * 2u); };
    if (S1MODE) wait_vm<16>(); else wait_vm<4>();
    bar(); ST(2);
    { unsigned stage = 2 * STG, ck = ck0 + 2u;
#pragma nounroll
      for (unsigned c = 2; c < 9; ++c) { fillQ(ck, stage); ck = ck == 8u ? 0u : ck + 1u; stage = stage == 2 * STG ? 0u : stage + STG;
        wait_vm<44>(); bar(); ST(1 + c); } }
    const unsigned vV = prow * 256u + pch * 16u;
    const srd_t sWo = mk_srd(wo, NH * DH * DM * 2);
    const unsigned vWo = prow * (DM * 2u) + pch * 16u;
    constexpr unsigned last_n = 9u;
    { unsigned j = 1, jw = 0;
#pragma nounroll
      for (unsigned n = 0; n < 10; ++n) {
        if (TSTAG && n == 9u) { wait_vm<0>(); bar(); }
        const unsigned je = ((unsigned)(KVT >> (4u * n)) & 15u) + 1u;
#pragma nounroll
        for (; j < je; ++j) {
          if ((j & 1u) && j != 15u) { const unsigned t = (j + 1u) >> 1, ld = lds0 + ((SLOTK >> (4u * t)) & 15u) * 16384u;
#pragma unroll
            for (int i = 0; i < 4; ++i) dma4_nt(ld + i * 4096u, vK0 + 1024u, vK0 + 128u, vK1 + 1024u, vK1 + 128u, sK, t * 16384u + i * 4096u);
          } else { const unsigned t = (j - 1u) >> 1, ld = lds0 + ((SLOTV >> (4u * t)) & 15u) * 16384u;
#pragma unroll
            for (int i = 0; i < 4; ++i) dma4_nt(ld + i * 4096u, vV + 1024u, vV + 128u, vV + 1024u, vV + 128u, sV, t * 16384u + i * 4096u); } }
        const unsigned we = (unsigned)(0x8741000000ull >> (4u * n)) & 15u;
#pragma nounroll
        for (; jw < we; ++jw) { const unsigned ld = lds0 + ((SLOTW >> (4u * jw)) & 15u) * 16384u, so = (g4 * 8u + jw) * 128u * (DM * 2u) + ct * 128u;
#pragma unroll
          for (int k = 0; k < 4; ++k) dma4(ld + k * 4096u, vWo, vWo + 8192u, vWo + 16384u, vWo + 24576u, sWo, so + k * 32u * (DM * 2u)); }
        if (n == last_n) break;
        if (n < 6u) wait_vm<32>(); else if (n == 6u) wait_vm<48>(); else wait_vm<62>();
        bar(); ST(n == 0u ? 10u : 11u + n); } }
    asm volatile("" : "+v"(ep)); tag = mix64(rfl64(ep));
    ST(20); ST(21);
  } else {
  const unsigned rsw0 = 64u * (r & 7u) + 16u * (hh ^ ((r >> 2) & 3u)), rsw1 = rsw0 ^ 32u;
  f32x16 qa[4] = {}, o[4] = {};
  asm volatile("" : "+v"(qa[0]), "+v"(qa[1]), "+v"(qa[2]), "+v"(qa[3]), "+v"(o[0]), "+v"(o[1]), "+v"(o[2]), "+v"(o[3]));
  const char* wup;
  { const unsigned k = lane & 7u, sel = lane >> 3;
    wup = sel == 0u ? (const char*)(k & 1u ? vc : kc) + (size_t)(b * NH + h) * (SK * DH * 2) + (k >> 1) * 32768u
        : sel == 1u ? (const char*)wo + (size_t)(g4 * 1024u + k * 128u) * (DM * 2u) + ct * 128u
        : sel == 2u ? ws + ((b * 7u + k % 7u) * NH + h) * 8192u
        : sel == 3u ? ws + ((8u * rt + k) * NH + g4 * 8u) * 8192u
        : sel == 4u ? ws + X_OFF + u * 131072u + (k & 1u) * 65536u
        : sel == 5u ? (const char*)out + (size_t)((8u * rt + k) * 32u) * (DM * 2u)
        : sel == 6u ? ws + FLAG_OFF + k * 32768u + 64u
        : (const char*)(pc0 & ~255ull) + min((ix * 8u + k) * 128u, (unsigned)CODEPF); }
  wait_lgkm<0>();
  if (S1MODE) wait_vm<0>(); else if (wid >= 4u) wait_vm<8>(); else wait_vm<4>();
  ST(1);
  if (S1MODE == 2) dma4(lds0 + STG + wid * 4096u, vX0, vX1 + 8192u, vX0 + 16384u, vX1 + 24576u, sX, (ck0 + 1u) * 128u + wid * 32u * DM * 2u);
  bar();
  if (S1MODE == 1) dma4(lds0 + STG + wid * 4096u, vX0, vX1 + 8192u, vX0 + 16384u, vX1 + 24576u, sX, (ck0 + 1u) * 128u + wid * 32u * DM * 2u);
  asm volatile("" : "+v"(ep)); ep = rfl64(ep);
  ST(2);
  const lds_cptr kb0 = (lds_cptr)lds + 2048u * (r >> 3) + rsw0, kb1 = (lds_cptr)lds + 2048u * (r >> 3) + rsw1;
  const lds_cptr vb = (lds_cptr)lds + 64u * (4u * hh + q4) + trcol;
  bf16x8 qf[8], pre[RA];
  f32x16 negm, S0, S1; u32x4 P0[2], P1[2]; float l = 0.f;
  auto rdK = [&](lds_cptr k0, lds_cptr k1, int kb, int i) { return rd128(((i & 1) ? k1 : k0) + 8192 * kb + 512 * (i >> 1)); };
  auto rdV = [&](lds_cptr v, int kb, int j) { return trd2(v, 8192 * kb + 4096 * (j >> 2) + 512 * (j & 3), 8192 * kb + 4096 * (j >> 2) + 2048 + 512 * (j & 3)); };
  auto sm_fix = [&](float ps, f32x16& Sc, f32x16& Sn, u32x4 (&P)[2]) {
    if (__builtin_expect(__any(!(ps <= SUM_THR)), 0)) {
      const float dl = fmaxf(swap_max(max16(Sc)), 0.f), f = __builtin_amdgcn_exp2f(-dl);
#pragma unroll
      for (int i = 0; i < 16; ++i) { Sc[i] -= dl; Sn[i] -= dl; negm[i] -= dl; }
#pragma unroll
      for (int db = 0; db < 4; ++db)
#pragma unroll
        for (int i = 0; i < 16; ++i) o[db][i] *= f;
      l *= f; ps = expo(Sc, P); }
    l += ps; };
  { lds_cptr wp = (lds_cptr)lds + STG_W + 2048u * hh + 64u * q4 + trcol;
    lds_cptr xp0 = (lds_cptr)lds + wid * 4096u + 1024u * (r >> 3) + rsw0, xp1 = (lds_cptr)lds + wid * 4096u + 1024u * (r >> 3) + rsw1;
    bf16x8 xf[4], af[5];
    auto rdA = [&](int i) { return trd2(wp, 4096 * (i >> 2) + 512 * (i & 3), 4096 * (i >> 2) + 512 * (i & 3) + 256); };
    xf[0] = rd128(xp0); af[0] = rdA(0); af[1] = rdA(1); af[2] = rdA(2); af[3] = rdA(3);
    unsigned stage = 0;
#pragma nounroll
    for (unsigned c = 0; c < 8; ++c) {
      if (__builtin_expect(c == K0C && wid == 3u, 0)) {
        { if (!NOWUP) asm volatile("s_mov_b32 m0, %0\n\ts_nop 0\n\tglobal_load_lds_dword %1, off" :: "s"(lds0 + 3u * STG), "v"(wup) : "memory");
#pragma unroll
          for (int i = 0; i < 4; ++i) dma4_nt(lds0 + 9u * 16384u + i * 4096u, vK0 + 1024u, vK0 + 128u, vK1 + 1024u, vK1 + 128u, sK, i * 4096u); } }
      if (PF && __builtin_expect(wid == 3u && c >= PF0 && c < PF0 + PF, 0)) {
        const unsigned i = c - PF0, so = ((i + 2u) >> 1) * 16384u + 1024u;
        if (i & 1u) touch2(lds0 + 3u * STG, lane * 128u, sV, so - 16384u, so - 8192u); else touch2(lds0 + 3u * STG, lane * 128u, sK, so, so + 8192u); }
      const unsigned nstage = stage == 2 * STG ? 0u : stage + STG; const int dlt = (int)nstage - (int)stage; stage = nstage;
      SBAR();
      static_for<12>([&](auto I) { constexpr int i = I.value;
        qa[i & 3] = MFMA(af[i % 5], xf[i >> 2], qa[i & 3]);
        af[(i + 4) % 5] = rdA(i + 4);
        if constexpr (i == 0) xf[1] = rd128(xp1); if constexpr (i == 3) xf[2] = rd128(xp0 + 512); if constexpr (i == 7) xf[3] = rd128(xp1 + 512);
        SBAR(); });
      qa[0] = MFMA(af[12 % 5], xf[3], qa[0]);
      wp += dlt; xp0 += dlt; xp1 += dlt; asm volatile("" : "+v"(wp), "+v"(xp0), "+v"(xp1));
      SBAR();
      asm volatile("s_bitcmp1_b32 %0, %1\n\ts_cbranch_scc1 1f\n\ts_waitcnt vmcnt(0)\n1:\n\ts_waitcnt lgkmcnt(0)\n\ts_barrier" :: "s"(0x78u), "s"(c) : "memory", "scc");
      const bf16x8 xl = xf[3], a13 = af[13 % 5], a14 = af[14 % 5], a15 = af[15 % 5];
      xf[0] = rd128(xp0); af[0] = rdA(0); af[1] = rdA(1);
      SBAR();
      qa[1] = MFMA(a13, xl, qa[1]); SBAR();
      af[2] = rdA(2);
      qa[2] = MFMA(a14, xl, qa[2]); SBAR();
      af[3] = rdA(3);
      qa[3] = MFMA(a15, xl, qa[3]); SBAR();
      ST(3 + c);
    }
    lds_cptr k0 = kb0 + 9 * 16384, k1 = kb1 + 9 * 16384; asm volatile("" : "+v"(k0), "+v"(k1));
    bf16x8 ag[16], f[16]; u32x4 qw[8]; float qs = QSCALE; asm volatile("" : "+s"(qs));
#pragma unroll
    for (int i = 0; i < 4; ++i) ag[i] = af[i];
    auto cvq = [&](int j) { float lo = qa[j >> 3][2 * (j & 7)] * qs; pinf(lo); float hi = qa[j >> 3][2 * (j & 7) + 1] * qs; unsigned w = cvtpk(lo, hi); pinu(w); qw[j >> 2][j & 3] = w; };
    SBAR();
    static_for<24>([&](auto G) { constexpr int g = G.value, n = g + 4;
      if constexpr (g < 4) qa[g] = MFMA(ag[g], xf[0], qa[g]);
      else if constexpr (g < 16) qa[(g - 4) / 3] = MFMA(ag[g], xf[1 + (g - 4) % 3], qa[(g - 4) / 3]);
      else { qf[g - 16] = __builtin_bit_cast(bf16x8, qw[g - 16]);
        if constexpr (g == 16) { const f32x16 z = {}; S0 = MFMA(f[0], qf[0], z); } else S0 = MFMA(f[g - 16], qf[g - 16], S0); pin16(S0); }
      if constexpr (g == 0) { xf[1] = rd128(xp1); xf[2] = rd128(xp0 + 512); } if constexpr (g == 1) xf[3] = rd128(xp1 + 512);
      if constexpr (n < 16) ag[n] = rdA(4 * (1 + (n - 4) % 3) + (n - 4) / 3); else f[n - 16] = rdK(k0, k1, (n - 16) >> 3, (n - 16) & 7);
      if constexpr (g >= 9 && g < 21) { constexpr int j0 = (g - 9) * 8 / 3, j1 = (g - 8) * 8 / 3; static_for<j1 - j0>([&](auto J) { cvq(j0 + J.value); }); }
      SBAR(); });
    ST(11);
#pragma unroll
    for (int i = 12; i < 16; ++i) f[i] = rdK(k0, k1, 1, i & 7);
    const float m = swap_max(max16(S0));
#pragma unroll
    for (int i = 0; i < 16; ++i) negm[i] = -m;
    SBAR();
    float e[16]; float s0 = 0.f, s1 = 0.f;
    static_for<9>([&](auto G) { constexpr int g = G.value;
      if constexpr (g == 0) { if (wid >= 4u) bar(); }
      if constexpr (g == A0BAR) { if (wid < 4u) bar();
        constexpr unsigned ko = ((SLOTK >> 4) & 15u) * 16384u;
#pragma unroll
        for (int i = 0; i < RA; ++i) pre[i] = rdK(kb0 + ko, kb1 + ko, 0, i);
        SBAR(); }
      if constexpr (g == 0) S1 = MFMA(f[8], qf[0], negm); else if constexpr (g < 8) S1 = MFMA(f[8 + g], qf[g], S1);
      if constexpr (g < 8) pin16(S1);
      if constexpr (g < 8) { e[2 * g] = __builtin_amdgcn_exp2f(S0[2 * g] - m); e[2 * g + 1] = __builtin_amdgcn_exp2f(S0[2 * g + 1] - m); pinf(e[2 * g]); pinf(e[2 * g + 1]); }
      if constexpr (g == 1) { s0 = e[0]; s1 = e[1]; } else if constexpr (g > 1) { s0 += e[2 * g - 2]; pinf(s0); s1 += e[2 * g - 1]; pinf(s1); }
      if constexpr (g >= 1) { unsigned w = cvtpk(e[2 * g - 2], e[2 * g - 1]); pinu(w); P0[(g - 1) >> 2][(g - 1) & 3] = w; }
      SBAR(); });
    l = s0 + s1; }
  ST(12);
  auto unit = [&](auto KB, auto LAST, lds_cptr k0, lds_cptr k1, lds_cptr v, lds_cptr nk0, lds_cptr nk1, f32x16& Sn, f32x16& Sc, const u32x4 (&Pp)[2], u32x4 (&Pc)[2]) {
    constexpr int kb = KB.value; constexpr bool last = LAST.value;
    float e[16]; float s0 = 0.f, s1 = 0.f; bf16x8 f[16 + RA];
#pragma unroll
    for (int i = 0; i < RA; ++i) f[i] = pre[i];
    static_for<16>([&](auto G) { constexpr int g = G.value; constexpr int n = g + RA; constexpr int gb = GB;
      if constexpr (last && GY != GB && g == GY) { bar_if(wid >> 2); SBAR(); }
      if constexpr (last && g == gb) { if constexpr (GY != GB) bar_if((wid >> 2) ^ 1u); else bar(); SBAR(); }
      if constexpr (last && g == 12) wait_lgkm<0>();
      else if constexpr ((g & 3) == 0) wait_lgkm<lds_ops(g + 4, g + RA, last)>();
      if constexpr (g == 0) Sn = MFMA(f[0], qf[0], negm); else if constexpr (g < 8) Sn = MFMA(f[g], qf[g], Sn);
      else o[g & 3] = MFMA(f[g], __builtin_bit_cast(bf16x8, Pp[(g >> 2) & 1]), o[g & 3]);
      if constexpr (n < 8) f[n] = rdK(k0, k1, kb, n); else if constexpr (n < 16) f[n] = rdV(v, kb, n - 8);
      else if constexpr (!last) f[n] = rdK(k0, k1, kb + 1, n - 16);
      else if constexpr (g >= gb) { constexpr int per = (RA + 15 - gb) / (16 - gb); static_for<per>([&](auto J) { constexpr int i = per * (g - gb) + J.value; if constexpr (i < RA) f[16 + i] = rdK(nk0, nk1, 0, i); }); }
      if constexpr (g == 0) { e[0] = __builtin_amdgcn_exp2f(Sc[0]); pinf(e[0]); }
      if constexpr (g < 15) { e[g + 1] = __builtin_amdgcn_exp2f(Sc[g + 1]); pinf(e[g + 1]); }
      if constexpr (g == 1) { s0 = e[0]; s1 = e[1]; } else if constexpr (g > 1) { if constexpr (g & 1) { s1 += e[g]; pinf(s1); } else { s0 += e[g]; pinf(s0); } }
      if constexpr (g & 1) { unsigned w = cvtpk(e[g - 1], e[g]); pinu(w); Pc[g >> 3][(g >> 1) & 3] = w; }
      SBAR(); });
#pragma unroll
    for (int i = 0; i < RA; ++i) pre[i] = f[16 + i];
    sm_fix(s0 + s1, Sc, Sn, Pc); };
  if (YPRIO && wid >= 4u) __builtin_amdgcn_s_setprio(1);
#pragma nounroll
  for (unsigned t = 1; t < 7; ++t) {
    const unsigned ko = ((SLOTK >> (4u * t)) & 15u) * 16384u, vo = ((SLOTV >> (4u * (t - 1u))) & 15u) * 16384u, nko = ((SLOTK >> (4u * ((t + 1u) & 7u))) & 15u) * 16384u;
    SBAR();
    unit(std::integral_constant<int, 0>{}, std::false_type{}, kb0 + ko, kb1 + ko, vb + vo, kb0, kb1, S0, S1, P0, P1);
    unit(std::integral_constant<int, 1>{}, std::true_type{}, kb0 + ko, kb1 + ko, vb + vo, kb0 + nko, kb1 + nko, S1, S0, P1, P0);
    if (t == 1u || t == 6u) ST(12 + t);
  }
  const unsigned me = (b * 7u + wid) * NH + h;
  {
    constexpr unsigned ko = ((SLOTK >> 28) & 15u) * 16384u;
    const lds_cptr k0 = kb0 + ko, k1 = kb1 + ko, v6 = vb + ((SLOTV >> 24) & 15u) * 16384u, v7 = vb + ((SLOTV >> 28) & 15u) * 16384u;
    u32x4 P2[2], P3[2];
    auto scaleP = [&](u32x4 (&P)[2], float f) {
#pragma unroll
      for (int i = 0; i < 8; ++i) { const unsigned w = P[i >> 2][i & 3]; P[i >> 2][i & 3] = cvtpk(__uint_as_float(w << 16) * f, __uint_as_float(w & 0xFFFF0000u) * f); } };
    auto fix = [&](auto NP, float ps, f32x16& Sc, f32x16& Sn, u32x4 (&P)[2]) {
      if (__builtin_expect(__any(!(ps <= SUM_THR)), 0)) {
        const float f = __builtin_amdgcn_exp2f(-fmaxf(swap_max(max16(Sc)), 0.f));
        scaleP(P0, f); if constexpr (NP.value > 1) scaleP(P1, f); if constexpr (NP.value > 2) scaleP(P2, f); }
      sm_fix(ps, Sc, Sn, P); };
    auto rdVt = [&](int i) { const int j = pv_order(i), db = j >> 3, un = (j >> 1) & 3, ks = j & 1; return rdV(un < 2 ? v6 : v7, un & 1, 4 * ks + db); };
    auto pvt = [&](auto I, const bf16x8& vf) { constexpr int j = pv_order(I.value), un = (j >> 1) & 3;
      const u32x4 pb = un == 0 ? P0[j & 1] : un == 1 ? P1[j & 1] : un == 2 ? P2[j & 1] : P3[j & 1];
      o[j >> 3] = MFMA(vf, __builtin_bit_cast(bf16x8, pb), o[j >> 3]); pin16(o[j >> 3]); };
    bf16x8 f[16], w[32];
#pragma unroll
    for (int i = 0; i < RA; ++i) f[i] = pre[i];
    auto qsm = [&](auto H, f32x16& Sn, const f32x16& Sc, u32x4 (&Pc)[2]) { constexpr int hf = H.value;
      float e[16]; float s0 = 0.f, s1 = 0.f;
      static_for<9>([&](auto G) { constexpr int g = G.value, n = 8 * hf + g + RA;
        if constexpr (hf < 2 && g < 8) { if constexpr (g == 0) Sn = MFMA(f[8 * hf], qf[0], negm); else Sn = MFMA(f[8 * hf + g], qf[g], Sn);
          pin16(Sn);
          if constexpr (n < 16) f[n] = rdK(k0, k1, n >> 3, n & 7); }
        if constexpr (hf == 1 && g >= 2 && g < 8) w[g - 2] = rdVt(g - 2);
        if constexpr (hf == 2 && g < 8) { pvt(G, w[g]); w[g + 6] = rdVt(g + 6); }
        if constexpr (g < 8) { e[2 * g] = __builtin_amdgcn_exp2f(Sc[2 * g]); e[2 * g + 1] = __builtin_amdgcn_exp2f(Sc[2 * g + 1]); pinf(e[2 * g]); pinf(e[2 * g + 1]); }
        if constexpr (g == 1) { s0 = e[0]; s1 = e[1]; } else if constexpr (g > 1) { s0 += e[2 * g - 2]; pinf(s0); s1 += e[2 * g - 1]; pinf(s1); }
        if constexpr (g >= 1) { unsigned pw = cvtpk(e[2 * g - 2], e[2 * g - 1]); pinu(pw); Pc[(g - 1) >> 2][(g - 1) & 3] = pw; }
        SBAR(); });
      return s0 + s1; };
    if (TSTAG && wid < 4u) __builtin_amdgcn_s_setprio(1);
    SBAR();
    fix(std::integral_constant<int, 1>{}, qsm(std::integral_constant<int, 0>{}, S0, S1, P1), S1, S0, P1);
    if (TSTAG) bar_if(wid >> 2);
    fix(std::integral_constant<int, 2>{}, qsm(std::integral_constant<int, 1>{}, S1, S0, P2), S0, S1, P2);
    if (TSTAG) bar_if((wid >> 2) ^ 1u); else bar();
    ST(19);
    fix(std::integral_constant<int, 3>{}, qsm(std::integral_constant<int, 2>{}, S0, S1, P3), S1, S0, P3);
    const float inv = __builtin_amdgcn_rcpf(swap_add(l));
    auto put = [&](int k8) { bstore_sc1(pack8(o[k8 >> 1], k8 & 1, inv), lane * 16u, sO, me * 8192u + k8 * 1024u); };
    static_for<24>([&](auto G) { constexpr int g = G.value + 8;
      pvt(std::integral_constant<int, g>{}, w[g]);
      if constexpr (g + 6 < 32) w[g + 6] = rdVt(g + 6);
      if constexpr (g == 14) put(0); if constexpr (g == 16) put(1); if constexpr (g == 19) put(2); if constexpr (g == 22) put(3); if constexpr (g == 26) put(4); if constexpr (g == 29) put(5);
      SBAR(); });
    ST(20);
    put(6); put(7); }
  ST(21);
  tag = mix64(ep);
  if (TSTAG) __builtin_amdgcn_s_setprio(0);
  }
  if (!TSTAG || wid < 7u) { wait_vm<0>();
  ST(22);
  bar(); }
  if (FMODE == 1) { if (tid < 16u) st_agent(ws + FLAG_OFF + (b * NH + h) * FLAG_STRIDE + tid * 8u, tag); }
  else if (FMODE == 2) { if (tid == 0) __hip_atomic_exchange((gu64*)(ws + FLAG_OFF + (b * NH + h) * FLAG_STRIDE), tag, __ATOMIC_RELAXED, __HIP_MEMORY_SCOPE_AGENT); }
  else if (tid == 0) st_agent(ws + FLAG_OFF + (b * NH + h) * FLAG_STRIDE, tag);
  if (RET && wid < 7u) { unsigned dummy = 0; touch1(dummy, lane * 128u + ((b * 7u + wid) * NH + h) * 8192u, sO, 0u); wait_vm<0>(); pinu(dummy); }
  ST(23);
  if (!has_tile) {
    if (bid == 249u && wid == 0u) {
#pragma nounroll
      for (unsigned i = 0; i < 4; ++i) poll_flags(ws + FLAG_OFF + (i * 64u + lane) * FLAG_STRIDE, tag);
      if (lane == 0) st_agent(ws + EPOCH_OFF, tag); }
    ST(24); ST(25); ST(26); ST(27); ST(28); ST(29); ST(30); ST(31); ST_OUT(ws, 0, 32); return; }
  const unsigned jq = (2u * g4 + 2u + wid) & 7u, G = 8u * rt + jq;
  const unsigned voffO = lane * 16u, soO = (G * NH + g4 * 8u) * 8192u;
  const char* fp = ws + FLAG_OFF + (((G * 37u) >> 8) * NH + g4 * 8u + (lane & 7u)) * FLAG_STRIDE;
  unsigned ready = 0;
  auto need = [&](unsigned m) {
    _Pragma("nounroll") for (unsigned spins = 0; (ready & m) != m && spins < SPIN_CAP; ++spins) { ready = (unsigned)__ballot(ld_agent(fp) == tag); if ((ready & m) != m) __builtin_amdgcn_s_sleep(PSLEEP); }
    asm volatile("" ::: "memory"); };
  const unsigned hd0 = HG == 3 ? (ct % 3u) * 3u : (ct * (8u / HG)) & 7u;
  if (ONEP) { if (wid == 0u) poll_flags(ws + FLAG_OFF + ((((8u * rt + (lane & 8u ? 7u : 0u)) * 37u) >> 8) * NH + g4 * 8u + (lane & 7u)) * FLAG_STRIDE, tag); bar(); }
  else { if (PDELAY) __builtin_amdgcn_s_sleep(PDELAY); need(LAZY ? 1u << hd0 : 0xFFu); }
  ST(24);
  const lds_cptr wb = (lds_cptr)lds + 1024u * hh + 64u * q4 + trcol;
  if (P2PRIO) { if ((wid & 2u) == 0u) __builtin_amdgcn_s_setprio(2); else if (wid < 4u) __builtin_amdgcn_s_setprio(1); else __builtin_amdgcn_s_setprio(0); } else if (YPRIO) __builtin_amdgcn_s_setprio(0);
  f32x16 acc[2] = {};
  { unsigned hd = hd0, so_c = soO + hd * 8192u; lds_cptr wp = wb + ((SLOTW >> (4u * hd)) & 15u) * 16384u;
    bf16x8 of[8], lf[4][2];
    auto rdW = [&](int s2) { lf[s2 & 3][0] = trd2(wp, (s2 & 7) * 2048, (s2 & 7) * 2048 + 256); lf[s2 & 3][1] = trd2(wp, (s2 & 7) * 2048 + 512, (s2 & 7) * 2048 + 768); };
#pragma unroll
    for (int i = 0; i < 8; ++i) { if (i < WO) bload_sc1(of[i], voffO, sO, so_c + i * 1024u); else of[i] = bf16x8{}; }
    rdW(0); rdW(1);
#pragma nounroll
    for (unsigned p = 0; p < 8; ++p) {
      hd = (hd + 1u) & 7u;
      if (TSTAG && p == 5u) __builtin_amdgcn_s_barrier();
      const unsigned so_n = p < 7u ? soO + hd * 8192u : O_BYTES;
      static_for<8>([&](auto S) { constexpr int s2 = S.value;
        if constexpr (LAZY && s2 == 8 - WO) need(1u << hd);
        if constexpr (s2 == 6) wp = wb + ((SLOTW >> (4u * hd)) & 15u) * 16384u;
        wait_vm1<WO - 1>(of[s2]);
        wait_lgkm<4>();
        acc[0] = MFMA(lf[s2 & 3][0], of[s2], acc[0]);
        rdW(s2 + 2);
        SBAR();
        acc[1] = MFMA(lf[s2 & 3][1], of[s2], acc[1]);
        SBAR();
        breload_sc1(of[(s2 + WO) & 7], voffO, sO, (s2 + WO < 8 ? so_c : so_n) + ((s2 + WO) & 7) * 1024u);
        SBAR(); });
      so_c = so_n;
    }
    wait_vm<0>();
#pragma unroll
    for (int i = 0; i < 8; ++i) pin(of[i]); }
  ST(28);
  const srd_t sX2 = mk_srd(ws + X_OFF, X_BYTES);
  const unsigned xb = (u * 8u + jq) * 4u;
  if (wid < 6u) {
#pragma unroll
    for (int i = 0; i < 4; ++i) bstore_sc1(pack8(acc[i >> 1], i & 1), voffO, sX2, (xb + g4) * 4096u + i * 1024u);
    wait_vm<0>();
    if (FMODE == 1) { if (lane < 16u) st_agent(ws + FLAG2_OFF + (xb + g4) * FLAG_STRIDE + lane * 8u, tag); }
    else if (FMODE == 2) { if (lane == 0) __hip_atomic_exchange((gu64*)(ws + FLAG2_OFF + (xb + g4) * FLAG_STRIDE), tag, __ATOMIC_RELAXED, __HIP_MEMORY_SCOPE_AGENT); }
    else if (lane == 0) st_agent(ws + FLAG2_OFF + (xb + g4) * FLAG_STRIDE, tag);
    ST(29); ST(30);
  } else {
    if (PDELAY2) __builtin_amdgcn_s_sleep(PDELAY2);
    poll_flags(ws + FLAG2_OFF + (xb + ((g4 + 1u + lane % 3u) & 3u)) * FLAG_STRIDE, tag);
    ST(29);
    bf16x8 xp[12];
#pragma unroll
    for (int i = 0; i < 12; ++i) bload_sc1(xp[i], voffO, sX2, (xb + ((g4 + 1u + (i >> 2)) & 3u)) * 4096u + (i & 3) * 1024u);
    wait_vm<0>();
#pragma unroll
    for (int j = 0; j < 12; ++j) { const int i = j & 3; pin(xp[j]); const u32x4 v = __builtin_bit_cast(u32x4, xp[j]);
#pragma unroll
      for (int e = 0; e < 4; ++e) { acc[i >> 1][8 * (i & 1) + 2 * e] += __uint_as_float(v[e] << 16); acc[i >> 1][8 * (i & 1) + 2 * e + 1] += __uint_as_float(v[e] & 0xFFFF0000u); } }
    ST(30);
    char* op = (char*)out + (size_t)(G * 32u + r) * (DM * 2u) + ct * 128u + hh * 16u;
#pragma unroll
    for (int i = 0; i < 4; ++i) *(u32x4*)(op + i * 32) = pack8(acc[i >> 1], i & 1);
  }
  ST(31); ST_OUT(ws, 0, 32);
}

extern "C" void kernel_launch(void* const* d_in, const int* in_sizes, int n_in, void* d_out, int out_size, void* d_ws, size_t ws_size, hipStream_t stream) {
  (void)in_sizes; (void)n_in; (void)out_size; (void)ws_size;
  (void)hipFuncSetAttribute((const void*)mha_kernel, hipFuncAttributeMaxDynamicSharedMemorySize, LDS_BYTES);
  const unsigned short* x = (const unsigned short*)d_in[0]; const unsigned short* kc = (const unsigned short*)d_in[1]; const unsigned short* vc = (const unsigned short*)d_in[2];
  const unsigned short* wq = (const unsigned short*)d_in[3]; const unsigned short* wo = (const unsigned short*)d_in[4];
  mha_kernel<<<256, 512, LDS_BYTES, stream>>>(x, kc, vc, wq, wo, (char*)d_ws, (unsigned short*)d_out);
}
```

```cpp
#include <hip/hip_runtime.h>
#include <stdint.h>
#include <utility>

typedef short    bf16x8 __attribute__((ext_vector_type(8)));
typedef short    s16x4  __attribute__((ext_vector_type(4)));
typedef float    f32x16 __attribute__((ext_vector_type(16)));
typedef float    f32x2  __attribute__((ext_vector_type(2)));
typedef __bf16   bf16x2 __attribute__((ext_vector_type(2)));
typedef unsigned u32x4  __attribute__((ext_vector_type(4)));
typedef __attribute__((address_space(3))) const char* lds_cptr;
typedef __attribute__((address_space(1))) unsigned long long gu64;
typedef __amdgpu_buffer_rsrc_t srd_t;

#define DI __device__ __forceinline__
#define MFMA(a, b, c) __builtin_amdgcn_mfma_f32_32x32x16_bf16((a), (b), (c), 0, 0, 0)
#define SBAR() __builtin_amdgcn_sched_barrier(0)

constexpr int NB = 8, SQ = 224, DM = 576, NH = 32, SK = 512, DH = 128;
constexpr int LDS_BYTES = 163840;
constexpr float QSCALE = 0.08838834764831845f * 1.4426950408889634f;
constexpr float SUM_THR = 4096.f;

template <int... Is, class F> DI void static_for_impl(std::integer_sequence<int, Is...>, F&& f) { (f(std::integral_constant<int, Is>{}), ...); }
template <int N, class F> DI void static_for(F&& f) { static_for_impl(std::make_integer_sequence<int, N>{}, f); }

DI unsigned rfl(unsigned x) { return __builtin_amdgcn_readfirstlane(x); }
DI unsigned cvtpk(float lo, float hi) { f32x2 v = {lo, hi}; bf16x2 b = __builtin_convertvector(v, bf16x2); return __builtin_bit_cast(unsigned, b); }
DI srd_t mk_srd(const void* p, unsigned bytes) { return __builtin_amdgcn_make_buffer_rsrc((void*)p, (short)0, (int)bytes, 0x00020000); }
DI void dma4(unsigned lds, unsigned v0, unsigned v1, unsigned v2, unsigned v3, srd_t srd, unsigned soff) {
  asm volatile("s_mov_b32 m0, %0\n\ts_nop 0\n\tbuffer_load_dwordx4 %1, %5, %6 offen lds\n\tbuffer_load_dwordx4 %2, %5, %6 offen offset:1024 lds\n\t"
               "buffer_load_dwordx4 %3, %5, %6 offen offset:2048 lds\n\tbuffer_load_dwordx4 %4, %5, %6 offen offset:3072 lds"
               :: "s"(lds), "v"(v0), "v"(v1), "v"(v2), "v"(v3), "s"(srd), "s"(soff) : "memory");
}
DI void dma4_nt(unsigned lds, unsigned v0, unsigned v1, unsigned v2, unsigned v3, srd_t srd, unsigned soff) {
  asm volatile("s_mov_b32 m0, %0\n\ts_nop 0\n\tbuffer_load_dwordx4 %1, %5, %6 offen nt lds\n\tbuffer_load_dwordx4 %2, %5, %6 offen offset:1024 nt lds\n\t"
               "buffer_load_dwordx4 %3, %5, %6 offen offset:2048 nt lds\n\tbuffer_load_dwordx4 %4, %5, %6 offen offset:3072 nt lds"
               :: "s"(lds), "v"(v0), "v"(v1), "v"(v2), "v"(v3), "s"(srd), "s"(soff) : "memory");
}
DI void bstore_sc1(const u32x4& d, unsigned voff, srd_t srd, unsigned soff) { asm volatile("buffer_store_dwordx4 %0, %1, %2, %3 offen sc1\n\ts_nop 1" :: "v"(d), "v"(voff), "s"(srd), "s"(soff) : "memory"); }
DI void bload_sc1(bf16x8& d, unsigned voff, srd_t srd, unsigned soff) { asm volatile("buffer_load_dwordx4 %0, %1, %2, %3 offen sc1" : "=v"(d) : "v"(voff), "s"(srd), "s"(soff) : "memory"); }
DI void breload_sc1(bf16x8& d, unsigned voff, srd_t srd, unsigned soff) { asm volatile("buffer_load_dwordx4 %0, %1, %2, %3 offen sc1" : "+v"(d) : "v"(voff), "s"(srd), "s"(soff) : "memory"); }
DI void pin(bf16x8& a) { asm volatile("" : "+v"(a)); }
DI void pin16(f32x16& a) { asm volatile("" : "+v"(a)); }
DI void pinf(float& a) { asm volatile("" : "+v"(a)); }
DI void pinu(unsigned& a) { asm volatile("" : "+v"(a)); }
template <int N> DI void wait_vm1(bf16x8& a) { asm volatile("s_waitcnt vmcnt(%1)" : "+v"(a) : "n"(N) : "memory"); }
template <int N> DI void wait_vm() { asm volatile("s_waitcnt vmcnt(%0)" :: "n"(N) : "memory"); }
template <int N> DI void wait_lgkm() { __builtin_amdgcn_s_waitcnt(0xC07F | (N << 8)); SBAR(); }
DI void bar_if(unsigned c) { asm volatile("s_cmp_eq_u32 %0, 0\n\ts_cbranch_scc1 1f\n\ts_waitcnt lgkmcnt(0)\n\ts_barrier\n1:" :: "s"(c) : "memory", "scc"); }
DI void bar() { asm volatile("s_waitcnt lgkmcnt(0)\n\ts_barrier" ::: "memory"); }
typedef short v4i16_t __attribute__((ext_vector_type(4)));
DI s16x4 trd(lds_cptr p) { return __builtin_amdgcn_ds_read_tr16_b64_v4i16((__attribute__((address_space(3))) v4i16_t*)p); }
DI bf16x8 trd2(lds_cptr p, int o0, int o1) { s16x4 a = trd(p + o0), b = trd(p + o1); return __builtin_shufflevector(a, b, 0, 1, 2, 3, 4, 5, 6, 7); }
DI bf16x8 rd128(lds_cptr p) { return *(const __attribute__((address_space(3))) bf16x8*)p; }
DI float swap_max(float m) { auto rr = __builtin_amdgcn_permlane32_swap(__float_as_uint(m), __float_as_uint(m), false, false); return fmaxf(__uint_as_float(rr[0]), __uint_as_float(rr[1])); }
DI float swap_add(float m) { auto rr = __builtin_amdgcn_permlane32_swap(__float_as_uint(m), __float_as_uint(m), false, false); return __uint_as_float(rr[0]) + __uint_as_float(rr[1]); }
DI float max16(const f32x16& s) { float a = fmaxf(fmaxf(s[0], s[1]), s[2]), b = fmaxf(fmaxf(s[3], s[4]), s[5]);
  a = fmaxf(fmaxf(a, s[6]), s[7]); b = fmaxf(fmaxf(b, s[8]), s[9]); a = fmaxf(fmaxf(a, s[10]), s[11]); b = fmaxf(fmaxf(b, s[12]), s[13]); a = fmaxf(fmaxf(a, s[14]), s[15]); return fmaxf(a, b); }
DI u32x4 pack8(const f32x16& a, int s, float f) { return u32x4{cvtpk(a[8 * s] * f, a[8 * s + 1] * f), cvtpk(a[8 * s + 2] * f, a[8 * s + 3] * f), cvtpk(a[8 * s + 4] * f, a[8 * s + 5] * f), cvtpk(a[8 * s + 6] * f, a[8 * s + 7] * f)}; }
DI u32x4 pack8(const f32x16& a, int s) { return u32x4{cvtpk(a[8 * s], a[8 * s + 1]), cvtpk(a[8 * s + 2], a[8 * s + 3]), cvtpk(a[8 * s + 4], a[8 * s + 5]), cvtpk(a[8 * s + 6], a[8 * s + 7])}; }

DI float expo(const f32x16& s, u32x4 (&p)[2]) {
  float e[16]; float s0 = 0.f, s1 = 0.f;
#pragma unroll
  for (int i = 0; i < 16; ++i) e[i] = __builtin_amdgcn_exp2f(s[i]);
#pragma unroll
  for (int i = 0; i < 16; i += 2) { s0 += e[i]; s1 += e[i + 1]; }
#pragma unroll
  for (int i = 0; i < 8; ++i) p[i >> 2][i & 3] = cvtpk(e[2 * i], e[2 * i + 1]);
  return s0 + s1;
}

constexpr unsigned SLOTK = 0x04204209u;
constexpr unsigned SLOTV = 0x31531531u;
constexpr unsigned SLOTW = 0x05429876u;
constexpr unsigned STG = 45056u, STG_W = 28672u;
constexpr unsigned O_BYTES = NB * SQ * NH * DH * 2;
constexpr unsigned X_OFF = O_BYTES, X_BYTES = 63u * 8u * 4u * 4096u;
constexpr unsigned FLAG_OFF = X_OFF + X_BYTES, FLAG_STRIDE = 128, FLAG2_OFF = FLAG_OFF + 256 * FLAG_STRIDE, EPOCH_OFF = FLAG2_OFF + 63 * 32 * FLAG_STRIDE;
constexpr unsigned SPIN_CAP = 1u << 20;
constexpr int RA = 7, WO = 6, A0BAR = 6, PSLEEP = 4, PDELAY = 20, PDELAY2 = 25;
constexpr unsigned SPARE = 3u * STG;
constexpr unsigned CODE_BYTES = 19200u;
constexpr int lds_ops(int a, int b, bool last) { int n = 0; for (int i = a; i < b; ++i) n += i >= 16 ? (last ? 0 : 1) : (i >= 8 ? 2 : 1); return n; }
constexpr int pv_order(int i) { return i < 4 ? i : i < 8 ? i + 4 : i < 12 ? i - 4 : i; }
DI unsigned long long mix64(unsigned long long z) { z += 0x9E3779B97F4A7C15ull; z = (z ^ (z >> 30)) * 0xBF58476D1CE4E5B9ull; z = (z ^ (z >> 27)) * 0x94D049BB133111EBull; return z ^ (z >> 31); }
DI unsigned long long rfl64(unsigned long long v) { return ((unsigned long long)rfl((unsigned)(v >> 32)) << 32) | rfl((unsigned)v); }
DI unsigned long long ld_agent(const char* p) { return __hip_atomic_load((gu64*)p, __ATOMIC_RELAXED, __HIP_MEMORY_SCOPE_AGENT); }
DI void st_agent(char* p, unsigned long long v) { __hip_atomic_store((gu64*)p, v, __ATOMIC_RELAXED, __HIP_MEMORY_SCOPE_AGENT); }
DI void poll_flags(const char* p, unsigned long long tag) {
  _Pragma("nounroll") for (unsigned spins = 0; spins < SPIN_CAP; ++spins) { if (__all(ld_agent(p) == tag)) break; __builtin_amdgcn_s_sleep(PSLEEP); }
  asm volatile("" ::: "memory");
}

__global__ __launch_bounds__(512, 2) void mha_kernel(const unsigned short* __restrict__ x, const unsigned short* __restrict__ kc, const unsigned short* __restrict__ vc,
                                                     const unsigned short* __restrict__ wq, const unsigned short* __restrict__ wo, char* ws, unsigned short* __restrict__ out) {
  extern __shared__ __attribute__((aligned(16))) char lds[];
  unsigned long long pc0; asm volatile("s_getpc_b64 %0" : "=s"(pc0));
  const unsigned tid = threadIdx.x, lane = tid & 63u, r = lane & 31u, hh = lane >> 5;
  const unsigned wid = rfl(tid >> 6);
  const unsigned bid = blockIdx.x, xcd = bid & 7u, ix = bid >> 3;
  const unsigned b = (xcd & 1u) * 4u + (ix & 3u), h = (xcd >> 1) * 8u + (ix >> 2);
  const unsigned lds0 = (unsigned)(uintptr_t)lds;
  {
    const char* cp = (const char*)(pc0 & ~255ull) + min((ix * 8u + wid) * 128u, CODE_BYTES);
    asm volatile("s_mov_b32 m0, %0\n\ts_nop 0\n\tglobal_load_lds_dword %1, %2" :: "s"(lds0 + SPARE), "v"(0u), "s"(cp) : "memory"); }
  const srd_t sW = mk_srd((const char*)wq - 1024, DM * NH * DH * 2 + 1024);
  const srd_t sX = mk_srd(x + (size_t)b * SQ * DM, SQ * DM * 2);
  const unsigned prow = (lane >> 2) & 7u, pch = 4u * hh + (lane & 3u);
  const unsigned pchx0 = 4u * hh + ((lane & 3u) ^ ((lane >> 4) & 1u)), pchx1 = pchx0 ^ 2u;
  const unsigned vW = prow * 8192u + pch * 16u, vX0 = prow * (DM * 2u) + pchx0 * 16u, vX1 = prow * (DM * 2u) + pchx1 * 16u;
  const unsigned ck0 = 3u * (((ix & 3u) + (ix >> 2)) % 3u);
  unsigned long long ep; asm volatile("global_load_dwordx2 %0, %1, %2 sc1" : "=v"(ep) : "v"(0u), "s"(ws + EPOCH_OFF) : "memory");
  dma4(lds0 + (wid < 7u ? wid * 4096u : SPARE), vX0, vX1 + 8192u, vX0 + 16384u, vX1 + 24576u, sX, ck0 * 128u + min(wid, 6u) * 32u * DM * 2u);
  dma4(lds0 + (wid >> 2) * STG + STG_W + (wid & 3u) * 4096u, vW + 1024u, vW + 128u, vW + 65536u - 1024u, vW + 65664u - 2048u, sW, (ck0 + (wid >> 2)) * 64u * 8192u + h * 256u + (wid & 3u) * 131072u);
  const srd_t sK = mk_srd((const char*)(kc + (size_t)(b * NH + h) * SK * DH) - 1024, SK * DH * 2 + 1024);
  const srd_t sV = mk_srd((const char*)(vc + (size_t)(b * NH + h) * SK * DH) - 1024, SK * DH * 2 + 1024);
  const unsigned vK0 = prow * 256u + pchx0 * 16u, vK1 = prow * 256u + pchx1 * 16u;
  const unsigned q4 = (lane & 15u) >> 2, p4 = lane & 3u, blk = (lane >> 4) & 1u;
  const unsigned trcol = 32u * blk + 16u * (p4 & 1u) + 8u * (p4 >> 1);
  const srd_t sO = mk_srd(ws, O_BYTES);
  unsigned long long tag;
  const unsigned g4 = xcd >> 1, u0 = (xcd & 1u) * 32u + ix;
  const bool has_tile = u0 < 63u;
  const unsigned u = has_tile ? u0 : 62u, rt = u / 9u, ct = u - rt * 9u;

  if (wid == 7u) {
    auto fillQ = [&](unsigned c, unsigned stage) {
      const unsigned lw = lds0 + stage + STG_W, sw = c * 64u * 8192u + h * 256u, lx = lds0 + stage, sx = c * 128u;
#pragma unroll
      for (int i = 0; i < 4; ++i) dma4(lw + i * 4096u, vW + 1024u, vW + 128u, vW + 65536u - 1024u, vW + 65664u - 2048u, sW, sw + i * 131072u);
#pragma unroll
      for (int i = 0; i < 7; ++i) dma4(lx + i * 4096u, vX0, vX1 + 8192u, vX0 + 16384u, vX1 + 24576u, sX, sx + i * 32u * DM * 2u); };
    bar();
    { unsigned stage = 2 * STG, ck = ck0 + 2u;
#pragma nounroll
      for (unsigned c = 2; c < 9; ++c) { fillQ(ck, stage); ck = ck == 8u ? 0u : ck + 1u; stage = stage == 2 * STG ? 0u : stage + STG;
        wait_vm<44>(); bar(); } }
    const unsigned vV = prow * 256u + pch * 16u;
    const srd_t sWo = mk_srd(wo, NH * DH * DM * 2);
    const unsigned vWo = prow * (DM * 2u) + pch * 16u;
    { unsigned j = 1, jw = 0;
#pragma nounroll
      for (unsigned n = 0; n < 10; ++n) {
        if (n == 9u) { wait_vm<0>(); bar(); }
        const unsigned je = ((unsigned)(0xFFFECA8642ull >> (4u * n)) & 15u) + 1u;
#pragma nounroll
        for (; j < je; ++j) {
          if ((j & 1u) && j != 15u) { const unsigned t = (j + 1u) >> 1, ld = lds0 + ((SLOTK >> (4u * t)) & 15u) * 16384u;
#pragma unroll
            for (int i = 0; i < 4; ++i) dma4_nt(ld + i * 4096u, vK0 + 1024u, vK0 + 128u, vK1 + 1024u, vK1 + 128u, sK, t * 16384u + i * 4096u);
          } else { const unsigned t = (j - 1u) >> 1, ld = lds0 + ((SLOTV >> (4u * t)) & 15u) * 16384u;
#pragma unroll
            for (int i = 0; i < 4; ++i) dma4_nt(ld + i * 4096u, vV + 1024u, vV + 128u, vV + 1024u, vV + 128u, sV, t * 16384u + i * 4096u); } }
        const unsigned we = (unsigned)(0x8741000000ull >> (4u * n)) & 15u;
#pragma nounroll
        for (; jw < we; ++jw) { const unsigned ld = lds0 + ((SLOTW >> (4u * jw)) & 15u) * 16384u, so = (g4 * 8u + jw) * 128u * (DM * 2u) + ct * 128u;
#pragma unroll
          for (int k = 0; k < 4; ++k) dma4(ld + k * 4096u, vWo, vWo + 8192u, vWo + 16384u, vWo + 24576u, sWo, so + k * 32u * (DM * 2u)); }
        if (n == 9u) break;
        if (n < 6u) wait_vm<32>(); else if (n == 6u) wait_vm<48>(); else wait_vm<62>();
        bar(); } }
    asm volatile("" : "+v"(ep)); tag = mix64(rfl64(ep));

  } else {
  const unsigned rsw0 = 64u * (r & 7u) + 16u * (hh ^ ((r >> 2) & 3u)), rsw1 = rsw0 ^ 32u;
  f32x16 qa[4] = {}, o[4] = {};
  asm volatile("" : "+v"(qa[0]), "+v"(qa[1]), "+v"(qa[2]), "+v"(qa[3]), "+v"(o[0]), "+v"(o[1]), "+v"(o[2]), "+v"(o[3]));
  wait_lgkm<0>();
  wait_vm<4>(); if (wid < 4u) wait_vm<0>();

  dma4(lds0 + STG + wid * 4096u, vX0, vX1 + 8192u, vX0 + 16384u, vX1 + 24576u, sX, (ck0 + 1u) * 128u + wid * 32u * DM * 2u);
  bar();
  asm volatile("" : "+v"(ep)); ep = rfl64(ep);

  const lds_cptr kb0 = (lds_cptr)lds + 2048u * (r >> 3) + rsw0, kb1 = (lds_cptr)lds + 2048u * (r >> 3) + rsw1;
  const lds_cptr vb = (lds_cptr)lds + 64u * (4u * hh + q4) + trcol;
  bf16x8 qf[8], pre[RA];
  f32x16 negm, S0, S1; u32x4 P0[2], P1[2]; float l = 0.f;
  auto rdK = [&](lds_cptr k0, lds_cptr k1, int kb, int i) { return rd128(((i & 1) ? k1 : k0) + 8192 * kb + 512 * (i >> 1)); };
  auto rdV = [&](lds_cptr v, int kb, int j) { return trd2(v, 8192 * kb + 4096 * (j >> 2) + 512 * (j & 3), 8192 * kb + 4096 * (j >> 2) + 2048 + 512 * (j & 3)); };
  auto sm_fix = [&](float ps, f32x16& Sc, f32x16& Sn, u32x4 (&P)[2]) {
    if (__builtin_expect(__any(!(ps <= SUM_THR)), 0)) {
      const float dl = fmaxf(swap_max(max16(Sc)), 0.f), f = __builtin_amdgcn_exp2f(-dl);
#pragma unroll
      for (int i = 0; i < 16; ++i) { Sc[i] -= dl; Sn[i] -= dl; negm[i] -= dl; }
#pragma unroll
      for (int db = 0; db < 4; ++db)
#pragma unroll
        for (int i = 0; i < 16; ++i) o[db][i] *= f;
      l *= f; ps = expo(Sc, P); }
    l += ps; };
  { lds_cptr wp = (lds_cptr)lds + STG_W + 2048u * hh + 64u * q4 + trcol;
    lds_cptr xp0 = (lds_cptr)lds + wid * 4096u + 1024u * (r >> 3) + rsw0, xp1 = (lds_cptr)lds + wid * 4096u + 1024u * (r >> 3) + rsw1;
    bf16x8 xf[4], af[5];
    auto rdA = [&](int i) { return trd2(wp, 4096 * (i >> 2) + 512 * (i & 3), 4096 * (i >> 2) + 512 * (i & 3) + 256); };
    xf[0] = rd128(xp0); af[0] = rdA(0); af[1] = rdA(1); af[2] = rdA(2); af[3] = rdA(3);
    unsigned stage = 0;
#pragma nounroll
    for (unsigned c = 0; c < 8; ++c) {
      if (__builtin_expect(c == 3u && wid == 3u, 0)) {
#pragma unroll
        for (int i = 0; i < 4; ++i) dma4_nt(lds0 + 9u * 16384u + i * 4096u, vK0 + 1024u, vK0 + 128u, vK1 + 1024u, vK1 + 128u, sK, i * 4096u);
        unsigned ln = lane; asm volatile("" : "+v"(ln));
        const unsigned k = ln & 7u, sel = ln >> 3;
        const char* wup = sel == 0u ? (const char*)vc + (size_t)(b * NH + h) * (SK * DH * 2) + k * 16384u
                        : sel == 1u ? (const char*)wo + (size_t)(g4 * 1024u + k * 128u) * (DM * 2u) + ct * 128u
                        : sel == 2u ? (const char*)out + (size_t)((8u * rt + k) * 32u) * (DM * 2u)
                        : ws + (sel == 3u ? ((b * 7u + k) * NH + h) * 8192u : sel == 4u ? ((8u * rt + k) * NH + g4 * 8u) * 8192u : sel == 5u ? X_OFF + u * 131072u + k * 16384u : FLAG_OFF + k * 32768u + 64u);
        asm volatile("s_mov_b32 m0, %0\n\ts_nop 0\n\tglobal_load_lds_dword %1, off sc1" :: "s"(lds0 + SPARE), "v"(wup) : "memory"); }
      const unsigned nstage = stage == 2 * STG ? 0u : stage + STG; const int dlt = (int)nstage - (int)stage; stage = nstage;
      SBAR();
      static_for<12>([&](auto I) { constexpr int i = I.value;
        qa[i & 3] = MFMA(af[i % 5], xf[i >> 2], qa[i & 3]);
        af[(i + 4) % 5] = rdA(i + 4);
        if constexpr (i == 0) xf[1] = rd128(xp1); if constexpr (i == 3) xf[2] = rd128(xp0 + 512); if constexpr (i == 7) xf[3] = rd128(xp1 + 512);
        SBAR(); });
      qa[0] = MFMA(af[12 % 5], xf[3], qa[0]);
      wp += dlt; xp0 += dlt; xp1 += dlt; asm volatile("" : "+v"(wp), "+v"(xp0), "+v"(xp1));
      SBAR();
      asm volatile("s_bitcmp1_b32 %0, %1\n\ts_cbranch_scc1 1f\n\ts_waitcnt vmcnt(0)\n1:\n\ts_waitcnt lgkmcnt(0)\n\ts_barrier" :: "s"(0x78u), "s"(c) : "memory", "scc");
      const bf16x8 xl = xf[3], a13 = af[13 % 5], a14 = af[14 % 5], a15 = af[15 % 5];
      xf[0] = rd128(xp0); af[0] = rdA(0); af[1] = rdA(1);
      SBAR();
      qa[1] = MFMA(a13, xl, qa[1]); SBAR();
      af[2] = rdA(2);
      qa[2] = MFMA(a14, xl, qa[2]); SBAR();
      af[3] = rdA(3);
      qa[3] = MFMA(a15, xl, qa[3]); SBAR();

    }
    lds_cptr k0 = kb0 + 9 * 16384, k1 = kb1 + 9 * 16384; asm volatile("" : "+v"(k0), "+v"(k1));
    bf16x8 ag[16], f[16]; u32x4 qw[8]; float qs = QSCALE; asm volatile("" : "+s"(qs));
#pragma unroll
    for (int i = 0; i < 4; ++i) ag[i] = af[i];
    auto cvq = [&](int j) { float lo = qa[j >> 3][2 * (j & 7)] * qs; pinf(lo); float hi = qa[j >> 3][2 * (j & 7) + 1] * qs; unsigned w = cvtpk(lo, hi); pinu(w); qw[j >> 2][j & 3] = w; };
    SBAR();
    static_for<24>([&](auto G) { constexpr int g = G.value, n = g + 4;
      if constexpr (g < 4) qa[g] = MFMA(ag[g], xf[0], qa[g]);
      else if constexpr (g < 16) qa[(g - 4) / 3] = MFMA(ag[g], xf[1 + (g - 4) % 3], qa[(g - 4) / 3]);
      else { qf[g - 16] = __builtin_bit_cast(bf16x8, qw[g - 16]);
        if constexpr (g == 16) { const f32x16 z = {}; S0 = MFMA(f[0], qf[0], z); } else S0 = MFMA(f[g - 16], qf[g - 16], S0); pin16(S0); }
      if constexpr (g == 0) { xf[1] = rd128(xp1); xf[2] = rd128(xp0 + 512); } if constexpr (g == 1) xf[3] = rd128(xp1 + 512);
      if constexpr (n < 16) ag[n] = rdA(4 * (1 + (n - 4) % 3) + (n - 4) / 3); else f[n - 16] = rdK(k0, k1, (n - 16) >> 3, (n - 16) & 7);
      if constexpr (g >= 9 && g < 21) { constexpr int j0 = (g - 9) * 8 / 3, j1 = (g - 8) * 8 / 3; static_for<j1 - j0>([&](auto J) { cvq(j0 + J.value); }); }
      SBAR(); });

#pragma unroll
    for (int i = 12; i < 16; ++i) f[i] = rdK(k0, k1, 1, i & 7);
    const float m = swap_max(max16(S0));
#pragma unroll
    for (int i = 0; i < 16; ++i) negm[i] = -m;
    SBAR();
    float e[16]; float s0 = 0.f, s1 = 0.f;
    static_for<9>([&](auto G) { constexpr int g = G.value;
      if constexpr (g == 0) { if (wid >= 4u) bar(); }
      if constexpr (g == A0BAR) { if (wid < 4u) bar();
        constexpr unsigned ko = ((SLOTK >> 4) & 15u) * 16384u;
#pragma unroll
        for (int i = 0; i < RA; ++i) pre[i] = rdK(kb0 + ko, kb1 + ko, 0, i);
        SBAR(); }
      if constexpr (g == 0) S1 = MFMA(f[8], qf[0], negm); else if constexpr (g < 8) S1 = MFMA(f[8 + g], qf[g], S1);
      if constexpr (g < 8) pin16(S1);
      if constexpr (g < 8) { e[2 * g] = __builtin_amdgcn_exp2f(S0[2 * g] - m); e[2 * g + 1] = __builtin_amdgcn_exp2f(S0[2 * g + 1] - m); pinf(e[2 * g]); pinf(e[2 * g + 1]); }
      if constexpr (g == 1) { s0 = e[0]; s1 = e[1]; } else if constexpr (g > 1) { s0 += e[2 * g - 2]; pinf(s0); s1 += e[2 * g - 1]; pinf(s1); }
      if constexpr (g >= 1) { unsigned w = cvtpk(e[2 * g - 2], e[2 * g - 1]); pinu(w); P0[(g - 1) >> 2][(g - 1) & 3] = w; }
      SBAR(); });
    l = s0 + s1; }

  auto unit = [&](auto KB, auto LAST, lds_cptr k0, lds_cptr k1, lds_cptr v, lds_cptr nk0, lds_cptr nk1, f32x16& Sn, f32x16& Sc, const u32x4 (&Pp)[2], u32x4 (&Pc)[2]) {
    constexpr int kb = KB.value; constexpr bool last = LAST.value;
    float e[16]; float s0 = 0.f, s1 = 0.f; bf16x8 f[16 + RA];
#pragma unroll
    for (int i = 0; i < RA; ++i) f[i] = pre[i];
    static_for<16>([&](auto G) { constexpr int g = G.value; constexpr int n = g + RA; constexpr int gb = 12;
      if constexpr (last && g == gb) { bar(); wait_lgkm<0>(); }
      else if constexpr ((g & 3) == 0) wait_lgkm<lds_ops(g + 4, g + RA, last)>();
      if constexpr (g == 0) Sn = MFMA(f[0], qf[0], negm); else if constexpr (g < 8) Sn = MFMA(f[g], qf[g], Sn);
      else o[g & 3] = MFMA(f[g], __builtin_bit_cast(bf16x8, Pp[(g >> 2) & 1]), o[g & 3]);
      if constexpr (n < 8) f[n] = rdK(k0, k1, kb, n); else if constexpr (n < 16) f[n] = rdV(v, kb, n - 8);
      else if constexpr (!last) f[n] = rdK(k0, k1, kb + 1, n - 16);
      else if constexpr (g >= gb) { constexpr int per = (RA + 15 - gb) / (16 - gb); static_for<per>([&](auto J) { constexpr int i = per * (g - gb) + J.value; if constexpr (i < RA) f[16 + i] = rdK(nk0, nk1, 0, i); }); }
      if constexpr (g == 0) { e[0] = __builtin_amdgcn_exp2f(Sc[0]); pinf(e[0]); }
      if constexpr (g < 15) { e[g + 1] = __builtin_amdgcn_exp2f(Sc[g + 1]); pinf(e[g + 1]); }
      if constexpr (g == 1) { s0 = e[0]; s1 = e[1]; } else if constexpr (g > 1) { if constexpr (g & 1) { s1 += e[g]; pinf(s1); } else { s0 += e[g]; pinf(s0); } }
      if constexpr (g & 1) { unsigned w = cvtpk(e[g - 1], e[g]); pinu(w); Pc[g >> 3][(g >> 1) & 3] = w; }
      SBAR(); });
#pragma unroll
    for (int i = 0; i < RA; ++i) pre[i] = f[16 + i];
    sm_fix(s0 + s1, Sc, Sn, Pc); };
#pragma nounroll
  for (unsigned t = 1; t < 7; ++t) {
    const unsigned ko = ((SLOTK >> (4u * t)) & 15u) * 16384u, vo = ((SLOTV >> (4u * (t - 1u))) & 15u) * 16384u, nko = ((SLOTK >> (4u * ((t + 1u) & 7u))) & 15u) * 16384u;
    SBAR();
    unit(std::integral_constant<int, 0>{}, std::false_type{}, kb0 + ko, kb1 + ko, vb + vo, kb0, kb1, S0, S1, P0, P1);
    unit(std::integral_constant<int, 1>{}, std::true_type{}, kb0 + ko, kb1 + ko, vb + vo, kb0 + nko, kb1 + nko, S1, S0, P1, P0);
  }
  const unsigned me = (b * 7u + wid) * NH + h;
  {
    constexpr unsigned ko = ((SLOTK >> 28) & 15u) * 16384u;
    const lds_cptr k0 = kb0 + ko, k1 = kb1 + ko, v6 = vb + ((SLOTV >> 24) & 15u) * 16384u, v7 = vb + ((SLOTV >> 28) & 15u) * 16384u;
    u32x4 P2[2], P3[2];
    auto scaleP = [&](u32x4 (&P)[2], float f) {
#pragma unroll
      for (int i = 0; i < 8; ++i) { const unsigned w = P[i >> 2][i & 3]; P[i >> 2][i & 3] = cvtpk(__uint_as_float(w << 16) * f, __uint_as_float(w & 0xFFFF0000u) * f); } };
    auto fix = [&](auto NP, float ps, f32x16& Sc, f32x16& Sn, u32x4 (&P)[2]) {
      if (__builtin_expect(__any(!(ps <= SUM_THR)), 0)) {
        const float f = __builtin_amdgcn_exp2f(-fmaxf(swap_max(max16(Sc)), 0.f));
        scaleP(P0, f); if constexpr (NP.value > 1) scaleP(P1, f); if constexpr (NP.value > 2) scaleP(P2, f); }
      sm_fix(ps, Sc, Sn, P); };
    auto rdVt = [&](int i) { const int j = pv_order(i), db = j >> 3, un = (j >> 1) & 3, ks = j & 1; return rdV(un < 2 ? v6 : v7, un & 1, 4 * ks + db); };
    auto pvt = [&](auto I, const bf16x8& vf) { constexpr int j = pv_order(I.value), un = (j >> 1) & 3;
      const u32x4 pb = un == 0 ? P0[j & 1] : un == 1 ? P1[j & 1] : un == 2 ? P2[j & 1] : P3[j & 1];
      o[j >> 3] = MFMA(vf, __builtin_bit_cast(bf16x8, pb), o[j >> 3]); pin16(o[j >> 3]); };
    bf16x8 f[16], w[32];
#pragma unroll
    for (int i = 0; i < RA; ++i) f[i] = pre[i];
    auto qsm = [&](auto H, f32x16& Sn, const f32x16& Sc, u32x4 (&Pc)[2]) { constexpr int hf = H.value;
      float e[16]; float s0 = 0.f, s1 = 0.f;
      static_for<9>([&](auto G) { constexpr int g = G.value, n = 8 * hf + g + RA;
        if constexpr (hf < 2 && g < 8) { if constexpr (g == 0) Sn = MFMA(f[8 * hf], qf[0], negm); else Sn = MFMA(f[8 * hf + g], qf[g], Sn);
          pin16(Sn);
          if constexpr (n < 16) f[n] = rdK(k0, k1, n >> 3, n & 7); }
        if constexpr (hf == 1 && g >= 2 && g < 8) w[g - 2] = rdVt(g - 2);
        if constexpr (hf == 2 && g < 8) { pvt(G, w[g]); w[g + 6] = rdVt(g + 6); }
        if constexpr (g < 8) { e[2 * g] = __builtin_amdgcn_exp2f(Sc[2 * g]); e[2 * g + 1] = __builtin_amdgcn_exp2f(Sc[2 * g + 1]); pinf(e[2 * g]); pinf(e[2 * g + 1]); }
        if constexpr (g == 1) { s0 = e[0]; s1 = e[1]; } else if constexpr (g > 1) { s0 += e[2 * g - 2]; pinf(s0); s1 += e[2 * g - 1]; pinf(s1); }
        if constexpr (g >= 1) { unsigned pw = cvtpk(e[2 * g - 2], e[2 * g - 1]); pinu(pw); Pc[(g - 1) >> 2][(g - 1) & 3] = pw; }
        SBAR(); });
      return s0 + s1; };
    if (wid < 4u) __builtin_amdgcn_s_setprio(1);
    SBAR();
    fix(std::integral_constant<int, 1>{}, qsm(std::integral_constant<int, 0>{}, S0, S1, P1), S1, S0, P1);
    bar_if(wid >> 2);
    fix(std::integral_constant<int, 2>{}, qsm(std::integral_constant<int, 1>{}, S1, S0, P2), S0, S1, P2);
    bar_if((wid >> 2) ^ 1u);

    fix(std::integral_constant<int, 3>{}, qsm(std::integral_constant<int, 2>{}, S0, S1, P3), S1, S0, P3);
    const float inv = __builtin_amdgcn_rcpf(swap_add(l));
    auto put = [&](int k8) { bstore_sc1(pack8(o[k8 >> 1], k8 & 1, inv), lane * 16u, sO, me * 8192u + k8 * 1024u); };
    static_for<24>([&](auto G) { constexpr int g = G.value + 8;
      pvt(std::integral_constant<int, g>{}, w[g]);
      if constexpr (g + 6 < 32) w[g + 6] = rdVt(g + 6);
      if constexpr (g == 14) put(0); if constexpr (g == 16) put(1); if constexpr (g == 19) put(2); if constexpr (g == 22) put(3); if constexpr (g == 26) put(4); if constexpr (g == 29) put(5);
      SBAR(); });

    put(6); put(7); }

  tag = mix64(ep);
  __builtin_amdgcn_s_setprio(0);
  }
  if (wid < 7u) { wait_vm<0>();

  bar(); }
  if (tid == 0) st_agent(ws + FLAG_OFF + (b * NH + h) * FLAG_STRIDE, tag);

  if (!has_tile) {
    if (bid == 249u && wid == 0u) {
#pragma nounroll
      for (unsigned i = 0; i < 4; ++i) poll_flags(ws + FLAG_OFF + (i * 64u + lane) * FLAG_STRIDE, tag);
      if (lane == 0) st_agent(ws + EPOCH_OFF, tag); }
    return; }
  const unsigned jq = (2u * g4 + 2u + wid) & 7u, G = 8u * rt + jq;
  const unsigned voffO = lane * 16u, soO = (G * NH + g4 * 8u) * 8192u;
  const char* fp = ws + FLAG_OFF + (((G * 37u) >> 8) * NH + g4 * 8u + (lane & 7u)) * FLAG_STRIDE;
  unsigned ready = 0;
  auto need = [&](unsigned m) {
    _Pragma("nounroll") for (unsigned spins = 0; (ready & m) != m && spins < SPIN_CAP; ++spins) { ready = (unsigned)__ballot(ld_agent(fp) == tag); if ((ready & m) != m) __builtin_amdgcn_s_sleep(PSLEEP); }
    asm volatile("" ::: "memory"); };
  __builtin_amdgcn_s_sleep(PDELAY);
  need(1u);

  const lds_cptr wb = (lds_cptr)lds + 1024u * hh + 64u * q4 + trcol;
  f32x16 acc[2] = {};
  { unsigned hd = 0, so_c = soO + hd * 8192u; lds_cptr wp = wb + ((SLOTW >> (4u * hd)) & 15u) * 16384u;
    bf16x8 of[8], lf[4][2];
    auto rdW = [&](int s2) { lf[s2 & 3][0] = trd2(wp, (s2 & 7) * 2048, (s2 & 7) * 2048 + 256); lf[s2 & 3][1] = trd2(wp, (s2 & 7) * 2048 + 512, (s2 & 7) * 2048 + 768); };
#pragma unroll
    for (int i = 0; i < 8; ++i) { if (i < WO) bload_sc1(of[i], voffO, sO, so_c + i * 1024u); else of[i] = bf16x8{}; }
    rdW(0); rdW(1);
#pragma nounroll
    for (unsigned p = 0; p < 8; ++p) {
      hd = (hd + 1u) & 7u;
      if (p == 5u) __builtin_amdgcn_s_barrier();
      const unsigned so_n = p < 7u ? soO + hd * 8192u : O_BYTES;
      static_for<8>([&](auto S) { constexpr int s2 = S.value;
        if constexpr (s2 == 8 - WO) need(1u << hd);
        if constexpr (s2 == 6) wp = wb + ((SLOTW >> (4u * hd)) & 15u) * 16384u;
        wait_vm1<WO - 1>(of[s2]);
        wait_lgkm<4>();
        acc[0] = MFMA(lf[s2 & 3][0], of[s2], acc[0]);
        rdW(s2 + 2);
        SBAR();
        acc[1] = MFMA(lf[s2 & 3][1], of[s2], acc[1]);
        SBAR();
        breload_sc1(of[(s2 + WO) & 7], voffO, sO, (s2 + WO < 8 ? so_c : so_n) + ((s2 + WO) & 7) * 1024u);
        SBAR(); });
      so_c = so_n;
    }
    wait_vm<0>();
#pragma unroll
    for (int i = 0; i < 8; ++i) pin(of[i]); }

  const srd_t sX2 = mk_srd(ws + X_OFF, X_BYTES);
  const unsigned xb = (u * 8u + jq) * 4u;
  if (wid < 6u) {
#pragma unroll
    for (int i = 0; i < 4; ++i) bstore_sc1(pack8(acc[i >> 1], i & 1), voffO, sX2, (xb + g4) * 4096u + i * 1024u);
    wait_vm<0>();
    if (lane == 0) st_agent(ws + FLAG2_OFF + (xb + g4) * FLAG_STRIDE, tag);

  } else {
    __builtin_amdgcn_s_sleep(PDELAY2);
    poll_flags(ws + FLAG2_OFF + (xb + ((g4 + 1u + lane % 3u) & 3u)) * FLAG_STRIDE, tag);

    bf16x8 xp[12];
#pragma unroll
    for (int i = 0; i < 12; ++i) bload_sc1(xp[i], voffO, sX2, (xb + ((g4 + 1u + (i >> 2)) & 3u)) * 4096u + (i & 3) * 1024u);
    wait_vm<0>();
#pragma unroll
    for (int j = 0; j < 12; ++j) { const int i = j & 3; pin(xp[j]); const u32x4 v = __builtin_bit_cast(u32x4, xp[j]);
#pragma unroll
      for (int e = 0; e < 4; ++e) { acc[i >> 1][8 * (i & 1) + 2 * e] += __uint_as_float(v[e] << 16); acc[i >> 1][8 * (i & 1) + 2 * e + 1] += __uint_as_float(v[e] & 0xFFFF0000u); } }

    char* op = (char*)out + (size_t)(G * 32u + r) * (DM * 2u) + ct * 128u + hh * 16u;
#pragma unroll
    for (int i = 0; i < 4; ++i) *(u32x4*)(op + i * 32) = pack8(acc[i >> 1], i & 1);
  }

}

extern "C" void kernel_launch(void* const* d_in, const int* in_sizes, int n_in, void* d_out, int out_size, void* d_ws, size_t ws_size, hipStream_t stream) {
  (void)in_sizes; (void)n_in; (void)out_size; (void)ws_size;
  (void)hipFuncSetAttribute((const void*)mha_kernel, hipFuncAttributeMaxDynamicSharedMemorySize, LDS_BYTES);
  const unsigned short* x = (const unsigned short*)d_in[0]; const unsigned short* kc = (const unsigned short*)d_in[1]; const unsigned short* vc = (const unsigned short*)d_in[2];
  const unsigned short* wq = (const unsigned short*)d_in[3]; const unsigned short* wo = (const unsigned short*)d_in[4];
  mha_kernel<<<256, 512, LDS_BYTES, stream>>>(x, kc, vc, wq, wo, (char*)d_ws, (unsigned short*)d_out);
}
```

```cpp
#define QG 1
#include <hip/hip_runtime.h>
#include <stdint.h>
#include <utility>

typedef short    bf16x8 __attribute__((ext_vector_type(8)));
typedef short    s16x4  __attribute__((ext_vector_type(4)));
typedef float    f32x16 __attribute__((ext_vector_type(16)));
typedef float    f32x2  __attribute__((ext_vector_type(2)));
typedef __bf16   bf16x2 __attribute__((ext_vector_type(2)));
typedef unsigned u32x4  __attribute__((ext_vector_type(4)));
typedef __attribute__((address_space(3))) const char* lds_cptr;
typedef __attribute__((address_space(1))) unsigned long long gu64;
typedef __amdgpu_buffer_rsrc_t srd_t;

#define DI __device__ __forceinline__
#define MFMA(a, b, c) __builtin_amdgcn_mfma_f32_32x32x16_bf16((a), (b), (c), 0, 0, 0)
#define SBAR() __builtin_amdgcn_sched_barrier(0)

constexpr int NB = 8, SQ = 224, DM = 576, NH = 32, SK = 512, DH = 128;
constexpr int LDS_BYTES = 163840;
constexpr float QSCALE = 0.08838834764831845f * 1.4426950408889634f;
constexpr float SUM_THR = 4096.f;

template <int... Is, class F> DI void static_for_impl(std::integer_sequence<int, Is...>, F&& f) { (f(std::integral_constant<int, Is>{}), ...); }
template <int N, class F> DI void static_for(F&& f) { static_for_impl(std::make_integer_sequence<int, N>{}, f); }

DI unsigned rfl(unsigned x) { return __builtin_amdgcn_readfirstlane(x); }
DI unsigned cvtpk(float lo, float hi) { f32x2 v = {lo, hi}; bf16x2 b = __builtin_convertvector(v, bf16x2); return __builtin_bit_cast(unsigned, b); }
DI srd_t mk_srd(const void* p, unsigned bytes) { return __builtin_amdgcn_make_buffer_rsrc((void*)p, (short)0, (int)bytes, 0x00020000); }
DI void dma4(unsigned lds, unsigned v0, unsigned v1, unsigned v2, unsigned v3, srd_t srd, unsigned soff) {
  asm volatile("s_mov_b32 m0, %0\n\ts_nop 0\n\tbuffer_load_dwordx4 %1, %5, %6 offen lds\n\tbuffer_load_dwordx4 %2, %5, %6 offen offset:1024 lds\n\t"
               "buffer_load_dwordx4 %3, %5, %6 offen offset:2048 lds\n\tbuffer_load_dwordx4 %4, %5, %6 offen offset:3072 lds"
               :: "s"(lds), "v"(v0), "v"(v1), "v"(v2), "v"(v3), "s"(srd), "s"(soff) : "memory");
}
DI void dma4_nt(unsigned lds, unsigned v0, unsigned v1, unsigned v2, unsigned v3, srd_t srd, unsigned soff) {
  asm volatile("s_mov_b32 m0, %0\n\ts_nop 0\n\tbuffer_load_dwordx4 %1, %5, %6 offen nt lds\n\tbuffer_load_dwordx4 %2, %5, %6 offen offset:1024 nt lds\n\t"
               "buffer_load_dwordx4 %3, %5, %6 offen offset:2048 nt lds\n\tbuffer_load_dwordx4 %4, %5, %6 offen offset:3072 nt lds"
               :: "s"(lds), "v"(v0), "v"(v1), "v"(v2), "v"(v3), "s"(srd), "s"(soff) : "memory");
}
DI void bstore_sc1(const u32x4& d, unsigned voff, srd_t srd, unsigned soff) { asm volatile("buffer_store_dwordx4 %0, %1, %2, %3 offen sc1\n\ts_nop 1" :: "v"(d), "v"(voff), "s"(srd), "s"(soff) : "memory"); }
DI void bload_sc1(bf16x8& d, unsigned voff, srd_t srd, unsigned soff) { asm volatile("buffer_load_dwordx4 %0, %1, %2, %3 offen sc1" : "=v"(d) : "v"(voff), "s"(srd), "s"(soff) : "memory"); }
DI void breload_sc1(bf16x8& d, unsigned voff, srd_t srd, unsigned soff) { asm volatile("buffer_load_dwordx4 %0, %1, %2, %3 offen sc1" : "+v"(d) : "v"(voff), "s"(srd), "s"(soff) : "memory"); }
DI void pin(bf16x8& a) { asm volatile("" : "+v"(a)); }
DI void pin16(f32x16& a) { asm volatile("" : "+v"(a)); }
DI void pinf(float& a) { asm volatile("" : "+v"(a)); }
DI void pinu(unsigned& a) { asm volatile("" : "+v"(a)); }
template <int N> DI void wait_vm1(bf16x8& a) { asm volatile("s_waitcnt vmcnt(%1)" : "+v"(a) : "n"(N) : "memory"); }
template <int N> DI void wait_vm() { asm volatile("s_waitcnt vmcnt(%0)" :: "n"(N) : "memory"); }
template <int N> DI void wait_lgkm() { __builtin_amdgcn_s_waitcnt(0xC07F | (N << 8)); SBAR(); }
DI void bar_if(unsigned c) { asm volatile("s_cmp_eq_u32 %0, 0\n\ts_cbranch_scc1 1f\n\ts_waitcnt lgkmcnt(0)\n\ts_barrier\n1:" :: "s"(c) : "memory", "scc"); }
DI void bar() { asm volatile("s_waitcnt lgkmcnt(0)\n\ts_barrier" ::: "memory"); }
typedef short v4i16_t __attribute__((ext_vector_type(4)));
DI s16x4 trd(lds_cptr p) { return __builtin_amdgcn_ds_read_tr16_b64_v4i16((__attribute__((address_space(3))) v4i16_t*)p); }
DI bf16x8 trd2(lds_cptr p, int o0, int o1) { s16x4 a = trd(p + o0), b = trd(p + o1); return __builtin_shufflevector(a, b, 0, 1, 2, 3, 4, 5, 6, 7); }
DI bf16x8 rd128(lds_cptr p) { return *(const __attribute__((address_space(3))) bf16x8*)p; }
DI float swap_max(float m) { auto rr = __builtin_amdgcn_permlane32_swap(__float_as_uint(m), __float_as_uint(m), false, false); return fmaxf(__uint_as_float(rr[0]), __uint_as_float(rr[1])); }
DI float swap_add(float m) { auto rr = __builtin_amdgcn_permlane32_swap(__float_as_uint(m), __float_as_uint(m), false, false); return __uint_as_float(rr[0]) + __uint_as_float(rr[1]); }
DI float max16(const f32x16& s) { float a = fmaxf(fmaxf(s[0], s[1]), s[2]), b = fmaxf(fmaxf(s[3], s[4]), s[5]);
  a = fmaxf(fmaxf(a, s[6]), s[7]); b = fmaxf(fmaxf(b, s[8]), s[9]); a = fmaxf(fmaxf(a, s[10]), s[11]); b = fmaxf(fmaxf(b, s[12]), s[13]); a = fmaxf(fmaxf(a, s[14]), s[15]); return fmaxf(a, b); }
DI u32x4 pack8(const f32x16& a, int s, float f) { return u32x4{cvtpk(a[8 * s] * f, a[8 * s + 1] * f), cvtpk(a[8 * s + 2] * f, a[8 * s + 3] * f), cvtpk(a[8 * s + 4] * f, a[8 * s + 5] * f), cvtpk(a[8 * s + 6] * f, a[8 * s + 7] * f)}; }
DI u32x4 pack8(const f32x16& a, int s) { return u32x4{cvtpk(a[8 * s], a[8 * s + 1]), cvtpk(a[8 * s + 2], a[8 * s + 3]), cvtpk(a[8 * s + 4], a[8 * s + 5]), cvtpk(a[8 * s + 6], a[8 * s + 7])}; }

DI float expo(const f32x16& s, u32x4 (&p)[2]) {
  float e[16]; float s0 = 0.f, s1 = 0.f;
#pragma unroll
  for (int i = 0; i < 16; ++i) e[i] = __builtin_amdgcn_exp2f(s[i]);
#pragma unroll
  for (int i = 0; i < 16; i += 2) { s0 += e[i]; s1 += e[i + 1]; }
#pragma unroll
  for (int i = 0; i < 8; ++i) p[i >> 2][i & 3] = cvtpk(e[2 * i], e[2 * i + 1]);
  return s0 + s1;
}

#define ST_DECL
#define ST(k)
#define ST_OUT(ws, lo, hi)

constexpr unsigned SLOTK = 0x04204209u;
constexpr unsigned SLOTV = 0x31531531u;
constexpr unsigned SLOTW = 0x05429876u;
constexpr unsigned STG = 45056u, STG_W = 28672u;
constexpr unsigned O_BYTES = NB * SQ * NH * DH * 2;
constexpr unsigned X_OFF = O_BYTES, X_BYTES = 63u * 8u * 4u * 4096u;
constexpr unsigned FLAG_OFF = X_OFF + X_BYTES, FLAG_STRIDE = 128, FLAG2_OFF = FLAG_OFF + 256 * FLAG_STRIDE, EPOCH_OFF = FLAG2_OFF + 63 * 32 * FLAG_STRIDE;
#define XOFF 0
#define KA 0u
constexpr unsigned SPIN_CAP = 1u << 20;
constexpr int RA = 7, WO = 6, A0BAR = 6, PSLEEP = 4, PDELAY = 20, PDELAY2 = 25;
constexpr unsigned SPARE = 3u * STG;
constexpr unsigned CODE_BYTES = 19200u;
constexpr int lds_ops(int a, int b, bool last) { int n = 0; for (int i = a; i < b; ++i) n += i >= 16 ? (last ? 0 : 1) : (i >= 8 ? 2 : 1); return n; }
constexpr int pv_order(int i) { return i < 4 ? i : i < 8 ? i + 4 : i < 12 ? i - 4 : i; }
DI unsigned long long mix64(unsigned long long z) { z += 0x9E3779B97F4A7C15ull; z = (z ^ (z >> 30)) * 0xBF58476D1CE4E5B9ull; z = (z ^ (z >> 27)) * 0x94D049BB133111EBull; return z ^ (z >> 31); }
DI unsigned long long rfl64(unsigned long long v) { return ((unsigned long long)rfl((unsigned)(v >> 32)) << 32) | rfl((unsigned)v); }
DI unsigned long long ld_agent(const char* p) { return __hip_atomic_load((gu64*)p, __ATOMIC_RELAXED, __HIP_MEMORY_SCOPE_AGENT); }
DI void st_agent(char* p, unsigned long long v) { __hip_atomic_store((gu64*)p, v, __ATOMIC_RELAXED, __HIP_MEMORY_SCOPE_AGENT); }
DI void poll_flags(const char* p, unsigned long long tag) {
  _Pragma("nounroll") for (unsigned spins = 0; spins < SPIN_CAP; ++spins) { if (__all(ld_agent(p) == tag)) break; __builtin_amdgcn_s_sleep(PSLEEP); }
  asm volatile("" ::: "memory");
}

__global__ __launch_bounds__(512, 2) void mha_kernel(const unsigned short* __restrict__ x, const unsigned short* __restrict__ kc, const unsigned short* __restrict__ vc,
                                                     const unsigned short* __restrict__ wq, const unsigned short* __restrict__ wo, char* ws, unsigned short* __restrict__ out) {
  extern __shared__ __attribute__((aligned(16))) char lds[];
  unsigned long long pc0; asm volatile("s_getpc_b64 %0" : "=s"(pc0));
  const unsigned tid = threadIdx.x, lane = tid & 63u, r = lane & 31u, hh = lane >> 5;
  const unsigned wid = rfl(tid >> 6);
  ST_DECL ST(0);
  const unsigned bid = blockIdx.x, xcd = bid & 7u, ix = bid >> 3;
  const unsigned b = (xcd & 1u) * 4u + (ix & 3u), h = (xcd >> 1) * 8u + (ix >> 2);
  const unsigned lds0 = (unsigned)(uintptr_t)lds;
  {
    const char* cp = (const char*)(pc0 & ~255ull) + min((ix * 8u + wid) * 128u, CODE_BYTES);
    asm volatile("s_mov_b32 m0, %0\n\ts_nop 0\n\tglobal_load_lds_dword %1, %2" :: "s"(lds0 + SPARE), "v"(0u), "s"(cp) : "memory"); }
  const srd_t sW = mk_srd((const char*)wq - 1024, DM * NH * DH * 2 + 1024);
  const srd_t sX = mk_srd(x + (size_t)b * SQ * DM, SQ * DM * 2);
  const unsigned prow = (lane >> 2) & 7u, pch = 4u * hh + (lane & 3u);
  const unsigned pchx0 = 4u * hh + ((lane & 3u) ^ ((lane >> 4) & 1u)), pchx1 = pchx0 ^ 2u;
  const unsigned vW = prow * 8192u + pch * 16u, vX0 = prow * (DM * 2u) + pchx0 * 16u, vX1 = prow * (DM * 2u) + pchx1 * 16u;
  const unsigned ck0 = (QG == 3 ? 3u * (((ix & 3u) + (ix >> 2)) % 3u) : QG == 2 ? 4u * (((ix & 3u) + (ix >> 2)) & 1u) : 0u) + (XOFF ? (0x10022110u >> (4u * xcd)) & 3u : 0u), ck1 = ck0 == 8u ? 0u : ck0 + 1u;
  unsigned long long ep; asm volatile("global_load_dwordx2 %0, %1, %2 sc1" : "=v"(ep) : "v"(0u), "s"(ws + EPOCH_OFF) : "memory");
  dma4(lds0 + (wid < 7u ? wid * 4096u : SPARE), vX0, vX1 + 8192u, vX0 + 16384u, vX1 + 24576u, sX, ck0 * 128u + min(wid, 6u) * 32u * DM * 2u);
  dma4(lds0 + (wid >> 2) * STG + STG_W + (wid & 3u) * 4096u, vW + 1024u, vW + 128u, vW + 65536u - 1024u, vW + 65664u - 2048u, sW, (wid < 4u ? ck0 : ck1) * 64u * 8192u + h * 256u + (wid & 3u) * 131072u);
  const srd_t sK = mk_srd((const char*)(kc + (size_t)(b * NH + h) * SK * DH) - 1024, SK * DH * 2 + 1024);
  const srd_t sV = mk_srd((const char*)(vc + (size_t)(b * NH + h) * SK * DH) - 1024, SK * DH * 2 + 1024);
  const unsigned vK0 = prow * 256u + pchx0 * 16u, vK1 = prow * 256u + pchx1 * 16u;
  const unsigned q4 = (lane & 15u) >> 2, p4 = lane & 3u, blk = (lane >> 4) & 1u;
  const unsigned trcol = 32u * blk + 16u * (p4 & 1u) + 8u * (p4 >> 1);
  const srd_t sO = mk_srd(ws, O_BYTES);
  unsigned long long tag;
  const unsigned g4 = xcd >> 1, u0 = (xcd & 1u) * 32u + ix;
  const bool has_tile = u0 < 63u;
  const unsigned u = has_tile ? u0 : 62u, rt = u / 9u, ct = u - rt * 9u;

  if (wid == 7u) {
    auto fillQ = [&](unsigned c, unsigned stage) {
      const unsigned lw = lds0 + stage + STG_W, sw = c * 64u * 8192u + h * 256u, lx = lds0 + stage, sx = c * 128u;
#pragma unroll
      for (int i = 0; i < 4; ++i) dma4(lw + i * 4096u, vW + 1024u, vW + 128u, vW + 65536u - 1024u, vW + 65664u - 2048u, sW, sw + i * 131072u);
#pragma unroll
      for (int i = 0; i < 7; ++i) dma4(lx + i * 4096u, vX0, vX1 + 8192u, vX0 + 16384u, vX1 + 24576u, sX, sx + i * 32u * DM * 2u); };
    bar(); ST(2);
    { unsigned stage = 2 * STG, ck = ck1 == 8u ? 0u : ck1 + 1u;
#pragma nounroll
      for (unsigned c = 2; c < 9; ++c) { fillQ(ck, stage); ck = ck == 8u ? 0u : ck + 1u; stage = stage == 2 * STG ? 0u : stage + STG;
        wait_vm<44>(); bar(); ST(1 + c); } }
    const unsigned vV = prow * 256u + pch * 16u;
    const srd_t sWo = mk_srd(wo, NH * DH * DM * 2);
    const unsigned vWo = prow * (DM * 2u) + pch * 16u;
    { unsigned j = 1, jw = 0;
#pragma nounroll
      for (unsigned n = 0; n < 10; ++n) {
        if (n == 9u) { wait_vm<0>(); bar(); }
        const unsigned je = ((unsigned)(0xFFFECA8642ull >> (4u * n)) & 15u) + 1u;
#pragma nounroll
        for (; j < je; ++j) {
          if ((j & 1u) && j != 15u) { const unsigned t = (j + 1u) >> 1, ld = lds0 + ((SLOTK >> (4u * t)) & 15u) * 16384u;
#pragma unroll
            for (int i = 0; i < 4; ++i) dma4_nt(ld + i * 4096u, vK0 + 1024u, vK0 + 128u, vK1 + 1024u, vK1 + 128u, sK, t * 16384u + i * 4096u);
          } else { const unsigned t = (j - 1u) >> 1, ld = lds0 + ((SLOTV >> (4u * t)) & 15u) * 16384u;
#pragma unroll
            for (int i = 0; i < 4; ++i) dma4_nt(ld + i * 4096u, vV + 1024u, vV + 128u, vV + 1024u, vV + 128u, sV, t * 16384u + i * 4096u); } }
        const unsigned we = (unsigned)(0x8741000000ull >> (4u * n)) & 15u;
#pragma nounroll
        for (; jw < we; ++jw) { const unsigned ld = lds0 + ((SLOTW >> (4u * jw)) & 15u) * 16384u, so = (g4 * 8u + jw) * 128u * (DM * 2u) + ct * 128u;
#pragma unroll
          for (int k = 0; k < 4; ++k) dma4(ld + k * 4096u, vWo, vWo + 8192u, vWo + 16384u, vWo + 24576u, sWo, so + k * 32u * (DM * 2u)); }
        if (n == 9u) break;
        if (n < 6u) wait_vm<32>(); else if (n == 6u) wait_vm<48>(); else wait_vm<62>();
        bar(); ST(n == 0u ? 10u : 11u + n); } }
    asm volatile("" : "+v"(ep)); tag = mix64(rfl64(ep));
    ST(20); ST(21);
  } else {
  const unsigned rsw0 = 64u * (r & 7u) + 16u * (hh ^ ((r >> 2) & 3u)), rsw1 = rsw0 ^ 32u;
  f32x16 qa[4] = {}, o[4] = {};
  asm volatile("" : "+v"(qa[0]), "+v"(qa[1]), "+v"(qa[2]), "+v"(qa[3]), "+v"(o[0]), "+v"(o[1]), "+v"(o[2]), "+v"(o[3]));
  wait_lgkm<0>();
  wait_vm<4>(); if (wid < 4u) wait_vm<0>();
  ST(1);
  dma4(lds0 + STG + wid * 4096u, vX0, vX1 + 8192u, vX0 + 16384u, vX1 + 24576u, sX, ck1 * 128u + wid * 32u * DM * 2u);
  bar();
  asm volatile("" : "+v"(ep)); ep = rfl64(ep);
  ST(2);
  const lds_cptr kb0 = (lds_cptr)lds + 2048u * (r >> 3) + rsw0, kb1 = (lds_cptr)lds + 2048u * (r >> 3) + rsw1;
  const lds_cptr vb = (lds_cptr)lds + 64u * (4u * hh + q4) + trcol;
  bf16x8 qf[8], pre[RA];
  f32x16 negm, S0, S1; u32x4 P0[2], P1[2]; float l = 0.f;
  auto rdK = [&](lds_cptr k0, lds_cptr k1, int kb, int i) { return rd128(((i & 1) ? k1 : k0) + 8192 * kb + 512 * (i >> 1)); };
  auto rdV = [&](lds_cptr v, int kb, int j) { return trd2(v, 8192 * kb + 4096 * (j >> 2) + 512 * (j & 3), 8192 * kb + 4096 * (j >> 2) + 2048 + 512 * (j & 3)); };
  auto sm_fix = [&](float ps, f32x16& Sc, f32x16& Sn, u32x4 (&P)[2]) {
    if (__builtin_expect(__any(!(ps <= SUM_THR)), 0)) {
      const float dl = fmaxf(swap_max(max16(Sc)), 0.f), f = __builtin_amdgcn_exp2f(-dl);
#pragma unroll
      for (int i = 0; i < 16; ++i) { Sc[i] -= dl; Sn[i] -= dl; negm[i] -= dl; }
#pragma unroll
      for (int db = 0; db < 4; ++db)
#pragma unroll
        for (int i = 0; i < 16; ++i) o[db][i] *= f;
      l *= f; ps = expo(Sc, P); }
    l += ps; };
  { lds_cptr wp = (lds_cptr)lds + STG_W + 2048u * hh + 64u * q4 + trcol;
    lds_cptr xp0 = (lds_cptr)lds + wid * 4096u + 1024u * (r >> 3) + rsw0, xp1 = (lds_cptr)lds + wid * 4096u + 1024u * (r >> 3) + rsw1;
    bf16x8 xf[4], af[5];
    auto rdA = [&](int i) { return trd2(wp, 4096 * (i >> 2) + 512 * (i & 3), 4096 * (i >> 2) + 512 * (i & 3) + 256); };
    xf[0] = rd128(xp0); af[0] = rdA(0); af[1] = rdA(1); af[2] = rdA(2); af[3] = rdA(3);
    unsigned stage = 0;
#pragma nounroll
    for (unsigned c = 0; c < 8; ++c) {
      if (__builtin_expect(c == 3u && wid == 3u, 0)) {
#pragma unroll
        for (int i = 0; i < 4; ++i) dma4_nt(lds0 + 9u * 16384u + i * 4096u, vK0 + 1024u, vK0 + 128u, vK1 + 1024u, vK1 + 128u, sK, i * 4096u);
        unsigned ln = lane; asm volatile("" : "+v"(ln));
        const unsigned k = ln & 7u, sel = ln >> 3;
        const char* wup = sel == 0u ? (const char*)vc + (size_t)(b * NH + h) * (SK * DH * 2) + k * 16384u
                        : sel == 1u ? (const char*)wo + (size_t)(g4 * 1024u + k * 128u) * (DM * 2u) + ct * 128u
                        : sel == 2u ? (const char*)out + (size_t)((8u * rt + k) * 32u) * (DM * 2u)
                        : ws + (sel == 3u ? ((b * 7u + k) * NH + h) * 8192u : sel == 4u ? ((8u * rt + k) * NH + g4 * 8u) * 8192u : sel == 5u ? X_OFF + u * 131072u + k * 16384u : FLAG_OFF + k * 32768u + 64u);
        asm volatile("s_mov_b32 m0, %0\n\ts_nop 0\n\tglobal_load_lds_dword %1, off sc1" :: "s"(lds0 + SPARE), "v"(wup) : "memory"); }
      if (KA && __builtin_expect(c >= 4u && c < 7u && wid == 3u, 0))
        asm volatile("s_mov_b32 m0, %0\n\ts_nop 0\n\tbuffer_load_dword %1, %2, %3 offen nt lds" :: "s"(lds0 + SPARE), "v"((lane & (KA - 1u)) * 128u), "s"(sK), "s"(17408u + (c - 4u) * KA * 128u) : "memory");
      const unsigned nstage = stage == 2 * STG ? 0u : stage + STG; const int dlt = (int)nstage - (int)stage; stage = nstage;
      SBAR();
      static_for<12>([&](auto I) { constexpr int i = I.value;
        qa[i & 3] = MFMA(af[i % 5], xf[i >> 2], qa[i & 3]);
        af[(i + 4) % 5] = rdA(i + 4);
        if constexpr (i == 0) xf[1] = rd128(xp1); if constexpr (i == 3) xf[2] = rd128(xp0 + 512); if constexpr (i == 7) xf[3] = rd128(xp1 + 512);
        SBAR(); });
      qa[0] = MFMA(af[12 % 5], xf[3], qa[0]);
      wp += dlt; xp0 += dlt; xp1 += dlt; asm volatile("" : "+v"(wp), "+v"(xp0), "+v"(xp1));
      SBAR();
      asm volatile("s_bitcmp1_b32 %0, %1\n\ts_cbranch_scc1 1f\n\ts_waitcnt vmcnt(0)\n1:\n\ts_waitcnt lgkmcnt(0)\n\ts_barrier" :: "s"(0x78u), "s"(c) : "memory", "scc");
      const bf16x8 xl = xf[3], a13 = af[13 % 5], a14 = af[14 % 5], a15 = af[15 % 5];
      xf[0] = rd128(xp0); af[0] = rdA(0); af[1] = rdA(1);
      SBAR();
      qa[1] = MFMA(a13, xl, qa[1]); SBAR();
      af[2] = rdA(2);
      qa[2] = MFMA(a14, xl, qa[2]); SBAR();
      af[3] = rdA(3);
      qa[3] = MFMA(a15, xl, qa[3]); SBAR();
      ST(3 + c);
    }
    lds_cptr k0 = kb0 + 9 * 16384, k1 = kb1 + 9 * 16384; asm volatile("" : "+v"(k0), "+v"(k1));
    bf16x8 ag[16], f[16]; u32x4 qw[8]; float qs = QSCALE; asm volatile("" : "+s"(qs));
#pragma unroll
    for (int i = 0; i < 4; ++i) ag[i] = af[i];
    auto cvq = [&](int j) { float lo = qa[j >> 3][2 * (j & 7)] * qs; pinf(lo); float hi = qa[j >> 3][2 * (j & 7) + 1] * qs; unsigned w = cvtpk(lo, hi); pinu(w); qw[j >> 2][j & 3] = w; };
    SBAR();
    static_for<24>([&](auto G) { constexpr int g = G.value, n = g + 4;
      if constexpr (g < 4) qa[g] = MFMA(ag[g], xf[0], qa[g]);
      else if constexpr (g < 16) qa[(g - 4) / 3] = MFMA(ag[g], xf[1 + (g - 4) % 3], qa[(g - 4) / 3]);
      else { qf[g - 16] = __builtin_bit_cast(bf16x8, qw[g - 16]);
        if constexpr (g == 16) { const f32x16 z = {}; S0 = MFMA(f[0], qf[0], z); } else S0 = MFMA(f[g - 16], qf[g - 16], S0); pin16(S0); }
      if constexpr (g == 0) { xf[1] = rd128(xp1); xf[2] = rd128(xp0 + 512); } if constexpr (g == 1) xf[3] = rd128(xp1 + 512);
      if constexpr (n < 16) ag[n] = rdA(4 * (1 + (n - 4) % 3) + (n - 4) / 3); else f[n - 16] = rdK(k0, k1, (n - 16) >> 3, (n - 16) & 7);
      if constexpr (g >= 9 && g < 21) { constexpr int j0 = (g - 9) * 8 / 3, j1 = (g - 8) * 8 / 3; static_for<j1 - j0>([&](auto J) { cvq(j0 + J.value); }); }
      SBAR(); });
    ST(11);
#pragma unroll
    for (int i = 12; i < 16; ++i) f[i] = rdK(k0, k1, 1, i & 7);
    const float m = swap_max(max16(S0));
#pragma unroll
    for (int i = 0; i < 16; ++i) negm[i] = -m;
    SBAR();
    float e[16]; float s0 = 0.f, s1 = 0.f;
    static_for<9>([&](auto G) { constexpr int g = G.value;
      if constexpr (g == 0) { if (wid >= 4u) bar(); }
      if constexpr (g == A0BAR) { if (wid < 4u) bar();
        constexpr unsigned ko = ((SLOTK >> 4) & 15u) * 16384u;
#pragma unroll
        for (int i = 0; i < RA; ++i) pre[i] = rdK(kb0 + ko, kb1 + ko, 0, i);
        SBAR(); }
      if constexpr (g == 0) S1 = MFMA(f[8], qf[0], negm); else if constexpr (g < 8) S1 = MFMA(f[8 + g], qf[g], S1);
      if constexpr (g < 8) pin16(S1);
      if constexpr (g < 8) { e[2 * g] = __builtin_amdgcn_exp2f(S0[2 * g] - m); e[2 * g + 1] = __builtin_amdgcn_exp2f(S0[2 * g + 1] - m); pinf(e[2 * g]); pinf(e[2 * g + 1]); }
      if constexpr (g == 1) { s0 = e[0]; s1 = e[1]; } else if constexpr (g > 1) { s0 += e[2 * g - 2]; pinf(s0); s1 += e[2 * g - 1]; pinf(s1); }
      if constexpr (g >= 1) { unsigned w = cvtpk(e[2 * g - 2], e[2 * g - 1]); pinu(w); P0[(g - 1) >> 2][(g - 1) & 3] = w; }
      SBAR(); });
    l = s0 + s1; }
  ST(12);
  auto unit = [&](auto KB, auto LAST, lds_cptr k0, lds_cptr k1, lds_cptr v, lds_cptr nk0, lds_cptr nk1, f32x16& Sn, f32x16& Sc, const u32x4 (&Pp)[2], u32x4 (&Pc)[2]) {
    constexpr int kb = KB.value; constexpr bool last = LAST.value;
    float e[16]; float s0 = 0.f, s1 = 0.f; bf16x8 f[16 + RA];
#pragma unroll
    for (int i = 0; i < RA; ++i) f[i] = pre[i];
    static_for<16>([&](auto G) { constexpr int g = G.value; constexpr int n = g + RA; constexpr int gb = 12;
      if constexpr (last && g == gb) { bar(); wait_lgkm<0>(); }
      else if constexpr ((g & 3) == 0) wait_lgkm<lds_ops(g + 4, g + RA, last)>();
      if constexpr (g == 0) Sn = MFMA(f[0], qf[0], negm); else if constexpr (g < 8) Sn = MFMA(f[g], qf[g], Sn);
      else o[g & 3] = MFMA(f[g], __builtin_bit_cast(bf16x8, Pp[(g >> 2) & 1]), o[g & 3]);
      if constexpr (n < 8) f[n] = rdK(k0, k1, kb, n); else if constexpr (n < 16) f[n] = rdV(v, kb, n - 8);
      else if constexpr (!last) f[n] = rdK(k0, k1, kb + 1, n - 16);
      else if constexpr (g >= gb) { constexpr int per = (RA + 15 - gb) / (16 - gb); static_for<per>([&](auto J) { constexpr int i = per * (g - gb) + J.value; if constexpr (i < RA) f[16 + i] = rdK(nk0, nk1, 0, i); }); }
      if constexpr (g == 0) { e[0] = __builtin_amdgcn_exp2f(Sc[0]); pinf(e[0]); }
      if constexpr (g < 15) { e[g + 1] = __builtin_amdgcn_exp2f(Sc[g + 1]); pinf(e[g + 1]); }
      if constexpr (g == 1) { s0 = e[0]; s1 = e[1]; } else if constexpr (g > 1) { if constexpr (g & 1) { s1 += e[g]; pinf(s1); } else { s0 += e[g]; pinf(s0); } }
      if constexpr (g & 1) { unsigned w = cvtpk(e[g - 1], e[g]); pinu(w); Pc[g >> 3][(g >> 1) & 3] = w; }
      SBAR(); });
#pragma unroll
    for (int i = 0; i < RA; ++i) pre[i] = f[16 + i];
    sm_fix(s0 + s1, Sc, Sn, Pc); };
#pragma nounroll
  for (unsigned t = 1; t < 7; ++t) {
    const unsigned ko = ((SLOTK >> (4u * t)) & 15u) * 16384u, vo = ((SLOTV >> (4u * (t - 1u))) & 15u) * 16384u, nko = ((SLOTK >> (4u * ((t + 1u) & 7u))) & 15u) * 16384u;
    SBAR();
    unit(std::integral_constant<int, 0>{}, std::false_type{}, kb0 + ko, kb1 + ko, vb + vo, kb0, kb1, S0, S1, P0, P1);
    unit(std::integral_constant<int, 1>{}, std::true_type{}, kb0 + ko, kb1 + ko, vb + vo, kb0 + nko, kb1 + nko, S1, S0, P1, P0);
    if (t == 1u || t == 6u) ST(12 + t);
  }
  const unsigned me = (b * 7u + wid) * NH + h;
  {
    constexpr unsigned ko = ((SLOTK >> 28) & 15u) * 16384u;
    const lds_cptr k0 = kb0 + ko, k1 = kb1 + ko, v6 = vb + ((SLOTV >> 24) & 15u) * 16384u, v7 = vb + ((SLOTV >> 28) & 15u) * 16384u;
    u32x4 P2[2], P3[2];
    auto scaleP = [&](u32x4 (&P)[2], float f) {
#pragma unroll
      for (int i = 0; i < 8; ++i) { const unsigned w = P[i >> 2][i & 3]; P[i >> 2][i & 3] = cvtpk(__uint_as_float(w << 16) * f, __uint_as_float(w & 0xFFFF0000u) * f); } };
    auto fix = [&](auto NP, float ps, f32x16& Sc, f32x16& Sn, u32x4 (&P)[2]) {
      if (__builtin_expect(__any(!(ps <= SUM_THR)), 0)) {
        const float f = __builtin_amdgcn_exp2f(-fmaxf(swap_max(max16(Sc)), 0.f));
        scaleP(P0, f); if constexpr (NP.value > 1) scaleP(P1, f); if constexpr (NP.value > 2) scaleP(P2, f); }
      sm_fix(ps, Sc, Sn, P); };
    auto rdVt = [&](int i) { const int j = pv_order(i), db = j >> 3, un = (j >> 1) & 3, ks = j & 1; return rdV(un < 2 ? v6 : v7, un & 1, 4 * ks + db); };
    auto pvt = [&](auto I, const bf16x8& vf) { constexpr int j = pv_order(I.value), un = (j >> 1) & 3;
      const u32x4 pb = un == 0 ? P0[j & 1] : un == 1 ? P1[j & 1] : un == 2 ? P2[j & 1] : P3[j & 1];
      o[j >> 3] = MFMA(vf, __builtin_bit_cast(bf16x8, pb), o[j >> 3]); pin16(o[j >> 3]); };
    bf16x8 f[16], w[32];
#pragma unroll
    for (int i = 0; i < RA; ++i) f[i] = pre[i];
    auto qsm = [&](auto H, f32x16& Sn, const f32x16& Sc, u32x4 (&Pc)[2]) { constexpr int hf = H.value;
      float e[16]; float s0 = 0.f, s1 = 0.f;
      static_for<9>([&](auto G) { constexpr int g = G.value, n = 8 * hf + g + RA;
        if constexpr (hf < 2 && g < 8) { if constexpr (g == 0) Sn = MFMA(f[8 * hf], qf[0], negm); else Sn = MFMA(f[8 * hf + g], qf[g], Sn);
          pin16(Sn);
          if constexpr (n < 16) f[n] = rdK(k0, k1, n >> 3, n & 7); }
        if constexpr (hf == 1 && g >= 2 && g < 8) w[g - 2] = rdVt(g - 2);
        if constexpr (hf == 2 && g < 8) { pvt(G, w[g]); w[g + 6] = rdVt(g + 6); }
        if constexpr (g < 8) { e[2 * g] = __builtin_amdgcn_exp2f(Sc[2 * g]); e[2 * g + 1] = __builtin_amdgcn_exp2f(Sc[2 * g + 1]); pinf(e[2 * g]); pinf(e[2 * g + 1]); }
        if constexpr (g == 1) { s0 = e[0]; s1 = e[1]; } else if constexpr (g > 1) { s0 += e[2 * g - 2]; pinf(s0); s1 += e[2 * g - 1]; pinf(s1); }
        if constexpr (g >= 1) { unsigned pw = cvtpk(e[2 * g - 2], e[2 * g - 1]); pinu(pw); Pc[(g - 1) >> 2][(g - 1) & 3] = pw; }
        SBAR(); });
      return s0 + s1; };
    if (wid < 4u) __builtin_amdgcn_s_setprio(1);
    SBAR();
    fix(std::integral_constant<int, 1>{}, qsm(std::integral_constant<int, 0>{}, S0, S1, P1), S1, S0, P1);
    bar_if(wid >> 2);
    fix(std::integral_constant<int, 2>{}, qsm(std::integral_constant<int, 1>{}, S1, S0, P2), S0, S1, P2);
    bar_if((wid >> 2) ^ 1u);
    ST(19);
    fix(std::integral_constant<int, 3>{}, qsm(std::integral_constant<int, 2>{}, S0, S1, P3), S1, S0, P3);
    const float inv = __builtin_amdgcn_rcpf(swap_add(l));
    auto put = [&](int k8) { bstore_sc1(pack8(o[k8 >> 1], k8 & 1, inv), lane * 16u, sO, me * 8192u + k8 * 1024u); };
    static_for<24>([&](auto G) { constexpr int g = G.value + 8;
      pvt(std::integral_constant<int, g>{}, w[g]);
      if constexpr (g + 6 < 32) w[g + 6] = rdVt(g + 6);
      if constexpr (g == 14) put(0); if constexpr (g == 16) put(1); if constexpr (g == 19) put(2); if constexpr (g == 22) put(3); if constexpr (g == 26) put(4); if constexpr (g == 29) put(5);
      SBAR(); });
    ST(20);
    put(6); put(7); }
  ST(21);
  tag = mix64(ep);
  __builtin_amdgcn_s_setprio(0);
  }
  if (wid < 7u) { wait_vm<0>();
  ST(22);
  bar(); }
  if (tid == 0) st_agent(ws + FLAG_OFF + (b * NH + h) * FLAG_STRIDE, tag);
  ST(23);
  if (!has_tile) {
    if (bid == 249u && wid == 0u) {
#pragma nounroll
      for (unsigned i = 0; i < 4; ++i) poll_flags(ws + FLAG_OFF + (i * 64u + lane) * FLAG_STRIDE, tag);
      if (lane == 0) st_agent(ws + EPOCH_OFF, tag); }
    ST(24); ST(25); ST(26); ST(27); ST(28); ST(29); ST(30); ST(31); ST_OUT(ws, 0, 32); return; }
  const unsigned jq = (2u * g4 + 2u + wid) & 7u, G = 8u * rt + jq;
  const unsigned voffO = lane * 16u, soO = (G * NH + g4 * 8u) * 8192u;
  const char* fp = ws + FLAG_OFF + (((G * 37u) >> 8) * NH + g4 * 8u + (lane & 7u)) * FLAG_STRIDE;
  unsigned ready = 0;
  auto need = [&](unsigned m) {
    _Pragma("nounroll") for (unsigned spins = 0; (ready & m) != m && spins < SPIN_CAP; ++spins) { ready = (unsigned)__ballot(ld_agent(fp) == tag); if ((ready & m) != m) __builtin_amdgcn_s_sleep(PSLEEP); }
    asm volatile("" ::: "memory"); };
  __builtin_amdgcn_s_sleep(PDELAY);
  need(1u);
  ST(24);
  const lds_cptr wb = (lds_cptr)lds + 1024u * hh + 64u * q4 + trcol;
  f32x16 acc[2] = {};
  { unsigned hd = 0, so_c = soO + hd * 8192u; lds_cptr wp = wb + ((SLOTW >> (4u * hd)) & 15u) * 16384u;
    bf16x8 of[8], lf[4][2];
    auto rdW = [&](int s2) { lf[s2 & 3][0] = trd2(wp, (s2 & 7) * 2048, (s2 & 7) * 2048 + 256); lf[s2 & 3][1] = trd2(wp, (s2 & 7) * 2048 + 512, (s2 & 7) * 2048 + 768); };
#pragma unroll
    for (int i = 0; i < 8; ++i) { if (i < WO) bload_sc1(of[i], voffO, sO, so_c + i * 1024u); else of[i] = bf16x8{}; }
    rdW(0); rdW(1);
#pragma nounroll
    for (unsigned p = 0; p < 8; ++p) {
      hd = (hd + 1u) & 7u;
      if (p == 5u) __builtin_amdgcn_s_barrier();
      const unsigned so_n = p < 7u ? soO + hd * 8192u : O_BYTES;
      static_for<8>([&](auto S) { constexpr int s2 = S.value;
        if constexpr (s2 == 8 - WO) need(1u << hd);
        if constexpr (s2 == 6) wp = wb + ((SLOTW >> (4u * hd)) & 15u) * 16384u;
        wait_vm1<WO - 1>(of[s2]);
        wait_lgkm<4>();
        acc[0] = MFMA(lf[s2 & 3][0], of[s2], acc[0]);
        rdW(s2 + 2);
        SBAR();
        acc[1] = MFMA(lf[s2 & 3][1], of[s2], acc[1]);
        SBAR();
        breload_sc1(of[(s2 + WO) & 7], voffO, sO, (s2 + WO < 8 ? so_c : so_n) + ((s2 + WO) & 7) * 1024u);
        SBAR(); });
      so_c = so_n;
    }
    wait_vm<0>();
#pragma unroll
    for (int i = 0; i < 8; ++i) pin(of[i]); }
  ST(28);
  const srd_t sX2 = mk_srd(ws + X_OFF, X_BYTES);
  const unsigned xb = (u * 8u + jq) * 4u;
  if (wid < 6u) {
#pragma unroll
    for (int i = 0; i < 4; ++i) bstore_sc1(pack8(acc[i >> 1], i & 1), voffO, sX2, (xb + g4) * 4096u + i * 1024u);
    wait_vm<0>();
    if (lane == 0) st_agent(ws + FLAG2_OFF + (xb + g4) * FLAG_STRIDE, tag);
    ST(29); ST(30);
  } else {
    __builtin_amdgcn_s_sleep(PDELAY2);
    poll_flags(ws + FLAG2_OFF + (xb + ((g4 + 1u + lane % 3u) & 3u)) * FLAG_STRIDE, tag);
    ST(29);
    bf16x8 xp[12];
#pragma unroll
    for (int i = 0; i < 12; ++i) bload_sc1(xp[i], voffO, sX2, (xb + ((g4 + 1u + (i >> 2)) & 3u)) * 4096u + (i & 3) * 1024u);
    wait_vm<0>();
#pragma unroll
    for (int j = 0; j < 12; ++j) { const int i = j & 3; pin(xp[j]); const u32x4 v = __builtin_bit_cast(u32x4, xp[j]);
#pragma unroll
      for (int e = 0; e < 4; ++e) { acc[i >> 1][8 * (i & 1) + 2 * e] += __uint_as_float(v[e] << 16); acc[i >> 1][8 * (i & 1) + 2 * e + 1] += __uint_as_float(v[e] & 0xFFFF0000u); } }
    ST(30);
    char* op = (char*)out + (size_t)(G * 32u + r) * (DM * 2u) + ct * 128u + hh * 16u;
#pragma unroll
    for (int i = 0; i < 4; ++i) *(u32x4*)(op + i * 32) = pack8(acc[i >> 1], i & 1);
  }
  ST(31); ST_OUT(ws, 0, 32);
}

extern "C" void kernel_launch(void* const* d_in, const int* in_sizes, int n_in, void* d_out, int out_size, void* d_ws, size_t ws_size, hipStream_t stream) {
  (void)in_sizes; (void)n_in; (void)out_size; (void)ws_size;
  (void)hipFuncSetAttribute((const void*)mha_kernel, hipFuncAttributeMaxDynamicSharedMemorySize, LDS_BYTES);
  const unsigned short* x = (const unsigned short*)d_in[0]; const unsigned short* kc = (const unsigned short*)d_in[1]; const unsigned short* vc = (const unsigned short*)d_in[2];
  const unsigned short* wq = (const unsigned short*)d_in[3]; const unsigned short* wo = (const unsigned short*)d_in[4];
  mha_kernel<<<256, 512, LDS_BYTES, stream>>>(x, kc, vc, wq, wo, (char*)d_ws, (unsigned short*)d_out);
}
```

```cpp
#include <hip/hip_runtime.h>
#include <stdint.h>
#include <utility>

typedef short    bf16x8 __attribute__((ext_vector_type(8)));
typedef short    s16x4  __attribute__((ext_vector_type(4)));
typedef float    f32x16 __attribute__((ext_vector_type(16)));
typedef float    f32x2  __attribute__((ext_vector_type(2)));
typedef __bf16   bf16x2 __attribute__((ext_vector_type(2)));
typedef unsigned u32x4  __attribute__((ext_vector_type(4)));
typedef __attribute__((address_space(3))) const char* lds_cptr;
typedef __attribute__((address_space(1))) unsigned long long gu64;
typedef __amdgpu_buffer_rsrc_t srd_t;

#define DI __device__ __forceinline__
#define MFMA(a, b, c) __builtin_amdgcn_mfma_f32_32x32x16_bf16((a), (b), (c), 0, 0, 0)
#define SBAR() __builtin_amdgcn_sched_barrier(0)

constexpr int NB = 8, SQ = 224, DM = 576, NH = 32, SK = 512, DH = 128;
constexpr int LDS_BYTES = 163840;
constexpr float QSCALE = 0.08838834764831845f * 1.4426950408889634f;
constexpr float SUM_THR = 4096.f;

template <int... Is, class F> DI void static_for_impl(std::integer_sequence<int, Is...>, F&& f) { (f(std::integral_constant<int, Is>{}), ...); }
template <int N, class F> DI void static_for(F&& f) { static_for_impl(std::make_integer_sequence<int, N>{}, f); }

DI unsigned rfl(unsigned x) { return __builtin_amdgcn_readfirstlane(x); }
DI unsigned cvtpk(float lo, float hi) { f32x2 v = {lo, hi}; bf16x2 b = __builtin_convertvector(v, bf16x2); return __builtin_bit_cast(unsigned, b); }
DI srd_t mk_srd(const void* p, unsigned bytes) { return __builtin_amdgcn_make_buffer_rsrc((void*)p, (short)0, (int)bytes, 0x00020000); }
DI void dma4(unsigned lds, unsigned v0, unsigned v1, unsigned v2, unsigned v3, srd_t srd, unsigned soff) {
  asm volatile("s_mov_b32 m0, %0\n\ts_nop 0\n\tbuffer_load_dwordx4 %1, %5, %6 offen lds\n\tbuffer_load_dwordx4 %2, %5, %6 offen offset:1024 lds\n\t"
               "buffer_load_dwordx4 %3, %5, %6 offen offset:2048 lds\n\tbuffer_load_dwordx4 %4, %5, %6 offen offset:3072 lds"
               :: "s"(lds), "v"(v0), "v"(v1), "v"(v2), "v"(v3), "s"(srd), "s"(soff) : "memory");
}
DI void dma4_nt(unsigned lds, unsigned v0, unsigned v1, unsigned v2, unsigned v3, srd_t srd, unsigned soff) {
  asm volatile("s_mov_b32 m0, %0\n\ts_nop 0\n\tbuffer_load_dwordx4 %1, %5, %6 offen nt lds\n\tbuffer_load_dwordx4 %2, %5, %6 offen offset:1024 nt lds\n\t"
               "buffer_load_dwordx4 %3, %5, %6 offen offset:2048 nt lds\n\tbuffer_load_dwordx4 %4, %5, %6 offen offset:3072 nt lds"
               :: "s"(lds), "v"(v0), "v"(v1), "v"(v2), "v"(v3), "s"(srd), "s"(soff) : "memory");
}
DI void bstore_sc1(const u32x4& d, unsigned voff, srd_t srd, unsigned soff) { asm volatile("buffer_store_dwordx4 %0, %1, %2, %3 offen sc1\n\ts_nop 1" :: "v"(d), "v"(voff), "s"(srd), "s"(soff) : "memory"); }
DI void bload_sc1(bf16x8& d, unsigned voff, srd_t srd, unsigned soff) { asm volatile("buffer_load_dwordx4 %0, %1, %2, %3 offen sc1" : "=v"(d) : "v"(voff), "s"(srd), "s"(soff) : "memory"); }
DI void breload_sc1(bf16x8& d, unsigned voff, srd_t srd, unsigned soff) { asm volatile("buffer_load_dwordx4 %0, %1, %2, %3 offen sc1" : "+v"(d) : "v"(voff), "s"(srd), "s"(soff) : "memory"); }
DI void pin(bf16x8& a) { asm volatile("" : "+v"(a)); }
DI void pin16(f32x16& a) { asm volatile("" : "+v"(a)); }
DI void pinf(float& a) { asm volatile("" : "+v"(a)); }
DI void pinu(unsigned& a) { asm volatile("" : "+v"(a)); }
template <int N> DI void wait_vm1(bf16x8& a) { asm volatile("s_waitcnt vmcnt(%1)" : "+v"(a) : "n"(N) : "memory"); }
template <int N> DI void wait_vm() { asm volatile("s_waitcnt vmcnt(%0)" :: "n"(N) : "memory"); }
template <int N> DI void wait_lgkm() { __builtin_amdgcn_s_waitcnt(0xC07F | (N << 8)); SBAR(); }
DI void bar_if(unsigned c) { asm volatile("s_cmp_eq_u32 %0, 0\n\ts_cbranch_scc1 1f\n\ts_waitcnt lgkmcnt(0)\n\ts_barrier\n1:" :: "s"(c) : "memory", "scc"); }
DI void bar() { asm volatile("s_waitcnt lgkmcnt(0)\n\ts_barrier" ::: "memory"); }
typedef short v4i16_t __attribute__((ext_vector_type(4)));
DI s16x4 trd(lds_cptr p) { return __builtin_amdgcn_ds_read_tr16_b64_v4i16((__attribute__((address_space(3))) v4i16_t*)p); }
DI bf16x8 trd2(lds_cptr p, int o0, int o1) { s16x4 a = trd(p + o0), b = trd(p + o1); return __builtin_shufflevector(a, b, 0, 1, 2, 3, 4, 5, 6, 7); }
DI bf16x8 rd128(lds_cptr p) { return *(const __attribute__((address_space(3))) bf16x8*)p; }
DI float swap_max(float m) { auto rr = __builtin_amdgcn_permlane32_swap(__float_as_uint(m), __float_as_uint(m), false, false); return fmaxf(__uint_as_float(rr[0]), __uint_as_float(rr[1])); }
DI float swap_add(float m) { auto rr = __builtin_amdgcn_permlane32_swap(__float_as_uint(m), __float_as_uint(m), false, false); return __uint_as_float(rr[0]) + __uint_as_float(rr[1]); }
DI float max16(const f32x16& s) { float a = fmaxf(fmaxf(s[0], s[1]), s[2]), b = fmaxf(fmaxf(s[3], s[4]), s[5]);
  a = fmaxf(fmaxf(a, s[6]), s[7]); b = fmaxf(fmaxf(b, s[8]), s[9]); a = fmaxf(fmaxf(a, s[10]), s[11]); b = fmaxf(fmaxf(b, s[12]), s[13]); a = fmaxf(fmaxf(a, s[14]), s[15]); return fmaxf(a, b); }
DI u32x4 pack8(const f32x16& a, int s, float f) { return u32x4{cvtpk(a[8 * s] * f, a[8 * s + 1] * f), cvtpk(a[8 * s + 2] * f, a[8 * s + 3] * f), cvtpk(a[8 * s + 4] * f, a[8 * s + 5] * f), cvtpk(a[8 * s + 6] * f, a[8 * s + 7] * f)}; }
DI u32x4 pack8(const f32x16& a, int s) { return u32x4{cvtpk(a[8 * s], a[8 * s + 1]), cvtpk(a[8 * s + 2], a[8 * s + 3]), cvtpk(a[8 * s + 4], a[8 * s + 5]), cvtpk(a[8 * s + 6], a[8 * s + 7])}; }

DI float expo(const f32x16& s, u32x4 (&p)[2]) {
  float e[16]; float s0 = 0.f, s1 = 0.f;
#pragma unroll
  for (int i = 0; i < 16; ++i) e[i] = __builtin_amdgcn_exp2f(s[i]);
#pragma unroll
  for (int i = 0; i < 16; i += 2) { s0 += e[i]; s1 += e[i + 1]; }
#pragma unroll
  for (int i = 0; i < 8; ++i) p[i >> 2][i & 3] = cvtpk(e[2 * i], e[2 * i + 1]);
  return s0 + s1;
}

constexpr unsigned SLOTK = 0x04204209u;
constexpr unsigned SLOTV = 0x31531531u;
constexpr unsigned SLOTW = 0x05429876u;
constexpr unsigned STG = 45056u, STG_W = 28672u;
constexpr unsigned O_BYTES = NB * SQ * NH * DH * 2;
constexpr unsigned X_OFF = O_BYTES, X_BYTES = 63u * 8u * 4u * 4096u;
constexpr unsigned FLAG_OFF = X_OFF + X_BYTES, FLAG_STRIDE = 128, FLAG2_OFF = FLAG_OFF + 256 * FLAG_STRIDE, EPOCH_OFF = FLAG2_OFF + 63 * 32 * FLAG_STRIDE;
constexpr unsigned SPIN_CAP = 1u << 20;
constexpr int RA = 7, WO = 6, A0BAR = 6, PSLEEP = 4, PDELAY = 20, PDELAY2 = 25;
constexpr unsigned SPARE = 3u * STG;
constexpr unsigned CODE_BYTES = 19200u;
constexpr int lds_ops(int a, int b, bool last) { int n = 0; for (int i = a; i < b; ++i) n += i >= 16 ? (last ? 0 : 1) : (i >= 8 ? 2 : 1); return n; }
constexpr int pv_order(int i) { return i < 4 ? i : i < 8 ? i + 4 : i < 12 ? i - 4 : i; }
DI unsigned long long mix64(unsigned long long z) { z += 0x9E3779B97F4A7C15ull; z = (z ^ (z >> 30)) * 0xBF58476D1CE4E5B9ull; z = (z ^ (z >> 27)) * 0x94D049BB133111EBull; return z ^ (z >> 31); }
DI unsigned long long rfl64(unsigned long long v) { return ((unsigned long long)rfl((unsigned)(v >> 32)) << 32) | rfl((unsigned)v); }
DI unsigned long long ld_agent(const char* p) { return __hip_atomic_load((gu64*)p, __ATOMIC_RELAXED, __HIP_MEMORY_SCOPE_AGENT); }
DI void st_agent(char* p, unsigned long long v) { __hip_atomic_store((gu64*)p, v, __ATOMIC_RELAXED, __HIP_MEMORY_SCOPE_AGENT); }
DI void poll_flags(const char* p, unsigned long long tag) {
  _Pragma("nounroll") for (unsigned spins = 0; spins < SPIN_CAP; ++spins) { if (__all(ld_agent(p) == tag)) break; __builtin_amdgcn_s_sleep(PSLEEP); }
  asm volatile("" ::: "memory");
}

__global__ __launch_bounds__(512, 2) void mha_kernel(const unsigned short* __restrict__ x, const unsigned short* __restrict__ kc, const unsigned short* __restrict__ vc,
                                                     const unsigned short* __restrict__ wq, const unsigned short* __restrict__ wo, char* ws, unsigned short* __restrict__ out) {
  extern __shared__ __attribute__((aligned(16))) char lds[];
  unsigned long long pc0; asm volatile("s_getpc_b64 %0" : "=s"(pc0));
  const unsigned tid = threadIdx.x, lane = tid & 63u, r = lane & 31u, hh = lane >> 5;
  const unsigned wid = rfl(tid >> 6);
  const unsigned bid = blockIdx.x, xcd = bid & 7u, ix = bid >> 3;
  const unsigned b = (xcd & 1u) * 4u + (ix & 3u), h = (xcd >> 1) * 8u + (ix >> 2);
  const unsigned lds0 = (unsigned)(uintptr_t)lds;
  {
    const char* cp = (const char*)(pc0 & ~255ull) + min((ix * 8u + wid) * 128u, CODE_BYTES);
    asm volatile("s_mov_b32 m0, %0\n\ts_nop 0\n\tglobal_load_lds_dword %1, %2" :: "s"(lds0 + SPARE), "v"(0u), "s"(cp) : "memory"); }
  const srd_t sW = mk_srd((const char*)wq - 1024, DM * NH * DH * 2 + 1024);
  const srd_t sX = mk_srd(x + (size_t)b * SQ * DM, SQ * DM * 2);
  const unsigned prow = (lane >> 2) & 7u, pch = 4u * hh + (lane & 3u);
  const unsigned pchx0 = 4u * hh + ((lane & 3u) ^ ((lane >> 4) & 1u)), pchx1 = pchx0 ^ 2u;
  const unsigned vW = prow * 8192u + pch * 16u, vX0 = prow * (DM * 2u) + pchx0 * 16u, vX1 = prow * (DM * 2u) + pchx1 * 16u;
  const unsigned ck0 = 3u * (((ix & 3u) + (ix >> 2)) % 3u);
  unsigned long long ep; asm volatile("global_load_dwordx2 %0, %1, %2 sc1" : "=v"(ep) : "v"(0u), "s"(ws + EPOCH_OFF) : "memory");
  dma4(lds0 + (wid < 7u ? wid * 4096u : SPARE), vX0, vX1 + 8192u, vX0 + 16384u, vX1 + 24576u, sX, ck0 * 128u + min(wid, 6u) * 32u * DM * 2u);
  dma4(lds0 + (wid >> 2) * STG + STG_W + (wid & 3u) * 4096u, vW + 1024u, vW + 128u, vW + 65536u - 1024u, vW + 65664u - 2048u, sW, (ck0 + (wid >> 2)) * 64u * 8192u + h * 256u + (wid & 3u) * 131072u);
  const srd_t sK = mk_srd((const char*)(kc + (size_t)(b * NH + h) * SK * DH) - 1024, SK * DH * 2 + 1024);
  const srd_t sV = mk_srd((const char*)(vc + (size_t)(b * NH + h) * SK * DH) - 1024, SK * DH * 2 + 1024);
  const unsigned vK0 = prow * 256u + pchx0 * 16u, vK1 = prow * 256u + pchx1 * 16u;
  const unsigned q4 = (lane & 15u) >> 2, p4 = lane & 3u, blk = (lane >> 4) & 1u;
  const unsigned trcol = 32u * blk + 16u * (p4 & 1u) + 8u * (p4 >> 1);
  const srd_t sO = mk_srd(ws, O_BYTES);
  unsigned long long tag;
  const unsigned g4 = xcd >> 1, u0 = (xcd & 1u) * 32u + ix;
  const bool has_tile = u0 < 63u;
  const unsigned u = has_tile ? u0 : 62u, rt = u / 9u, ct = u - rt * 9u;

  if (wid == 7u) {
    auto fillQ = [&](unsigned c, unsigned stage) {
      const unsigned lw = lds0 + stage + STG_W, sw = c * 64u * 8192u + h * 256u, lx = lds0 + stage, sx = c * 128u;
#pragma unroll
      for (int i = 0; i < 4; ++i) dma4(lw + i * 4096u, vW + 1024u, vW + 128u, vW + 65536u - 1024u, vW + 65664u - 2048u, sW, sw + i * 131072u);
#pragma unroll
      for (int i = 0; i < 7; ++i) dma4(lx + i * 4096u, vX0, vX1 + 8192u, vX0 + 16384u, vX1 + 24576u, sX, sx + i * 32u * DM * 2u); };
    bar();
    { unsigned stage = 2 * STG, ck = ck0 + 2u;
#pragma nounroll
      for (unsigned c = 2; c < 9; ++c) { fillQ(ck, stage); ck = ck == 8u ? 0u : ck + 1u; stage = stage == 2 * STG ? 0u : stage + STG;
        wait_vm<44>(); bar(); } }
    const unsigned vV = prow * 256u + pch * 16u;
    const srd_t sWo = mk_srd(wo, NH * DH * DM * 2);
    const unsigned vWo = prow * (DM * 2u) + pch * 16u;
    { unsigned j = 1, jw = 0;
#pragma nounroll
      for (unsigned n = 0; n < 10; ++n) {
        if (n == 9u) { wait_vm<0>(); bar(); }
        const unsigned je = ((unsigned)(0xFFFECA8642ull >> (4u * n)) & 15u) + 1u;
#pragma nounroll
        for (; j < je; ++j) {
          if ((j & 1u) && j != 15u) { const unsigned t = (j + 1u) >> 1, ld = lds0 + ((SLOTK >> (4u * t)) & 15u) * 16384u;
#pragma unroll
            for (int i = 0; i < 4; ++i) dma4_nt(ld + i * 4096u, vK0 + 1024u, vK0 + 128u, vK1 + 1024u, vK1 + 128u, sK, t * 16384u + i * 4096u);
          } else { const unsigned t = (j - 1u) >> 1, ld = lds0 + ((SLOTV >> (4u * t)) & 15u) * 16384u;
#pragma unroll
            for (int i = 0; i < 4; ++i) dma4_nt(ld + i * 4096u, vV + 1024u, vV + 128u, vV + 1024u, vV + 128u, sV, t * 16384u + i * 4096u); } }
        const unsigned we = (unsigned)(0x8741000000ull >> (4u * n)) & 15u;
#pragma nounroll
        for (; jw < we; ++jw) { const unsigned ld = lds0 + ((SLOTW >> (4u * jw)) & 15u) * 16384u, so = (g4 * 8u + jw) * 128u * (DM * 2u) + ct * 128u;
#pragma unroll
          for (int k = 0; k < 4; ++k) dma4(ld + k * 4096u, vWo, vWo + 8192u, vWo + 16384u, vWo + 24576u, sWo, so + k * 32u * (DM * 2u)); }
        if (n == 9u) break;
        if (n < 6u) wait_vm<32>(); else if (n == 6u) wait_vm<48>(); else wait_vm<62>();
        bar(); } }
    asm volatile("" : "+v"(ep)); tag = mix64(rfl64(ep));
  } else {
  const unsigned rsw0 = 64u * (r & 7u) + 16u * (hh ^ ((r >> 2) & 3u)), rsw1 = rsw0 ^ 32u;
  f32x16 qa[4] = {}, o[4] = {};
  asm volatile("" : "+v"(qa[0]), "+v"(qa[1]), "+v"(qa[2]), "+v"(qa[3]), "+v"(o[0]), "+v"(o[1]), "+v"(o[2]), "+v"(o[3]));
  wait_lgkm<0>();
  wait_vm<4>(); if (wid < 4u) wait_vm<0>();

  dma4(lds0 + STG + wid * 4096u, vX0, vX1 + 8192u, vX0 + 16384u, vX1 + 24576u, sX, (ck0 + 1u) * 128u + wid * 32u * DM * 2u);
  bar();
  asm volatile("" : "+v"(ep)); ep = rfl64(ep);

  const lds_cptr kb0 = (lds_cptr)lds + 2048u * (r >> 3) + rsw0, kb1 = (lds_cptr)lds + 2048u * (r >> 3) + rsw1;
  const lds_cptr vb = (lds_cptr)lds + 64u * (4u * hh + q4) + trcol;
  bf16x8 qf[8], pre[RA];
  f32x16 negm, S0, S1; u32x4 P0[2], P1[2]; float l = 0.f;
  auto rdK = [&](lds_cptr k0, lds_cptr k1, int kb, int i) { return rd128(((i & 1) ? k1 : k0) + 8192 * kb + 512 * (i >> 1)); };
  auto rdV = [&](lds_cptr v, int kb, int j) { return trd2(v, 8192 * kb + 4096 * (j >> 2) + 512 * (j & 3), 8192 * kb + 4096 * (j >> 2) + 2048 + 512 * (j & 3)); };
  auto sm_fix = [&](float ps, f32x16& Sc, f32x16& Sn, u32x4 (&P)[2]) {
    if (__builtin_expect(__any(!(ps <= SUM_THR)), 0)) {
      const float dl = fmaxf(swap_max(max16(Sc)), 0.f), f = __builtin_amdgcn_exp2f(-dl);
#pragma unroll
      for (int i = 0; i < 16; ++i) { Sc[i] -= dl; Sn[i] -= dl; negm[i] -= dl; }
#pragma unroll
      for (int db = 0; db < 4; ++db)
#pragma unroll
        for (int i = 0; i < 16; ++i) o[db][i] *= f;
      l *= f; ps = expo(Sc, P); }
    l += ps; };
  { lds_cptr wp = (lds_cptr)lds + STG_W + 2048u * hh + 64u * q4 + trcol;
    lds_cptr xp0 = (lds_cptr)lds + wid * 4096u + 1024u * (r >> 3) + rsw0, xp1 = (lds_cptr)lds + wid * 4096u + 1024u * (r >> 3) + rsw1;
    bf16x8 xf[4], af[5];
    auto rdA = [&](int i) { return trd2(wp, 4096 * (i >> 2) + 512 * (i & 3), 4096 * (i >> 2) + 512 * (i & 3) + 256); };
    xf[0] = rd128(xp0); af[0] = rdA(0); af[1] = rdA(1); af[2] = rdA(2); af[3] = rdA(3);
    unsigned stage = 0;
#pragma nounroll
    for (unsigned c = 0; c < 8; ++c) {
      if (__builtin_expect(c == 3u && wid == 3u, 0)) {
#pragma unroll
        for (int i = 0; i < 4; ++i) dma4_nt(lds0 + 9u * 16384u + i * 4096u, vK0 + 1024u, vK0 + 128u, vK1 + 1024u, vK1 + 128u, sK, i * 4096u);
        unsigned ln = lane; asm volatile("" : "+v"(ln));
        const unsigned k = ln & 7u, sel = ln >> 3;
        const char* wup = sel == 0u ? (const char*)vc + (size_t)(b * NH + h) * (SK * DH * 2) + k * 16384u
                        : sel == 1u ? (const char*)wo + (size_t)(g4 * 1024u + k * 128u) * (DM * 2u) + ct * 128u
                        : sel == 2u ? (const char*)out + (size_t)((8u * rt + k) * 32u) * (DM * 2u)
                        : ws + (sel == 3u ? ((b * 7u + k) * NH + h) * 8192u : sel == 4u ? ((8u * rt + k) * NH + g4 * 8u) * 8192u : sel == 5u ? X_OFF + u * 131072u + k * 16384u : FLAG_OFF + k * 32768u + 64u);
        asm volatile("s_mov_b32 m0, %0\n\ts_nop 0\n\tglobal_load_lds_dword %1, off sc1" :: "s"(lds0 + SPARE), "v"(wup) : "memory"); }
      const unsigned nstage = stage == 2 * STG ? 0u : stage + STG; const int dlt = (int)nstage - (int)stage; stage = nstage;
      SBAR();
      static_for<12>([&](auto I) { constexpr int i = I.value;
        qa[i & 3] = MFMA(af[i % 5], xf[i >> 2], qa[i & 3]);
        af[(i + 4) % 5] = rdA(i + 4);
        if constexpr (i == 0) xf[1] = rd128(xp1); if constexpr (i == 3) xf[2] = rd128(xp0 + 512); if constexpr (i == 7) xf[3] = rd128(xp1 + 512);
        SBAR(); });
      qa[0] = MFMA(af[12 % 5], xf[3], qa[0]);
      wp += dlt; xp0 += dlt; xp1 += dlt; asm volatile("" : "+v"(wp), "+v"(xp0), "+v"(xp1));
      SBAR();
      asm volatile("s_bitcmp1_b32 %0, %1\n\ts_cbranch_scc1 1f\n\ts_waitcnt vmcnt(0)\n1:\n\ts_waitcnt lgkmcnt(0)\n\ts_barrier" :: "s"(0x78u), "s"(c) : "memory", "scc");
      const bf16x8 xl = xf[3], a13 = af[13 % 5], a14 = af[14 % 5], a15 = af[15 % 5];
      xf[0] = rd128(xp0); af[0] = rdA(0); af[1] = rdA(1);
      SBAR();
      qa[1] = MFMA(a13, xl, qa[1]); SBAR();
      af[2] = rdA(2);
      qa[2] = MFMA(a14, xl, qa[2]); SBAR();
      af[3] = rdA(3);
      qa[3] = MFMA(a15, xl, qa[3]); SBAR();
    }
    lds_cptr k0 = kb0 + 9 * 16384, k1 = kb1 + 9 * 16384; asm volatile("" : "+v"(k0), "+v"(k1));
    bf16x8 ag[16], f[16]; u32x4 qw[8]; float qs = QSCALE; asm volatile("" : "+s"(qs));
#pragma unroll
    for (int i = 0; i < 4; ++i) ag[i] = af[i];
    auto cvq = [&](int j) { float lo = qa[j >> 3][2 * (j & 7)] * qs; pinf(lo); float hi = qa[j >> 3][2 * (j & 7) + 1] * qs; unsigned w = cvtpk(lo, hi); pinu(w); qw[j >> 2][j & 3] = w; };
    SBAR();
    static_for<24>([&](auto G) { constexpr int g = G.value, n = g + 4;
      if constexpr (g < 4) qa[g] = MFMA(ag[g], xf[0], qa[g]);
      else if constexpr (g < 16) qa[(g - 4) / 3] = MFMA(ag[g], xf[1 + (g - 4) % 3], qa[(g - 4) / 3]);
      else { qf[g - 16] = __builtin_bit_cast(bf16x8, qw[g - 16]);
        if constexpr (g == 16) { const f32x16 z = {}; S0 = MFMA(f[0], qf[0], z); } else S0 = MFMA(f[g - 16], qf[g - 16], S0); pin16(S0); }
      if constexpr (g == 0) { xf[1] = rd128(xp1); xf[2] = rd128(xp0 + 512); } if constexpr (g == 1) xf[3] = rd128(xp1 + 512);
      if constexpr (n < 16) ag[n] = rdA(4 * (1 + (n - 4) % 3) + (n - 4) / 3); else f[n - 16] = rdK(k0, k1, (n - 16) >> 3, (n - 16) & 7);
      if constexpr (g >= 9 && g < 21) { constexpr int j0 = (g - 9) * 8 / 3, j1 = (g - 8) * 8 / 3; static_for<j1 - j0>([&](auto J) { cvq(j0 + J.value); }); }
      SBAR(); });

#pragma unroll
    for (int i = 12; i < 16; ++i) f[i] = rdK(k0, k1, 1, i & 7);
    const float m = swap_max(max16(S0));
#pragma unroll
    for (int i = 0; i < 16; ++i) negm[i] = -m;
    SBAR();
    float e[16]; float s0 = 0.f, s1 = 0.f;
    static_for<9>([&](auto G) { constexpr int g = G.value;
      if constexpr (g == 0) { if (wid >= 4u) bar(); }
      if constexpr (g == A0BAR) { if (wid < 4u) bar();
        constexpr unsigned ko = ((SLOTK >> 4) & 15u) * 16384u;
#pragma unroll
        for (int i = 0; i < RA; ++i) pre[i] = rdK(kb0 + ko, kb1 + ko, 0, i);
        SBAR(); }
      if constexpr (g == 0) S1 = MFMA(f[8], qf[0], negm); else if constexpr (g < 8) S1 = MFMA(f[8 + g], qf[g], S1);
      if constexpr (g < 8) pin16(S1);
      if constexpr (g < 8) { e[2 * g] = __builtin_amdgcn_exp2f(S0[2 * g] - m); e[2 * g + 1] = __builtin_amdgcn_exp2f(S0[2 * g + 1] - m); pinf(e[2 * g]); pinf(e[2 * g + 1]); }
      if constexpr (g == 1) { s0 = e[0]; s1 = e[1]; } else if constexpr (g > 1) { s0 += e[2 * g - 2]; pinf(s0); s1 += e[2 * g - 1]; pinf(s1); }
      if constexpr (g >= 1) { unsigned w = cvtpk(e[2 * g - 2], e[2 * g - 1]); pinu(w); P0[(g - 1) >> 2][(g - 1) & 3] = w; }
      SBAR(); });
    l = s0 + s1; }

  auto unit = [&](auto KB, auto LAST, lds_cptr k0, lds_cptr k1, lds_cptr v, lds_cptr nk0, lds_cptr nk1, f32x16& Sn, f32x16& Sc, const u32x4 (&Pp)[2], u32x4 (&Pc)[2]) {
    constexpr int kb = KB.value; constexpr bool last = LAST.value;
    float e[16]; float s0 = 0.f, s1 = 0.f; bf16x8 f[16 + RA];
#pragma unroll
    for (int i = 0; i < RA; ++i) f[i] = pre[i];
    static_for<16>([&](auto G) { constexpr int g = G.value; constexpr int n = g + RA; constexpr int gb = 12;
      if constexpr (last && g == gb) { bar(); wait_lgkm<0>(); }
      else if constexpr ((g & 3) == 0) wait_lgkm<lds_ops(g + 4, g + RA, last)>();
      if constexpr (g == 0) Sn = MFMA(f[0], qf[0], negm); else if constexpr (g < 8) Sn = MFMA(f[g], qf[g], Sn);
      else o[g & 3] = MFMA(f[g], __builtin_bit_cast(bf16x8, Pp[(g >> 2) & 1]), o[g & 3]);
      if constexpr (n < 8) f[n] = rdK(k0, k1, kb, n); else if constexpr (n < 16) f[n] = rdV(v, kb, n - 8);
      else if constexpr (!last) f[n] = rdK(k0, k1, kb + 1, n - 16);
      else if constexpr (g >= gb) { constexpr int per = (RA + 15 - gb) / (16 - gb); static_for<per>([&](auto J) { constexpr int i = per * (g - gb) + J.value; if constexpr (i < RA) f[16 + i] = rdK(nk0, nk1, 0, i); }); }
      if constexpr (g == 0) { e[0] = __builtin_amdgcn_exp2f(Sc[0]); pinf(e[0]); }
      if constexpr (g < 15) { e[g + 1] = __builtin_amdgcn_exp2f(Sc[g + 1]); pinf(e[g + 1]); }
      if constexpr (g == 1) { s0 = e[0]; s1 = e[1]; } else if constexpr (g > 1) { if constexpr (g & 1) { s1 += e[g]; pinf(s1); } else { s0 += e[g]; pinf(s0); } }
      if constexpr (g & 1) { unsigned w = cvtpk(e[g - 1], e[g]); pinu(w); Pc[g >> 3][(g >> 1) & 3] = w; }
      SBAR(); });
#pragma unroll
    for (int i = 0; i < RA; ++i) pre[i] = f[16 + i];
    sm_fix(s0 + s1, Sc, Sn, Pc); };
#pragma nounroll
  for (unsigned t = 1; t < 7; ++t) {
    const unsigned ko = ((SLOTK >> (4u * t)) & 15u) * 16384u, vo = ((SLOTV >> (4u * (t - 1u))) & 15u) * 16384u, nko = ((SLOTK >> (4u * ((t + 1u) & 7u))) & 15u) * 16384u;
    SBAR();
    unit(std::integral_constant<int, 0>{}, std::false_type{}, kb0 + ko, kb1 + ko, vb + vo, kb0, kb1, S0, S1, P0, P1);
    unit(std::integral_constant<int, 1>{}, std::true_type{}, kb0 + ko, kb1 + ko, vb + vo, kb0 + nko, kb1 + nko, S1, S0, P1, P0);
  }
  const unsigned me = (b * 7u + wid) * NH + h;
  {
    constexpr unsigned ko = ((SLOTK >> 28) & 15u) * 16384u;
    const lds_cptr k0 = kb0 + ko, k1 = kb1 + ko, v6 = vb + ((SLOTV >> 24) & 15u) * 16384u, v7 = vb + ((SLOTV >> 28) & 15u) * 16384u;
    u32x4 P2[2], P3[2];
    auto scaleP = [&](u32x4 (&P)[2], float f) {
#pragma unroll
      for (int i = 0; i < 8; ++i) { const unsigned w = P[i >> 2][i & 3]; P[i >> 2][i & 3] = cvtpk(__uint_as_float(w << 16) * f, __uint_as_float(w & 0xFFFF0000u) * f); } };
    auto fix = [&](auto NP, float ps, f32x16& Sc, f32x16& Sn, u32x4 (&P)[2]) {
      if (__builtin_expect(__any(!(ps <= SUM_THR)), 0)) {
        const float f = __builtin_amdgcn_exp2f(-fmaxf(swap_max(max16(Sc)), 0.f));
        scaleP(P0, f); if constexpr (NP.value > 1) scaleP(P1, f); if constexpr (NP.value > 2) scaleP(P2, f); }
      sm_fix(ps, Sc, Sn, P); };
    auto rdVt = [&](int i) { const int j = pv_order(i), db = j >> 3, un = (j >> 1) & 3, ks = j & 1; return rdV(un < 2 ? v6 : v7, un & 1, 4 * ks + db); };
    auto pvt = [&](auto I, const bf16x8& vf) { constexpr int j = pv_order(I.value), un = (j >> 1) & 3;
      const u32x4 pb = un == 0 ? P0[j & 1] : un == 1 ? P1[j & 1] : un == 2 ? P2[j & 1] : P3[j & 1];
      o[j >> 3] = MFMA(vf, __builtin_bit_cast(bf16x8, pb), o[j >> 3]); pin16(o[j >> 3]); };
    bf16x8 f[16], w[32];
#pragma unroll
    for (int i = 0; i < RA; ++i) f[i] = pre[i];
    auto qsm = [&](auto H, f32x16& Sn, const f32x16& Sc, u32x4 (&Pc)[2]) { constexpr int hf = H.value;
      float e[16]; float s0 = 0.f, s1 = 0.f;
      static_for<9>([&](auto G) { constexpr int g = G.value, n = 8 * hf + g + RA;
        if constexpr (hf < 2 && g < 8) { if constexpr (g == 0) Sn = MFMA(f[8 * hf], qf[0], negm); else Sn = MFMA(f[8 * hf + g], qf[g], Sn);
          pin16(Sn);
          if constexpr (n < 16) f[n] = rdK(k0, k1, n >> 3, n & 7); }
        if constexpr (hf == 1 && g >= 2 && g < 8) w[g - 2] = rdVt(g - 2);
        if constexpr (hf == 2 && g < 8) { pvt(G, w[g]); w[g + 6] = rdVt(g + 6); }
        if constexpr (g < 8) { e[2 * g] = __builtin_amdgcn_exp2f(Sc[2 * g]); e[2 * g + 1] = __builtin_amdgcn_exp2f(Sc[2 * g + 1]); pinf(e[2 * g]); pinf(e[2 * g + 1]); }
        if constexpr (g == 1) { s0 = e[0]; s1 = e[1]; } else if constexpr (g > 1) { s0 += e[2 * g - 2]; pinf(s0); s1 += e[2 * g - 1]; pinf(s1); }
        if constexpr (g >= 1) { unsigned pw = cvtpk(e[2 * g - 2], e[2 * g - 1]); pinu(pw); Pc[(g - 1) >> 2][(g - 1) & 3] = pw; }
        SBAR(); });
      return s0 + s1; };
    if (wid < 4u) __builtin_amdgcn_s_setprio(1);
    SBAR();
    fix(std::integral_constant<int, 1>{}, qsm(std::integral_constant<int, 0>{}, S0, S1, P1), S1, S0, P1);
    bar_if(wid >> 2);
    fix(std::integral_constant<int, 2>{}, qsm(std::integral_constant<int, 1>{}, S1, S0, P2), S0, S1, P2);
    bar_if((wid >> 2) ^ 1u);

    fix(std::integral_constant<int, 3>{}, qsm(std::integral_constant<int, 2>{}, S0, S1, P3), S1, S0, P3);
    const float inv = __builtin_amdgcn_rcpf(swap_add(l));
    auto put = [&](int k8) { bstore_sc1(pack8(o[k8 >> 1], k8 & 1, inv), lane * 16u, sO, me * 8192u + k8 * 1024u); };
    static_for<24>([&](auto G) { constexpr int g = G.value + 8;
      pvt(std::integral_constant<int, g>{}, w[g]);
      if constexpr (g + 6 < 32) w[g + 6] = rdVt(g + 6);
      if constexpr (g == 14) put(0); if constexpr (g == 16) put(1); if constexpr (g == 19) put(2); if constexpr (g == 22) put(3); if constexpr (g == 26) put(4); if constexpr (g == 29) put(5);
      SBAR(); });

    put(6); put(7); }

  tag = mix64(ep);
  __builtin_amdgcn_s_setprio(0);
  }
  if (wid < 7u) { wait_vm<0>(); bar(); }
  if (tid == 0) st_agent(ws + FLAG_OFF + (b * NH + h) * FLAG_STRIDE, tag);

  if (!has_tile) {
    if (bid == 249u && wid == 0u) {
#pragma nounroll
      for (unsigned i = 0; i < 4; ++i) poll_flags(ws + FLAG_OFF + (i * 64u + lane) * FLAG_STRIDE, tag);
      if (lane == 0) st_agent(ws + EPOCH_OFF, tag); }
    return; }
  const unsigned jq = (2u * g4 + 2u + wid) & 7u, G = 8u * rt + jq;
  const unsigned voffO = lane * 16u, soO = (G * NH + g4 * 8u) * 8192u;
  const char* fp = ws + FLAG_OFF + (((G * 37u) >> 8) * NH + g4 * 8u + (lane & 7u)) * FLAG_STRIDE;
  unsigned ready = 0;
  auto need = [&](unsigned m) {
    _Pragma("nounroll") for (unsigned spins = 0; (ready & m) != m && spins < SPIN_CAP; ++spins) { ready = (unsigned)__ballot(ld_agent(fp) == tag); if ((ready & m) != m) __builtin_amdgcn_s_sleep(PSLEEP); }
    asm volatile("" ::: "memory"); };
  __builtin_amdgcn_s_sleep(PDELAY);
  need(1u);

  const lds_cptr wb = (lds_cptr)lds + 1024u * hh + 64u * q4 + trcol;
  f32x16 acc[2] = {};
  { unsigned hd = 0, so_c = soO + hd * 8192u; lds_cptr wp = wb + ((SLOTW >> (4u * hd)) & 15u) * 16384u;
    bf16x8 of[8], lf[4][2];
    auto rdW = [&](int s2) { lf[s2 & 3][0] = trd2(wp, (s2 & 7) * 2048, (s2 & 7) * 2048 + 256); lf[s2 & 3][1] = trd2(wp, (s2 & 7) * 2048 + 512, (s2 & 7) * 2048 + 768); };
#pragma unroll
    for (int i = 0; i < 8; ++i) { if (i < WO) bload_sc1(of[i], voffO, sO, so_c + i * 1024u); else of[i] = bf16x8{}; }
    rdW(0); rdW(1);
#pragma nounroll
    for (unsigned p = 0; p < 8; ++p) {
      hd = (hd + 1u) & 7u;
      if (p == 5u) __builtin_amdgcn_s_barrier();
      const unsigned so_n = p < 7u ? soO + hd * 8192u : O_BYTES;
      static_for<8>([&](auto S) { constexpr int s2 = S.value;
        if constexpr (s2 == 8 - WO) need(1u << hd);
        if constexpr (s2 == 6) wp = wb + ((SLOTW >> (4u * hd)) & 15u) * 16384u;
        wait_vm1<WO - 1>(of[s2]);
        wait_lgkm<4>();
        acc[0] = MFMA(lf[s2 & 3][0], of[s2], acc[0]);
        rdW(s2 + 2);
        SBAR();
        acc[1] = MFMA(lf[s2 & 3][1], of[s2], acc[1]);
        SBAR();
        breload_sc1(of[(s2 + WO) & 7], voffO, sO, (s2 + WO < 8 ? so_c : so_n) + ((s2 + WO) & 7) * 1024u);
        SBAR(); });
      so_c = so_n;
    }
    wait_vm<0>();
#pragma unroll
    for (int i = 0; i < 8; ++i) pin(of[i]); }

  const srd_t sX2 = mk_srd(ws + X_OFF, X_BYTES);
  const unsigned xb = (u * 8u + jq) * 4u;
  if (wid < 6u) {
#pragma unroll
    for (int i = 0; i < 4; ++i) bstore_sc1(pack8(acc[i >> 1], i & 1), voffO, sX2, (xb + g4) * 4096u + i * 1024u);
    wait_vm<0>();
    if (lane == 0) st_agent(ws + FLAG2_OFF + (xb + g4) * FLAG_STRIDE, tag);
  } else {
    __builtin_amdgcn_s_sleep(PDELAY2);
    poll_flags(ws + FLAG2_OFF + (xb + ((g4 + 1u + lane % 3u) & 3u)) * FLAG_STRIDE, tag);

    bf16x8 xp[12];
#pragma unroll
    for (int i = 0; i < 12; ++i) bload_sc1(xp[i], voffO, sX2, (xb + ((g4 + 1u + (i >> 2)) & 3u)) * 4096u + (i & 3) * 1024u);
    wait_vm<0>();
#pragma unroll
    for (int j = 0; j < 12; ++j) { const int i = j & 3; pin(xp[j]); const u32x4 v = __builtin_bit_cast(u32x4, xp[j]);
#pragma unroll
      for (int e = 0; e < 4; ++e) { acc[i >> 1][8 * (i & 1) + 2 * e] += __uint_as_float(v[e] << 16); acc[i >> 1][8 * (i & 1) + 2 * e + 1] += __uint_as_float(v[e] & 0xFFFF0000u); } }

    char* op = (char*)out + (size_t)(G * 32u + r) * (DM * 2u) + ct * 128u + hh * 16u;
#pragma unroll
    for (int i = 0; i < 4; ++i) *(u32x4*)(op + i * 32) = pack8(acc[i >> 1], i & 1);
  }
}

extern "C" void kernel_launch(void* const* d_in, const int* in_sizes, int n_in, void* d_out, int out_size, void* d_ws, size_t ws_size, hipStream_t stream) {
  (void)in_sizes; (void)n_in; (void)out_size; (void)ws_size;
  (void)hipFuncSetAttribute((const void*)mha_kernel, hipFuncAttributeMaxDynamicSharedMemorySize, LDS_BYTES);
  const unsigned short* x = (const unsigned short*)d_in[0]; const unsigned short* kc = (const unsigned short*)d_in[1]; const unsigned short* vc = (const unsigned short*)d_in[2];
  const unsigned short* wq = (const unsigned short*)d_in[3]; const unsigned short* wo = (const unsigned short*)d_in[4];
  mha_kernel<<<256, 512, LDS_BYTES, stream>>>(x, kc, vc, wq, wo, (char*)d_ws, (unsigned short*)d_out);
}
```

```cpp
#include <hip/hip_runtime.h>
#include <stdint.h>
#include <utility>

typedef short    bf16x8 __attribute__((ext_vector_type(8)));
typedef short    s16x4  __attribute__((ext_vector_type(4)));
typedef float    f32x16 __attribute__((ext_vector_type(16)));
typedef float    f32x2  __attribute__((ext_vector_type(2)));
typedef __bf16   bf16x2 __attribute__((ext_vector_type(2)));
typedef unsigned u32x4  __attribute__((ext_vector_type(4)));
typedef __attribute__((address_space(3))) const char* lds_cptr;
typedef __attribute__((address_space(1))) unsigned long long gu64;
typedef __amdgpu_buffer_rsrc_t srd_t;

#define DI __device__ __forceinline__
#define MFMA(a, b, c) __builtin_amdgcn_mfma_f32_32x32x16_bf16((a), (b), (c), 0, 0, 0)
#define SBAR() __builtin_amdgcn_sched_barrier(0)

constexpr int NB = 8, SQ = 224, DM = 576, NH = 32, SK = 512, DH = 128;
constexpr int LDS_BYTES = 163840;
constexpr float QSCALE = 0.08838834764831845f * 1.4426950408889634f;
constexpr float SUM_THR = 4096.f;

template <int... Is, class F> DI void static_for_impl(std::integer_sequence<int, Is...>, F&& f) { (f(std::integral_constant<int, Is>{}), ...); }
template <int N, class F> DI void static_for(F&& f) { static_for_impl(std::make_integer_sequence<int, N>{}, f); }

DI unsigned rfl(unsigned x) { return __builtin_amdgcn_readfirstlane(x); }
DI unsigned cvtpk(float lo, float hi) { f32x2 v = {lo, hi}; bf16x2 b = __builtin_convertvector(v, bf16x2); return __builtin_bit_cast(unsigned, b); }
DI srd_t mk_srd(const void* p, unsigned bytes) { return __builtin_amdgcn_make_buffer_rsrc((void*)p, (short)0, (int)bytes, 0x00020000); }
DI void dma4(unsigned lds, unsigned v0, unsigned v1, unsigned v2, unsigned v3, srd_t srd, unsigned soff) {
  asm volatile("s_mov_b32 m0, %0\n\ts_nop 0\n\tbuffer_load_dwordx4 %1, %5, %6 offen lds\n\tbuffer_load_dwordx4 %2, %5, %6 offen offset:1024 lds\n\t"
               "buffer_load_dwordx4 %3, %5, %6 offen offset:2048 lds\n\tbuffer_load_dwordx4 %4, %5, %6 offen offset:3072 lds"
               :: "s"(lds), "v"(v0), "v"(v1), "v"(v2), "v"(v3), "s"(srd), "s"(soff) : "memory");
}
DI void dma4_nt(unsigned lds, unsigned v0, unsigned v1, unsigned v2, unsigned v3, srd_t srd, unsigned soff) {
  asm volatile("s_mov_b32 m0, %0\n\ts_nop 0\n\tbuffer_load_dwordx4 %1, %5, %6 offen nt lds\n\tbuffer_load_dwordx4 %2, %5, %6 offen offset:1024 nt lds\n\t"
               "buffer_load_dwordx4 %3, %5, %6 offen offset:2048 nt lds\n\tbuffer_load_dwordx4 %4, %5, %6 offen offset:3072 nt lds"
               :: "s"(lds), "v"(v0), "v"(v1), "v"(v2), "v"(v3), "s"(srd), "s"(soff) : "memory");
}
DI void touch2(unsigned lds, unsigned voff, srd_t srd, unsigned s0, unsigned s1) { asm volatile("s_mov_b32 m0, %0\n\ts_nop 0\n\tbuffer_load_dword %1, %2, %3 offen nt lds\n\tbuffer_load_dword %1, %2, %4 offen nt lds" :: "s"(lds), "v"(voff), "s"(srd), "s"(s0), "s"(s1) : "memory"); }
DI void bstore_sc1(const u32x4& d, unsigned voff, srd_t srd, unsigned soff) { asm volatile("buffer_store_dwordx4 %0, %1, %2, %3 offen sc1\n\ts_nop 1" :: "v"(d), "v"(voff), "s"(srd), "s"(soff) : "memory"); }
DI void bload_sc1(bf16x8& d, unsigned voff, srd_t srd, unsigned soff) { asm volatile("buffer_load_dwordx4 %0, %1, %2, %3 offen sc1" : "=v"(d) : "v"(voff), "s"(srd), "s"(soff) : "memory"); }
DI void breload_sc1(bf16x8& d, unsigned voff, srd_t srd, unsigned soff) { asm volatile("buffer_load_dwordx4 %0, %1, %2, %3 offen sc1" : "+v"(d) : "v"(voff), "s"(srd), "s"(soff) : "memory"); }
DI void pin(bf16x8& a) { asm volatile("" : "+v"(a)); }
DI void pin16(f32x16& a) { asm volatile("" : "+v"(a)); }
DI void pinf(float& a) { asm volatile("" : "+v"(a)); }
DI void pinu(unsigned& a) { asm volatile("" : "+v"(a)); }
template <int N> DI void wait_vm1(bf16x8& a) { asm volatile("s_waitcnt vmcnt(%1)" : "+v"(a) : "n"(N) : "memory"); }
template <int N> DI void wait_vm() { asm volatile("s_waitcnt vmcnt(%0)" :: "n"(N) : "memory"); }
template <int N> DI void wait_lgkm() { __builtin_amdgcn_s_waitcnt(0xC07F | (N << 8)); SBAR(); }
DI void bar_if(unsigned c) { asm volatile("s_cmp_eq_u32 %0, 0\n\ts_cbranch_scc1 1f\n\ts_waitcnt lgkmcnt(0)\n\ts_barrier\n1:" :: "s"(c) : "memory", "scc"); }
DI void bar() { asm volatile("s_waitcnt lgkmcnt(0)\n\ts_barrier" ::: "memory"); }
typedef short v4i16_t __attribute__((ext_vector_type(4)));
DI s16x4 trd(lds_cptr p) { return __builtin_amdgcn_ds_read_tr16_b64_v4i16((__attribute__((address_space(3))) v4i16_t*)p); }
DI bf16x8 trd2(lds_cptr p, int o0, int o1) { s16x4 a = trd(p + o0), b = trd(p + o1); return __builtin_shufflevector(a, b, 0, 1, 2, 3, 4, 5, 6, 7); }
DI bf16x8 rd128(lds_cptr p) { return *(const __attribute__((address_space(3))) bf16x8*)p; }
DI float swap_max(float m) { auto rr = __builtin_amdgcn_permlane32_swap(__float_as_uint(m), __float_as_uint(m), false, false); return fmaxf(__uint_as_float(rr[0]), __uint_as_float(rr[1])); }
DI float swap_add(float m) { auto rr = __builtin_amdgcn_permlane32_swap(__float_as_uint(m), __float_as_uint(m), false, false); return __uint_as_float(rr[0]) + __uint_as_float(rr[1]); }
DI float max16(const f32x16& s) { float a = fmaxf(fmaxf(s[0], s[1]), s[2]), b = fmaxf(fmaxf(s[3], s[4]), s[5]);
  a = fmaxf(fmaxf(a, s[6]), s[7]); b = fmaxf(fmaxf(b, s[8]), s[9]); a = fmaxf(fmaxf(a, s[10]), s[11]); b = fmaxf(fmaxf(b, s[12]), s[13]); a = fmaxf(fmaxf(a, s[14]), s[15]); return fmaxf(a, b); }
DI u32x4 pack8(const f32x16& a, int s, float f) { return u32x4{cvtpk(a[8 * s] * f, a[8 * s + 1] * f), cvtpk(a[8 * s + 2] * f, a[8 * s + 3] * f), cvtpk(a[8 * s + 4] * f, a[8 * s + 5] * f), cvtpk(a[8 * s + 6] * f, a[8 * s + 7] * f)}; }
DI u32x4 pack8(const f32x16& a, int s) { return u32x4{cvtpk(a[8 * s], a[8 * s + 1]), cvtpk(a[8 * s + 2], a[8 * s + 3]), cvtpk(a[8 * s + 4], a[8 * s + 5]), cvtpk(a[8 * s + 6], a[8 * s + 7])}; }

DI float expo(const f32x16& s, u32x4 (&p)[2]) {
  float e[16]; float s0 = 0.f, s1 = 0.f;
#pragma unroll
  for (int i = 0; i < 16; ++i) e[i] = __builtin_amdgcn_exp2f(s[i]);
#pragma unroll
  for (int i = 0; i < 16; i += 2) { s0 += e[i]; s1 += e[i + 1]; }
#pragma unroll
  for (int i = 0; i < 8; ++i) p[i >> 2][i & 3] = cvtpk(e[2 * i], e[2 * i + 1]);
  return s0 + s1;
}

#define ST_DECL
#define ST(k)
#define ST_OUT(ws, lo, hi)

constexpr unsigned SLOTK = 0x04204209u;
constexpr unsigned SLOTV = 0x31531531u;
constexpr unsigned SLOTW = 0x05429876u;
constexpr unsigned STG = 45056u, STG_W = 28672u;
constexpr unsigned O_BYTES = NB * SQ * NH * DH * 2;
constexpr unsigned X_OFF = O_BYTES, X_BYTES = 63u * 8u * 4u * 4096u;
constexpr unsigned FLAG_OFF = X_OFF + X_BYTES, FLAG_STRIDE = 128, FLAG2_OFF = FLAG_OFF + 256 * FLAG_STRIDE, EPOCH_OFF = FLAG2_OFF + 63 * 32 * FLAG_STRIDE;
constexpr unsigned SPIN_CAP = 1u << 20;
constexpr int RA = 7, WO = 6, A0BAR = 6, PSLEEP = 4, PDELAY = 20, PDELAY2 = 25;
constexpr unsigned SPARE = 3u * STG;
constexpr unsigned CODE_BYTES = 19200u;
constexpr int lds_ops(int a, int b, bool last) { int n = 0; for (int i = a; i < b; ++i) n += i >= 16 ? (last ? 0 : 1) : (i >= 8 ? 2 : 1); return n; }
constexpr int pv_order(int i) { return i < 4 ? i : i < 8 ? i + 4 : i < 12 ? i - 4 : i; }
DI unsigned long long mix64(unsigned long long z) { z += 0x9E3779B97F4A7C15ull; z = (z ^ (z >> 30)) * 0xBF58476D1CE4E5B9ull; z = (z ^ (z >> 27)) * 0x94D049BB133111EBull; return z ^ (z >> 31); }
DI unsigned long long rfl64(unsigned long long v) { return ((unsigned long long)rfl((unsigned)(v >> 32)) << 32) | rfl((unsigned)v); }
DI unsigned long long ld_agent(const char* p) { return __hip_atomic_load((gu64*)p, __ATOMIC_RELAXED, __HIP_MEMORY_SCOPE_AGENT); }
DI void st_agent(char* p, unsigned long long v) { __hip_atomic_store((gu64*)p, v, __ATOMIC_RELAXED, __HIP_MEMORY_SCOPE_AGENT); }
DI void poll_flags(const char* p, unsigned long long tag) {
  _Pragma("nounroll") for (unsigned spins = 0; spins < SPIN_CAP; ++spins) { if (__all(ld_agent(p) == tag)) break; __builtin_amdgcn_s_sleep(PSLEEP); }
  asm volatile("" ::: "memory");
}

__global__ __launch_bounds__(512, 2) void mha_kernel(const unsigned short* __restrict__ x, const unsigned short* __restrict__ kc, const unsigned short* __restrict__ vc,
                                                     const unsigned short* __restrict__ wq, const unsigned short* __restrict__ wo, char* ws, unsigned short* __restrict__ out) {
  extern __shared__ __attribute__((aligned(16))) char lds[];
  unsigned long long pc0; asm volatile("s_getpc_b64 %0" : "=s"(pc0));
  const unsigned tid = threadIdx.x, lane = tid & 63u, r = lane & 31u, hh = lane >> 5;
  const unsigned wid = rfl(tid >> 6);
  ST_DECL ST(0);
  const unsigned bid = blockIdx.x, xcd = bid & 7u, ix = bid >> 3;
  const unsigned b = (xcd & 1u) * 4u + (ix & 3u), h = (xcd >> 1) * 8u + (ix >> 2);
  const unsigned lds0 = (unsigned)(uintptr_t)lds;
  {
    const char* cp = (const char*)(pc0 & ~255ull) + min((ix * 8u + wid) * 128u, CODE_BYTES);
    asm volatile("s_mov_b32 m0, %0\n\ts_nop 0\n\tglobal_load_lds_dword %1, %2" :: "s"(lds0 + SPARE), "v"(0u), "s"(cp) : "memory"); }
  const srd_t sW = mk_srd((const char*)wq - 1024, DM * NH * DH * 2 + 1024);
  const srd_t sX = mk_srd(x + (size_t)b * SQ * DM, SQ * DM * 2);
  const unsigned prow = (lane >> 2) & 7u, pch = 4u * hh + (lane & 3u);
  const unsigned pchx0 = 4u * hh + ((lane & 3u) ^ ((lane >> 4) & 1u)), pchx1 = pchx0 ^ 2u;
  const unsigned vW = prow * 8192u + pch * 16u, vX0 = prow * (DM * 2u) + pchx0 * 16u, vX1 = prow * (DM * 2u) + pchx1 * 16u;
  const unsigned ck0 = 3u * (((ix & 3u) + (ix >> 2)) % 3u);
  unsigned long long ep; asm volatile("global_load_dwordx2 %0, %1, %2 sc1" : "=v"(ep) : "v"(0u), "s"(ws + EPOCH_OFF) : "memory");
  dma4(lds0 + (wid < 7u ? wid * 4096u : SPARE), vX0, vX1 + 8192u, vX0 + 16384u, vX1 + 24576u, sX, ck0 * 128u + min(wid, 6u) * 32u * DM * 2u);
  dma4(lds0 + (wid >> 2) * STG + STG_W + (wid & 3u) * 4096u, vW + 1024u, vW + 128u, vW + 65536u - 1024u, vW + 65664u - 2048u, sW, (ck0 + (wid >> 2)) * 64u * 8192u + h * 256u + (wid & 3u) * 131072u);
  const srd_t sK = mk_srd((const char*)(kc + (size_t)(b * NH + h) * SK * DH) - 1024, SK * DH * 2 + 1024);
  const srd_t sV = mk_srd((const char*)(vc + (size_t)(b * NH + h) * SK * DH) - 1024, SK * DH * 2 + 1024);
  const unsigned vK0 = prow * 256u + pchx0 * 16u, vK1 = prow * 256u + pchx1 * 16u;
  const unsigned q4 = (lane & 15u) >> 2, p4 = lane & 3u, blk = (lane >> 4) & 1u;
  const unsigned trcol = 32u * blk + 16u * (p4 & 1u) + 8u * (p4 >> 1);
  const srd_t sO = mk_srd(ws, O_BYTES);
  unsigned long long tag;
  const unsigned g4 = xcd >> 1, u0 = (xcd & 1u) * 32u + ix;
  const bool has_tile = u0 < 63u;
  const unsigned u = has_tile ? u0 : 62u, rt = u / 9u, ct = u - rt * 9u;

  auto touchKV = [&](unsigned j) {
    if (j & 1u) { const unsigned so = ((j + 1u) >> 1) * 16384u + 1024u; touch2(lds0 + SPARE, lane * 128u, sK, so, so + 8192u); }
    else { const unsigned so = ((j - 1u) >> 1) * 16384u + 1024u; touch2(lds0 + SPARE, lane * 128u, sV, so, so + 8192u); } };

  if (wid == 7u) {
    auto fillQ = [&](unsigned c, unsigned stage) {
      const unsigned lw = lds0 + stage + STG_W, sw = c * 64u * 8192u + h * 256u, lx = lds0 + stage, sx = c * 128u;
#pragma unroll
      for (int i = 0; i < 4; ++i) dma4(lw + i * 4096u, vW + 1024u, vW + 128u, vW + 65536u - 1024u, vW + 65664u - 2048u, sW, sw + i * 131072u);
#pragma unroll
      for (int i = 0; i < 7; ++i) dma4(lx + i * 4096u, vX0, vX1 + 8192u, vX0 + 16384u, vX1 + 24576u, sX, sx + i * 32u * DM * 2u); };
    bar(); ST(2);
    { unsigned stage = 2 * STG, ck = ck0 + 2u;
#pragma nounroll
      for (unsigned c = 2; c < 9; ++c) { fillQ(ck, stage); ck = ck == 8u ? 0u : ck + 1u; stage = stage == 2 * STG ? 0u : stage + STG;
        wait_vm<44>(); bar(); ST(1 + c); } }
    const unsigned vV = prow * 256u + pch * 16u;
    const srd_t sWo = mk_srd(wo, NH * DH * DM * 2);
    const unsigned vWo = prow * (DM * 2u) + pch * 16u;
    { unsigned j = 1, jw = 0;
#pragma nounroll
      for (unsigned n = 0; n < 10; ++n) {
        if (n == 9u) { wait_vm<0>(); bar(); }
        const unsigned je = ((unsigned)(0xFFFECA8642ull >> (4u * n)) & 15u) + 1u;
#pragma nounroll
        for (; j < je; ++j) {
          if ((j & 1u) && j != 15u) { const unsigned t = (j + 1u) >> 1, ld = lds0 + ((SLOTK >> (4u * t)) & 15u) * 16384u;
#pragma unroll
            for (int i = 0; i < 4; ++i) dma4_nt(ld + i * 4096u, vK0 + 1024u, vK0 + 128u, vK1 + 1024u, vK1 + 128u, sK, t * 16384u + i * 4096u);
          } else { const unsigned t = (j - 1u) >> 1, ld = lds0 + ((SLOTV >> (4u * t)) & 15u) * 16384u;
#pragma unroll
            for (int i = 0; i < 4; ++i) dma4_nt(ld + i * 4096u, vV + 1024u, vV + 128u, vV + 1024u, vV + 128u, sV, t * 16384u + i * 4096u); } }
        const unsigned we = (unsigned)(0x8741000000ull >> (4u * n)) & 15u;
#pragma nounroll
        for (; jw < we; ++jw) { const unsigned ld = lds0 + ((SLOTW >> (4u * jw)) & 15u) * 16384u, so = (g4 * 8u + jw) * 128u * (DM * 2u) + ct * 128u;
#pragma unroll
          for (int k = 0; k < 4; ++k) dma4(ld + k * 4096u, vWo, vWo + 8192u, vWo + 16384u, vWo + 24576u, sWo, so + k * 32u * (DM * 2u)); }
        if (n == 9u) break;
        if (n < 6u) wait_vm<32>(); else if (n == 6u) wait_vm<48>(); else wait_vm<62>();
        bar(); ST(n == 0u ? 10u : 11u + n); } }
    asm volatile("" : "+v"(ep)); tag = mix64(rfl64(ep));
    ST(20); ST(21);
  } else {
  const unsigned rsw0 = 64u * (r & 7u) + 16u * (hh ^ ((r >> 2) & 3u)), rsw1 = rsw0 ^ 32u;
  f32x16 qa[4] = {}, o[4] = {};
  asm volatile("" : "+v"(qa[0]), "+v"(qa[1]), "+v"(qa[2]), "+v"(qa[3]), "+v"(o[0]), "+v"(o[1]), "+v"(o[2]), "+v"(o[3]));
  wait_lgkm<0>();
  wait_vm<4>(); if (wid < 4u) wait_vm<0>();
  ST(1);
  dma4(lds0 + STG + wid * 4096u, vX0, vX1 + 8192u, vX0 + 16384u, vX1 + 24576u, sX, (ck0 + 1u) * 128u + wid * 32u * DM * 2u);
  bar();
  asm volatile("" : "+v"(ep)); ep = rfl64(ep);
  ST(2);
  const lds_cptr kb0 = (lds_cptr)lds + 2048u * (r >> 3) + rsw0, kb1 = (lds_cptr)lds + 2048u * (r >> 3) + rsw1;
  const lds_cptr vb = (lds_cptr)lds + 64u * (4u * hh + q4) + trcol;
  bf16x8 qf[8], pre[RA];
  f32x16 negm, S0, S1; u32x4 P0[2], P1[2]; float l = 0.f;
  auto rdK = [&](lds_cptr k0, lds_cptr k1, int kb, int i) { return rd128(((i & 1) ? k1 : k0) + 8192 * kb + 512 * (i >> 1)); };
  auto rdV = [&](lds_cptr v, int kb, int j) { return trd2(v, 8192 * kb + 4096 * (j >> 2) + 512 * (j & 3), 8192 * kb + 4096 * (j >> 2) + 2048 + 512 * (j & 3)); };
  auto sm_fix = [&](float ps, f32x16& Sc, f32x16& Sn, u32x4 (&P)[2]) {
    if (__builtin_expect(__any(!(ps <= SUM_THR)), 0)) {
      const float dl = fmaxf(swap_max(max16(Sc)), 0.f), f = __builtin_amdgcn_exp2f(-dl);
#pragma unroll
      for (int i = 0; i < 16; ++i) { Sc[i] -= dl; Sn[i] -= dl; negm[i] -= dl; }
#pragma unroll
      for (int db = 0; db < 4; ++db)
#pragma unroll
        for (int i = 0; i < 16; ++i) o[db][i] *= f;
      l *= f; ps = expo(Sc, P); }
    l += ps; };
  { lds_cptr wp = (lds_cptr)lds + STG_W + 2048u * hh + 64u * q4 + trcol;
    lds_cptr xp0 = (lds_cptr)lds + wid * 4096u + 1024u * (r >> 3) + rsw0, xp1 = (lds_cptr)lds + wid * 4096u + 1024u * (r >> 3) + rsw1;
    bf16x8 xf[4], af[5];
    auto rdA = [&](int i) { return trd2(wp, 4096 * (i >> 2) + 512 * (i & 3), 4096 * (i >> 2) + 512 * (i & 3) + 256); };
    xf[0] = rd128(xp0); af[0] = rdA(0); af[1] = rdA(1); af[2] = rdA(2); af[3] = rdA(3);
    unsigned stage = 0;
#pragma nounroll
    for (unsigned c = 0; c < 8; ++c) {
      if (__builtin_expect(c == 3u && wid == 3u, 0)) {
#pragma unroll
        for (int i = 0; i < 4; ++i) dma4_nt(lds0 + 9u * 16384u + i * 4096u, vK0 + 1024u, vK0 + 128u, vK1 + 1024u, vK1 + 128u, sK, i * 4096u);
        unsigned ln = lane; asm volatile("" : "+v"(ln));
        const unsigned k = ln & 7u, sel = ln >> 3;
        const char* wup = sel == 0u ? (const char*)vc + (size_t)(b * NH + h) * (SK * DH * 2) + k * 16384u
                        : sel == 1u ? (const char*)wo + (size_t)(g4 * 1024u + k * 128u) * (DM * 2u) + ct * 128u
                        : sel == 2u ? (const char*)out + (size_t)((8u * rt + k) * 32u) * (DM * 2u)
                        : ws + (sel == 3u ? ((b * 7u + k) * NH + h) * 8192u : sel == 4u ? ((8u * rt + k) * NH + g4 * 8u) * 8192u : sel == 5u ? X_OFF + u * 131072u + k * 16384u : FLAG_OFF + k * 32768u + 64u);
        asm volatile("s_mov_b32 m0, %0\n\ts_nop 0\n\tglobal_load_lds_dword %1, off sc1" :: "s"(lds0 + SPARE), "v"(wup) : "memory"); }
      if (__builtin_expect((c == 4u || c == 5u) && wid == 3u, 0)) touchKV(c - 1u);
      const unsigned nstage = stage == 2 * STG ? 0u : stage + STG; const int dlt = (int)nstage - (int)stage; stage = nstage;
      SBAR();
      static_for<12>([&](auto I) { constexpr int i = I.value;
        qa[i & 3] = MFMA(af[i % 5], xf[i >> 2], qa[i & 3]);
        af[(i + 4) % 5] = rdA(i + 4);
        if constexpr (i == 0) xf[1] = rd128(xp1); if constexpr (i == 3) xf[2] = rd128(xp0 + 512); if constexpr (i == 7) xf[3] = rd128(xp1 + 512);
        SBAR(); });
      qa[0] = MFMA(af[12 % 5], xf[3], qa[0]);
      wp += dlt; xp0 += dlt; xp1 += dlt; asm volatile("" : "+v"(wp), "+v"(xp0), "+v"(xp1));
      SBAR();
      asm volatile("s_bitcmp1_b32 %0, %1\n\ts_cbranch_scc1 1f\n\ts_waitcnt vmcnt(0)\n1:\n\ts_waitcnt lgkmcnt(0)\n\ts_barrier" :: "s"(0x78u), "s"(c) : "memory", "scc");
      const bf16x8 xl = xf[3], a13 = af[13 % 5], a14 = af[14 % 5], a15 = af[15 % 5];
      xf[0] = rd128(xp0); af[0] = rdA(0); af[1] = rdA(1);
      SBAR();
      qa[1] = MFMA(a13, xl, qa[1]); SBAR();
      af[2] = rdA(2);
      qa[2] = MFMA(a14, xl, qa[2]); SBAR();
      af[3] = rdA(3);
      qa[3] = MFMA(a15, xl, qa[3]); SBAR();
      ST(3 + c);
    }
    lds_cptr k0 = kb0 + 9 * 16384, k1 = kb1 + 9 * 16384; asm volatile("" : "+v"(k0), "+v"(k1));
    bf16x8 ag[16], f[16]; u32x4 qw[8]; float qs = QSCALE; asm volatile("" : "+s"(qs));
#pragma unroll
    for (int i = 0; i < 4; ++i) ag[i] = af[i];
    auto cvq = [&](int j) { float lo = qa[j >> 3][2 * (j & 7)] * qs; pinf(lo); float hi = qa[j >> 3][2 * (j & 7) + 1] * qs; unsigned w = cvtpk(lo, hi); pinu(w); qw[j >> 2][j & 3] = w; };
    SBAR();
    static_for<24>([&](auto G) { constexpr int g = G.value, n = g + 4;
      if constexpr (g < 4) qa[g] = MFMA(ag[g], xf[0], qa[g]);
      else if constexpr (g < 16) qa[(g - 4) / 3] = MFMA(ag[g], xf[1 + (g - 4) % 3], qa[(g - 4) / 3]);
      else { qf[g - 16] = __builtin_bit_cast(bf16x8, qw[g - 16]);
        if constexpr (g == 16) { const f32x16 z = {}; S0 = MFMA(f[0], qf[0], z); } else S0 = MFMA(f[g - 16], qf[g - 16], S0); pin16(S0); }
      if constexpr (g == 0) { xf[1] = rd128(xp1); xf[2] = rd128(xp0 + 512); } if constexpr (g == 1) xf[3] = rd128(xp1 + 512);
      if constexpr (n < 16) ag[n] = rdA(4 * (1 + (n - 4) % 3) + (n - 4) / 3); else f[n - 16] = rdK(k0, k1, (n - 16) >> 3, (n - 16) & 7);
      if constexpr (g >= 9 && g < 21) { constexpr int j0 = (g - 9) * 8 / 3, j1 = (g - 8) * 8 / 3; static_for<j1 - j0>([&](auto J) { cvq(j0 + J.value); }); }
      SBAR(); });
    ST(11);
#pragma unroll
    for (int i = 12; i < 16; ++i) f[i] = rdK(k0, k1, 1, i & 7);
    const float m = swap_max(max16(S0));
#pragma unroll
    for (int i = 0; i < 16; ++i) negm[i] = -m;
    SBAR();
    float e[16]; float s0 = 0.f, s1 = 0.f;
    static_for<9>([&](auto G) { constexpr int g = G.value;
      if constexpr (g == 0) { if (wid >= 4u) bar(); }
      if constexpr (g == A0BAR) { if (wid < 4u) bar();
        constexpr unsigned ko = ((SLOTK >> 4) & 15u) * 16384u;
#pragma unroll
        for (int i = 0; i < RA; ++i) pre[i] = rdK(kb0 + ko, kb1 + ko, 0, i);
        SBAR(); }
      if constexpr (g == 0) S1 = MFMA(f[8], qf[0], negm); else if constexpr (g < 8) S1 = MFMA(f[8 + g], qf[g], S1);
      if constexpr (g < 8) pin16(S1);
      if constexpr (g < 8) { e[2 * g] = __builtin_amdgcn_exp2f(S0[2 * g] - m); e[2 * g + 1] = __builtin_amdgcn_exp2f(S0[2 * g + 1] - m); pinf(e[2 * g]); pinf(e[2 * g + 1]); }
      if constexpr (g == 1) { s0 = e[0]; s1 = e[1]; } else if constexpr (g > 1) { s0 += e[2 * g - 2]; pinf(s0); s1 += e[2 * g - 1]; pinf(s1); }
      if constexpr (g >= 1) { unsigned w = cvtpk(e[2 * g - 2], e[2 * g - 1]); pinu(w); P0[(g - 1) >> 2][(g - 1) & 3] = w; }
      SBAR(); });
    l = s0 + s1; }
  ST(12);
  auto unit = [&](auto KB, auto LAST, lds_cptr k0, lds_cptr k1, lds_cptr v, lds_cptr nk0, lds_cptr nk1, f32x16& Sn, f32x16& Sc, const u32x4 (&Pp)[2], u32x4 (&Pc)[2]) {
    constexpr int kb = KB.value; constexpr bool last = LAST.value;
    float e[16]; float s0 = 0.f, s1 = 0.f; bf16x8 f[16 + RA];
#pragma unroll
    for (int i = 0; i < RA; ++i) f[i] = pre[i];
    static_for<16>([&](auto G) { constexpr int g = G.value; constexpr int n = g + RA; constexpr int gb = 12;
      if constexpr (last && g == gb) { bar(); wait_lgkm<0>(); }
      else if constexpr ((g & 3) == 0) wait_lgkm<lds_ops(g + 4, g + RA, last)>();
      if constexpr (g == 0) Sn = MFMA(f[0], qf[0], negm); else if constexpr (g < 8) Sn = MFMA(f[g], qf[g], Sn);
      else o[g & 3] = MFMA(f[g], __builtin_bit_cast(bf16x8, Pp[(g >> 2) & 1]), o[g & 3]);
      if constexpr (n < 8) f[n] = rdK(k0, k1, kb, n); else if constexpr (n < 16) f[n] = rdV(v, kb, n - 8);
      else if constexpr (!last) f[n] = rdK(k0, k1, kb + 1, n - 16);
      else if constexpr (g >= gb) { constexpr int per = (RA + 15 - gb) / (16 - gb); static_for<per>([&](auto J) { constexpr int i = per * (g - gb) + J.value; if constexpr (i < RA) f[16 + i] = rdK(nk0, nk1, 0, i); }); }
      if constexpr (g == 0) { e[0] = __builtin_amdgcn_exp2f(Sc[0]); pinf(e[0]); }
      if constexpr (g < 15) { e[g + 1] = __builtin_amdgcn_exp2f(Sc[g + 1]); pinf(e[g + 1]); }
      if constexpr (g == 1) { s0 = e[0]; s1 = e[1]; } else if constexpr (g > 1) { if constexpr (g & 1) { s1 += e[g]; pinf(s1); } else { s0 += e[g]; pinf(s0); } }
      if constexpr (g & 1) { unsigned w = cvtpk(e[g - 1], e[g]); pinu(w); Pc[g >> 3][(g >> 1) & 3] = w; }
      SBAR(); });
#pragma unroll
    for (int i = 0; i < RA; ++i) pre[i] = f[16 + i];
    sm_fix(s0 + s1, Sc, Sn, Pc); };
#pragma nounroll
  for (unsigned t = 1; t < 7; ++t) {
    const unsigned ko = ((SLOTK >> (4u * t)) & 15u) * 16384u, vo = ((SLOTV >> (4u * (t - 1u))) & 15u) * 16384u, nko = ((SLOTK >> (4u * ((t + 1u) & 7u))) & 15u) * 16384u;
    SBAR();
    unit(std::integral_constant<int, 0>{}, std::false_type{}, kb0 + ko, kb1 + ko, vb + vo, kb0, kb1, S0, S1, P0, P1);
    unit(std::integral_constant<int, 1>{}, std::true_type{}, kb0 + ko, kb1 + ko, vb + vo, kb0 + nko, kb1 + nko, S1, S0, P1, P0);
    if (t == 1u || t == 6u) ST(12 + t);
  }
  const unsigned me = (b * 7u + wid) * NH + h;
  {
    constexpr unsigned ko = ((SLOTK >> 28) & 15u) * 16384u;
    const lds_cptr k0 = kb0 + ko, k1 = kb1 + ko, v6 = vb + ((SLOTV >> 24) & 15u) * 16384u, v7 = vb + ((SLOTV >> 28) & 15u) * 16384u;
    u32x4 P2[2], P3[2];
    auto scaleP = [&](u32x4 (&P)[2], float f) {
#pragma unroll
      for (int i = 0; i < 8; ++i) { const unsigned w = P[i >> 2][i & 3]; P[i >> 2][i & 3] = cvtpk(__uint_as_float(w << 16) * f, __uint_as_float(w & 0xFFFF0000u) * f); } };
    auto fix = [&](auto NP, float ps, f32x16& Sc, f32x16& Sn, u32x4 (&P)[2]) {
      if (__builtin_expect(__any(!(ps <= SUM_THR)), 0)) {
        const float f = __builtin_amdgcn_exp2f(-fmaxf(swap_max(max16(Sc)), 0.f));
        scaleP(P0, f); if constexpr (NP.value > 1) scaleP(P1, f); if constexpr (NP.value > 2) scaleP(P2, f); }
      sm_fix(ps, Sc, Sn, P); };
    auto rdVt = [&](int i) { const int j = pv_order(i), db = j >> 3, un = (j >> 1) & 3, ks = j & 1; return rdV(un < 2 ? v6 : v7, un & 1, 4 * ks + db); };
    auto pvt = [&](auto I, const bf16x8& vf) { constexpr int j = pv_order(I.value), un = (j >> 1) & 3;
      const u32x4 pb = un == 0 ? P0[j & 1] : un == 1 ? P1[j & 1] : un == 2 ? P2[j & 1] : P3[j & 1];
      o[j >> 3] = MFMA(vf, __builtin_bit_cast(bf16x8, pb), o[j >> 3]); pin16(o[j >> 3]); };
    bf16x8 f[16], w[32];
#pragma unroll
    for (int i = 0; i < RA; ++i) f[i] = pre[i];
    auto qsm = [&](auto H, f32x16& Sn, const f32x16& Sc, u32x4 (&Pc)[2]) { constexpr int hf = H.value;
      float e[16]; float s0 = 0.f, s1 = 0.f;
      static_for<9>([&](auto G) { constexpr int g = G.value, n = 8 * hf + g + RA;
        if constexpr (hf < 2 && g < 8) { if constexpr (g == 0) Sn = MFMA(f[8 * hf], qf[0], negm); else Sn = MFMA(f[8 * hf + g], qf[g], Sn);
          pin16(Sn);
          if constexpr (n < 16) f[n] = rdK(k0, k1, n >> 3, n & 7); }
        if constexpr (hf == 1 && g >= 2 && g < 8) w[g - 2] = rdVt(g - 2);
        if constexpr (hf == 2 && g < 8) { pvt(G, w[g]); w[g + 6] = rdVt(g + 6); }
        if constexpr (g < 8) { e[2 * g] = __builtin_amdgcn_exp2f(Sc[2 * g]); e[2 * g + 1] = __builtin_amdgcn_exp2f(Sc[2 * g + 1]); pinf(e[2 * g]); pinf(e[2 * g + 1]); }
        if constexpr (g == 1) { s0 = e[0]; s1 = e[1]; } else if constexpr (g > 1) { s0 += e[2 * g - 2]; pinf(s0); s1 += e[2 * g - 1]; pinf(s1); }
        if constexpr (g >= 1) { unsigned pw = cvtpk(e[2 * g - 2], e[2 * g - 1]); pinu(pw); Pc[(g - 1) >> 2][(g - 1) & 3] = pw; }
        SBAR(); });
      return s0 + s1; };
    if (wid < 4u) __builtin_amdgcn_s_setprio(1);
    SBAR();
    fix(std::integral_constant<int, 1>{}, qsm(std::integral_constant<int, 0>{}, S0, S1, P1), S1, S0, P1);
    bar_if(wid >> 2);
    fix(std::integral_constant<int, 2>{}, qsm(std::integral_constant<int, 1>{}, S1, S0, P2), S0, S1, P2);
    bar_if((wid >> 2) ^ 1u);
    ST(19);
    fix(std::integral_constant<int, 3>{}, qsm(std::integral_constant<int, 2>{}, S0, S1, P3), S1, S0, P3);
    const float inv = __builtin_amdgcn_rcpf(swap_add(l));
    auto put = [&](int k8) { bstore_sc1(pack8(o[k8 >> 1], k8 & 1, inv), lane * 16u, sO, me * 8192u + k8 * 1024u); };
    static_for<24>([&](auto G) { constexpr int g = G.value + 8;
      pvt(std::integral_constant<int, g>{}, w[g]);
      if constexpr (g + 6 < 32) w[g + 6] = rdVt(g + 6);
      if constexpr (g == 14) put(0); if constexpr (g == 16) put(1); if constexpr (g == 19) put(2); if constexpr (g == 22) put(3); if constexpr (g == 26) put(4); if constexpr (g == 29) put(5);
      SBAR(); });
    ST(20);
    put(6); put(7); }
  ST(21);
  tag = mix64(ep);
  __builtin_amdgcn_s_setprio(0);
  }
  if (wid < 7u) { wait_vm<0>();
  ST(22);
  bar(); }
  if (tid == 0) st_agent(ws + FLAG_OFF + (b * NH + h) * FLAG_STRIDE, tag);
  ST(23);
  if (!has_tile) {
    if (bid == 249u && wid == 0u) {
#pragma nounroll
      for (unsigned i = 0; i < 4; ++i) poll_flags(ws + FLAG_OFF + (i * 64u + lane) * FLAG_STRIDE, tag);
      if (lane == 0) st_agent(ws + EPOCH_OFF, tag); }
    ST(24); ST(25); ST(26); ST(27); ST(28); ST(29); ST(30); ST(31); ST_OUT(ws, 0, 32); return; }
  const unsigned jq = (2u * g4 + 2u + wid) & 7u, G = 8u * rt + jq;
  const unsigned voffO = lane * 16u, soO = (G * NH + g4 * 8u) * 8192u;
  const char* fp = ws + FLAG_OFF + (((G * 37u) >> 8) * NH + g4 * 8u + (lane & 7u)) * FLAG_STRIDE;
  unsigned ready = 0;
  auto need = [&](unsigned m) {
    _Pragma("nounroll") for (unsigned spins = 0; (ready & m) != m && spins < SPIN_CAP; ++spins) { ready = (unsigned)__ballot(ld_agent(fp) == tag); if ((ready & m) != m) __builtin_amdgcn_s_sleep(PSLEEP); }
    asm volatile("" ::: "memory"); };
  __builtin_amdgcn_s_sleep(PDELAY);
  need(1u);
  ST(24);
  const lds_cptr wb = (lds_cptr)lds + 1024u * hh + 64u * q4 + trcol;
  f32x16 acc[2] = {};
  { unsigned hd = 0, so_c = soO + hd * 8192u; lds_cptr wp = wb + ((SLOTW >> (4u * hd)) & 15u) * 16384u;
    bf16x8 of[8], lf[4][2];
    auto rdW = [&](int s2) { lf[s2 & 3][0] = trd2(wp, (s2 & 7) * 2048, (s2 & 7) * 2048 + 256); lf[s2 & 3][1] = trd2(wp, (s2 & 7) * 2048 + 512, (s2 & 7) * 2048 + 768); };
#pragma unroll
    for (int i = 0; i < 8; ++i) { if (i < WO) bload_sc1(of[i], voffO, sO, so_c + i * 1024u); else of[i] = bf16x8{}; }
    rdW(0); rdW(1);
#pragma nounroll
    for (unsigned p = 0; p < 8; ++p) {
      hd = (hd + 1u) & 7u;
      if (p == 5u) __builtin_amdgcn_s_barrier();
      const unsigned so_n = p < 7u ? soO + hd * 8192u : O_BYTES;
      static_for<8>([&](auto S) { constexpr int s2 = S.value;
        if constexpr (s2 == 8 - WO) need(1u << hd);
        if constexpr (s2 == 6) wp = wb + ((SLOTW >> (4u * hd)) & 15u) * 16384u;
        wait_vm1<WO - 1>(of[s2]);
        wait_lgkm<4>();
        acc[0] = MFMA(lf[s2 & 3][0], of[s2], acc[0]);
        rdW(s2 + 2);
        SBAR();
        acc[1] = MFMA(lf[s2 & 3][1], of[s2], acc[1]);
        SBAR();
        breload_sc1(of[(s2 + WO) & 7], voffO, sO, (s2 + WO < 8 ? so_c : so_n) + ((s2 + WO) & 7) * 1024u);
        SBAR(); });
      so_c = so_n;
    }
    wait_vm<0>();
#pragma unroll
    for (int i = 0; i < 8; ++i) pin(of[i]); }
  ST(28);
  const srd_t sX2 = mk_srd(ws + X_OFF, X_BYTES);
  const unsigned xb = (u * 8u + jq) * 4u;
  if (wid < 6u) {
#pragma unroll
    for (int i = 0; i < 4; ++i) bstore_sc1(pack8(acc[i >> 1], i & 1), voffO, sX2, (xb + g4) * 4096u + i * 1024u);
    wait_vm<0>();
    if (lane == 0) st_agent(ws + FLAG2_OFF + (xb + g4) * FLAG_STRIDE, tag);
    ST(29); ST(30);
  } else {
    __builtin_amdgcn_s_sleep(PDELAY2);
    poll_flags(ws + FLAG2_OFF + (xb + ((g4 + 1u + lane % 3u) & 3u)) * FLAG_STRIDE, tag);
    ST(29);
    bf16x8 xp[12];
#pragma unroll
    for (int i = 0; i < 12; ++i) bload_sc1(xp[i], voffO, sX2, (xb + ((g4 + 1u + (i >> 2)) & 3u)) * 4096u + (i & 3) * 1024u);
    wait_vm<0>();
#pragma unroll
    for (int j = 0; j < 12; ++j) { const int i = j & 3; pin(xp[j]); const u32x4 v = __builtin_bit_cast(u32x4, xp[j]);
#pragma unroll
      for (int e = 0; e < 4; ++e) { acc[i >> 1][8 * (i & 1) + 2 * e] += __uint_as_float(v[e] << 16); acc[i >> 1][8 * (i & 1) + 2 * e + 1] += __uint_as_float(v[e] & 0xFFFF0000u); } }
    ST(30);
    char* op = (char*)out + (size_t)(G * 32u + r) * (DM * 2u) + ct * 128u + hh * 16u;
#pragma unroll
    for (int i = 0; i < 4; ++i) *(u32x4*)(op + i * 32) = pack8(acc[i >> 1], i & 1);
  }
  ST(31); ST_OUT(ws, 0, 32);
}

extern "C" void kernel_launch(void* const* d_in, const int* in_sizes, int n_in, void* d_out, int out_size, void* d_ws, size_t ws_size, hipStream_t stream) {
  (void)in_sizes; (void)n_in; (void)out_size; (void)ws_size;
  (void)hipFuncSetAttribute((const void*)mha_kernel, hipFuncAttributeMaxDynamicSharedMemorySize, LDS_BYTES);
  const unsigned short* x = (const unsigned short*)d_in[0]; const unsigned short* kc = (const unsigned short*)d_in[1]; const unsigned short* vc = (const unsigned short*)d_in[2];
  const unsigned short* wq = (const unsigned short*)d_in[3]; const unsigned short* wo = (const unsigned short*)d_in[4];
  mha_kernel<<<256, 512, LDS_BYTES, stream>>>(x, kc, vc, wq, wo, (char*)d_ws, (unsigned short*)d_out);
}
```

```cpp
#define PKC 6u
#define PVC 7u
#include <hip/hip_runtime.h>
#include <stdint.h>
#include <utility>

typedef short    bf16x8 __attribute__((ext_vector_type(8)));
typedef short    s16x4  __attribute__((ext_vector_type(4)));
typedef float    f32x16 __attribute__((ext_vector_type(16)));
typedef float    f32x2  __attribute__((ext_vector_type(2)));
typedef __bf16   bf16x2 __attribute__((ext_vector_type(2)));
typedef unsigned u32x4  __attribute__((ext_vector_type(4)));
typedef __attribute__((address_space(3))) const char* lds_cptr;
typedef __attribute__((address_space(1))) unsigned long long gu64;
typedef __amdgpu_buffer_rsrc_t srd_t;

#define DI __device__ __forceinline__
#define MFMA(a, b, c) __builtin_amdgcn_mfma_f32_32x32x16_bf16((a), (b), (c), 0, 0, 0)
#define SBAR() __builtin_amdgcn_sched_barrier(0)

constexpr int NB = 8, SQ = 224, DM = 576, NH = 32, SK = 512, DH = 128;
constexpr int LDS_BYTES = 163840;
constexpr float QSCALE = 0.08838834764831845f * 1.4426950408889634f;
constexpr float SUM_THR = 4096.f;

template <int... Is, class F> DI void static_for_impl(std::integer_sequence<int, Is...>, F&& f) { (f(std::integral_constant<int, Is>{}), ...); }
template <int N, class F> DI void static_for(F&& f) { static_for_impl(std::make_integer_sequence<int, N>{}, f); }

DI unsigned rfl(unsigned x) { return __builtin_amdgcn_readfirstlane(x); }
DI unsigned cvtpk(float lo, float hi) { f32x2 v = {lo, hi}; bf16x2 b = __builtin_convertvector(v, bf16x2); return __builtin_bit_cast(unsigned, b); }
DI srd_t mk_srd(const void* p, unsigned bytes) { return __builtin_amdgcn_make_buffer_rsrc((void*)p, (short)0, (int)bytes, 0x00020000); }
DI void dma4(unsigned lds, unsigned v0, unsigned v1, unsigned v2, unsigned v3, srd_t srd, unsigned soff) {
  asm volatile("s_mov_b32 m0, %0\n\ts_nop 0\n\tbuffer_load_dwordx4 %1, %5, %6 offen lds\n\tbuffer_load_dwordx4 %2, %5, %6 offen offset:1024 lds\n\t"
               "buffer_load_dwordx4 %3, %5, %6 offen offset:2048 lds\n\tbuffer_load_dwordx4 %4, %5, %6 offen offset:3072 lds"
               :: "s"(lds), "v"(v0), "v"(v1), "v"(v2), "v"(v3), "s"(srd), "s"(soff) : "memory");
}
DI void dma4_nt(unsigned lds, unsigned v0, unsigned v1, unsigned v2, unsigned v3, srd_t srd, unsigned soff) {
  asm volatile("s_mov_b32 m0, %0\n\ts_nop 0\n\tbuffer_load_dwordx4 %1, %5, %6 offen nt lds\n\tbuffer_load_dwordx4 %2, %5, %6 offen offset:1024 nt lds\n\t"
               "buffer_load_dwordx4 %3, %5, %6 offen offset:2048 nt lds\n\tbuffer_load_dwordx4 %4, %5, %6 offen offset:3072 nt lds"
               :: "s"(lds), "v"(v0), "v"(v1), "v"(v2), "v"(v3), "s"(srd), "s"(soff) : "memory");
}
DI void touch2(unsigned lds, unsigned voff, srd_t srd, unsigned s0, unsigned s1) { asm volatile("s_mov_b32 m0, %0\n\ts_nop 0\n\tbuffer_load_dword %1, %2, %3 offen nt lds\n\tbuffer_load_dword %1, %2, %4 offen nt lds" :: "s"(lds), "v"(voff), "s"(srd), "s"(s0), "s"(s1) : "memory"); }
DI void breload_nt(bf16x8& d, unsigned voff, srd_t srd, unsigned soff) { asm volatile("buffer_load_dwordx4 %0, %1, %2, %3 offen nt" : "=v"(d) : "v"(voff), "s"(srd), "s"(soff) : "memory"); }
DI void bstore_sc1(const u32x4& d, unsigned voff, srd_t srd, unsigned soff) { asm volatile("buffer_store_dwordx4 %0, %1, %2, %3 offen sc1\n\ts_nop 1" :: "v"(d), "v"(voff), "s"(srd), "s"(soff) : "memory"); }
DI void bload_sc1(bf16x8& d, unsigned voff, srd_t srd, unsigned soff) { asm volatile("buffer_load_dwordx4 %0, %1, %2, %3 offen sc1" : "=v"(d) : "v"(voff), "s"(srd), "s"(soff) : "memory"); }
DI void breload_sc1(bf16x8& d, unsigned voff, srd_t srd, unsigned soff) { asm volatile("buffer_load_dwordx4 %0, %1, %2, %3 offen sc1" : "+v"(d) : "v"(voff), "s"(srd), "s"(soff) : "memory"); }
DI void pin(bf16x8& a) { asm volatile("" : "+v"(a)); }
DI void pin16(f32x16& a) { asm volatile("" : "+v"(a)); }
DI void pinf(float& a) { asm volatile("" : "+v"(a)); }
DI void pinu(unsigned& a) { asm volatile("" : "+v"(a)); }
template <int N> DI void wait_vm1(bf16x8& a) { asm volatile("s_waitcnt vmcnt(%1)" : "+v"(a) : "n"(N) : "memory"); }
template <int N> DI void wait_vm() { asm volatile("s_waitcnt vmcnt(%0)" :: "n"(N) : "memory"); }
template <int N> DI void wait_lgkm() { __builtin_amdgcn_s_waitcnt(0xC07F | (N << 8)); SBAR(); }
DI void bar_if(unsigned c) { asm volatile("s_cmp_eq_u32 %0, 0\n\ts_cbranch_scc1 1f\n\ts_waitcnt lgkmcnt(0)\n\ts_barrier\n1:" :: "s"(c) : "memory", "scc"); }
DI void bar() { asm volatile("s_waitcnt lgkmcnt(0)\n\ts_barrier" ::: "memory"); }
typedef short v4i16_t __attribute__((ext_vector_type(4)));
DI s16x4 trd(lds_cptr p) { return __builtin_amdgcn_ds_read_tr16_b64_v4i16((__attribute__((address_space(3))) v4i16_t*)p); }
DI bf16x8 trd2(lds_cptr p, int o0, int o1) { s16x4 a = trd(p + o0), b = trd(p + o1); return __builtin_shufflevector(a, b, 0, 1, 2, 3, 4, 5, 6, 7); }
DI bf16x8 rd128(lds_cptr p) { return *(const __attribute__((address_space(3))) bf16x8*)p; }
DI float swap_max(float m) { auto rr = __builtin_amdgcn_permlane32_swap(__float_as_uint(m), __float_as_uint(m), false, false); return fmaxf(__uint_as_float(rr[0]), __uint_as_float(rr[1])); }
DI float swap_add(float m) { auto rr = __builtin_amdgcn_permlane32_swap(__float_as_uint(m), __float_as_uint(m), false, false); return __uint_as_float(rr[0]) + __uint_as_float(rr[1]); }
DI float max16(const f32x16& s) { float a = fmaxf(fmaxf(s[0], s[1]), s[2]), b = fmaxf(fmaxf(s[3], s[4]), s[5]);
  a = fmaxf(fmaxf(a, s[6]), s[7]); b = fmaxf(fmaxf(b, s[8]), s[9]); a = fmaxf(fmaxf(a, s[10]), s[11]); b = fmaxf(fmaxf(b, s[12]), s[13]); a = fmaxf(fmaxf(a, s[14]), s[15]); return fmaxf(a, b); }
DI u32x4 pack8(const f32x16& a, int s, float f) { return u32x4{cvtpk(a[8 * s] * f, a[8 * s + 1] * f), cvtpk(a[8 * s + 2] * f, a[8 * s + 3] * f), cvtpk(a[8 * s + 4] * f, a[8 * s + 5] * f), cvtpk(a[8 * s + 6] * f, a[8 * s + 7] * f)}; }
DI u32x4 pack8(const f32x16& a, int s) { return u32x4{cvtpk(a[8 * s], a[8 * s + 1]), cvtpk(a[8 * s + 2], a[8 * s + 3]), cvtpk(a[8 * s + 4], a[8 * s + 5]), cvtpk(a[8 * s + 6], a[8 * s + 7])}; }

DI float expo(const f32x16& s, u32x4 (&p)[2]) {
  float e[16]; float s0 = 0.f, s1 = 0.f;
#pragma unroll
  for (int i = 0; i < 16; ++i) e[i] = __builtin_amdgcn_exp2f(s[i]);
#pragma unroll
  for (int i = 0; i < 16; i += 2) { s0 += e[i]; s1 += e[i + 1]; }
#pragma unroll
  for (int i = 0; i < 8; ++i) p[i >> 2][i & 3] = cvtpk(e[2 * i], e[2 * i + 1]);
  return s0 + s1;
}

#define ST_DECL
#define ST(k)
#define ST_OUT(ws, lo, hi)

constexpr unsigned SLOTK = 0x31864209u;
constexpr unsigned SLOTV = 0x54207531u;
constexpr unsigned SLOTW = 0x32108769u;
constexpr unsigned STG = 45056u, STG_W = 28672u;
constexpr unsigned O_BYTES = NB * SQ * NH * DH * 2;
constexpr unsigned X_OFF = O_BYTES, X_BYTES = 63u * 8u * 4u * 4096u;
constexpr unsigned FLAG_OFF = X_OFF + X_BYTES, FLAG_STRIDE = 128, FLAG2_OFF = FLAG_OFF + 256 * FLAG_STRIDE, EPOCH_OFF = FLAG2_OFF + 63 * 32 * FLAG_STRIDE;
#define WOT 0x87741000000ull
constexpr unsigned SPIN_CAP = 1u << 20;
constexpr int RA = 7, WO = 6, A0BAR = 6, PSLEEP = 4, PDELAY = 20, PDELAY2 = 25;
constexpr unsigned SPARE = 3u * STG;
constexpr unsigned CODE_BYTES = 21504u;
constexpr int lds_ops(int a, int b, bool last) { int n = 0; for (int i = a; i < b; ++i) n += i >= 16 ? (last ? 0 : 1) : (i >= 8 ? 2 : 1); return n; }
constexpr int pv_order(int i) { return i < 4 ? i : i < 8 ? i + 4 : i < 12 ? i - 4 : i; }
DI unsigned long long mix64(unsigned long long z) { z += 0x9E3779B97F4A7C15ull; z = (z ^ (z >> 30)) * 0xBF58476D1CE4E5B9ull; z = (z ^ (z >> 27)) * 0x94D049BB133111EBull; return z ^ (z >> 31); }
DI unsigned long long rfl64(unsigned long long v) { return ((unsigned long long)rfl((unsigned)(v >> 32)) << 32) | rfl((unsigned)v); }
DI unsigned long long ld_agent(const char* p) { return __hip_atomic_load((gu64*)p, __ATOMIC_RELAXED, __HIP_MEMORY_SCOPE_AGENT); }
DI void st_agent(char* p, unsigned long long v) { __hip_atomic_store((gu64*)p, v, __ATOMIC_RELAXED, __HIP_MEMORY_SCOPE_AGENT); }
DI void poll_flags(const char* p, unsigned long long tag) {
  _Pragma("nounroll") for (unsigned spins = 0; spins < SPIN_CAP; ++spins) { if (__all(ld_agent(p) == tag)) break; __builtin_amdgcn_s_sleep(PSLEEP); }
  asm volatile("" ::: "memory");
}

__global__ __launch_bounds__(512, 2) void mha_kernel(const unsigned short* __restrict__ x, const unsigned short* __restrict__ kc, const unsigned short* __restrict__ vc,
                                                     const unsigned short* __restrict__ wq, const unsigned short* __restrict__ wo, char* ws, unsigned short* __restrict__ out) {
  extern __shared__ __attribute__((aligned(16))) char lds[];
  unsigned long long pc0; asm volatile("s_getpc_b64 %0" : "=s"(pc0));
  const unsigned tid = threadIdx.x, lane = tid & 63u, r = lane & 31u, hh = lane >> 5;
  const unsigned wid = rfl(tid >> 6);
  ST_DECL ST(0);
  const unsigned bid = blockIdx.x, xcd = bid & 7u, ix = bid >> 3;
  const unsigned b = (xcd & 1u) * 4u + (ix & 3u), h = (xcd >> 1) * 8u + (ix >> 2);
  const unsigned lds0 = (unsigned)(uintptr_t)lds;
  {
    const char* cp = (const char*)(pc0 & ~255ull) + min((ix * 8u + wid) * 128u, CODE_BYTES);
    asm volatile("s_mov_b32 m0, %0\n\ts_nop 0\n\tglobal_load_lds_dword %1, %2" :: "s"(lds0 + SPARE), "v"(0u), "s"(cp) : "memory"); }
  const srd_t sW = mk_srd((const char*)wq - 1024, DM * NH * DH * 2 + 1024);
  const srd_t sX = mk_srd(x + (size_t)b * SQ * DM, SQ * DM * 2);
  const unsigned prow = (lane >> 2) & 7u, pch = 4u * hh + (lane & 3u);
  const unsigned pchx0 = 4u * hh + ((lane & 3u) ^ ((lane >> 4) & 1u)), pchx1 = pchx0 ^ 2u;
  const unsigned vW = prow * 8192u + pch * 16u, vX0 = prow * (DM * 2u) + pchx0 * 16u, vX1 = prow * (DM * 2u) + pchx1 * 16u;
  const unsigned ck0 = 3u * (((ix & 3u) + (ix >> 2)) % 3u);
  unsigned long long ep; asm volatile("global_load_dwordx2 %0, %1, %2 sc1" : "=v"(ep) : "v"(0u), "s"(ws + EPOCH_OFF) : "memory");
  dma4(lds0 + (wid < 7u ? wid * 4096u : SPARE), vX0, vX1 + 8192u, vX0 + 16384u, vX1 + 24576u, sX, ck0 * 128u + min(wid, 6u) * 32u * DM * 2u);
  dma4(lds0 + (wid >> 2) * STG + STG_W + (wid & 3u) * 4096u, vW + 1024u, vW + 128u, vW + 65536u - 1024u, vW + 65664u - 2048u, sW, (ck0 + (wid >> 2)) * 64u * 8192u + h * 256u + (wid & 3u) * 131072u);
  const srd_t sK = mk_srd((const char*)(kc + (size_t)(b * NH + h) * SK * DH) - 1024, SK * DH * 2 + 1024);
  const srd_t sV = mk_srd((const char*)(vc + (size_t)(b * NH + h) * SK * DH) - 1024, SK * DH * 2 + 1024);
  const unsigned vK0 = prow * 256u + pchx0 * 16u, vK1 = prow * 256u + pchx1 * 16u;
  const unsigned q4 = (lane & 15u) >> 2, p4 = lane & 3u, blk = (lane >> 4) & 1u;
  const unsigned trcol = 32u * blk + 16u * (p4 & 1u) + 8u * (p4 >> 1);
  const srd_t sO = mk_srd(ws, O_BYTES);
  unsigned long long tag;
  const unsigned g4 = xcd >> 1, u0 = (xcd & 1u) * 32u + ix;
  const bool has_tile = u0 < 63u;
  const unsigned u = has_tile ? u0 : 62u, rt = u / 9u, ct = u - rt * 9u;

  if (wid == 7u) {
    auto fillQ = [&](unsigned c, unsigned stage) {
      const unsigned lw = lds0 + stage + STG_W, sw = c * 64u * 8192u + h * 256u, lx = lds0 + stage, sx = c * 128u;
#pragma unroll
      for (int i = 0; i < 4; ++i) dma4(lw + i * 4096u, vW + 1024u, vW + 128u, vW + 65536u - 1024u, vW + 65664u - 2048u, sW, sw + i * 131072u);
#pragma unroll
      for (int i = 0; i < 7; ++i) dma4(lx + i * 4096u, vX0, vX1 + 8192u, vX0 + 16384u, vX1 + 24576u, sX, sx + i * 32u * DM * 2u); };
    const unsigned vV = prow * 256u + pch * 16u;
    bf16x8 pk[16], pv[16];
    { unsigned stage = 2 * STG, ck = ck0 + 2u;
      auto seg = [&](unsigned c0, unsigned c1) {
#pragma nounroll
        for (unsigned c = c0; c < c1; ++c) { bar(); fillQ(ck, stage); ck = ck == 8u ? 0u : ck + 1u; stage = stage == 2 * STG ? 0u : stage + STG; wait_vm<44>(); } };
      seg(2, PKC + 1u);
#pragma unroll
      for (int i = 0; i < 16; ++i) breload_nt(pk[i], ((i >> 1) & 1) ? vK1 : vK0, sK, 1024u + 32768u + (i >> 1) * 2048u + (i & 1) * 128u);
      seg(PKC + 1u, PVC + 1u);
#pragma unroll
      for (int i = 0; i < 16; ++i) breload_nt(pv[i], vV, sV, 1024u + 16384u + (i >> 1) * 2048u + (i & 1) * 128u);
      seg(PVC + 1u, 9);
      bar(); }
    const srd_t sWo = mk_srd(wo, NH * DH * DM * 2);
    const unsigned vWo = prow * (DM * 2u) + pch * 16u;
    { unsigned j = 1, jw = 0;
      auto kv = [&](unsigned n0, unsigned n1) {
#pragma nounroll
      for (unsigned n = n0; n < n1; ++n) {
        if (n == 10u) { wait_vm<0>(); bar(); }
        if (n == 1u) j = 5u;
        const unsigned je = ((unsigned)(0xFFFFFECA852ull >> (4u * n)) & 15u) + 1u;
#pragma nounroll
        for (; j < je; ++j) {
          if ((j & 1u) && j != 15u) { const unsigned t = (j + 1u) >> 1, ld = lds0 + ((SLOTK >> (4u * t)) & 15u) * 16384u;
#pragma unroll
            for (int i = 0; i < 4; ++i) dma4_nt(ld + i * 4096u, vK0 + 1024u, vK0 + 128u, vK1 + 1024u, vK1 + 128u, sK, t * 16384u + i * 4096u);
          } else { const unsigned t = (j - 1u) >> 1, ld = lds0 + ((SLOTV >> (4u * t)) & 15u) * 16384u;
#pragma unroll
            for (int i = 0; i < 4; ++i) dma4_nt(ld + i * 4096u, vV + 1024u, vV + 128u, vV + 1024u, vV + 128u, sV, t * 16384u + i * 4096u); } }
        const unsigned we = (unsigned)(WOT >> (4u * n)) & 15u;
#pragma nounroll
        for (; jw < we; ++jw) { const unsigned ld = lds0 + ((SLOTW >> (4u * jw)) & 15u) * 16384u, so = (g4 * 8u + jw) * 128u * (DM * 2u) + ct * 128u;
#pragma unroll
          for (int k = 0; k < 4; ++k) dma4(ld + k * 4096u, vWo, vWo + 8192u, vWo + 16384u, vWo + 24576u, sWo, so + k * 32u * (DM * 2u)); }
        if (n >= 9u) continue;
        if (n == 0u) wait_vm<32>(); else if (n == 1u) wait_vm<16>(); else wait_vm<62>();
        bar(); } };
      kv(0, 1);
        { __attribute__((address_space(3))) char* lw = (__attribute__((address_space(3))) char*)lds + lane * 16u;
#pragma unroll
          for (int i = 0; i < 16; ++i) { pin(pk[i]); *(__attribute__((address_space(3))) bf16x8*)(lw + 2 * 16384 + i * 1024) = pk[i]; }
#pragma unroll
          for (int i = 0; i < 16; ++i) { pin(pv[i]); *(__attribute__((address_space(3))) bf16x8*)(lw + 3 * 16384 + i * 1024) = pv[i]; } }
      kv(1, 11); }
    asm volatile("" : "+v"(ep)); tag = mix64(rfl64(ep));
    ST(20); ST(21);
  } else {
  const unsigned rsw0 = 64u * (r & 7u) + 16u * (hh ^ ((r >> 2) & 3u)), rsw1 = rsw0 ^ 32u;
  f32x16 qa[4] = {}, o[4] = {};
  asm volatile("" : "+v"(qa[0]), "+v"(qa[1]), "+v"(qa[2]), "+v"(qa[3]), "+v"(o[0]), "+v"(o[1]), "+v"(o[2]), "+v"(o[3]));
  wait_lgkm<0>();
  wait_vm<4>(); if (wid < 4u) wait_vm<0>();
  ST(1);
  dma4(lds0 + STG + wid * 4096u, vX0, vX1 + 8192u, vX0 + 16384u, vX1 + 24576u, sX, (ck0 + 1u) * 128u + wid * 32u * DM * 2u);
  bar();
  asm volatile("" : "+v"(ep)); ep = rfl64(ep);
  ST(2);
  const lds_cptr kb0 = (lds_cptr)lds + 2048u * (r >> 3) + rsw0, kb1 = (lds_cptr)lds + 2048u * (r >> 3) + rsw1;
  const lds_cptr vb = (lds_cptr)lds + 64u * (4u * hh + q4) + trcol;
  bf16x8 qf[8], pre[RA];
  f32x16 negm, S0, S1; u32x4 P0[2], P1[2]; float l = 0.f;
  auto rdK = [&](lds_cptr k0, lds_cptr k1, int kb, int i) { return rd128(((i & 1) ? k1 : k0) + 8192 * kb + 512 * (i >> 1)); };
  auto rdV = [&](lds_cptr v, int kb, int j) { return trd2(v, 8192 * kb + 4096 * (j >> 2) + 512 * (j & 3), 8192 * kb + 4096 * (j >> 2) + 2048 + 512 * (j & 3)); };
  auto sm_fix = [&](float ps, f32x16& Sc, f32x16& Sn, u32x4 (&P)[2]) {
    if (__builtin_expect(__any(!(ps <= SUM_THR)), 0)) {
      const float dl = fmaxf(swap_max(max16(Sc)), 0.f), f = __builtin_amdgcn_exp2f(-dl);
#pragma unroll
      for (int i = 0; i < 16; ++i) { Sc[i] -= dl; Sn[i] -= dl; negm[i] -= dl; }
#pragma unroll
      for (int db = 0; db < 4; ++db)
#pragma unroll
        for (int i = 0; i < 16; ++i) o[db][i] *= f;
      l *= f; ps = expo(Sc, P); }
    l += ps; };
  { lds_cptr wp = (lds_cptr)lds + STG_W + 2048u * hh + 64u * q4 + trcol;
    lds_cptr xp0 = (lds_cptr)lds + wid * 4096u + 1024u * (r >> 3) + rsw0, xp1 = (lds_cptr)lds + wid * 4096u + 1024u * (r >> 3) + rsw1;
    bf16x8 xf[4], af[5];
    auto rdA = [&](int i) { return trd2(wp, 4096 * (i >> 2) + 512 * (i & 3), 4096 * (i >> 2) + 512 * (i & 3) + 256); };
    xf[0] = rd128(xp0); af[0] = rdA(0); af[1] = rdA(1); af[2] = rdA(2); af[3] = rdA(3);
    unsigned stage = 0;
#pragma nounroll
    for (unsigned c = 0; c < 8; ++c) {
      if (__builtin_expect(c == 3u && wid == 3u, 0)) {
#pragma unroll
        for (int i = 0; i < 4; ++i) dma4_nt(lds0 + 9u * 16384u + i * 4096u, vK0 + 1024u, vK0 + 128u, vK1 + 1024u, vK1 + 128u, sK, i * 4096u);
        unsigned ln = lane; asm volatile("" : "+v"(ln));
        const unsigned k = ln & 7u, sel = ln >> 3;
        const char* wup = sel == 0u ? (const char*)vc + (size_t)(b * NH + h) * (SK * DH * 2) + k * 16384u
                        : sel == 1u ? (const char*)wo + (size_t)(g4 * 1024u + k * 128u) * (DM * 2u) + ct * 128u
                        : sel == 2u ? (const char*)out + (size_t)((8u * rt + k) * 32u) * (DM * 2u)
                        : ws + (sel == 3u ? ((b * 7u + k) * NH + h) * 8192u : sel == 4u ? ((8u * rt + k) * NH + g4 * 8u) * 8192u : sel == 5u ? X_OFF + u * 131072u + k * 16384u : FLAG_OFF + k * 32768u + 64u);
        asm volatile("s_mov_b32 m0, %0\n\ts_nop 0\n\tglobal_load_lds_dword %1, off sc1" :: "s"(lds0 + SPARE), "v"(wup) : "memory"); }
      const unsigned nstage = stage == 2 * STG ? 0u : stage + STG; const int dlt = (int)nstage - (int)stage; stage = nstage;
      SBAR();
      static_for<12>([&](auto I) { constexpr int i = I.value;
        qa[i & 3] = MFMA(af[i % 5], xf[i >> 2], qa[i & 3]);
        af[(i + 4) % 5] = rdA(i + 4);
        if constexpr (i == 0) xf[1] = rd128(xp1); if constexpr (i == 3) xf[2] = rd128(xp0 + 512); if constexpr (i == 7) xf[3] = rd128(xp1 + 512);
        SBAR(); });
      qa[0] = MFMA(af[12 % 5], xf[3], qa[0]);
      wp += dlt; xp0 += dlt; xp1 += dlt; asm volatile("" : "+v"(wp), "+v"(xp0), "+v"(xp1));
      SBAR();
      asm volatile("s_bitcmp1_b32 %0, %1\n\ts_cbranch_scc1 1f\n\ts_waitcnt vmcnt(0)\n1:\n\ts_waitcnt lgkmcnt(0)\n\ts_barrier" :: "s"(0x78u), "s"(c) : "memory", "scc");
      const bf16x8 xl = xf[3], a13 = af[13 % 5], a14 = af[14 % 5], a15 = af[15 % 5];
      xf[0] = rd128(xp0); af[0] = rdA(0); af[1] = rdA(1);
      SBAR();
      qa[1] = MFMA(a13, xl, qa[1]); SBAR();
      af[2] = rdA(2);
      qa[2] = MFMA(a14, xl, qa[2]); SBAR();
      af[3] = rdA(3);
      qa[3] = MFMA(a15, xl, qa[3]); SBAR();
      ST(3 + c);
    }
    lds_cptr k0 = kb0 + 9 * 16384, k1 = kb1 + 9 * 16384; asm volatile("" : "+v"(k0), "+v"(k1));
    bf16x8 ag[16], f[16]; u32x4 qw[8]; float qs = QSCALE; asm volatile("" : "+s"(qs));
#pragma unroll
    for (int i = 0; i < 4; ++i) ag[i] = af[i];
    auto cvq = [&](int j) { float lo = qa[j >> 3][2 * (j & 7)] * qs; pinf(lo); float hi = qa[j >> 3][2 * (j & 7) + 1] * qs; unsigned w = cvtpk(lo, hi); pinu(w); qw[j >> 2][j & 3] = w; };
    SBAR();
    static_for<24>([&](auto G) { constexpr int g = G.value, n = g + 4;
      if constexpr (g < 4) qa[g] = MFMA(ag[g], xf[0], qa[g]);
      else if constexpr (g < 16) qa[(g - 4) / 3] = MFMA(ag[g], xf[1 + (g - 4) % 3], qa[(g - 4) / 3]);
      else { qf[g - 16] = __builtin_bit_cast(bf16x8, qw[g - 16]);
        if constexpr (g == 16) { const f32x16 z = {}; S0 = MFMA(f[0], qf[0], z); } else S0 = MFMA(f[g - 16], qf[g - 16], S0); pin16(S0); }
      if constexpr (g == 0) { xf[1] = rd128(xp1); xf[2] = rd128(xp0 + 512); } if constexpr (g == 1) xf[3] = rd128(xp1 + 512);
      if constexpr (n < 16) ag[n] = rdA(4 * (1 + (n - 4) % 3) + (n - 4) / 3); else f[n - 16] = rdK(k0, k1, (n - 16) >> 3, (n - 16) & 7);
      if constexpr (g >= 9 && g < 21) { constexpr int j0 = (g - 9) * 8 / 3, j1 = (g - 8) * 8 / 3; static_for<j1 - j0>([&](auto J) { cvq(j0 + J.value); }); }
      SBAR(); });
    ST(11);
#pragma unroll
    for (int i = 12; i < 16; ++i) f[i] = rdK(k0, k1, 1, i & 7);
    const float m = swap_max(max16(S0));
#pragma unroll
    for (int i = 0; i < 16; ++i) negm[i] = -m;
    SBAR();
    float e[16]; float s0 = 0.f, s1 = 0.f;
    static_for<9>([&](auto G) { constexpr int g = G.value;
      if constexpr (g == 0) { if (wid >= 4u) bar(); }
      if constexpr (g == A0BAR) { if (wid < 4u) bar();
        constexpr unsigned ko = ((SLOTK >> 4) & 15u) * 16384u;
#pragma unroll
        for (int i = 0; i < RA; ++i) pre[i] = rdK(kb0 + ko, kb1 + ko, 0, i);
        SBAR(); }
      if constexpr (g == 0) S1 = MFMA(f[8], qf[0], negm); else if constexpr (g < 8) S1 = MFMA(f[8 + g], qf[g], S1);
      if constexpr (g < 8) pin16(S1);
      if constexpr (g < 8) { e[2 * g] = __builtin_amdgcn_exp2f(S0[2 * g] - m); e[2 * g + 1] = __builtin_amdgcn_exp2f(S0[2 * g + 1] - m); pinf(e[2 * g]); pinf(e[2 * g + 1]); }
      if constexpr (g == 1) { s0 = e[0]; s1 = e[1]; } else if constexpr (g > 1) { s0 += e[2 * g - 2]; pinf(s0); s1 += e[2 * g - 1]; pinf(s1); }
      if constexpr (g >= 1) { unsigned w = cvtpk(e[2 * g - 2], e[2 * g - 1]); pinu(w); P0[(g - 1) >> 2][(g - 1) & 3] = w; }
      SBAR(); });
    l = s0 + s1; }
  ST(12);
  auto unit = [&](auto KB, auto LAST, lds_cptr k0, lds_cptr k1, lds_cptr v, lds_cptr nk0, lds_cptr nk1, f32x16& Sn, f32x16& Sc, const u32x4 (&Pp)[2], u32x4 (&Pc)[2]) {
    constexpr int kb = KB.value; constexpr bool last = LAST.value;
    float e[16]; float s0 = 0.f, s1 = 0.f; bf16x8 f[16 + RA];
#pragma unroll
    for (int i = 0; i < RA; ++i) f[i] = pre[i];
    static_for<16>([&](auto G) { constexpr int g = G.value; constexpr int n = g + RA; constexpr int gb = 12;
      if constexpr (last && g == gb) { bar(); wait_lgkm<0>(); }
      else if constexpr ((g & 3) == 0) wait_lgkm<lds_ops(g + 4, g + RA, last)>();
      if constexpr (g == 0) Sn = MFMA(f[0], qf[0], negm); else if constexpr (g < 8) Sn = MFMA(f[g], qf[g], Sn);
      else o[g & 3] = MFMA(f[g], __builtin_bit_cast(bf16x8, Pp[(g >> 2) & 1]), o[g & 3]);
      if constexpr (n < 8) f[n] = rdK(k0, k1, kb, n); else if constexpr (n < 16) f[n] = rdV(v, kb, n - 8);
      else if constexpr (!last) f[n] = rdK(k0, k1, kb + 1, n - 16);
      else if constexpr (g >= gb) { constexpr int per = (RA + 15 - gb) / (16 - gb); static_for<per>([&](auto J) { constexpr int i = per * (g - gb) + J.value; if constexpr (i < RA) f[16 + i] = rdK(nk0, nk1, 0, i); }); }
      if constexpr (g == 0) { e[0] = __builtin_amdgcn_exp2f(Sc[0]); pinf(e[0]); }
      if constexpr (g < 15) { e[g + 1] = __builtin_amdgcn_exp2f(Sc[g + 1]); pinf(e[g + 1]); }
      if constexpr (g == 1) { s0 = e[0]; s1 = e[1]; } else if constexpr (g > 1) { if constexpr (g & 1) { s1 += e[g]; pinf(s1); } else { s0 += e[g]; pinf(s0); } }
      if constexpr (g & 1) { unsigned w = cvtpk(e[g - 1], e[g]); pinu(w); Pc[g >> 3][(g >> 1) & 3] = w; }
      SBAR(); });
#pragma unroll
    for (int i = 0; i < RA; ++i) pre[i] = f[16 + i];
    sm_fix(s0 + s1, Sc, Sn, Pc); };
#pragma nounroll
  for (unsigned t = 1; t < 7; ++t) {
    const unsigned ko = ((SLOTK >> (4u * t)) & 15u) * 16384u, vo = ((SLOTV >> (4u * (t - 1u))) & 15u) * 16384u, nko = ((SLOTK >> (4u * ((t + 1u) & 7u))) & 15u) * 16384u;
    SBAR();
    unit(std::integral_constant<int, 0>{}, std::false_type{}, kb0 + ko, kb1 + ko, vb + vo, kb0, kb1, S0, S1, P0, P1);
    unit(std::integral_constant<int, 1>{}, std::true_type{}, kb0 + ko, kb1 + ko, vb + vo, kb0 + nko, kb1 + nko, S1, S0, P1, P0);
    if (t == 1u || t == 6u) ST(12 + t);
  }
  const unsigned me = (b * 7u + wid) * NH + h;
  {
    constexpr unsigned ko = ((SLOTK >> 28) & 15u) * 16384u;
    const lds_cptr k0 = kb0 + ko, k1 = kb1 + ko, v6 = vb + ((SLOTV >> 24) & 15u) * 16384u, v7 = vb + ((SLOTV >> 28) & 15u) * 16384u;
    u32x4 P2[2], P3[2];
    auto scaleP = [&](u32x4 (&P)[2], float f) {
#pragma unroll
      for (int i = 0; i < 8; ++i) { const unsigned w = P[i >> 2][i & 3]; P[i >> 2][i & 3] = cvtpk(__uint_as_float(w << 16) * f, __uint_as_float(w & 0xFFFF0000u) * f); } };
    auto fix = [&](auto NP, float ps, f32x16& Sc, f32x16& Sn, u32x4 (&P)[2]) {
      if (__builtin_expect(__any(!(ps <= SUM_THR)), 0)) {
        const float f = __builtin_amdgcn_exp2f(-fmaxf(swap_max(max16(Sc)), 0.f));
        scaleP(P0, f); if constexpr (NP.value > 1) scaleP(P1, f); if constexpr (NP.value > 2) scaleP(P2, f); }
      sm_fix(ps, Sc, Sn, P); };
    auto rdVt = [&](int i) { const int j = pv_order(i), db = j >> 3, un = (j >> 1) & 3, ks = j & 1; return rdV(un < 2 ? v6 : v7, un & 1, 4 * ks + db); };
    auto pvt = [&](auto I, const bf16x8& vf) { constexpr int j = pv_order(I.value), un = (j >> 1) & 3;
      const u32x4 pb = un == 0 ? P0[j & 1] : un == 1 ? P1[j & 1] : un == 2 ? P2[j & 1] : P3[j & 1];
      o[j >> 3] = MFMA(vf, __builtin_bit_cast(bf16x8, pb), o[j >> 3]); pin16(o[j >> 3]); };
    bf16x8 f[16], w[32];
#pragma unroll
    for (int i = 0; i < RA; ++i) f[i] = pre[i];
    auto qsm = [&](auto H, f32x16& Sn, const f32x16& Sc, u32x4 (&Pc)[2]) { constexpr int hf = H.value;
      float e[16]; float s0 = 0.f, s1 = 0.f;
      static_for<9>([&](auto G) { constexpr int g = G.value, n = 8 * hf + g + RA;
        if constexpr (hf < 2 && g < 8) { if constexpr (g == 0) Sn = MFMA(f[8 * hf], qf[0], negm); else Sn = MFMA(f[8 * hf + g], qf[g], Sn);
          pin16(Sn);
          if constexpr (n < 16) f[n] = rdK(k0, k1, n >> 3, n & 7); }
        if constexpr (hf == 1 && g >= 2 && g < 8) w[g - 2] = rdVt(g - 2);
        if constexpr (hf == 2 && g < 8) { pvt(G, w[g]); w[g + 6] = rdVt(g + 6); }
        if constexpr (g < 8) { e[2 * g] = __builtin_amdgcn_exp2f(Sc[2 * g]); e[2 * g + 1] = __builtin_amdgcn_exp2f(Sc[2 * g + 1]); pinf(e[2 * g]); pinf(e[2 * g + 1]); }
        if constexpr (g == 1) { s0 = e[0]; s1 = e[1]; } else if constexpr (g > 1) { s0 += e[2 * g - 2]; pinf(s0); s1 += e[2 * g - 1]; pinf(s1); }
        if constexpr (g >= 1) { unsigned pw = cvtpk(e[2 * g - 2], e[2 * g - 1]); pinu(pw); Pc[(g - 1) >> 2][(g - 1) & 3] = pw; }
        SBAR(); });
      return s0 + s1; };
    if (wid < 4u) __builtin_amdgcn_s_setprio(1);
    SBAR();
    fix(std::integral_constant<int, 1>{}, qsm(std::integral_constant<int, 0>{}, S0, S1, P1), S1, S0, P1);
    bar_if(wid >> 2);
    fix(std::integral_constant<int, 2>{}, qsm(std::integral_constant<int, 1>{}, S1, S0, P2), S0, S1, P2);
    bar_if((wid >> 2) ^ 1u);
    ST(19);
    fix(std::integral_constant<int, 3>{}, qsm(std::integral_constant<int, 2>{}, S0, S1, P3), S1, S0, P3);
    const float inv = __builtin_amdgcn_rcpf(swap_add(l));
    auto put = [&](int k8) { bstore_sc1(pack8(o[k8 >> 1], k8 & 1, inv), lane * 16u, sO, me * 8192u + k8 * 1024u); };
    static_for<24>([&](auto G) { constexpr int g = G.value + 8;
      pvt(std::integral_constant<int, g>{}, w[g]);
      if constexpr (g + 6 < 32) w[g + 6] = rdVt(g + 6);
      if constexpr (g == 14) put(0); if constexpr (g == 16) put(1); if constexpr (g == 19) put(2); if constexpr (g == 22) put(3); if constexpr (g == 26) put(4); if constexpr (g == 29) put(5);
      SBAR(); });
    ST(20);
    put(6); put(7); }
  ST(21);
  tag = mix64(ep);
  __builtin_amdgcn_s_setprio(0);
  }
  if (wid < 7u) { wait_vm<0>();
  ST(22);
  bar(); }
  if (tid == 0) st_agent(ws + FLAG_OFF + (b * NH + h) * FLAG_STRIDE, tag);
  ST(23);
  if (!has_tile) {
    if (bid == 249u && wid == 0u) {
#pragma nounroll
      for (unsigned i = 0; i < 4; ++i) poll_flags(ws + FLAG_OFF + (i * 64u + lane) * FLAG_STRIDE, tag);
      if (lane == 0) st_agent(ws + EPOCH_OFF, tag); }
    ST(24); ST(25); ST(26); ST(27); ST(28); ST(29); ST(30); ST(31); ST_OUT(ws, 0, 32); return; }
  const unsigned jq = (2u * g4 + 2u + wid) & 7u, G = 8u * rt + jq;
  const unsigned voffO = lane * 16u, soO = (G * NH + g4 * 8u) * 8192u;
  const char* fp = ws + FLAG_OFF + (((G * 37u) >> 8) * NH + g4 * 8u + (lane & 7u)) * FLAG_STRIDE;
  unsigned ready = 0;
  auto need = [&](unsigned m) {
    _Pragma("nounroll") for (unsigned spins = 0; (ready & m) != m && spins < SPIN_CAP; ++spins) { ready = (unsigned)__ballot(ld_agent(fp) == tag); if ((ready & m) != m) __builtin_amdgcn_s_sleep(PSLEEP); }
    asm volatile("" ::: "memory"); };
  __builtin_amdgcn_s_sleep(PDELAY);
  need(1u);
  ST(24);
  const lds_cptr wb = (lds_cptr)lds + 1024u * hh + 64u * q4 + trcol;
  f32x16 acc[2] = {};
  { unsigned hd = 0, so_c = soO + hd * 8192u; lds_cptr wp = wb + ((SLOTW >> (4u * hd)) & 15u) * 16384u;
    bf16x8 of[8], lf[4][2];
    auto rdW = [&](int s2) { lf[s2 & 3][0] = trd2(wp, (s2 & 7) * 2048, (s2 & 7) * 2048 + 256); lf[s2 & 3][1] = trd2(wp, (s2 & 7) * 2048 + 512, (s2 & 7) * 2048 + 768); };
#pragma unroll
    for (int i = 0; i < 8; ++i) { if (i < WO) bload_sc1(of[i], voffO, sO, so_c + i * 1024u); else of[i] = bf16x8{}; }
    rdW(0); rdW(1);
#pragma nounroll
    for (unsigned p = 0; p < 8; ++p) {
      hd = (hd + 1u) & 7u;
      if (p == 5u) __builtin_amdgcn_s_barrier();
      const unsigned so_n = p < 7u ? soO + hd * 8192u : O_BYTES;
      static_for<8>([&](auto S) { constexpr int s2 = S.value;
        if constexpr (s2 == 8 - WO) need(1u << hd);
        if constexpr (s2 == 6) wp = wb + ((SLOTW >> (4u * hd)) & 15u) * 16384u;
        wait_vm1<WO - 1>(of[s2]);
        wait_lgkm<4>();
        acc[0] = MFMA(lf[s2 & 3][0], of[s2], acc[0]);
        rdW(s2 + 2);
        SBAR();
        acc[1] = MFMA(lf[s2 & 3][1], of[s2], acc[1]);
        SBAR();
        breload_sc1(of[(s2 + WO) & 7], voffO, sO, (s2 + WO < 8 ? so_c : so_n) + ((s2 + WO) & 7) * 1024u);
        SBAR(); });
      so_c = so_n;
    }
    wait_vm<0>();
#pragma unroll
    for (int i = 0; i < 8; ++i) pin(of[i]); }
  ST(28);
  const srd_t sX2 = mk_srd(ws + X_OFF, X_BYTES);
  const unsigned xb = (u * 8u + jq) * 4u;
  if (wid < 6u) {
#pragma unroll
    for (int i = 0; i < 4; ++i) bstore_sc1(pack8(acc[i >> 1], i & 1), voffO, sX2, (xb + g4) * 4096u + i * 1024u);
    wait_vm<0>();
    if (lane == 0) st_agent(ws + FLAG2_OFF + (xb + g4) * FLAG_STRIDE, tag);
    ST(29); ST(30);
  } else {
    __builtin_amdgcn_s_sleep(PDELAY2);
    poll_flags(ws + FLAG2_OFF + (xb + ((g4 + 1u + lane % 3u) & 3u)) * FLAG_STRIDE, tag);
    ST(29);
    bf16x8 xp[12];
#pragma unroll
    for (int i = 0; i < 12; ++i) bload_sc1(xp[i], voffO, sX2, (xb + ((g4 + 1u + (i >> 2)) & 3u)) * 4096u + (i & 3) * 1024u);
    wait_vm<0>();
#pragma unroll
    for (int j = 0; j < 12; ++j) { const int i = j & 3; pin(xp[j]); const u32x4 v = __builtin_bit_cast(u32x4, xp[j]);
#pragma unroll
      for (int e = 0; e < 4; ++e) { acc[i >> 1][8 * (i & 1) + 2 * e] += __uint_as_float(v[e] << 16); acc[i >> 1][8 * (i & 1) + 2 * e + 1] += __uint_as_float(v[e] & 0xFFFF0000u); } }
    ST(30);
    char* op = (char*)out + (size_t)(G * 32u + r) * (DM * 2u) + ct * 128u + hh * 16u;
#pragma unroll
    for (int i = 0; i < 4; ++i) *(u32x4*)(op + i * 32) = pack8(acc[i >> 1], i & 1);
  }
  ST(31); ST_OUT(ws, 0, 32);
}

extern "C" void kernel_launch(void* const* d_in, const int* in_sizes, int n_in, void* d_out, int out_size, void* d_ws, size_t ws_size, hipStream_t stream) {
  (void)in_sizes; (void)n_in; (void)out_size; (void)ws_size;
  (void)hipFuncSetAttribute((const void*)mha_kernel, hipFuncAttributeMaxDynamicSharedMemorySize, LDS_BYTES);
  const unsigned short* x = (const unsigned short*)d_in[0]; const unsigned short* kc = (const unsigned short*)d_in[1]; const unsigned short* vc = (const unsigned short*)d_in[2];
  const unsigned short* wq = (const unsigned short*)d_in[3]; const unsigned short* wo = (const unsigned short*)d_in[4];
  mha_kernel<<<256, 512, LDS_BYTES, stream>>>(x, kc, vc, wq, wo, (char*)d_ws, (unsigned short*)d_out);
}
```

```cpp
#include <hip/hip_runtime.h>
#include <stdint.h>
#include <utility>

typedef short    bf16x8 __attribute__((ext_vector_type(8)));
typedef short    s16x4  __attribute__((ext_vector_type(4)));
typedef float    f32x16 __attribute__((ext_vector_type(16)));
typedef float    f32x2  __attribute__((ext_vector_type(2)));
typedef __bf16   bf16x2 __attribute__((ext_vector_type(2)));
typedef unsigned u32x4  __attribute__((ext_vector_type(4)));
typedef __attribute__((address_space(3))) const char* lds_cptr;
typedef __attribute__((address_space(1))) unsigned long long gu64;
typedef __amdgpu_buffer_rsrc_t srd_t;

#define DI __device__ __forceinline__
#define MFMA(a, b, c) __builtin_amdgcn_mfma_f32_32x32x16_bf16((a), (b), (c), 0, 0, 0)
#define SBAR() __builtin_amdgcn_sched_barrier(0)

constexpr int NB = 8, SQ = 224, DM = 576, NH = 32, SK = 512, DH = 128;
constexpr int LDS_BYTES = 163840;
constexpr float QSCALE = 0.08838834764831845f * 1.4426950408889634f;
constexpr float SUM_THR = 4096.f;

template <int... Is, class F> DI void static_for_impl(std::integer_sequence<int, Is...>, F&& f) { (f(std::integral_constant<int, Is>{}), ...); }
template <int N, class F> DI void static_for(F&& f) { static_for_impl(std::make_integer_sequence<int, N>{}, f); }

DI unsigned rfl(unsigned x) { return __builtin_amdgcn_readfirstlane(x); }
DI unsigned cvtpk(float lo, float hi) { f32x2 v = {lo, hi}; bf16x2 b = __builtin_convertvector(v, bf16x2); return __builtin_bit_cast(unsigned, b); }
DI srd_t mk_srd(const void* p, unsigned bytes) { return __builtin_amdgcn_make_buffer_rsrc((void*)p, (short)0, (int)bytes, 0x00020000); }
DI void dma4(unsigned lds, unsigned v0, unsigned v1, unsigned v2, unsigned v3, srd_t srd, unsigned soff) {
  asm volatile("s_mov_b32 m0, %0\n\ts_nop 0\n\tbuffer_load_dwordx4 %1, %5, %6 offen lds\n\tbuffer_load_dwordx4 %2, %5, %6 offen offset:1024 lds\n\t"
               "buffer_load_dwordx4 %3, %5, %6 offen offset:2048 lds\n\tbuffer_load_dwordx4 %4, %5, %6 offen offset:3072 lds"
               :: "s"(lds), "v"(v0), "v"(v1), "v"(v2), "v"(v3), "s"(srd), "s"(soff) : "memory");
}
DI void dma4_nt(unsigned lds, unsigned v0, unsigned v1, unsigned v2, unsigned v3, srd_t srd, unsigned soff) {
  asm volatile("s_mov_b32 m0, %0\n\ts_nop 0\n\tbuffer_load_dwordx4 %1, %5, %6 offen nt lds\n\tbuffer_load_dwordx4 %2, %5, %6 offen offset:1024 nt lds\n\t"
               "buffer_load_dwordx4 %3, %5, %6 offen offset:2048 nt lds\n\tbuffer_load_dwordx4 %4, %5, %6 offen offset:3072 nt lds"
               :: "s"(lds), "v"(v0), "v"(v1), "v"(v2), "v"(v3), "s"(srd), "s"(soff) : "memory");
}
DI void bstore_sc1(const u32x4& d, unsigned voff, srd_t srd, unsigned soff) { asm volatile("buffer_store_dwordx4 %0, %1, %2, %3 offen sc1\n\ts_nop 1" :: "v"(d), "v"(voff), "s"(srd), "s"(soff) : "memory"); }
DI void bload_sc1(bf16x8& d, unsigned voff, srd_t srd, unsigned soff) { asm volatile("buffer_load_dwordx4 %0, %1, %2, %3 offen sc1" : "=v"(d) : "v"(voff), "s"(srd), "s"(soff) : "memory"); }
DI void breload_sc1(bf16x8& d, unsigned voff, srd_t srd, unsigned soff) { asm volatile("buffer_load_dwordx4 %0, %1, %2, %3 offen sc1" : "+v"(d) : "v"(voff), "s"(srd), "s"(soff) : "memory"); }
DI void pin(bf16x8& a) { asm volatile("" : "+v"(a)); }
DI void pin16(f32x16& a) { asm volatile("" : "+v"(a)); }
DI void pinf(float& a) { asm volatile("" : "+v"(a)); }
DI void pinu(unsigned& a) { asm volatile("" : "+v"(a)); }
template <int N> DI void wait_vm1(bf16x8& a) { asm volatile("s_waitcnt vmcnt(%1)" : "+v"(a) : "n"(N) : "memory"); }
template <int N> DI void wait_vm() { asm volatile("s_waitcnt vmcnt(%0)" :: "n"(N) : "memory"); }
template <int N> DI void wait_lgkm() { __builtin_amdgcn_s_waitcnt(0xC07F | (N << 8)); SBAR(); }
DI void bar_if(unsigned c) { asm volatile("s_cmp_eq_u32 %0, 0\n\ts_cbranch_scc1 1f\n\ts_waitcnt lgkmcnt(0)\n\ts_barrier\n1:" :: "s"(c) : "memory", "scc"); }
DI void bar() { asm volatile("s_waitcnt lgkmcnt(0)\n\ts_barrier" ::: "memory"); }
typedef short v4i16_t __attribute__((ext_vector_type(4)));
DI s16x4 trd(lds_cptr p) { return __builtin_amdgcn_ds_read_tr16_b64_v4i16((__attribute__((address_space(3))) v4i16_t*)p); }
DI bf16x8 trd2(lds_cptr p, int o0, int o1) { s16x4 a = trd(p + o0), b = trd(p + o1); return __builtin_shufflevector(a, b, 0, 1, 2, 3, 4, 5, 6, 7); }
DI bf16x8 rd128(lds_cptr p) { return *(const __attribute__((address_space(3))) bf16x8*)p; }
DI float swap_max(float m) { auto rr = __builtin_amdgcn_permlane32_swap(__float_as_uint(m), __float_as_uint(m), false, false); return fmaxf(__uint_as_float(rr[0]), __uint_as_float(rr[1])); }
DI float swap_add(float m) { auto rr = __builtin_amdgcn_permlane32_swap(__float_as_uint(m), __float_as_uint(m), false, false); return __uint_as_float(rr[0]) + __uint_as_float(rr[1]); }
DI float max16(const f32x16& s) { float a = fmaxf(fmaxf(s[0], s[1]), s[2]), b = fmaxf(fmaxf(s[3], s[4]), s[5]);
  a = fmaxf(fmaxf(a, s[6]), s[7]); b = fmaxf(fmaxf(b, s[8]), s[9]); a = fmaxf(fmaxf(a, s[10]), s[11]); b = fmaxf(fmaxf(b, s[12]), s[13]); a = fmaxf(fmaxf(a, s[14]), s[15]); return fmaxf(a, b); }
DI u32x4 pack8(const f32x16& a, int s, float f) { return u32x4{cvtpk(a[8 * s] * f, a[8 * s + 1] * f), cvtpk(a[8 * s + 2] * f, a[8 * s + 3] * f), cvtpk(a[8 * s + 4] * f, a[8 * s + 5] * f), cvtpk(a[8 * s + 6] * f, a[8 * s + 7] * f)}; }
DI u32x4 pack8(const f32x16& a, int s) { return u32x4{cvtpk(a[8 * s], a[8 * s + 1]), cvtpk(a[8 * s + 2], a[8 * s + 3]), cvtpk(a[8 * s + 4], a[8 * s + 5]), cvtpk(a[8 * s + 6], a[8 * s + 7])}; }

DI float expo(const f32x16& s, u32x4 (&p)[2]) {
  float e[16]; float s0 = 0.f, s1 = 0.f;
#pragma unroll
  for (int i = 0; i < 16; ++i) e[i] = __builtin_amdgcn_exp2f(s[i]);
#pragma unroll
  for (int i = 0; i < 16; i += 2) { s0 += e[i]; s1 += e[i + 1]; }
#pragma unroll
  for (int i = 0; i < 8; ++i) p[i >> 2][i & 3] = cvtpk(e[2 * i], e[2 * i + 1]);
  return s0 + s1;
}

#define ST_DECL
#define ST(k)
#define ST_OUT(ws, lo, hi)

constexpr unsigned SLOTK = 0x04204209u;
constexpr unsigned SLOTV = 0x31531531u;
constexpr unsigned SLOTW = 0x05429876u;
constexpr unsigned STG = 45056u, STG_W = 28672u;
constexpr unsigned O_BYTES = NB * SQ * NH * DH * 2;
constexpr unsigned X_OFF = O_BYTES, X_BYTES = 63u * 8u * 4u * 4096u;
constexpr unsigned FLAG_OFF = X_OFF + X_BYTES, FLAG_STRIDE = 128, FLAG2_OFF = FLAG_OFF + 256 * FLAG_STRIDE, EPOCH_OFF = FLAG2_OFF + 63 * 32 * FLAG_STRIDE;
#define XOFF 0
#define KA 0u
#define QG 1
#define PACE 0
#define PACEA 0
#define K0S 1
#define NOWUP 0
#define WUPC 3u
#define RAMP 0
constexpr unsigned SPIN_CAP = 1u << 20;
constexpr int RA = 7, WO = 6, A0BAR = 6, PSLEEP = 4, PDELAY = 20, PDELAY2 = 25;
constexpr unsigned SPARE = 3u * STG;
constexpr unsigned CODE_BYTES = 19200u;
constexpr int lds_ops(int a, int b, bool last) { int n = 0; for (int i = a; i < b; ++i) n += i >= 16 ? (last ? 0 : 1) : (i >= 8 ? 2 : 1); return n; }
constexpr int pv_order(int i) { return i < 4 ? i : i < 8 ? i + 4 : i < 12 ? i - 4 : i; }
DI unsigned long long mix64(unsigned long long z) { z += 0x9E3779B97F4A7C15ull; z = (z ^ (z >> 30)) * 0xBF58476D1CE4E5B9ull; z = (z ^ (z >> 27)) * 0x94D049BB133111EBull; return z ^ (z >> 31); }
DI unsigned long long rfl64(unsigned long long v) { return ((unsigned long long)rfl((unsigned)(v >> 32)) << 32) | rfl((unsigned)v); }
DI unsigned long long ld_agent(const char* p) { return __hip_atomic_load((gu64*)p, __ATOMIC_RELAXED, __HIP_MEMORY_SCOPE_AGENT); }
DI void st_agent(char* p, unsigned long long v) { __hip_atomic_store((gu64*)p, v, __ATOMIC_RELAXED, __HIP_MEMORY_SCOPE_AGENT); }
DI void poll_flags(const char* p, unsigned long long tag) {
  _Pragma("nounroll") for (unsigned spins = 0; spins < SPIN_CAP; ++spins) { if (__all(ld_agent(p) == tag)) break; __builtin_amdgcn_s_sleep(PSLEEP); }
  asm volatile("" ::: "memory");
}

__global__ __launch_bounds__(512, 2) void mha_kernel(const unsigned short* __restrict__ x, const unsigned short* __restrict__ kc, const unsigned short* __restrict__ vc,
                                                     const unsigned short* __restrict__ wq, const unsigned short* __restrict__ wo, char* ws, unsigned short* __restrict__ out) {
  extern __shared__ __attribute__((aligned(16))) char lds[];
  unsigned long long pc0; asm volatile("s_getpc_b64 %0" : "=s"(pc0));
  const unsigned tid = threadIdx.x, lane = tid & 63u, r = lane & 31u, hh = lane >> 5;
  const unsigned wid = rfl(tid >> 6);
  ST_DECL ST(0);
  const unsigned bid = blockIdx.x, xcd = bid & 7u, ix = bid >> 3;
  const unsigned b = (xcd & 1u) * 4u + (ix & 3u), h = (xcd >> 1) * 8u + (ix >> 2);
  const unsigned lds0 = (unsigned)(uintptr_t)lds;
  {
    const char* cp = (const char*)(pc0 & ~255ull) + min((ix * 8u + wid) * 128u, CODE_BYTES);
    asm volatile("s_mov_b32 m0, %0\n\ts_nop 0\n\tglobal_load_lds_dword %1, %2" :: "s"(lds0 + SPARE), "v"(0u), "s"(cp) : "memory"); }
  const srd_t sW = mk_srd((const char*)wq - 1024, DM * NH * DH * 2 + 1024);
  const srd_t sX = mk_srd(x + (size_t)b * SQ * DM, SQ * DM * 2);
  const unsigned prow = (lane >> 2) & 7u, pch = 4u * hh + (lane & 3u);
  const unsigned pchx0 = 4u * hh + ((lane & 3u) ^ ((lane >> 4) & 1u)), pchx1 = pchx0 ^ 2u;
  const unsigned vW = prow * 8192u + pch * 16u, vX0 = prow * (DM * 2u) + pchx0 * 16u, vX1 = prow * (DM * 2u) + pchx1 * 16u;
  const unsigned ck0 = (QG == 3 ? 3u * (((ix & 3u) + (ix >> 2)) % 3u) : QG == 2 ? 4u * (((ix & 3u) + (ix >> 2)) & 1u) : 0u) + (XOFF ? (0x10022110u >> (4u * xcd)) & 3u : 0u), ck1 = ck0 == 8u ? 0u : ck0 + 1u;
  unsigned long long ep; asm volatile("global_load_dwordx2 %0, %1, %2 sc1" : "=v"(ep) : "v"(0u), "s"(ws + EPOCH_OFF) : "memory");
  dma4(lds0 + (wid < 7u ? wid * 4096u : SPARE), vX0, vX1 + 8192u, vX0 + 16384u, vX1 + 24576u, sX, ck0 * 128u + min(wid, 6u) * 32u * DM * 2u);
  dma4(lds0 + (wid >> 2) * STG + STG_W + (wid & 3u) * 4096u, vW + 1024u, vW + 128u, vW + 65536u - 1024u, vW + 65664u - 2048u, sW, (wid < 4u ? ck0 : ck1) * 64u * 8192u + h * 256u + (wid & 3u) * 131072u);
  const srd_t sK = mk_srd((const char*)(kc + (size_t)(b * NH + h) * SK * DH) - 1024, SK * DH * 2 + 1024);
  const srd_t sV = mk_srd((const char*)(vc + (size_t)(b * NH + h) * SK * DH) - 1024, SK * DH * 2 + 1024);
  const unsigned vK0 = prow * 256u + pchx0 * 16u, vK1 = prow * 256u + pchx1 * 16u;
  const unsigned q4 = (lane & 15u) >> 2, p4 = lane & 3u, blk = (lane >> 4) & 1u;
  const unsigned trcol = 32u * blk + 16u * (p4 & 1u) + 8u * (p4 >> 1);
  const srd_t sO = mk_srd(ws, O_BYTES);
  unsigned long long tag;
  const unsigned g4 = xcd >> 1, u0 = (xcd & 1u) * 32u + ix;
  const bool has_tile = u0 < 63u;
  const unsigned u = has_tile ? u0 : 62u, rt = u / 9u, ct = u - rt * 9u;

  if (wid == 7u) {
    auto fillQ = [&](unsigned c, unsigned stage) {
      const unsigned lw = lds0 + stage + STG_W, sw = c * 64u * 8192u + h * 256u, lx = lds0 + stage, sx = c * 128u;
#pragma unroll
      for (int i = 0; i < 4; ++i) dma4(lw + i * 4096u, vW + 1024u, vW + 128u, vW + 65536u - 1024u, vW + 65664u - 2048u, sW, sw + i * 131072u);
#pragma unroll
      for (int i = 0; i < 7; ++i) dma4(lx + i * 4096u, vX0, vX1 + 8192u, vX0 + 16384u, vX1 + 24576u, sX, sx + i * 32u * DM * 2u); };
    bar(); ST(2);
    { unsigned stage = 2 * STG, ck = ck1 == 8u ? 0u : ck1 + 1u;
#pragma nounroll
      for (unsigned c = 2; c < 9; ++c) { fillQ(ck, stage); ck = ck == 8u ? 0u : ck + 1u; stage = stage == 2 * STG ? 0u : stage + STG;
        wait_vm<44>(); bar(); ST(1 + c); } }
    const unsigned vV = prow * 256u + pch * 16u;
    const srd_t sWo = mk_srd(wo, NH * DH * DM * 2);
    const unsigned vWo = prow * (DM * 2u) + pch * 16u;
    auto pace = [&](unsigned n) { if (n == 0u) { if (PACEA) __builtin_amdgcn_s_sleep(PACEA); } else if (n < 6u) { if (PACE) __builtin_amdgcn_s_sleep(PACE); } };
    { unsigned j = 1, jw = 0;
#pragma nounroll
      for (unsigned n = 0; n < 10; ++n) {
        if (n == 9u) { wait_vm<0>(); bar(); }
        const unsigned je = ((unsigned)(0xFFFECA8642ull >> (4u * n)) & 15u) + 1u;
#pragma nounroll
        for (; j < je; ++j) {
          if ((j & 1u) && j != 15u) { const unsigned t = (j + 1u) >> 1, ld = lds0 + ((SLOTK >> (4u * t)) & 15u) * 16384u;
#pragma unroll
            for (int i = 0; i < 4; ++i) dma4_nt(ld + i * 4096u, vK0 + 1024u, vK0 + 128u, vK1 + 1024u, vK1 + 128u, sK, t * 16384u + i * 4096u), pace(n);
          } else { const unsigned t = (j - 1u) >> 1, ld = lds0 + ((SLOTV >> (4u * t)) & 15u) * 16384u;
#pragma unroll
            for (int i = 0; i < 4; ++i) dma4_nt(ld + i * 4096u, vV + 1024u, vV + 128u, vV + 1024u, vV + 128u, sV, t * 16384u + i * 4096u), pace(n); } }
        const unsigned we = (unsigned)(0x8741000000ull >> (4u * n)) & 15u;
#pragma nounroll
        for (; jw < we; ++jw) { const unsigned ld = lds0 + ((SLOTW >> (4u * jw)) & 15u) * 16384u, so = (g4 * 8u + jw) * 128u * (DM * 2u) + ct * 128u;
#pragma unroll
          for (int k = 0; k < 4; ++k) dma4(ld + k * 4096u, vWo, vWo + 8192u, vWo + 16384u, vWo + 24576u, sWo, so + k * 32u * (DM * 2u)); }
        if (n == 9u) break;
        if (n < 6u) wait_vm<32>(); else if (n == 6u) wait_vm<48>(); else wait_vm<62>();
        bar(); ST(n == 0u ? 10u : 11u + n); } }
    asm volatile("" : "+v"(ep)); tag = mix64(rfl64(ep));
    ST(20); ST(21);
  } else {
  const unsigned rsw0 = 64u * (r & 7u) + 16u * (hh ^ ((r >> 2) & 3u)), rsw1 = rsw0 ^ 32u;
  f32x16 qa[4] = {}, o[4] = {};
  asm volatile("" : "+v"(qa[0]), "+v"(qa[1]), "+v"(qa[2]), "+v"(qa[3]), "+v"(o[0]), "+v"(o[1]), "+v"(o[2]), "+v"(o[3]));
  wait_lgkm<0>();
  wait_vm<4>(); if (wid < 4u) wait_vm<0>();
  ST(1);
  dma4(lds0 + STG + wid * 4096u, vX0, vX1 + 8192u, vX0 + 16384u, vX1 + 24576u, sX, ck1 * 128u + wid * 32u * DM * 2u);
  bar();
  asm volatile("" : "+v"(ep)); ep = rfl64(ep);
  ST(2);
  const lds_cptr kb0 = (lds_cptr)lds + 2048u * (r >> 3) + rsw0, kb1 = (lds_cptr)lds + 2048u * (r >> 3) + rsw1;
  const lds_cptr vb = (lds_cptr)lds + 64u * (4u * hh + q4) + trcol;
  bf16x8 qf[8], pre[RA];
  f32x16 negm, S0, S1; u32x4 P0[2], P1[2]; float l = 0.f;
  auto rdK = [&](lds_cptr k0, lds_cptr k1, int kb, int i) { return rd128(((i & 1) ? k1 : k0) + 8192 * kb + 512 * (i >> 1)); };
  auto rdV = [&](lds_cptr v, int kb, int j) { return trd2(v, 8192 * kb + 4096 * (j >> 2) + 512 * (j & 3), 8192 * kb + 4096 * (j >> 2) + 2048 + 512 * (j & 3)); };
  auto sm_fix = [&](float ps, f32x16& Sc, f32x16& Sn, u32x4 (&P)[2]) {
    if (__builtin_expect(__any(!(ps <= SUM_THR)), 0)) {
      const float dl = fmaxf(swap_max(max16(Sc)), 0.f), f = __builtin_amdgcn_exp2f(-dl);
#pragma unroll
      for (int i = 0; i < 16; ++i) { Sc[i] -= dl; Sn[i] -= dl; negm[i] -= dl; }
#pragma unroll
      for (int db = 0; db < 4; ++db)
#pragma unroll
        for (int i = 0; i < 16; ++i) o[db][i] *= f;
      l *= f; ps = expo(Sc, P); }
    l += ps; };
  { lds_cptr wp = (lds_cptr)lds + STG_W + 2048u * hh + 64u * q4 + trcol;
    lds_cptr xp0 = (lds_cptr)lds + wid * 4096u + 1024u * (r >> 3) + rsw0, xp1 = (lds_cptr)lds + wid * 4096u + 1024u * (r >> 3) + rsw1;
    bf16x8 xf[4], af[5];
    auto rdA = [&](int i) { return trd2(wp, 4096 * (i >> 2) + 512 * (i & 3), 4096 * (i >> 2) + 512 * (i & 3) + 256); };
    xf[0] = rd128(xp0); af[0] = rdA(0); af[1] = rdA(1); af[2] = rdA(2); af[3] = rdA(3);
    unsigned stage = 0;
#pragma nounroll
    for (unsigned c = 0; c < 8; ++c) {
      if (!NOWUP && __builtin_expect(c == WUPC && wid == 3u, 0)) {
#pragma unroll
        for (int i = 0; i < (K0S ? 0 : 4); ++i) dma4_nt(lds0 + 9u * 16384u + i * 4096u, vK0 + 1024u, vK0 + 128u, vK1 + 1024u, vK1 + 128u, sK, i * 4096u);
        unsigned ln = lane; asm volatile("" : "+v"(ln));
        const unsigned k = ln & 7u, sel = ln >> 3;
        const char* wup = sel == 0u ? (const char*)vc + (size_t)(b * NH + h) * (SK * DH * 2) + k * 16384u
                        : sel == 1u ? (const char*)wo + (size_t)(g4 * 1024u + k * 128u) * (DM * 2u) + ct * 128u
                        : sel == 2u ? (const char*)out + (size_t)((8u * rt + k) * 32u) * (DM * 2u)
                        : ws + (sel == 3u ? ((b * 7u + k) * NH + h) * 8192u : sel == 4u ? ((8u * rt + k) * NH + g4 * 8u) * 8192u : sel == 5u ? X_OFF + u * 131072u + k * 16384u : FLAG_OFF + k * 32768u + 64u);
        asm volatile("s_mov_b32 m0, %0\n\ts_nop 0\n\tglobal_load_lds_dword %1, off sc1" :: "s"(lds0 + SPARE), "v"(wup) : "memory"); }
      if (RAMP && c == 0u) { for (unsigned i = 0; i < ((ix + wid) & 7u); ++i) __builtin_amdgcn_s_sleep(RAMP); }
      if (K0S && __builtin_expect(c >= 3u && c < 7u && wid == 3u, 0)) dma4_nt(lds0 + 9u * 16384u + (c - 3u) * 4096u, vK0 + 1024u, vK0 + 128u, vK1 + 1024u, vK1 + 128u, sK, (c - 3u) * 4096u);
      if (KA && __builtin_expect(c >= 4u && c < 7u && wid == 3u, 0))
        asm volatile("s_mov_b32 m0, %0\n\ts_nop 0\n\tbuffer_load_dword %1, %2, %3 offen nt lds" :: "s"(lds0 + SPARE), "v"((lane & (KA - 1u)) * 128u), "s"(sK), "s"(17408u + (c - 4u) * KA * 128u) : "memory");
      const unsigned nstage = stage == 2 * STG ? 0u : stage + STG; const int dlt = (int)nstage - (int)stage; stage = nstage;
      SBAR();
      static_for<12>([&](auto I) { constexpr int i = I.value;
        qa[i & 3] = MFMA(af[i % 5], xf[i >> 2], qa[i & 3]);
        af[(i + 4) % 5] = rdA(i + 4);
        if constexpr (i == 0) xf[1] = rd128(xp1); if constexpr (i == 3) xf[2] = rd128(xp0 + 512); if constexpr (i == 7) xf[3] = rd128(xp1 + 512);
        SBAR(); });
      qa[0] = MFMA(af[12 % 5], xf[3], qa[0]);
      wp += dlt; xp0 += dlt; xp1 += dlt; asm volatile("" : "+v"(wp), "+v"(xp0), "+v"(xp1));
      SBAR();
      asm volatile("s_bitcmp1_b32 %0, %1\n\ts_cbranch_scc1 1f\n\ts_waitcnt vmcnt(0)\n1:\n\ts_waitcnt lgkmcnt(0)\n\ts_barrier" :: "s"(0x78u), "s"(c) : "memory", "scc");
      const bf16x8 xl = xf[3], a13 = af[13 % 5], a14 = af[14 % 5], a15 = af[15 % 5];
      xf[0] = rd128(xp0); af[0] = rdA(0); af[1] = rdA(1);
      SBAR();
      qa[1] = MFMA(a13, xl, qa[1]); SBAR();
      af[2] = rdA(2);
      qa[2] = MFMA(a14, xl, qa[2]); SBAR();
      af[3] = rdA(3);
      qa[3] = MFMA(a15, xl, qa[3]); SBAR();
      ST(3 + c);
    }
    lds_cptr k0 = kb0 + 9 * 16384, k1 = kb1 + 9 * 16384; asm volatile("" : "+v"(k0), "+v"(k1));
    bf16x8 ag[16], f[16]; u32x4 qw[8]; float qs = QSCALE; asm volatile("" : "+s"(qs));
#pragma unroll
    for (int i = 0; i < 4; ++i) ag[i] = af[i];
    auto cvq = [&](int j) { float lo = qa[j >> 3][2 * (j & 7)] * qs; pinf(lo); float hi = qa[j >> 3][2 * (j & 7) + 1] * qs; unsigned w = cvtpk(lo, hi); pinu(w); qw[j >> 2][j & 3] = w; };
    SBAR();
    static_for<24>([&](auto G) { constexpr int g = G.value, n = g + 4;
      if constexpr (g < 4) qa[g] = MFMA(ag[g], xf[0], qa[g]);
      else if constexpr (g < 16) qa[(g - 4) / 3] = MFMA(ag[g], xf[1 + (g - 4) % 3], qa[(g - 4) / 3]);
      else { qf[g - 16] = __builtin_bit_cast(bf16x8, qw[g - 16]);
        if constexpr (g == 16) { const f32x16 z = {}; S0 = MFMA(f[0], qf[0], z); } else S0 = MFMA(f[g - 16], qf[g - 16], S0); pin16(S0); }
      if constexpr (g == 0) { xf[1] = rd128(xp1); xf[2] = rd128(xp0 + 512); } if constexpr (g == 1) xf[3] = rd128(xp1 + 512);
      if constexpr (n < 16) ag[n] = rdA(4 * (1 + (n - 4) % 3) + (n - 4) / 3); else f[n - 16] = rdK(k0, k1, (n - 16) >> 3, (n - 16) & 7);
      if constexpr (g >= 9 && g < 21) { constexpr int j0 = (g - 9) * 8 / 3, j1 = (g - 8) * 8 / 3; static_for<j1 - j0>([&](auto J) { cvq(j0 + J.value); }); }
      SBAR(); });
    ST(11);
#pragma unroll
    for (int i = 12; i < 16; ++i) f[i] = rdK(k0, k1, 1, i & 7);
    const float m = swap_max(max16(S0));
#pragma unroll
    for (int i = 0; i < 16; ++i) negm[i] = -m;
    SBAR();
    float e[16]; float s0 = 0.f, s1 = 0.f;
    static_for<9>([&](auto G) { constexpr int g = G.value;
      if constexpr (g == 0) { if (wid >= 4u) bar(); }
      if constexpr (g == A0BAR) { if (wid < 4u) bar();
        constexpr unsigned ko = ((SLOTK >> 4) & 15u) * 16384u;
#pragma unroll
        for (int i = 0; i < RA; ++i) pre[i] = rdK(kb0 + ko, kb1 + ko, 0, i);
        SBAR(); }
      if constexpr (g == 0) S1 = MFMA(f[8], qf[0], negm); else if constexpr (g < 8) S1 = MFMA(f[8 + g], qf[g], S1);
      if constexpr (g < 8) pin16(S1);
      if constexpr (g < 8) { e[2 * g] = __builtin_amdgcn_exp2f(S0[2 * g] - m); e[2 * g + 1] = __builtin_amdgcn_exp2f(S0[2 * g + 1] - m); pinf(e[2 * g]); pinf(e[2 * g + 1]); }
      if constexpr (g == 1) { s0 = e[0]; s1 = e[1]; } else if constexpr (g > 1) { s0 += e[2 * g - 2]; pinf(s0); s1 += e[2 * g - 1]; pinf(s1); }
      if constexpr (g >= 1) { unsigned w = cvtpk(e[2 * g - 2], e[2 * g - 1]); pinu(w); P0[(g - 1) >> 2][(g - 1) & 3] = w; }
      SBAR(); });
    l = s0 + s1; }
  ST(12);
  auto unit = [&](auto KB, auto LAST, lds_cptr k0, lds_cptr k1, lds_cptr v, lds_cptr nk0, lds_cptr nk1, f32x16& Sn, f32x16& Sc, const u32x4 (&Pp)[2], u32x4 (&Pc)[2]) {
    constexpr int kb = KB.value; constexpr bool last = LAST.value;
    float e[16]; float s0 = 0.f, s1 = 0.f; bf16x8 f[16 + RA];
#pragma unroll
    for (int i = 0; i < RA; ++i) f[i] = pre[i];
    static_for<16>([&](auto G) { constexpr int g = G.value; constexpr int n = g + RA; constexpr int gb = 12;
      if constexpr (last && g == gb) { bar(); wait_lgkm<0>(); }
      else if constexpr ((g & 3) == 0) wait_lgkm<lds_ops(g + 4, g + RA, last)>();
      if constexpr (g == 0) Sn = MFMA(f[0], qf[0], negm); else if constexpr (g < 8) Sn = MFMA(f[g], qf[g], Sn);
      else o[g & 3] = MFMA(f[g], __builtin_bit_cast(bf16x8, Pp[(g >> 2) & 1]), o[g & 3]);
      if constexpr (n < 8) f[n] = rdK(k0, k1, kb, n); else if constexpr (n < 16) f[n] = rdV(v, kb, n - 8);
      else if constexpr (!last) f[n] = rdK(k0, k1, kb + 1, n - 16);
      else if constexpr (g >= gb) { constexpr int per = (RA + 15 - gb) / (16 - gb); static_for<per>([&](auto J) { constexpr int i = per * (g - gb) + J.value; if constexpr (i < RA) f[16 + i] = rdK(nk0, nk1, 0, i); }); }
      if constexpr (g == 0) { e[0] = __builtin_amdgcn_exp2f(Sc[0]); pinf(e[0]); }
      if constexpr (g < 15) { e[g + 1] = __builtin_amdgcn_exp2f(Sc[g + 1]); pinf(e[g + 1]); }
      if constexpr (g == 1) { s0 = e[0]; s1 = e[1]; } else if constexpr (g > 1) { if constexpr (g & 1) { s1 += e[g]; pinf(s1); } else { s0 += e[g]; pinf(s0); } }
      if constexpr (g & 1) { unsigned w = cvtpk(e[g - 1], e[g]); pinu(w); Pc[g >> 3][(g >> 1) & 3] = w; }
      SBAR(); });
#pragma unroll
    for (int i = 0; i < RA; ++i) pre[i] = f[16 + i];
    sm_fix(s0 + s1, Sc, Sn, Pc); };
#pragma nounroll
  for (unsigned t = 1; t < 7; ++t) {
    const unsigned ko = ((SLOTK >> (4u * t)) & 15u) * 16384u, vo = ((SLOTV >> (4u * (t - 1u))) & 15u) * 16384u, nko = ((SLOTK >> (4u * ((t + 1u) & 7u))) & 15u) * 16384u;
    SBAR();
    unit(std::integral_constant<int, 0>{}, std::false_type{}, kb0 + ko, kb1 + ko, vb + vo, kb0, kb1, S0, S1, P0, P1);
    unit(std::integral_constant<int, 1>{}, std::true_type{}, kb0 + ko, kb1 + ko, vb + vo, kb0 + nko, kb1 + nko, S1, S0, P1, P0);
    if (t == 1u || t == 6u) ST(12 + t);
  }
  const unsigned me = (b * 7u + wid) * NH + h;
  {
    constexpr unsigned ko = ((SLOTK >> 28) & 15u) * 16384u;
    const lds_cptr k0 = kb0 + ko, k1 = kb1 + ko, v6 = vb + ((SLOTV >> 24) & 15u) * 16384u, v7 = vb + ((SLOTV >> 28) & 15u) * 16384u;
    u32x4 P2[2], P3[2];
    auto scaleP = [&](u32x4 (&P)[2], float f) {
#pragma unroll
      for (int i = 0; i < 8; ++i) { const unsigned w = P[i >> 2][i & 3]; P[i >> 2][i & 3] = cvtpk(__uint_as_float(w << 16) * f, __uint_as_float(w & 0xFFFF0000u) * f); } };
    auto fix = [&](auto NP, float ps, f32x16& Sc, f32x16& Sn, u32x4 (&P)[2]) {
      if (__builtin_expect(__any(!(ps <= SUM_THR)), 0)) {
        const float f = __builtin_amdgcn_exp2f(-fmaxf(swap_max(max16(Sc)), 0.f));
        scaleP(P0, f); if constexpr (NP.value > 1) scaleP(P1, f); if constexpr (NP.value > 2) scaleP(P2, f); }
      sm_fix(ps, Sc, Sn, P); };
    auto rdVt = [&](int i) { const int j = pv_order(i), db = j >> 3, un = (j >> 1) & 3, ks = j & 1; return rdV(un < 2 ? v6 : v7, un & 1, 4 * ks + db); };
    auto pvt = [&](auto I, const bf16x8& vf) { constexpr int j = pv_order(I.value), un = (j >> 1) & 3;
      const u32x4 pb = un == 0 ? P0[j & 1] : un == 1 ? P1[j & 1] : un == 2 ? P2[j & 1] : P3[j & 1];
      o[j >> 3] = MFMA(vf, __builtin_bit_cast(bf16x8, pb), o[j >> 3]); pin16(o[j >> 3]); };
    bf16x8 f[16], w[32];
#pragma unroll
    for (int i = 0; i < RA; ++i) f[i] = pre[i];
    auto qsm = [&](auto H, f32x16& Sn, const f32x16& Sc, u32x4 (&Pc)[2]) { constexpr int hf = H.value;
      float e[16]; float s0 = 0.f, s1 = 0.f;
      static_for<9>([&](auto G) { constexpr int g = G.value, n = 8 * hf + g + RA;
        if constexpr (hf < 2 && g < 8) { if constexpr (g == 0) Sn = MFMA(f[8 * hf], qf[0], negm); else Sn = MFMA(f[8 * hf + g], qf[g], Sn);
          pin16(Sn);
          if constexpr (n < 16) f[n] = rdK(k0, k1, n >> 3, n & 7); }
        if constexpr (hf == 1 && g >= 2 && g < 8) w[g - 2] = rdVt(g - 2);
        if constexpr (hf == 2 && g < 8) { pvt(G, w[g]); w[g + 6] = rdVt(g + 6); }
        if constexpr (g < 8) { e[2 * g] = __builtin_amdgcn_exp2f(Sc[2 * g]); e[2 * g + 1] = __builtin_amdgcn_exp2f(Sc[2 * g + 1]); pinf(e[2 * g]); pinf(e[2 * g + 1]); }
        if constexpr (g == 1) { s0 = e[0]; s1 = e[1]; } else if constexpr (g > 1) { s0 += e[2 * g - 2]; pinf(s0); s1 += e[2 * g - 1]; pinf(s1); }
        if constexpr (g >= 1) { unsigned pw = cvtpk(e[2 * g - 2], e[2 * g - 1]); pinu(pw); Pc[(g - 1) >> 2][(g - 1) & 3] = pw; }
        SBAR(); });
      return s0 + s1; };
    if (wid < 4u) __builtin_amdgcn_s_setprio(1);
    SBAR();
    fix(std::integral_constant<int, 1>{}, qsm(std::integral_constant<int, 0>{}, S0, S1, P1), S1, S0, P1);
    bar_if(wid >> 2);
    fix(std::integral_constant<int, 2>{}, qsm(std::integral_constant<int, 1>{}, S1, S0, P2), S0, S1, P2);
    bar_if((wid >> 2) ^ 1u);
    ST(19);
    fix(std::integral_constant<int, 3>{}, qsm(std::integral_constant<int, 2>{}, S0, S1, P3), S1, S0, P3);
    const float inv = __builtin_amdgcn_rcpf(swap_add(l));
    auto put = [&](int k8) { bstore_sc1(pack8(o[k8 >> 1], k8 & 1, inv), lane * 16u, sO, me * 8192u + k8 * 1024u); };
    static_for<24>([&](auto G) { constexpr int g = G.value + 8;
      pvt(std::integral_constant<int, g>{}, w[g]);
      if constexpr (g + 6 < 32) w[g + 6] = rdVt(g + 6);
      if constexpr (g == 14) put(0); if constexpr (g == 16) put(1); if constexpr (g == 19) put(2); if constexpr (g == 22) put(3); if constexpr (g == 26) put(4); if constexpr (g == 29) put(5);
      SBAR(); });
    ST(20);
    put(6); put(7); }
  ST(21);
  tag = mix64(ep);
  __builtin_amdgcn_s_setprio(0);
  }
  if (wid < 7u) { wait_vm<0>();
  ST(22);
  bar(); }
  if (tid == 0) st_agent(ws + FLAG_OFF + (b * NH + h) * FLAG_STRIDE, tag);
  ST(23);
  if (!has_tile) {
    if (bid == 249u && wid == 0u) {
#pragma nounroll
      for (unsigned i = 0; i < 4; ++i) poll_flags(ws + FLAG_OFF + (i * 64u + lane) * FLAG_STRIDE, tag);
      if (lane == 0) st_agent(ws + EPOCH_OFF, tag); }
    ST(24); ST(25); ST(26); ST(27); ST(28); ST(29); ST(30); ST(31); ST_OUT(ws, 0, 32); return; }
  const unsigned jq = (2u * g4 + 2u + wid) & 7u, G = 8u * rt + jq;
  const unsigned voffO = lane * 16u, soO = (G * NH + g4 * 8u) * 8192u;
  const char* fp = ws + FLAG_OFF + (((G * 37u) >> 8) * NH + g4 * 8u + (lane & 7u)) * FLAG_STRIDE;
  unsigned ready = 0;
  auto need = [&](unsigned m) {
    _Pragma("nounroll") for (unsigned spins = 0; (ready & m) != m && spins < SPIN_CAP; ++spins) { ready = (unsigned)__ballot(ld_agent(fp) == tag); if ((ready & m) != m) __builtin_amdgcn_s_sleep(PSLEEP); }
    asm volatile("" ::: "memory"); };
  __builtin_amdgcn_s_sleep(PDELAY);
  need(1u);
  ST(24);
  const lds_cptr wb = (lds_cptr)lds + 1024u * hh + 64u * q4 + trcol;
  f32x16 acc[2] = {};
  { unsigned hd = 0, so_c = soO + hd * 8192u; lds_cptr wp = wb + ((SLOTW >> (4u * hd)) & 15u) * 16384u;
    bf16x8 of[8], lf[4][2];
    auto rdW = [&](int s2) { lf[s2 & 3][0] = trd2(wp, (s2 & 7) * 2048, (s2 & 7) * 2048 + 256); lf[s2 & 3][1] = trd2(wp, (s2 & 7) * 2048 + 512, (s2 & 7) * 2048 + 768); };
#pragma unroll
    for (int i = 0; i < 8; ++i) { if (i < WO) bload_sc1(of[i], voffO, sO, so_c + i * 1024u); else of[i] = bf16x8{}; }
    rdW(0); rdW(1);
#pragma nounroll
    for (unsigned p = 0; p < 8; ++p) {
      hd = (hd + 1u) & 7u;
      if (p == 5u) __builtin_amdgcn_s_barrier();
      const unsigned so_n = p < 7u ? soO + hd * 8192u : O_BYTES;
      static_for<8>([&](auto S) { constexpr int s2 = S.value;
        if constexpr (s2 == 8 - WO) need(1u << hd);
        if constexpr (s2 == 6) wp = wb + ((SLOTW >> (4u * hd)) & 15u) * 16384u;
        wait_vm1<WO - 1>(of[s2]);
        wait_lgkm<4>();
        acc[0] = MFMA(lf[s2 & 3][0], of[s2], acc[0]);
        rdW(s2 + 2);
        SBAR();
        acc[1] = MFMA(lf[s2 & 3][1], of[s2], acc[1]);
        SBAR();
        breload_sc1(of[(s2 + WO) & 7], voffO, sO, (s2 + WO < 8 ? so_c : so_n) + ((s2 + WO) & 7) * 1024u);
        SBAR(); });
      so_c = so_n;
    }
    wait_vm<0>();
#pragma unroll
    for (int i = 0; i < 8; ++i) pin(of[i]); }
  ST(28);
  const srd_t sX2 = mk_srd(ws + X_OFF, X_BYTES);
  const unsigned xb = (u * 8u + jq) * 4u;
  if (wid < 6u) {
#pragma unroll
    for (int i = 0; i < 4; ++i) bstore_sc1(pack8(acc[i >> 1], i & 1), voffO, sX2, (xb + g4) * 4096u + i * 1024u);
    wait_vm<0>();
    if (lane == 0) st_agent(ws + FLAG2_OFF + (xb + g4) * FLAG_STRIDE, tag);
    ST(29); ST(30);
  } else {
    __builtin_amdgcn_s_sleep(PDELAY2);
    poll_flags(ws + FLAG2_OFF + (xb + ((g4 + 1u + lane % 3u) & 3u)) * FLAG_STRIDE, tag);
    ST(29);
    bf16x8 xp[12];
#pragma unroll
    for (int i = 0; i < 12; ++i) bload_sc1(xp[i], voffO, sX2, (xb + ((g4 + 1u + (i >> 2)) & 3u)) * 4096u + (i & 3) * 1024u);
    wait_vm<0>();
#pragma unroll
    for (int j = 0; j < 12; ++j) { const int i = j & 3; pin(xp[j]); const u32x4 v = __builtin_bit_cast(u32x4, xp[j]);
#pragma unroll
      for (int e = 0; e < 4; ++e) { acc[i >> 1][8 * (i & 1) + 2 * e] += __uint_as_float(v[e] << 16); acc[i >> 1][8 * (i & 1) + 2 * e + 1] += __uint_as_float(v[e] & 0xFFFF0000u); } }
    ST(30);
    char* op = (char*)out + (size_t)(G * 32u + r) * (DM * 2u) + ct * 128u + hh * 16u;
#pragma unroll
    for (int i = 0; i < 4; ++i) *(u32x4*)(op + i * 32) = pack8(acc[i >> 1], i & 1);
  }
  ST(31); ST_OUT(ws, 0, 32);
}

extern "C" void kernel_launch(void* const* d_in, const int* in_sizes, int n_in, void* d_out, int out_size, void* d_ws, size_t ws_size, hipStream_t stream) {
  (void)in_sizes; (void)n_in; (void)out_size; (void)ws_size;
  (void)hipFuncSetAttribute((const void*)mha_kernel, hipFuncAttributeMaxDynamicSharedMemorySize, LDS_BYTES);
  const unsigned short* x = (const unsigned short*)d_in[0]; const unsigned short* kc = (const unsigned short*)d_in[1]; const unsigned short* vc = (const unsigned short*)d_in[2];
  const unsigned short* wq = (const unsigned short*)d_in[3]; const unsigned short* wo = (const unsigned short*)d_in[4];
  mha_kernel<<<256, 512, LDS_BYTES, stream>>>(x, kc, vc, wq, wo, (char*)d_ws, (unsigned short*)d_out);
}
```

```cpp
#define RAMPW 3
#include <hip/hip_runtime.h>
#include <stdint.h>
#include <utility>

typedef short    bf16x8 __attribute__((ext_vector_type(8)));
typedef short    s16x4  __attribute__((ext_vector_type(4)));
typedef float    f32x16 __attribute__((ext_vector_type(16)));
typedef float    f32x2  __attribute__((ext_vector_type(2)));
typedef __bf16   bf16x2 __attribute__((ext_vector_type(2)));
typedef unsigned u32x4  __attribute__((ext_vector_type(4)));
typedef __attribute__((address_space(3))) const char* lds_cptr;
typedef __attribute__((address_space(1))) unsigned long long gu64;
typedef __amdgpu_buffer_rsrc_t srd_t;

#define DI __device__ __forceinline__
#define MFMA(a, b, c) __builtin_amdgcn_mfma_f32_32x32x16_bf16((a), (b), (c), 0, 0, 0)
#define SBAR() __builtin_amdgcn_sched_barrier(0)

constexpr int NB = 8, SQ = 224, DM = 576, NH = 32, SK = 512, DH = 128;
constexpr int LDS_BYTES = 163840;
constexpr float QSCALE = 0.08838834764831845f * 1.4426950408889634f;
constexpr float SUM_THR = 4096.f;

template <int... Is, class F> DI void static_for_impl(std::integer_sequence<int, Is...>, F&& f) { (f(std::integral_constant<int, Is>{}), ...); }
template <int N, class F> DI void static_for(F&& f) { static_for_impl(std::make_integer_sequence<int, N>{}, f); }

DI unsigned rfl(unsigned x) { return __builtin_amdgcn_readfirstlane(x); }
DI unsigned cvtpk(float lo, float hi) { f32x2 v = {lo, hi}; bf16x2 b = __builtin_convertvector(v, bf16x2); return __builtin_bit_cast(unsigned, b); }
DI srd_t mk_srd(const void* p, unsigned bytes) { return __builtin_amdgcn_make_buffer_rsrc((void*)p, (short)0, (int)bytes, 0x00020000); }
DI void dma4(unsigned lds, unsigned v0, unsigned v1, unsigned v2, unsigned v3, srd_t srd, unsigned soff) {
  asm volatile("s_mov_b32 m0, %0\n\ts_nop 0\n\tbuffer_load_dwordx4 %1, %5, %6 offen lds\n\tbuffer_load_dwordx4 %2, %5, %6 offen offset:1024 lds\n\t"
               "buffer_load_dwordx4 %3, %5, %6 offen offset:2048 lds\n\tbuffer_load_dwordx4 %4, %5, %6 offen offset:3072 lds"
               :: "s"(lds), "v"(v0), "v"(v1), "v"(v2), "v"(v3), "s"(srd), "s"(soff) : "memory");
}
DI void dma4_nt(unsigned lds, unsigned v0, unsigned v1, unsigned v2, unsigned v3, srd_t srd, unsigned soff) {
  asm volatile("s_mov_b32 m0, %0\n\ts_nop 0\n\tbuffer_load_dwordx4 %1, %5, %6 offen nt lds\n\tbuffer_load_dwordx4 %2, %5, %6 offen offset:1024 nt lds\n\t"
               "buffer_load_dwordx4 %3, %5, %6 offen offset:2048 nt lds\n\tbuffer_load_dwordx4 %4, %5, %6 offen offset:3072 nt lds"
               :: "s"(lds), "v"(v0), "v"(v1), "v"(v2), "v"(v3), "s"(srd), "s"(soff) : "memory");
}
DI void bstore_sc1(const u32x4& d, unsigned voff, srd_t srd, unsigned soff) { asm volatile("buffer_store_dwordx4 %0, %1, %2, %3 offen sc1\n\ts_nop 1" :: "v"(d), "v"(voff), "s"(srd), "s"(soff) : "memory"); }
DI void bload_sc1(bf16x8& d, unsigned voff, srd_t srd, unsigned soff) { asm volatile("buffer_load_dwordx4 %0, %1, %2, %3 offen sc1" : "=v"(d) : "v"(voff), "s"(srd), "s"(soff) : "memory"); }
DI void breload_sc1(bf16x8& d, unsigned voff, srd_t srd, unsigned soff) { asm volatile("buffer_load_dwordx4 %0, %1, %2, %3 offen sc1" : "+v"(d) : "v"(voff), "s"(srd), "s"(soff) : "memory"); }
DI void pin(bf16x8& a) { asm volatile("" : "+v"(a)); }
DI void pin16(f32x16& a) { asm volatile("" : "+v"(a)); }
DI void pinf(float& a) { asm volatile("" : "+v"(a)); }
DI void pinu(unsigned& a) { asm volatile("" : "+v"(a)); }
template <int N> DI void wait_vm1(bf16x8& a) { asm volatile("s_waitcnt vmcnt(%1)" : "+v"(a) : "n"(N) : "memory"); }
template <int N> DI void wait_vm() { asm volatile("s_waitcnt vmcnt(%0)" :: "n"(N) : "memory"); }
template <int N> DI void wait_lgkm() { __builtin_amdgcn_s_waitcnt(0xC07F | (N << 8)); SBAR(); }
DI void bar_if(unsigned c) { asm volatile("s_cmp_eq_u32 %0, 0\n\ts_cbranch_scc1 1f\n\ts_waitcnt lgkmcnt(0)\n\ts_barrier\n1:" :: "s"(c) : "memory", "scc"); }
DI void bar() { asm volatile("s_waitcnt lgkmcnt(0)\n\ts_barrier" ::: "memory"); }
typedef short v4i16_t __attribute__((ext_vector_type(4)));
DI s16x4 trd(lds_cptr p) { return __builtin_amdgcn_ds_read_tr16_b64_v4i16((__attribute__((address_space(3))) v4i16_t*)p); }
DI bf16x8 trd2(lds_cptr p, int o0, int o1) { s16x4 a = trd(p + o0), b = trd(p + o1); return __builtin_shufflevector(a, b, 0, 1, 2, 3, 4, 5, 6, 7); }
DI bf16x8 rd128(lds_cptr p) { return *(const __attribute__((address_space(3))) bf16x8*)p; }
DI float swap_max(float m) { auto rr = __builtin_amdgcn_permlane32_swap(__float_as_uint(m), __float_as_uint(m), false, false); return fmaxf(__uint_as_float(rr[0]), __uint_as_float(rr[1])); }
DI float swap_add(float m) { auto rr = __builtin_amdgcn_permlane32_swap(__float_as_uint(m), __float_as_uint(m), false, false); return __uint_as_float(rr[0]) + __uint_as_float(rr[1]); }
DI float max16(const f32x16& s) { float a = fmaxf(fmaxf(s[0], s[1]), s[2]), b = fmaxf(fmaxf(s[3], s[4]), s[5]);
  a = fmaxf(fmaxf(a, s[6]), s[7]); b = fmaxf(fmaxf(b, s[8]), s[9]); a = fmaxf(fmaxf(a, s[10]), s[11]); b = fmaxf(fmaxf(b, s[12]), s[13]); a = fmaxf(fmaxf(a, s[14]), s[15]); return fmaxf(a, b); }
DI u32x4 pack8(const f32x16& a, int s, float f) { return u32x4{cvtpk(a[8 * s] * f, a[8 * s + 1] * f), cvtpk(a[8 * s + 2] * f, a[8 * s + 3] * f), cvtpk(a[8 * s + 4] * f, a[8 * s + 5] * f), cvtpk(a[8 * s + 6] * f, a[8 * s + 7] * f)}; }
DI u32x4 pack8(const f32x16& a, int s) { return u32x4{cvtpk(a[8 * s], a[8 * s + 1]), cvtpk(a[8 * s + 2], a[8 * s + 3]), cvtpk(a[8 * s + 4], a[8 * s + 5]), cvtpk(a[8 * s + 6], a[8 * s + 7])}; }

DI float expo(const f32x16& s, u32x4 (&p)[2]) {
  float e[16]; float s0 = 0.f, s1 = 0.f;
#pragma unroll
  for (int i = 0; i < 16; ++i) e[i] = __builtin_amdgcn_exp2f(s[i]);
#pragma unroll
  for (int i = 0; i < 16; i += 2) { s0 += e[i]; s1 += e[i + 1]; }
#pragma unroll
  for (int i = 0; i < 8; ++i) p[i >> 2][i & 3] = cvtpk(e[2 * i], e[2 * i + 1]);
  return s0 + s1;
}

#define ST_DECL
#define ST(k)
#define ST_OUT(ws, lo, hi)

constexpr unsigned SLOTK = 0x04204209u;
constexpr unsigned SLOTV = 0x31531531u;
constexpr unsigned SLOTW = 0x05429876u;
constexpr unsigned STG = 45056u, STG_W = 28672u;
constexpr unsigned O_BYTES = NB * SQ * NH * DH * 2;
constexpr unsigned X_OFF = O_BYTES, X_BYTES = 63u * 8u * 4u * 4096u;
constexpr unsigned FLAG_OFF = X_OFF + X_BYTES, FLAG_STRIDE = 128, FLAG2_OFF = FLAG_OFF + 256 * FLAG_STRIDE, EPOCH_OFF = FLAG2_OFF + 63 * 32 * FLAG_STRIDE;
#define XOFF 0
#define KA 0u
#define QG 1
#define PACE 0
#define PACEA 0
#define K0S 1
#define NOWUP 0
#define WUPC 3u
#define RAMP 0
#define HOLD 0
#define SRAMP 0
#define PRE 0u
constexpr unsigned SPIN_CAP = 1u << 20;
constexpr int RA = 7, WO = 6, A0BAR = 6, PSLEEP = 4, PDELAY = 20, PDELAY2 = 25;
constexpr unsigned SPARE = 3u * STG;
constexpr unsigned CODE_BYTES = 19200u;
constexpr int lds_ops(int a, int b, bool last) { int n = 0; for (int i = a; i < b; ++i) n += i >= 16 ? (last ? 0 : 1) : (i >= 8 ? 2 : 1); return n; }
constexpr int pv_order(int i) { return i < 4 ? i : i < 8 ? i + 4 : i < 12 ? i - 4 : i; }
DI unsigned long long mix64(unsigned long long z) { z += 0x9E3779B97F4A7C15ull; z = (z ^ (z >> 30)) * 0xBF58476D1CE4E5B9ull; z = (z ^ (z >> 27)) * 0x94D049BB133111EBull; return z ^ (z >> 31); }
DI unsigned long long rfl64(unsigned long long v) { return ((unsigned long long)rfl((unsigned)(v >> 32)) << 32) | rfl((unsigned)v); }
DI unsigned long long ld_agent(const char* p) { return __hip_atomic_load((gu64*)p, __ATOMIC_RELAXED, __HIP_MEMORY_SCOPE_AGENT); }
DI void st_agent(char* p, unsigned long long v) { __hip_atomic_store((gu64*)p, v, __ATOMIC_RELAXED, __HIP_MEMORY_SCOPE_AGENT); }
DI void poll_flags(const char* p, unsigned long long tag) {
  _Pragma("nounroll") for (unsigned spins = 0; spins < SPIN_CAP; ++spins) { if (__all(ld_agent(p) == tag)) break; __builtin_amdgcn_s_sleep(PSLEEP); }
  asm volatile("" ::: "memory");
}

__global__ __launch_bounds__(512, 2) void mha_kernel(const unsigned short* __restrict__ x, const unsigned short* __restrict__ kc, const unsigned short* __restrict__ vc,
                                                     const unsigned short* __restrict__ wq, const unsigned short* __restrict__ wo, char* ws, unsigned short* __restrict__ out) {
  extern __shared__ __attribute__((aligned(16))) char lds[];
  unsigned long long pc0; asm volatile("s_getpc_b64 %0" : "=s"(pc0));
  const unsigned tid = threadIdx.x, lane = tid & 63u, r = lane & 31u, hh = lane >> 5;
  const unsigned wid = rfl(tid >> 6);
  ST_DECL ST(0);
  const unsigned bid = blockIdx.x, xcd = bid & 7u, ix = bid >> 3;
  const unsigned b = (xcd & 1u) * 4u + (ix & 3u), h = (xcd >> 1) * 8u + (ix >> 2);
  const unsigned lds0 = (unsigned)(uintptr_t)lds;
  if (SRAMP) { for (unsigned i = 0; i < (ix & 7u); ++i) __builtin_amdgcn_s_sleep(SRAMP); }
  {
    const char* cp = (const char*)(pc0 & ~255ull) + min((ix * 8u + wid) * 128u, CODE_BYTES);
    asm volatile("s_mov_b32 m0, %0\n\ts_nop 0\n\tglobal_load_lds_dword %1, %2" :: "s"(lds0 + SPARE), "v"(0u), "s"(cp) : "memory"); }
  const srd_t sW = mk_srd((const char*)wq - 1024, DM * NH * DH * 2 + 1024);
  const srd_t sX = mk_srd(x + (size_t)b * SQ * DM, SQ * DM * 2);
  const unsigned prow = (lane >> 2) & 7u, pch = 4u * hh + (lane & 3u);
  const unsigned pchx0 = 4u * hh + ((lane & 3u) ^ ((lane >> 4) & 1u)), pchx1 = pchx0 ^ 2u;
  const unsigned vW = prow * 8192u + pch * 16u, vX0 = prow * (DM * 2u) + pchx0 * 16u, vX1 = prow * (DM * 2u) + pchx1 * 16u;
  const unsigned ck0 = (QG == 3 ? 3u * (((ix & 3u) + (ix >> 2)) % 3u) : QG == 2 ? 4u * (((ix & 3u) + (ix >> 2)) & 1u) : 0u) + (XOFF ? (0x10022110u >> (4u * xcd)) & 3u : 0u), ck1 = ck0 == 8u ? 0u : ck0 + 1u;
  unsigned long long ep; asm volatile("global_load_dwordx2 %0, %1, %2 sc1" : "=v"(ep) : "v"(0u), "s"(ws + EPOCH_OFF) : "memory");
  dma4(lds0 + (wid < 7u ? wid * 4096u : SPARE), vX0, vX1 + 8192u, vX0 + 16384u, vX1 + 24576u, sX, ck0 * 128u + min(wid, 6u) * 32u * DM * 2u);
  dma4(lds0 + (wid >> 2) * STG + STG_W + (wid & 3u) * 4096u, vW + 1024u, vW + 128u, vW + 65536u - 1024u, vW + 65664u - 2048u, sW, (wid < 4u ? ck0 : ck1) * 64u * 8192u + h * 256u + (wid & 3u) * 131072u);
  const srd_t sK = mk_srd((const char*)(kc + (size_t)(b * NH + h) * SK * DH) - 1024, SK * DH * 2 + 1024);
  const srd_t sV = mk_srd((const char*)(vc + (size_t)(b * NH + h) * SK * DH) - 1024, SK * DH * 2 + 1024);
  const unsigned vK0 = prow * 256u + pchx0 * 16u, vK1 = prow * 256u + pchx1 * 16u;
  const unsigned q4 = (lane & 15u) >> 2, p4 = lane & 3u, blk = (lane >> 4) & 1u;
  const unsigned trcol = 32u * blk + 16u * (p4 & 1u) + 8u * (p4 >> 1);
  const srd_t sO = mk_srd(ws, O_BYTES);
  unsigned long long tag;
  const unsigned g4 = xcd >> 1, u0 = (xcd & 1u) * 32u + ix;
  const bool has_tile = u0 < 63u;
  const unsigned u = has_tile ? u0 : 62u, rt = u / 9u, ct = u - rt * 9u;

  if (wid == 7u) {
    auto fillQ = [&](unsigned c, unsigned stage) {
      const unsigned lw = lds0 + stage + STG_W, sw = c * 64u * 8192u + h * 256u, lx = lds0 + stage, sx = c * 128u;
#pragma unroll
      for (int i = 0; i < 4; ++i) dma4(lw + i * 4096u, vW + 1024u, vW + 128u, vW + 65536u - 1024u, vW + 65664u - 2048u, sW, sw + i * 131072u);
#pragma unroll
      for (int i = 0; i < 7; ++i) dma4(lx + i * 4096u, vX0, vX1 + 8192u, vX0 + 16384u, vX1 + 24576u, sX, sx + i * 32u * DM * 2u); };
    bar(); ST(2);
    { unsigned stage = 2 * STG, ck = ck1 == 8u ? 0u : ck1 + 1u;
#pragma nounroll
      for (unsigned c = 2; c < 9; ++c) { fillQ(ck, stage); ck = ck == 8u ? 0u : ck + 1u; stage = stage == 2 * STG ? 0u : stage + STG;
        wait_vm<44>(); bar(); ST(1 + c); } }
    const unsigned vV = prow * 256u + pch * 16u;
    const srd_t sWo = mk_srd(wo, NH * DH * DM * 2);
    const unsigned vWo = prow * (DM * 2u) + pch * 16u;
    auto pace = [&](unsigned n) { if (n == 0u) { if (PACEA) __builtin_amdgcn_s_sleep(PACEA); } else if (n < 6u) { if (PACE) __builtin_amdgcn_s_sleep(PACE); } };
    { unsigned j = 1, jw = 0;
#pragma nounroll
      for (unsigned n = 0; n < 10; ++n) {
        if (n == 9u) { wait_vm<0>(); bar(); }
        const unsigned je = ((unsigned)(0xFFFECA8642ull >> (4u * n)) & 15u) + 1u;
#pragma nounroll
        for (; j < je; ++j) {
          if ((j & 1u) && j != 15u) { const unsigned t = (j + 1u) >> 1, ld = lds0 + ((SLOTK >> (4u * t)) & 15u) * 16384u;
#pragma unroll
            for (int i = 0; i < 4; ++i) dma4_nt(ld + i * 4096u, vK0 + 1024u, vK0 + 128u, vK1 + 1024u, vK1 + 128u, sK, t * 16384u + i * 4096u), pace(n);
          } else { const unsigned t = (j - 1u) >> 1, ld = lds0 + ((SLOTV >> (4u * t)) & 15u) * 16384u;
#pragma unroll
            for (int i = 0; i < 4; ++i) dma4_nt(ld + i * 4096u, vV + 1024u, vV + 128u, vV + 1024u, vV + 128u, sV, t * 16384u + i * 4096u), pace(n); } }
        const unsigned we = (unsigned)(0x8741000000ull >> (4u * n)) & 15u;
#pragma nounroll
        for (; jw < we; ++jw) { const unsigned ld = lds0 + ((SLOTW >> (4u * jw)) & 15u) * 16384u, so = (g4 * 8u + jw) * 128u * (DM * 2u) + ct * 128u;
#pragma unroll
          for (int k = 0; k < 4; ++k) dma4(ld + k * 4096u, vWo, vWo + 8192u, vWo + 16384u, vWo + 24576u, sWo, so + k * 32u * (DM * 2u)); }
        if (n == 9u) break;
        if (n < 6u) wait_vm<32>(); else if (n == 6u) wait_vm<48>(); else wait_vm<62>();
        bar(); ST(n == 0u ? 10u : 11u + n); } }
    asm volatile("" : "+v"(ep)); tag = mix64(rfl64(ep));
    ST(20); ST(21);
  } else {
  const unsigned rsw0 = 64u * (r & 7u) + 16u * (hh ^ ((r >> 2) & 3u)), rsw1 = rsw0 ^ 32u;
  f32x16 qa[4] = {}, o[4] = {};
  asm volatile("" : "+v"(qa[0]), "+v"(qa[1]), "+v"(qa[2]), "+v"(qa[3]), "+v"(o[0]), "+v"(o[1]), "+v"(o[2]), "+v"(o[3]));
  if (PRE) {
    unsigned z = (tid + 1u) * 2654435761u; u32x4 ja = {z & 0xBFFFBFFFu, (z * 3u) & 0xBFFFBFFFu, (z * 5u) & 0xBFFFBFFFu, (z * 7u) & 0xBFFFBFFFu}, jb = {(z * 9u) & 0xBFFFBFFFu, (z * 11u) & 0xBFFFBFFFu, (z * 13u) & 0xBFFFBFFFu, (z * 15u) & 0xBFFFBFFFu};
    f32x16 jc = {};
#pragma nounroll
    for (unsigned i = 0; i < PRE; ++i) {
      jc = MFMA(__builtin_bit_cast(bf16x8, ja), __builtin_bit_cast(bf16x8, jb), jc); jc = MFMA(__builtin_bit_cast(bf16x8, jb), __builtin_bit_cast(bf16x8, ja), jc); pin16(jc);
      ja[0] = ja[1]; ja[1] = jb[2]; jb[2] = jb[3] ^ 0x01230456u; jb[3] = ja[0];
      if (i * 3u < PRE) __builtin_amdgcn_s_sleep(4); else if (i * 3u < 2u * PRE) __builtin_amdgcn_s_sleep(2); } }
  wait_lgkm<0>();
  wait_vm<4>(); if (wid < 4u) wait_vm<0>();
  ST(1);
  dma4(lds0 + STG + wid * 4096u, vX0, vX1 + 8192u, vX0 + 16384u, vX1 + 24576u, sX, ck1 * 128u + wid * 32u * DM * 2u);
  bar();
  asm volatile("" : "+v"(ep)); ep = rfl64(ep);
  ST(2);
  const lds_cptr kb0 = (lds_cptr)lds + 2048u * (r >> 3) + rsw0, kb1 = (lds_cptr)lds + 2048u * (r >> 3) + rsw1;
  const lds_cptr vb = (lds_cptr)lds + 64u * (4u * hh + q4) + trcol;
  bf16x8 qf[8], pre[RA];
  f32x16 negm, S0, S1; u32x4 P0[2], P1[2]; float l = 0.f;
  auto rdK = [&](lds_cptr k0, lds_cptr k1, int kb, int i) { return rd128(((i & 1) ? k1 : k0) + 8192 * kb + 512 * (i >> 1)); };
  auto rdV = [&](lds_cptr v, int kb, int j) { return trd2(v, 8192 * kb + 4096 * (j >> 2) + 512 * (j & 3), 8192 * kb + 4096 * (j >> 2) + 2048 + 512 * (j & 3)); };
  auto sm_fix = [&](float ps, f32x16& Sc, f32x16& Sn, u32x4 (&P)[2]) {
    if (__builtin_expect(__any(!(ps <= SUM_THR)), 0)) {
      const float dl = fmaxf(swap_max(max16(Sc)), 0.f), f = __builtin_amdgcn_exp2f(-dl);
#pragma unroll
      for (int i = 0; i < 16; ++i) { Sc[i] -= dl; Sn[i] -= dl; negm[i] -= dl; }
#pragma unroll
      for (int db = 0; db < 4; ++db)
#pragma unroll
        for (int i = 0; i < 16; ++i) o[db][i] *= f;
      l *= f; ps = expo(Sc, P); }
    l += ps; };
  { lds_cptr wp = (lds_cptr)lds + STG_W + 2048u * hh + 64u * q4 + trcol;
    lds_cptr xp0 = (lds_cptr)lds + wid * 4096u + 1024u * (r >> 3) + rsw0, xp1 = (lds_cptr)lds + wid * 4096u + 1024u * (r >> 3) + rsw1;
    bf16x8 xf[4], af[5];
    auto rdA = [&](int i) { return trd2(wp, 4096 * (i >> 2) + 512 * (i & 3), 4096 * (i >> 2) + 512 * (i & 3) + 256); };
    xf[0] = rd128(xp0); af[0] = rdA(0); af[1] = rdA(1); af[2] = rdA(2); af[3] = rdA(3);
    unsigned stage = 0;
#pragma nounroll
    for (unsigned c = 0; c < 8; ++c) {
      if (!NOWUP && __builtin_expect(c == WUPC && wid == 3u, 0)) {
#pragma unroll
        for (int i = 0; i < (K0S ? 0 : 4); ++i) dma4_nt(lds0 + 9u * 16384u + i * 4096u, vK0 + 1024u, vK0 + 128u, vK1 + 1024u, vK1 + 128u, sK, i * 4096u);
        unsigned ln = lane; asm volatile("" : "+v"(ln));
        const unsigned k = ln & 7u, sel = ln >> 3;
        const char* wup = sel == 0u ? (const char*)vc + (size_t)(b * NH + h) * (SK * DH * 2) + k * 16384u
                        : sel == 1u ? (const char*)wo + (size_t)(g4 * 1024u + k * 128u) * (DM * 2u) + ct * 128u
                        : sel == 2u ? (const char*)out + (size_t)((8u * rt + k) * 32u) * (DM * 2u)
                        : ws + (sel == 3u ? ((b * 7u + k) * NH + h) * 8192u : sel == 4u ? ((8u * rt + k) * NH + g4 * 8u) * 8192u : sel == 5u ? X_OFF + u * 131072u + k * 16384u : FLAG_OFF + k * 32768u + 64u);
        asm volatile("s_mov_b32 m0, %0\n\ts_nop 0\n\tglobal_load_lds_dword %1, off sc1" :: "s"(lds0 + SPARE), "v"(wup) : "memory"); }
      if (HOLD && c == 0u) __builtin_amdgcn_s_sleep(HOLD);
      if (RAMPW && c == 0u) { for (unsigned i = 0; i < ((wid + ix) & 3u); ++i) __builtin_amdgcn_s_sleep(RAMPW); }
      if (RAMP && c == 0u) { for (unsigned i = 0; i < ((ix + wid) & 7u); ++i) __builtin_amdgcn_s_sleep(RAMP); }
      if (K0S && __builtin_expect(c >= 3u && c < 7u && wid == 3u, 0)) dma4_nt(lds0 + 9u * 16384u + (c - 3u) * 4096u, vK0 + 1024u, vK0 + 128u, vK1 + 1024u, vK1 + 128u, sK, (c - 3u) * 4096u);
      if (KA && __builtin_expect(c >= 4u && c < 7u && wid == 3u, 0))
        asm volatile("s_mov_b32 m0, %0\n\ts_nop 0\n\tbuffer_load_dword %1, %2, %3 offen nt lds" :: "s"(lds0 + SPARE), "v"((lane & (KA - 1u)) * 128u), "s"(sK), "s"(17408u + (c - 4u) * KA * 128u) : "memory");
      const unsigned nstage = stage == 2 * STG ? 0u : stage + STG; const int dlt = (int)nstage - (int)stage; stage = nstage;
      SBAR();
      static_for<12>([&](auto I) { constexpr int i = I.value;
        qa[i & 3] = MFMA(af[i % 5], xf[i >> 2], qa[i & 3]);
        af[(i + 4) % 5] = rdA(i + 4);
        if constexpr (i == 0) xf[1] = rd128(xp1); if constexpr (i == 3) xf[2] = rd128(xp0 + 512); if constexpr (i == 7) xf[3] = rd128(xp1 + 512);
        SBAR(); });
      qa[0] = MFMA(af[12 % 5], xf[3], qa[0]);
      wp += dlt; xp0 += dlt; xp1 += dlt; asm volatile("" : "+v"(wp), "+v"(xp0), "+v"(xp1));
      SBAR();
      asm volatile("s_bitcmp1_b32 %0, %1\n\ts_cbranch_scc1 1f\n\ts_waitcnt vmcnt(0)\n1:\n\ts_waitcnt lgkmcnt(0)\n\ts_barrier" :: "s"(0x78u), "s"(c) : "memory", "scc");
      const bf16x8 xl = xf[3], a13 = af[13 % 5], a14 = af[14 % 5], a15 = af[15 % 5];
      xf[0] = rd128(xp0); af[0] = rdA(0); af[1] = rdA(1);
      SBAR();
      qa[1] = MFMA(a13, xl, qa[1]); SBAR();
      af[2] = rdA(2);
      qa[2] = MFMA(a14, xl, qa[2]); SBAR();
      af[3] = rdA(3);
      qa[3] = MFMA(a15, xl, qa[3]); SBAR();
      ST(3 + c);
    }
    lds_cptr k0 = kb0 + 9 * 16384, k1 = kb1 + 9 * 16384; asm volatile("" : "+v"(k0), "+v"(k1));
    bf16x8 ag[16], f[16]; u32x4 qw[8]; float qs = QSCALE; asm volatile("" : "+s"(qs));
#pragma unroll
    for (int i = 0; i < 4; ++i) ag[i] = af[i];
    auto cvq = [&](int j) { float lo = qa[j >> 3][2 * (j & 7)] * qs; pinf(lo); float hi = qa[j >> 3][2 * (j & 7) + 1] * qs; unsigned w = cvtpk(lo, hi); pinu(w); qw[j >> 2][j & 3] = w; };
    SBAR();
    static_for<24>([&](auto G) { constexpr int g = G.value, n = g + 4;
      if constexpr (g < 4) qa[g] = MFMA(ag[g], xf[0], qa[g]);
      else if constexpr (g < 16) qa[(g - 4) / 3] = MFMA(ag[g], xf[1 + (g - 4) % 3], qa[(g - 4) / 3]);
      else { qf[g - 16] = __builtin_bit_cast(bf16x8, qw[g - 16]);
        if constexpr (g == 16) { const f32x16 z = {}; S0 = MFMA(f[0], qf[0], z); } else S0 = MFMA(f[g - 16], qf[g - 16], S0); pin16(S0); }
      if constexpr (g == 0) { xf[1] = rd128(xp1); xf[2] = rd128(xp0 + 512); } if constexpr (g == 1) xf[3] = rd128(xp1 + 512);
      if constexpr (n < 16) ag[n] = rdA(4 * (1 + (n - 4) % 3) + (n - 4) / 3); else f[n - 16] = rdK(k0, k1, (n - 16) >> 3, (n - 16) & 7);
      if constexpr (g >= 9 && g < 21) { constexpr int j0 = (g - 9) * 8 / 3, j1 = (g - 8) * 8 / 3; static_for<j1 - j0>([&](auto J) { cvq(j0 + J.value); }); }
      SBAR(); });
    ST(11);
#pragma unroll
    for (int i = 12; i < 16; ++i) f[i] = rdK(k0, k1, 1, i & 7);
    const float m = swap_max(max16(S0));
#pragma unroll
    for (int i = 0; i < 16; ++i) negm[i] = -m;
    SBAR();
    float e[16]; float s0 = 0.f, s1 = 0.f;
    static_for<9>([&](auto G) { constexpr int g = G.value;
      if constexpr (g == 0) { if (wid >= 4u) bar(); }
      if constexpr (g == A0BAR) { if (wid < 4u) bar();
        constexpr unsigned ko = ((SLOTK >> 4) & 15u) * 16384u;
#pragma unroll
        for (int i = 0; i < RA; ++i) pre[i] = rdK(kb0 + ko, kb1 + ko, 0, i);
        SBAR(); }
      if constexpr (g == 0) S1 = MFMA(f[8], qf[0], negm); else if constexpr (g < 8) S1 = MFMA(f[8 + g], qf[g], S1);
      if constexpr (g < 8) pin16(S1);
      if constexpr (g < 8) { e[2 * g] = __builtin_amdgcn_exp2f(S0[2 * g] - m); e[2 * g + 1] = __builtin_amdgcn_exp2f(S0[2 * g + 1] - m); pinf(e[2 * g]); pinf(e[2 * g + 1]); }
      if constexpr (g == 1) { s0 = e[0]; s1 = e[1]; } else if constexpr (g > 1) { s0 += e[2 * g - 2]; pinf(s0); s1 += e[2 * g - 1]; pinf(s1); }
      if constexpr (g >= 1) { unsigned w = cvtpk(e[2 * g - 2], e[2 * g - 1]); pinu(w); P0[(g - 1) >> 2][(g - 1) & 3] = w; }
      SBAR(); });
    l = s0 + s1; }
  ST(12);
  auto unit = [&](auto KB, auto LAST, lds_cptr k0, lds_cptr k1, lds_cptr v, lds_cptr nk0, lds_cptr nk1, f32x16& Sn, f32x16& Sc, const u32x4 (&Pp)[2], u32x4 (&Pc)[2]) {
    constexpr int kb = KB.value; constexpr bool last = LAST.value;
    float e[16]; float s0 = 0.f, s1 = 0.f; bf16x8 f[16 + RA];
#pragma unroll
    for (int i = 0; i < RA; ++i) f[i] = pre[i];
    static_for<16>([&](auto G) { constexpr int g = G.value; constexpr int n = g + RA; constexpr int gb = 12;
      if constexpr (last && g == gb) { bar(); wait_lgkm<0>(); }
      else if constexpr ((g & 3) == 0) wait_lgkm<lds_ops(g + 4, g + RA, last)>();
      if constexpr (g == 0) Sn = MFMA(f[0], qf[0], negm); else if constexpr (g < 8) Sn = MFMA(f[g], qf[g], Sn);
      else o[g & 3] = MFMA(f[g], __builtin_bit_cast(bf16x8, Pp[(g >> 2) & 1]), o[g & 3]);
      if constexpr (n < 8) f[n] = rdK(k0, k1, kb, n); else if constexpr (n < 16) f[n] = rdV(v, kb, n - 8);
      else if constexpr (!last) f[n] = rdK(k0, k1, kb + 1, n - 16);
      else if constexpr (g >= gb) { constexpr int per = (RA + 15 - gb) / (16 - gb); static_for<per>([&](auto J) { constexpr int i = per * (g - gb) + J.value; if constexpr (i < RA) f[16 + i] = rdK(nk0, nk1, 0, i); }); }
      if constexpr (g == 0) { e[0] = __builtin_amdgcn_exp2f(Sc[0]); pinf(e[0]); }
      if constexpr (g < 15) { e[g + 1] = __builtin_amdgcn_exp2f(Sc[g + 1]); pinf(e[g + 1]); }
      if constexpr (g == 1) { s0 = e[0]; s1 = e[1]; } else if constexpr (g > 1) { if constexpr (g & 1) { s1 += e[g]; pinf(s1); } else { s0 += e[g]; pinf(s0); } }
      if constexpr (g & 1) { unsigned w = cvtpk(e[g - 1], e[g]); pinu(w); Pc[g >> 3][(g >> 1) & 3] = w; }
      SBAR(); });
#pragma unroll
    for (int i = 0; i < RA; ++i) pre[i] = f[16 + i];
    sm_fix(s0 + s1, Sc, Sn, Pc); };
#pragma nounroll
  for (unsigned t = 1; t < 7; ++t) {
    const unsigned ko = ((SLOTK >> (4u * t)) & 15u) * 16384u, vo = ((SLOTV >> (4u * (t - 1u))) & 15u) * 16384u, nko = ((SLOTK >> (4u * ((t + 1u) & 7u))) & 15u) * 16384u;
    SBAR();
    unit(std::integral_constant<int, 0>{}, std::false_type{}, kb0 + ko, kb1 + ko, vb + vo, kb0, kb1, S0, S1, P0, P1);
    unit(std::integral_constant<int, 1>{}, std::true_type{}, kb0 + ko, kb1 + ko, vb + vo, kb0 + nko, kb1 + nko, S1, S0, P1, P0);
    if (t == 1u || t == 6u) ST(12 + t);
  }
  const unsigned me = (b * 7u + wid) * NH + h;
  {
    constexpr unsigned ko = ((SLOTK >> 28) & 15u) * 16384u;
    const lds_cptr k0 = kb0 + ko, k1 = kb1 + ko, v6 = vb + ((SLOTV >> 24) & 15u) * 16384u, v7 = vb + ((SLOTV >> 28) & 15u) * 16384u;
    u32x4 P2[2], P3[2];
    auto scaleP = [&](u32x4 (&P)[2], float f) {
#pragma unroll
      for (int i = 0; i < 8; ++i) { const unsigned w = P[i >> 2][i & 3]; P[i >> 2][i & 3] = cvtpk(__uint_as_float(w << 16) * f, __uint_as_float(w & 0xFFFF0000u) * f); } };
    auto fix = [&](auto NP, float ps, f32x16& Sc, f32x16& Sn, u32x4 (&P)[2]) {
      if (__builtin_expect(__any(!(ps <= SUM_THR)), 0)) {
        const float f = __builtin_amdgcn_exp2f(-fmaxf(swap_max(max16(Sc)), 0.f));
        scaleP(P0, f); if constexpr (NP.value > 1) scaleP(P1, f); if constexpr (NP.value > 2) scaleP(P2, f); }
      sm_fix(ps, Sc, Sn, P); };
    auto rdVt = [&](int i) { const int j = pv_order(i), db = j >> 3, un = (j >> 1) & 3, ks = j & 1; return rdV(un < 2 ? v6 : v7, un & 1, 4 * ks + db); };
    auto pvt = [&](auto I, const bf16x8& vf) { constexpr int j = pv_order(I.value), un = (j >> 1) & 3;
      const u32x4 pb = un == 0 ? P0[j & 1] : un == 1 ? P1[j & 1] : un == 2 ? P2[j & 1] : P3[j & 1];
      o[j >> 3] = MFMA(vf, __builtin_bit_cast(bf16x8, pb), o[j >> 3]); pin16(o[j >> 3]); };
    bf16x8 f[16], w[32];
#pragma unroll
    for (int i = 0; i < RA; ++i) f[i] = pre[i];
    auto qsm = [&](auto H, f32x16& Sn, const f32x16& Sc, u32x4 (&Pc)[2]) { constexpr int hf = H.value;
      float e[16]; float s0 = 0.f, s1 = 0.f;
      static_for<9>([&](auto G) { constexpr int g = G.value, n = 8 * hf + g + RA;
        if constexpr (hf < 2 && g < 8) { if constexpr (g == 0) Sn = MFMA(f[8 * hf], qf[0], negm); else Sn = MFMA(f[8 * hf + g], qf[g], Sn);
          pin16(Sn);
          if constexpr (n < 16) f[n] = rdK(k0, k1, n >> 3, n & 7); }
        if constexpr (hf == 1 && g >= 2 && g < 8) w[g - 2] = rdVt(g - 2);
        if constexpr (hf == 2 && g < 8) { pvt(G, w[g]); w[g + 6] = rdVt(g + 6); }
        if constexpr (g < 8) { e[2 * g] = __builtin_amdgcn_exp2f(Sc[2 * g]); e[2 * g + 1] = __builtin_amdgcn_exp2f(Sc[2 * g + 1]); pinf(e[2 * g]); pinf(e[2 * g + 1]); }
        if constexpr (g == 1) { s0 = e[0]; s1 = e[1]; } else if constexpr (g > 1) { s0 += e[2 * g - 2]; pinf(s0); s1 += e[2 * g - 1]; pinf(s1); }
        if constexpr (g >= 1) { unsigned pw = cvtpk(e[2 * g - 2], e[2 * g - 1]); pinu(pw); Pc[(g - 1) >> 2][(g - 1) & 3] = pw; }
        SBAR(); });
      return s0 + s1; };
    if (wid < 4u) __builtin_amdgcn_s_setprio(1);
    SBAR();
    fix(std::integral_constant<int, 1>{}, qsm(std::integral_constant<int, 0>{}, S0, S1, P1), S1, S0, P1);
    bar_if(wid >> 2);
    fix(std::integral_constant<int, 2>{}, qsm(std::integral_constant<int, 1>{}, S1, S0, P2), S0, S1, P2);
    bar_if((wid >> 2) ^ 1u);
    ST(19);
    fix(std::integral_constant<int, 3>{}, qsm(std::integral_constant<int, 2>{}, S0, S1, P3), S1, S0, P3);
    const float inv = __builtin_amdgcn_rcpf(swap_add(l));
    auto put = [&](int k8) { bstore_sc1(pack8(o[k8 >> 1], k8 & 1, inv), lane * 16u, sO, me * 8192u + k8 * 1024u); };
    static_for<24>([&](auto G) { constexpr int g = G.value + 8;
      pvt(std::integral_constant<int, g>{}, w[g]);
      if constexpr (g + 6 < 32) w[g + 6] = rdVt(g + 6);
      if constexpr (g == 14) put(0); if constexpr (g == 16) put(1); if constexpr (g == 19) put(2); if constexpr (g == 22) put(3); if constexpr (g == 26) put(4); if constexpr (g == 29) put(5);
      SBAR(); });
    ST(20);
    put(6); put(7); }
  ST(21);
  tag = mix64(ep);
  __builtin_amdgcn_s_setprio(0);
  }
  if (wid < 7u) { wait_vm<0>();
  ST(22);
  bar(); }
  if (tid == 0) st_agent(ws + FLAG_OFF + (b * NH + h) * FLAG_STRIDE, tag);
  ST(23);
  if (!has_tile) {
    if (bid == 249u && wid == 0u) {
#pragma nounroll
      for (unsigned i = 0; i < 4; ++i) poll_flags(ws + FLAG_OFF + (i * 64u + lane) * FLAG_STRIDE, tag);
      if (lane == 0) st_agent(ws + EPOCH_OFF, tag); }
    ST(24); ST(25); ST(26); ST(27); ST(28); ST(29); ST(30); ST(31); ST_OUT(ws, 0, 32); return; }
  const unsigned jq = (2u * g4 + 2u + wid) & 7u, G = 8u * rt + jq;
  const unsigned voffO = lane * 16u, soO = (G * NH + g4 * 8u) * 8192u;
  const char* fp = ws + FLAG_OFF + (((G * 37u) >> 8) * NH + g4 * 8u + (lane & 7u)) * FLAG_STRIDE;
  unsigned ready = 0;
  auto need = [&](unsigned m) {
    _Pragma("nounroll") for (unsigned spins = 0; (ready & m) != m && spins < SPIN_CAP; ++spins) { ready = (unsigned)__ballot(ld_agent(fp) == tag); if ((ready & m) != m) __builtin_amdgcn_s_sleep(PSLEEP); }
    asm volatile("" ::: "memory"); };
  __builtin_amdgcn_s_sleep(PDELAY);
  need(1u);
  ST(24);
  const lds_cptr wb = (lds_cptr)lds + 1024u * hh + 64u * q4 + trcol;
  f32x16 acc[2] = {};
  { unsigned hd = 0, so_c = soO + hd * 8192u; lds_cptr wp = wb + ((SLOTW >> (4u * hd)) & 15u) * 16384u;
    bf16x8 of[8], lf[4][2];
    auto rdW = [&](int s2) { lf[s2 & 3][0] = trd2(wp, (s2 & 7) * 2048, (s2 & 7) * 2048 + 256); lf[s2 & 3][1] = trd2(wp, (s2 & 7) * 2048 + 512, (s2 & 7) * 2048 + 768); };
#pragma unroll
    for (int i = 0; i < 8; ++i) { if (i < WO) bload_sc1(of[i], voffO, sO, so_c + i * 1024u); else of[i] = bf16x8{}; }
    rdW(0); rdW(1);
#pragma nounroll
    for (unsigned p = 0; p < 8; ++p) {
      hd = (hd + 1u) & 7u;
      if (p == 5u) __builtin_amdgcn_s_barrier();
      const unsigned so_n = p < 7u ? soO + hd * 8192u : O_BYTES;
      static_for<8>([&](auto S) { constexpr int s2 = S.value;
        if constexpr (s2 == 8 - WO) need(1u << hd);
        if constexpr (s2 == 6) wp = wb + ((SLOTW >> (4u * hd)) & 15u) * 16384u;
        wait_vm1<WO - 1>(of[s2]);
        wait_lgkm<4>();
        acc[0] = MFMA(lf[s2 & 3][0], of[s2], acc[0]);
        rdW(s2 + 2);
        SBAR();
        acc[1] = MFMA(lf[s2 & 3][1], of[s2], acc[1]);
        SBAR();
        breload_sc1(of[(s2 + WO) & 7], voffO, sO, (s2 + WO < 8 ? so_c : so_n) + ((s2 + WO) & 7) * 1024u);
        SBAR(); });
      so_c = so_n;
    }
    wait_vm<0>();
#pragma unroll
    for (int i = 0; i < 8; ++i) pin(of[i]); }
  ST(28);
  const srd_t sX2 = mk_srd(ws + X_OFF, X_BYTES);
  const unsigned xb = (u * 8u + jq) * 4u;
  if (wid < 6u) {
#pragma unroll
    for (int i = 0; i < 4; ++i) bstore_sc1(pack8(acc[i >> 1], i & 1), voffO, sX2, (xb + g4) * 4096u + i * 1024u);
    wait_vm<0>();
    if (lane == 0) st_agent(ws + FLAG2_OFF + (xb + g4) * FLAG_STRIDE, tag);
    ST(29); ST(30);
  } else {
    __builtin_amdgcn_s_sleep(PDELAY2);
    poll_flags(ws + FLAG2_OFF + (xb + ((g4 + 1u + lane % 3u) & 3u)) * FLAG_STRIDE, tag);
    ST(29);
    bf16x8 xp[12];
#pragma unroll
    for (int i = 0; i < 12; ++i) bload_sc1(xp[i], voffO, sX2, (xb + ((g4 + 1u + (i >> 2)) & 3u)) * 4096u + (i & 3) * 1024u);
    wait_vm<0>();
#pragma unroll
    for (int j = 0; j < 12; ++j) { const int i = j & 3; pin(xp[j]); const u32x4 v = __builtin_bit_cast(u32x4, xp[j]);
#pragma unroll
      for (int e = 0; e < 4; ++e) { acc[i >> 1][8 * (i & 1) + 2 * e] += __uint_as_float(v[e] << 16); acc[i >> 1][8 * (i & 1) + 2 * e + 1] += __uint_as_float(v[e] & 0xFFFF0000u); } }
    ST(30);
    char* op = (char*)out + (size_t)(G * 32u + r) * (DM * 2u) + ct * 128u + hh * 16u;
#pragma unroll
    for (int i = 0; i < 4; ++i) *(u32x4*)(op + i * 32) = pack8(acc[i >> 1], i & 1);
  }
  ST(31); ST_OUT(ws, 0, 32);
}

extern "C" void kernel_launch(void* const* d_in, const int* in_sizes, int n_in, void* d_out, int out_size, void* d_ws, size_t ws_size, hipStream_t stream) {
  (void)in_sizes; (void)n_in; (void)out_size; (void)ws_size;
  (void)hipFuncSetAttribute((const void*)mha_kernel, hipFuncAttributeMaxDynamicSharedMemorySize, LDS_BYTES);
  const unsigned short* x = (const unsigned short*)d_in[0]; const unsigned short* kc = (const unsigned short*)d_in[1]; const unsigned short* vc = (const unsigned short*)d_in[2];
  const unsigned short* wq = (const unsigned short*)d_in[3]; const unsigned short* wo = (const unsigned short*)d_in[4];
  mha_kernel<<<256, 512, LDS_BYTES, stream>>>(x, kc, vc, wq, wo, (char*)d_ws, (unsigned short*)d_out);
}
```

```cpp
#define R2 8
#define PD1 14
#include <hip/hip_runtime.h>
#include <stdint.h>
#include <utility>

typedef short    bf16x8 __attribute__((ext_vector_type(8)));
typedef short    s16x4  __attribute__((ext_vector_type(4)));
typedef float    f32x16 __attribute__((ext_vector_type(16)));
typedef float    f32x2  __attribute__((ext_vector_type(2)));
typedef __bf16   bf16x2 __attribute__((ext_vector_type(2)));
typedef unsigned u32x4  __attribute__((ext_vector_type(4)));
typedef __attribute__((address_space(3))) const char* lds_cptr;
typedef __attribute__((address_space(1))) unsigned long long gu64;
typedef __amdgpu_buffer_rsrc_t srd_t;

#define DI __device__ __forceinline__
#define MFMA(a, b, c) __builtin_amdgcn_mfma_f32_32x32x16_bf16((a), (b), (c), 0, 0, 0)
#define SBAR() __builtin_amdgcn_sched_barrier(0)

constexpr int NB = 8, SQ = 224, DM = 576, NH = 32, SK = 512, DH = 128;
constexpr int LDS_BYTES = 163840;
constexpr float QSCALE = 0.08838834764831845f * 1.4426950408889634f;
constexpr float SUM_THR = 4096.f;

template <int... Is, class F> DI void static_for_impl(std::integer_sequence<int, Is...>, F&& f) { (f(std::integral_constant<int, Is>{}), ...); }
template <int N, class F> DI void static_for(F&& f) { static_for_impl(std::make_integer_sequence<int, N>{}, f); }

DI unsigned rfl(unsigned x) { return __builtin_amdgcn_readfirstlane(x); }
DI unsigned cvtpk(float lo, float hi) { f32x2 v = {lo, hi}; bf16x2 b = __builtin_convertvector(v, bf16x2); return __builtin_bit_cast(unsigned, b); }
DI srd_t mk_srd(const void* p, unsigned bytes) { return __builtin_amdgcn_make_buffer_rsrc((void*)p, (short)0, (int)bytes, 0x00020000); }
DI void dma4(unsigned lds, unsigned v0, unsigned v1, unsigned v2, unsigned v3, srd_t srd, unsigned soff) {
  asm volatile("s_mov_b32 m0, %0\n\ts_nop 0\n\tbuffer_load_dwordx4 %1, %5, %6 offen lds\n\tbuffer_load_dwordx4 %2, %5, %6 offen offset:1024 lds\n\t"
               "buffer_load_dwordx4 %3, %5, %6 offen offset:2048 lds\n\tbuffer_load_dwordx4 %4, %5, %6 offen offset:3072 lds"
               :: "s"(lds), "v"(v0), "v"(v1), "v"(v2), "v"(v3), "s"(srd), "s"(soff) : "memory");
}
DI void dma4_nt(unsigned lds, unsigned v0, unsigned v1, unsigned v2, unsigned v3, srd_t srd, unsigned soff) {
  asm volatile("s_mov_b32 m0, %0\n\ts_nop 0\n\tbuffer_load_dwordx4 %1, %5, %6 offen nt lds\n\tbuffer_load_dwordx4 %2, %5, %6 offen offset:1024 nt lds\n\t"
               "buffer_load_dwordx4 %3, %5, %6 offen offset:2048 nt lds\n\tbuffer_load_dwordx4 %4, %5, %6 offen offset:3072 nt lds"
               :: "s"(lds), "v"(v0), "v"(v1), "v"(v2), "v"(v3), "s"(srd), "s"(soff) : "memory");
}
DI void bstore_sc1(const u32x4& d, unsigned voff, srd_t srd, unsigned soff) { asm volatile("buffer_store_dwordx4 %0, %1, %2, %3 offen sc1\n\ts_nop 1" :: "v"(d), "v"(voff), "s"(srd), "s"(soff) : "memory"); }
DI void bload_sc1(bf16x8& d, unsigned voff, srd_t srd, unsigned soff) { asm volatile("buffer_load_dwordx4 %0, %1, %2, %3 offen sc1" : "=v"(d) : "v"(voff), "s"(srd), "s"(soff) : "memory"); }
DI void breload_sc1(bf16x8& d, unsigned voff, srd_t srd, unsigned soff) { asm volatile("buffer_load_dwordx4 %0, %1, %2, %3 offen sc1" : "+v"(d) : "v"(voff), "s"(srd), "s"(soff) : "memory"); }
DI void pin(bf16x8& a) { asm volatile("" : "+v"(a)); }
DI void pin16(f32x16& a) { asm volatile("" : "+v"(a)); }
DI void pinf(float& a) { asm volatile("" : "+v"(a)); }
DI void pinu(unsigned& a) { asm volatile("" : "+v"(a)); }
template <int N> DI void wait_vm1(bf16x8& a) { asm volatile("s_waitcnt vmcnt(%1)" : "+v"(a) : "n"(N) : "memory"); }
template <int N> DI void wait_vm() { asm volatile("s_waitcnt vmcnt(%0)" :: "n"(N) : "memory"); }
template <int N> DI void wait_lgkm() { __builtin_amdgcn_s_waitcnt(0xC07F | (N << 8)); SBAR(); }
DI void bar_if(unsigned c) { asm volatile("s_cmp_eq_u32 %0, 0\n\ts_cbranch_scc1 1f\n\ts_waitcnt lgkmcnt(0)\n\ts_barrier\n1:" :: "s"(c) : "memory", "scc"); }
DI void bar() { asm volatile("s_waitcnt lgkmcnt(0)\n\ts_barrier" ::: "memory"); }
typedef short v4i16_t __attribute__((ext_vector_type(4)));
DI s16x4 trd(lds_cptr p) { return __builtin_amdgcn_ds_read_tr16_b64_v4i16((__attribute__((address_space(3))) v4i16_t*)p); }
DI bf16x8 trd2(lds_cptr p, int o0, int o1) { s16x4 a = trd(p + o0), b = trd(p + o1); return __builtin_shufflevector(a, b, 0, 1, 2, 3, 4, 5, 6, 7); }
DI bf16x8 rd128(lds_cptr p) { return *(const __attribute__((address_space(3))) bf16x8*)p; }
DI float swap_max(float m) { auto rr = __builtin_amdgcn_permlane32_swap(__float_as_uint(m), __float_as_uint(m), false, false); return fmaxf(__uint_as_float(rr[0]), __uint_as_float(rr[1])); }
DI float swap_add(float m) { auto rr = __builtin_amdgcn_permlane32_swap(__float_as_uint(m), __float_as_uint(m), false, false); return __uint_as_float(rr[0]) + __uint_as_float(rr[1]); }
DI float max16(const f32x16& s) { float a = fmaxf(fmaxf(s[0], s[1]), s[2]), b = fmaxf(fmaxf(s[3], s[4]), s[5]);
  a = fmaxf(fmaxf(a, s[6]), s[7]); b = fmaxf(fmaxf(b, s[8]), s[9]); a = fmaxf(fmaxf(a, s[10]), s[11]); b = fmaxf(fmaxf(b, s[12]), s[13]); a = fmaxf(fmaxf(a, s[14]), s[15]); return fmaxf(a, b); }
DI u32x4 pack8(const f32x16& a, int s, float f) { return u32x4{cvtpk(a[8 * s] * f, a[8 * s + 1] * f), cvtpk(a[8 * s + 2] * f, a[8 * s + 3] * f), cvtpk(a[8 * s + 4] * f, a[8 * s + 5] * f), cvtpk(a[8 * s + 6] * f, a[8 * s + 7] * f)}; }
DI u32x4 pack8(const f32x16& a, int s) { return u32x4{cvtpk(a[8 * s], a[8 * s + 1]), cvtpk(a[8 * s + 2], a[8 * s + 3]), cvtpk(a[8 * s + 4], a[8 * s + 5]), cvtpk(a[8 * s + 6], a[8 * s + 7])}; }

DI float expo(const f32x16& s, u32x4 (&p)[2]) {
  float e[16]; float s0 = 0.f, s1 = 0.f;
#pragma unroll
  for (int i = 0; i < 16; ++i) e[i] = __builtin_amdgcn_exp2f(s[i]);
#pragma unroll
  for (int i = 0; i < 16; i += 2) { s0 += e[i]; s1 += e[i + 1]; }
#pragma unroll
  for (int i = 0; i < 8; ++i) p[i >> 2][i & 3] = cvtpk(e[2 * i], e[2 * i + 1]);
  return s0 + s1;
}

#define ST_DECL
#define ST(k)
#define ST_OUT(ws, lo, hi)

constexpr unsigned SLOTK = 0x04204209u;
constexpr unsigned SLOTV = 0x31531531u;
constexpr unsigned SLOTW = 0x05429876u;
constexpr unsigned STG = 45056u, STG_W = 28672u;
constexpr unsigned O_BYTES = NB * SQ * NH * DH * 2;
constexpr unsigned X_OFF = O_BYTES, X_BYTES = 63u * 8u * 4u * 4096u;
constexpr unsigned FLAG_OFF = X_OFF + X_BYTES, FLAG_STRIDE = 128, FLAG2_OFF = FLAG_OFF + 256 * FLAG_STRIDE, EPOCH_OFF = FLAG2_OFF + 63 * 32 * FLAG_STRIDE;
#define XOFF 0
#define KA 0u
#define QG 1
#define PACE 0
#define PACEA 0
#define K0S 1
#define NOWUP 0
#define WUPC 3u
#define RAMP 0
#define HOLD 0
#define SRAMP 0
#define RAMPW 5
#define PRE 0u
constexpr unsigned SPIN_CAP = 1u << 20;
constexpr int RA = 7, WO = 6, A0BAR = 6, PSLEEP = 4, PDELAY = PD1, PDELAY2 = 25;
constexpr unsigned SPARE = 3u * STG;
constexpr unsigned CODE_BYTES = 19200u;
constexpr int lds_ops(int a, int b, bool last) { int n = 0; for (int i = a; i < b; ++i) n += i >= 16 ? (last ? 0 : 1) : (i >= 8 ? 2 : 1); return n; }
constexpr int pv_order(int i) { return i < 4 ? i : i < 8 ? i + 4 : i < 12 ? i - 4 : i; }
DI unsigned long long mix64(unsigned long long z) { z += 0x9E3779B97F4A7C15ull; z = (z ^ (z >> 30)) * 0xBF58476D1CE4E5B9ull; z = (z ^ (z >> 27)) * 0x94D049BB133111EBull; return z ^ (z >> 31); }
DI unsigned long long rfl64(unsigned long long v) { return ((unsigned long long)rfl((unsigned)(v >> 32)) << 32) | rfl((unsigned)v); }
DI unsigned long long ld_agent(const char* p) { return __hip_atomic_load((gu64*)p, __ATOMIC_RELAXED, __HIP_MEMORY_SCOPE_AGENT); }
DI void st_agent(char* p, unsigned long long v) { __hip_atomic_store((gu64*)p, v, __ATOMIC_RELAXED, __HIP_MEMORY_SCOPE_AGENT); }
DI void poll_flags(const char* p, unsigned long long tag) {
  _Pragma("nounroll") for (unsigned spins = 0; spins < SPIN_CAP; ++spins) { if (__all(ld_agent(p) == tag)) break; __builtin_amdgcn_s_sleep(PSLEEP); }
  asm volatile("" ::: "memory");
}

__global__ __launch_bounds__(512, 2) void mha_kernel(const unsigned short* __restrict__ x, const unsigned short* __restrict__ kc, const unsigned short* __restrict__ vc,
                                                     const unsigned short* __restrict__ wq, const unsigned short* __restrict__ wo, char* ws, unsigned short* __restrict__ out) {
  extern __shared__ __attribute__((aligned(16))) char lds[];
  unsigned long long pc0; asm volatile("s_getpc_b64 %0" : "=s"(pc0));
  const unsigned tid = threadIdx.x, lane = tid & 63u, r = lane & 31u, hh = lane >> 5;
  const unsigned wid = rfl(tid >> 6);
  ST_DECL ST(0);
  const unsigned bid = blockIdx.x, xcd = bid & 7u, ix = bid >> 3;
  const unsigned b = (xcd & 1u) * 4u + (ix & 3u), h = (xcd >> 1) * 8u + (ix >> 2);
  const unsigned lds0 = (unsigned)(uintptr_t)lds;
  if (SRAMP) { for (unsigned i = 0; i < (ix & 7u); ++i) __builtin_amdgcn_s_sleep(SRAMP); }
  {
    const char* cp = (const char*)(pc0 & ~255ull) + min((ix * 8u + wid) * 128u, CODE_BYTES);
    asm volatile("s_mov_b32 m0, %0\n\ts_nop 0\n\tglobal_load_lds_dword %1, %2" :: "s"(lds0 + SPARE), "v"(0u), "s"(cp) : "memory"); }
  const srd_t sW = mk_srd((const char*)wq - 1024, DM * NH * DH * 2 + 1024);
  const srd_t sX = mk_srd(x + (size_t)b * SQ * DM, SQ * DM * 2);
  const unsigned prow = (lane >> 2) & 7u, pch = 4u * hh + (lane & 3u);
  const unsigned pchx0 = 4u * hh + ((lane & 3u) ^ ((lane >> 4) & 1u)), pchx1 = pchx0 ^ 2u;
  const unsigned vW = prow * 8192u + pch * 16u, vX0 = prow * (DM * 2u) + pchx0 * 16u, vX1 = prow * (DM * 2u) + pchx1 * 16u;
  const unsigned ck0 = (QG == 3 ? 3u * (((ix & 3u) + (ix >> 2)) % 3u) : QG == 2 ? 4u * (((ix & 3u) + (ix >> 2)) & 1u) : 0u) + (XOFF ? (0x10022110u >> (4u * xcd)) & 3u : 0u), ck1 = ck0 == 8u ? 0u : ck0 + 1u;
  unsigned long long ep; asm volatile("global_load_dwordx2 %0, %1, %2 sc1" : "=v"(ep) : "v"(0u), "s"(ws + EPOCH_OFF) : "memory");
  dma4(lds0 + (wid < 7u ? wid * 4096u : SPARE), vX0, vX1 + 8192u, vX0 + 16384u, vX1 + 24576u, sX, ck0 * 128u + min(wid, 6u) * 32u * DM * 2u);
  dma4(lds0 + (wid >> 2) * STG + STG_W + (wid & 3u) * 4096u, vW + 1024u, vW + 128u, vW + 65536u - 1024u, vW + 65664u - 2048u, sW, (wid < 4u ? ck0 : ck1) * 64u * 8192u + h * 256u + (wid & 3u) * 131072u);
  const srd_t sK = mk_srd((const char*)(kc + (size_t)(b * NH + h) * SK * DH) - 1024, SK * DH * 2 + 1024);
  const srd_t sV = mk_srd((const char*)(vc + (size_t)(b * NH + h) * SK * DH) - 1024, SK * DH * 2 + 1024);
  const unsigned vK0 = prow * 256u + pchx0 * 16u, vK1 = prow * 256u + pchx1 * 16u;
  const unsigned q4 = (lane & 15u) >> 2, p4 = lane & 3u, blk = (lane >> 4) & 1u;
  const unsigned trcol = 32u * blk + 16u * (p4 & 1u) + 8u * (p4 >> 1);
  const srd_t sO = mk_srd(ws, O_BYTES);
  unsigned long long tag;
  const unsigned g4 = xcd >> 1, u0 = (xcd & 1u) * 32u + ix;
  const bool has_tile = u0 < 63u;
  const unsigned u = has_tile ? u0 : 62u, rt = u / 9u, ct = u - rt * 9u;

  if (wid == 7u) {
    auto fillQ = [&](unsigned c, unsigned stage) {
      const unsigned lw = lds0 + stage + STG_W, sw = c * 64u * 8192u + h * 256u, lx = lds0 + stage, sx = c * 128u;
#pragma unroll
      for (int i = 0; i < 4; ++i) dma4(lw + i * 4096u, vW + 1024u, vW + 128u, vW + 65536u - 1024u, vW + 65664u - 2048u, sW, sw + i * 131072u);
#pragma unroll
      for (int i = 0; i < 7; ++i) dma4(lx + i * 4096u, vX0, vX1 + 8192u, vX0 + 16384u, vX1 + 24576u, sX, sx + i * 32u * DM * 2u); };
    bar(); ST(2);
    { unsigned stage = 2 * STG, ck = ck1 == 8u ? 0u : ck1 + 1u;
#pragma nounroll
      for (unsigned c = 2; c < 9; ++c) { fillQ(ck, stage); ck = ck == 8u ? 0u : ck + 1u; stage = stage == 2 * STG ? 0u : stage + STG;
        wait_vm<44>(); bar(); ST(1 + c); } }
    const unsigned vV = prow * 256u + pch * 16u;
    const srd_t sWo = mk_srd(wo, NH * DH * DM * 2);
    const unsigned vWo = prow * (DM * 2u) + pch * 16u;
    auto pace = [&](unsigned n) { if (n == 0u) { if (PACEA) __builtin_amdgcn_s_sleep(PACEA); } else if (n < 6u) { if (PACE) __builtin_amdgcn_s_sleep(PACE); } };
    { unsigned j = 1, jw = 0;
#pragma nounroll
      for (unsigned n = 0; n < 10; ++n) {
        if (n == 9u) { wait_vm<0>(); bar(); }
        const unsigned je = ((unsigned)(0xFFFECA8642ull >> (4u * n)) & 15u) + 1u;
#pragma nounroll
        for (; j < je; ++j) {
          if ((j & 1u) && j != 15u) { const unsigned t = (j + 1u) >> 1, ld = lds0 + ((SLOTK >> (4u * t)) & 15u) * 16384u;
#pragma unroll
            for (int i = 0; i < 4; ++i) dma4_nt(ld + i * 4096u, vK0 + 1024u, vK0 + 128u, vK1 + 1024u, vK1 + 128u, sK, t * 16384u + i * 4096u), pace(n);
          } else { const unsigned t = (j - 1u) >> 1, ld = lds0 + ((SLOTV >> (4u * t)) & 15u) * 16384u;
#pragma unroll
            for (int i = 0; i < 4; ++i) dma4_nt(ld + i * 4096u, vV + 1024u, vV + 128u, vV + 1024u, vV + 128u, sV, t * 16384u + i * 4096u), pace(n); } }
        const unsigned we = (unsigned)(0x8741000000ull >> (4u * n)) & 15u;
#pragma nounroll
        for (; jw < we; ++jw) { const unsigned ld = lds0 + ((SLOTW >> (4u * jw)) & 15u) * 16384u, so = (g4 * 8u + jw) * 128u * (DM * 2u) + ct * 128u;
#pragma unroll
          for (int k = 0; k < 4; ++k) dma4(ld + k * 4096u, vWo, vWo + 8192u, vWo + 16384u, vWo + 24576u, sWo, so + k * 32u * (DM * 2u)); }
        if (n == 9u) break;
        if (n < 6u) wait_vm<32>(); else if (n == 6u) wait_vm<48>(); else wait_vm<62>();
        bar(); ST(n == 0u ? 10u : 11u + n); } }
    asm volatile("" : "+v"(ep)); tag = mix64(rfl64(ep));
    ST(20); ST(21);
  } else {
  const unsigned rsw0 = 64u * (r & 7u) + 16u * (hh ^ ((r >> 2) & 3u)), rsw1 = rsw0 ^ 32u;
  f32x16 qa[4] = {}, o[4] = {};
  asm volatile("" : "+v"(qa[0]), "+v"(qa[1]), "+v"(qa[2]), "+v"(qa[3]), "+v"(o[0]), "+v"(o[1]), "+v"(o[2]), "+v"(o[3]));
  if (PRE) {
    unsigned z = (tid + 1u) * 2654435761u; u32x4 ja = {z & 0xBFFFBFFFu, (z * 3u) & 0xBFFFBFFFu, (z * 5u) & 0xBFFFBFFFu, (z * 7u) & 0xBFFFBFFFu}, jb = {(z * 9u) & 0xBFFFBFFFu, (z * 11u) & 0xBFFFBFFFu, (z * 13u) & 0xBFFFBFFFu, (z * 15u) & 0xBFFFBFFFu};
    f32x16 jc = {};
#pragma nounroll
    for (unsigned i = 0; i < PRE; ++i) {
      jc = MFMA(__builtin_bit_cast(bf16x8, ja), __builtin_bit_cast(bf16x8, jb), jc); jc = MFMA(__builtin_bit_cast(bf16x8, jb), __builtin_bit_cast(bf16x8, ja), jc); pin16(jc);
      ja[0] = ja[1]; ja[1] = jb[2]; jb[2] = jb[3] ^ 0x01230456u; jb[3] = ja[0];
      if (i * 3u < PRE) __builtin_amdgcn_s_sleep(4); else if (i * 3u < 2u * PRE) __builtin_amdgcn_s_sleep(2); } }
  wait_lgkm<0>();
  wait_vm<4>(); if (wid < 4u) wait_vm<0>();
  ST(1);
  dma4(lds0 + STG + wid * 4096u, vX0, vX1 + 8192u, vX0 + 16384u, vX1 + 24576u, sX, ck1 * 128u + wid * 32u * DM * 2u);
  bar();
  asm volatile("" : "+v"(ep)); ep = rfl64(ep);
  ST(2);
  const lds_cptr kb0 = (lds_cptr)lds + 2048u * (r >> 3) + rsw0, kb1 = (lds_cptr)lds + 2048u * (r >> 3) + rsw1;
  const lds_cptr vb = (lds_cptr)lds + 64u * (4u * hh + q4) + trcol;
  bf16x8 qf[8], pre[RA];
  f32x16 negm, S0, S1; u32x4 P0[2], P1[2]; float l = 0.f;
  auto rdK = [&](lds_cptr k0, lds_cptr k1, int kb, int i) { return rd128(((i & 1) ? k1 : k0) + 8192 * kb + 512 * (i >> 1)); };
  auto rdV = [&](lds_cptr v, int kb, int j) { return trd2(v, 8192 * kb + 4096 * (j >> 2) + 512 * (j & 3), 8192 * kb + 4096 * (j >> 2) + 2048 + 512 * (j & 3)); };
  auto sm_fix = [&](float ps, f32x16& Sc, f32x16& Sn, u32x4 (&P)[2]) {
    if (__builtin_expect(__any(!(ps <= SUM_THR)), 0)) {
      const float dl = fmaxf(swap_max(max16(Sc)), 0.f), f = __builtin_amdgcn_exp2f(-dl);
#pragma unroll
      for (int i = 0; i < 16; ++i) { Sc[i] -= dl; Sn[i] -= dl; negm[i] -= dl; }
#pragma unroll
      for (int db = 0; db < 4; ++db)
#pragma unroll
        for (int i = 0; i < 16; ++i) o[db][i] *= f;
      l *= f; ps = expo(Sc, P); }
    l += ps; };
  { lds_cptr wp = (lds_cptr)lds + STG_W + 2048u * hh + 64u * q4 + trcol;
    lds_cptr xp0 = (lds_cptr)lds + wid * 4096u + 1024u * (r >> 3) + rsw0, xp1 = (lds_cptr)lds + wid * 4096u + 1024u * (r >> 3) + rsw1;
    bf16x8 xf[4], af[5];
    auto rdA = [&](int i) { return trd2(wp, 4096 * (i >> 2) + 512 * (i & 3), 4096 * (i >> 2) + 512 * (i & 3) + 256); };
    xf[0] = rd128(xp0); af[0] = rdA(0); af[1] = rdA(1); af[2] = rdA(2); af[3] = rdA(3);
    unsigned stage = 0;
#pragma nounroll
    for (unsigned c = 0; c < 8; ++c) {
      if (!NOWUP && __builtin_expect(c == WUPC && wid == 3u, 0)) {
#pragma unroll
        for (int i = 0; i < (K0S ? 0 : 4); ++i) dma4_nt(lds0 + 9u * 16384u + i * 4096u, vK0 + 1024u, vK0 + 128u, vK1 + 1024u, vK1 + 128u, sK, i * 4096u);
        unsigned ln = lane; asm volatile("" : "+v"(ln));
        const unsigned k = ln & 7u, sel = ln >> 3;
        const char* wup = sel == 0u ? (const char*)vc + (size_t)(b * NH + h) * (SK * DH * 2) + k * 16384u
                        : sel == 1u ? (const char*)wo + (size_t)(g4 * 1024u + k * 128u) * (DM * 2u) + ct * 128u
                        : sel == 2u ? (const char*)out + (size_t)((8u * rt + k) * 32u) * (DM * 2u)
                        : ws + (sel == 3u ? ((b * 7u + k) * NH + h) * 8192u : sel == 4u ? ((8u * rt + k) * NH + g4 * 8u) * 8192u : sel == 5u ? X_OFF + u * 131072u + k * 16384u : FLAG_OFF + k * 32768u + 64u);
        asm volatile("s_mov_b32 m0, %0\n\ts_nop 0\n\tglobal_load_lds_dword %1, off sc1" :: "s"(lds0 + SPARE), "v"(wup) : "memory"); }
      if (HOLD && c == 0u) __builtin_amdgcn_s_sleep(HOLD);
      if (RAMPW && c == 0u) { for (unsigned i = 0; i < ((wid + ix) & 3u); ++i) __builtin_amdgcn_s_sleep(RAMPW); }
      if (RAMP && c == 0u) { for (unsigned i = 0; i < ((ix + wid) & 7u); ++i) __builtin_amdgcn_s_sleep(RAMP); }
      if (K0S && __builtin_expect(c >= 3u && c < 7u && wid == 3u, 0)) dma4_nt(lds0 + 9u * 16384u + (c - 3u) * 4096u, vK0 + 1024u, vK0 + 128u, vK1 + 1024u, vK1 + 128u, sK, (c - 3u) * 4096u);
      if (KA && __builtin_expect(c >= 4u && c < 7u && wid == 3u, 0))
        asm volatile("s_mov_b32 m0, %0\n\ts_nop 0\n\tbuffer_load_dword %1, %2, %3 offen nt lds" :: "s"(lds0 + SPARE), "v"((lane & (KA - 1u)) * 128u), "s"(sK), "s"(17408u + (c - 4u) * KA * 128u) : "memory");
      const unsigned nstage = stage == 2 * STG ? 0u : stage + STG; const int dlt = (int)nstage - (int)stage; stage = nstage;
      SBAR();
      static_for<12>([&](auto I) { constexpr int i = I.value;
        qa[i & 3] = MFMA(af[i % 5], xf[i >> 2], qa[i & 3]);
        af[(i + 4) % 5] = rdA(i + 4);
        if constexpr (i == 0) xf[1] = rd128(xp1); if constexpr (i == 3) xf[2] = rd128(xp0 + 512); if constexpr (i == 7) xf[3] = rd128(xp1 + 512);
        SBAR(); });
      qa[0] = MFMA(af[12 % 5], xf[3], qa[0]);
      wp += dlt; xp0 += dlt; xp1 += dlt; asm volatile("" : "+v"(wp), "+v"(xp0), "+v"(xp1));
      SBAR();
      asm volatile("s_bitcmp1_b32 %0, %1\n\ts_cbranch_scc1 1f\n\ts_waitcnt vmcnt(0)\n1:\n\ts_waitcnt lgkmcnt(0)\n\ts_barrier" :: "s"(0x78u), "s"(c) : "memory", "scc");
      const bf16x8 xl = xf[3], a13 = af[13 % 5], a14 = af[14 % 5], a15 = af[15 % 5];
      xf[0] = rd128(xp0); af[0] = rdA(0); af[1] = rdA(1);
      SBAR();
      qa[1] = MFMA(a13, xl, qa[1]); SBAR();
      af[2] = rdA(2);
      qa[2] = MFMA(a14, xl, qa[2]); SBAR();
      af[3] = rdA(3);
      qa[3] = MFMA(a15, xl, qa[3]); SBAR();
      ST(3 + c);
    }
    lds_cptr k0 = kb0 + 9 * 16384, k1 = kb1 + 9 * 16384; asm volatile("" : "+v"(k0), "+v"(k1));
    bf16x8 ag[16], f[16]; u32x4 qw[8]; float qs = QSCALE; asm volatile("" : "+s"(qs));
#pragma unroll
    for (int i = 0; i < 4; ++i) ag[i] = af[i];
    auto cvq = [&](int j) { float lo = qa[j >> 3][2 * (j & 7)] * qs; pinf(lo); float hi = qa[j >> 3][2 * (j & 7) + 1] * qs; unsigned w = cvtpk(lo, hi); pinu(w); qw[j >> 2][j & 3] = w; };
    SBAR();
    static_for<24>([&](auto G) { constexpr int g = G.value, n = g + 4;
      if constexpr (g < 4) qa[g] = MFMA(ag[g], xf[0], qa[g]);
      else if constexpr (g < 16) qa[(g - 4) / 3] = MFMA(ag[g], xf[1 + (g - 4) % 3], qa[(g - 4) / 3]);
      else { qf[g - 16] = __builtin_bit_cast(bf16x8, qw[g - 16]);
        if constexpr (g == 16) { const f32x16 z = {}; S0 = MFMA(f[0], qf[0], z); } else S0 = MFMA(f[g - 16], qf[g - 16], S0); pin16(S0); }
      if constexpr (g == 0) { xf[1] = rd128(xp1); xf[2] = rd128(xp0 + 512); } if constexpr (g == 1) xf[3] = rd128(xp1 + 512);
      if constexpr (n < 16) ag[n] = rdA(4 * (1 + (n - 4) % 3) + (n - 4) / 3); else f[n - 16] = rdK(k0, k1, (n - 16) >> 3, (n - 16) & 7);
      if constexpr (g >= 9 && g < 21) { constexpr int j0 = (g - 9) * 8 / 3, j1 = (g - 8) * 8 / 3; static_for<j1 - j0>([&](auto J) { cvq(j0 + J.value); }); }
      SBAR(); });
    ST(11);
#pragma unroll
    for (int i = 12; i < 16; ++i) f[i] = rdK(k0, k1, 1, i & 7);
    const float m = swap_max(max16(S0));
#pragma unroll
    for (int i = 0; i < 16; ++i) negm[i] = -m;
    SBAR();
    float e[16]; float s0 = 0.f, s1 = 0.f;
    static_for<9>([&](auto G) { constexpr int g = G.value;
      if constexpr (g == 0) { if (wid >= 4u) bar(); }
      if constexpr (g == A0BAR) { if (wid < 4u) bar();
        constexpr unsigned ko = ((SLOTK >> 4) & 15u) * 16384u;
#pragma unroll
        for (int i = 0; i < RA; ++i) pre[i] = rdK(kb0 + ko, kb1 + ko, 0, i);
        SBAR(); }
      if constexpr (g == 0) S1 = MFMA(f[8], qf[0], negm); else if constexpr (g < 8) S1 = MFMA(f[8 + g], qf[g], S1);
      if constexpr (g < 8) pin16(S1);
      if constexpr (g < 8) { e[2 * g] = __builtin_amdgcn_exp2f(S0[2 * g] - m); e[2 * g + 1] = __builtin_amdgcn_exp2f(S0[2 * g + 1] - m); pinf(e[2 * g]); pinf(e[2 * g + 1]); }
      if constexpr (g == 1) { s0 = e[0]; s1 = e[1]; } else if constexpr (g > 1) { s0 += e[2 * g - 2]; pinf(s0); s1 += e[2 * g - 1]; pinf(s1); }
      if constexpr (g >= 1) { unsigned w = cvtpk(e[2 * g - 2], e[2 * g - 1]); pinu(w); P0[(g - 1) >> 2][(g - 1) & 3] = w; }
      SBAR(); });
    l = s0 + s1; }
  ST(12);
  auto unit = [&](auto KB, auto LAST, lds_cptr k0, lds_cptr k1, lds_cptr v, lds_cptr nk0, lds_cptr nk1, f32x16& Sn, f32x16& Sc, const u32x4 (&Pp)[2], u32x4 (&Pc)[2]) {
    constexpr int kb = KB.value; constexpr bool last = LAST.value;
    float e[16]; float s0 = 0.f, s1 = 0.f; bf16x8 f[16 + RA];
#pragma unroll
    for (int i = 0; i < RA; ++i) f[i] = pre[i];
    static_for<16>([&](auto G) { constexpr int g = G.value; constexpr int n = g + RA; constexpr int gb = 12;
      if constexpr (last && g == gb) { bar(); wait_lgkm<0>(); }
      else if constexpr ((g & 3) == 0) wait_lgkm<lds_ops(g + 4, g + RA, last)>();
      if constexpr (g == 0) Sn = MFMA(f[0], qf[0], negm); else if constexpr (g < 8) Sn = MFMA(f[g], qf[g], Sn);
      else o[g & 3] = MFMA(f[g], __builtin_bit_cast(bf16x8, Pp[(g >> 2) & 1]), o[g & 3]);
      if constexpr (n < 8) f[n] = rdK(k0, k1, kb, n); else if constexpr (n < 16) f[n] = rdV(v, kb, n - 8);
      else if constexpr (!last) f[n] = rdK(k0, k1, kb + 1, n - 16);
      else if constexpr (g >= gb) { constexpr int per = (RA + 15 - gb) / (16 - gb); static_for<per>([&](auto J) { constexpr int i = per * (g - gb) + J.value; if constexpr (i < RA) f[16 + i] = rdK(nk0, nk1, 0, i); }); }
      if constexpr (g == 0) { e[0] = __builtin_amdgcn_exp2f(Sc[0]); pinf(e[0]); }
      if constexpr (g < 15) { e[g + 1] = __builtin_amdgcn_exp2f(Sc[g + 1]); pinf(e[g + 1]); }
      if constexpr (g == 1) { s0 = e[0]; s1 = e[1]; } else if constexpr (g > 1) { if constexpr (g & 1) { s1 += e[g]; pinf(s1); } else { s0 += e[g]; pinf(s0); } }
      if constexpr (g & 1) { unsigned w = cvtpk(e[g - 1], e[g]); pinu(w); Pc[g >> 3][(g >> 1) & 3] = w; }
      SBAR(); });
#pragma unroll
    for (int i = 0; i < RA; ++i) pre[i] = f[16 + i];
    sm_fix(s0 + s1, Sc, Sn, Pc); };
#pragma nounroll
  for (unsigned t = 1; t < 7; ++t) {
    const unsigned ko = ((SLOTK >> (4u * t)) & 15u) * 16384u, vo = ((SLOTV >> (4u * (t - 1u))) & 15u) * 16384u, nko = ((SLOTK >> (4u * ((t + 1u) & 7u))) & 15u) * 16384u;
    SBAR();
    unit(std::integral_constant<int, 0>{}, std::false_type{}, kb0 + ko, kb1 + ko, vb + vo, kb0, kb1, S0, S1, P0, P1);
    unit(std::integral_constant<int, 1>{}, std::true_type{}, kb0 + ko, kb1 + ko, vb + vo, kb0 + nko, kb1 + nko, S1, S0, P1, P0);
    if (t == 1u || t == 6u) ST(12 + t);
  }
  const unsigned me = (b * 7u + wid) * NH + h;
  {
    constexpr unsigned ko = ((SLOTK >> 28) & 15u) * 16384u;
    const lds_cptr k0 = kb0 + ko, k1 = kb1 + ko, v6 = vb + ((SLOTV >> 24) & 15u) * 16384u, v7 = vb + ((SLOTV >> 28) & 15u) * 16384u;
    u32x4 P2[2], P3[2];
    auto scaleP = [&](u32x4 (&P)[2], float f) {
#pragma unroll
      for (int i = 0; i < 8; ++i) { const unsigned w = P[i >> 2][i & 3]; P[i >> 2][i & 3] = cvtpk(__uint_as_float(w << 16) * f, __uint_as_float(w & 0xFFFF0000u) * f); } };
    auto fix = [&](auto NP, float ps, f32x16& Sc, f32x16& Sn, u32x4 (&P)[2]) {
      if (__builtin_expect(__any(!(ps <= SUM_THR)), 0)) {
        const float f = __builtin_amdgcn_exp2f(-fmaxf(swap_max(max16(Sc)), 0.f));
        scaleP(P0, f); if constexpr (NP.value > 1) scaleP(P1, f); if constexpr (NP.value > 2) scaleP(P2, f); }
      sm_fix(ps, Sc, Sn, P); };
    auto rdVt = [&](int i) { const int j = pv_order(i), db = j >> 3, un = (j >> 1) & 3, ks = j & 1; return rdV(un < 2 ? v6 : v7, un & 1, 4 * ks + db); };
    auto pvt = [&](auto I, const bf16x8& vf) { constexpr int j = pv_order(I.value), un = (j >> 1) & 3;
      const u32x4 pb = un == 0 ? P0[j & 1] : un == 1 ? P1[j & 1] : un == 2 ? P2[j & 1] : P3[j & 1];
      o[j >> 3] = MFMA(vf, __builtin_bit_cast(bf16x8, pb), o[j >> 3]); pin16(o[j >> 3]); };
    bf16x8 f[16], w[32];
#pragma unroll
    for (int i = 0; i < RA; ++i) f[i] = pre[i];
    auto qsm = [&](auto H, f32x16& Sn, const f32x16& Sc, u32x4 (&Pc)[2]) { constexpr int hf = H.value;
      float e[16]; float s0 = 0.f, s1 = 0.f;
      static_for<9>([&](auto G) { constexpr int g = G.value, n = 8 * hf + g + RA;
        if constexpr (hf < 2 && g < 8) { if constexpr (g == 0) Sn = MFMA(f[8 * hf], qf[0], negm); else Sn = MFMA(f[8 * hf + g], qf[g], Sn);
          pin16(Sn);
          if constexpr (n < 16) f[n] = rdK(k0, k1, n >> 3, n & 7); }
        if constexpr (hf == 1 && g >= 2 && g < 8) w[g - 2] = rdVt(g - 2);
        if constexpr (hf == 2 && g < 8) { pvt(G, w[g]); w[g + 6] = rdVt(g + 6); }
        if constexpr (g < 8) { e[2 * g] = __builtin_amdgcn_exp2f(Sc[2 * g]); e[2 * g + 1] = __builtin_amdgcn_exp2f(Sc[2 * g + 1]); pinf(e[2 * g]); pinf(e[2 * g + 1]); }
        if constexpr (g == 1) { s0 = e[0]; s1 = e[1]; } else if constexpr (g > 1) { s0 += e[2 * g - 2]; pinf(s0); s1 += e[2 * g - 1]; pinf(s1); }
        if constexpr (g >= 1) { unsigned pw = cvtpk(e[2 * g - 2], e[2 * g - 1]); pinu(pw); Pc[(g - 1) >> 2][(g - 1) & 3] = pw; }
        SBAR(); });
      return s0 + s1; };
    if (wid < 4u) __builtin_amdgcn_s_setprio(1);
    SBAR();
    fix(std::integral_constant<int, 1>{}, qsm(std::integral_constant<int, 0>{}, S0, S1, P1), S1, S0, P1);
    bar_if(wid >> 2);
    fix(std::integral_constant<int, 2>{}, qsm(std::integral_constant<int, 1>{}, S1, S0, P2), S0, S1, P2);
    bar_if((wid >> 2) ^ 1u);
    ST(19);
    fix(std::integral_constant<int, 3>{}, qsm(std::integral_constant<int, 2>{}, S0, S1, P3), S1, S0, P3);
    const float inv = __builtin_amdgcn_rcpf(swap_add(l));
    auto put = [&](int k8) { bstore_sc1(pack8(o[k8 >> 1], k8 & 1, inv), lane * 16u, sO, me * 8192u + k8 * 1024u); };
    static_for<24>([&](auto G) { constexpr int g = G.value + 8;
      pvt(std::integral_constant<int, g>{}, w[g]);
      if constexpr (g + 6 < 32) w[g + 6] = rdVt(g + 6);
      if constexpr (g == 14) put(0); if constexpr (g == 16) put(1); if constexpr (g == 19) put(2); if constexpr (g == 22) put(3); if constexpr (g == 26) put(4); if constexpr (g == 29) put(5);
      SBAR(); });
    ST(20);
    put(6); put(7); }
  ST(21);
  tag = mix64(ep);
  __builtin_amdgcn_s_setprio(0);
  }
  if (wid < 7u) { wait_vm<0>();
  ST(22);
  bar(); }
  if (tid == 0) st_agent(ws + FLAG_OFF + (b * NH + h) * FLAG_STRIDE, tag);
  ST(23);
  if (!has_tile) {
    if (bid == 249u && wid == 0u) {
#pragma nounroll
      for (unsigned i = 0; i < 4; ++i) poll_flags(ws + FLAG_OFF + (i * 64u + lane) * FLAG_STRIDE, tag);
      if (lane == 0) st_agent(ws + EPOCH_OFF, tag); }
    ST(24); ST(25); ST(26); ST(27); ST(28); ST(29); ST(30); ST(31); ST_OUT(ws, 0, 32); return; }
  const unsigned jq = (2u * g4 + 2u + wid) & 7u, G = 8u * rt + jq;
  const unsigned voffO = lane * 16u, soO = (G * NH + g4 * 8u) * 8192u;
  const char* fp = ws + FLAG_OFF + (((G * 37u) >> 8) * NH + g4 * 8u + (lane & 7u)) * FLAG_STRIDE;
  unsigned ready = 0;
  auto need = [&](unsigned m) {
    _Pragma("nounroll") for (unsigned spins = 0; (ready & m) != m && spins < SPIN_CAP; ++spins) { ready = (unsigned)__ballot(ld_agent(fp) == tag); if ((ready & m) != m) __builtin_amdgcn_s_sleep(PSLEEP); }
    asm volatile("" ::: "memory"); };
  __builtin_amdgcn_s_sleep(PDELAY);
  if (R2) { for (unsigned i = 0; i < ((wid + ix) & 3u); ++i) __builtin_amdgcn_s_sleep(R2); }
  need(1u);
  ST(24);
  const lds_cptr wb = (lds_cptr)lds + 1024u * hh + 64u * q4 + trcol;
  f32x16 acc[2] = {};
  { unsigned hd = 0, so_c = soO + hd * 8192u; lds_cptr wp = wb + ((SLOTW >> (4u * hd)) & 15u) * 16384u;
    bf16x8 of[8], lf[4][2];
    auto rdW = [&](int s2) { lf[s2 & 3][0] = trd2(wp, (s2 & 7) * 2048, (s2 & 7) * 2048 + 256); lf[s2 & 3][1] = trd2(wp, (s2 & 7) * 2048 + 512, (s2 & 7) * 2048 + 768); };
#pragma unroll
    for (int i = 0; i < 8; ++i) { if (i < WO) bload_sc1(of[i], voffO, sO, so_c + i * 1024u); else of[i] = bf16x8{}; }
    rdW(0); rdW(1);
#pragma nounroll
    for (unsigned p = 0; p < 8; ++p) {
      hd = (hd + 1u) & 7u;
      if (p == 5u) __builtin_amdgcn_s_barrier();
      const unsigned so_n = p < 7u ? soO + hd * 8192u : O_BYTES;
      static_for<8>([&](auto S) { constexpr int s2 = S.value;
        if constexpr (s2 == 8 - WO) need(1u << hd);
        if constexpr (s2 == 6) wp = wb + ((SLOTW >> (4u * hd)) & 15u) * 16384u;
        wait_vm1<WO - 1>(of[s2]);
        wait_lgkm<4>();
        acc[0] = MFMA(lf[s2 & 3][0], of[s2], acc[0]);
        rdW(s2 + 2);
        SBAR();
        acc[1] = MFMA(lf[s2 & 3][1], of[s2], acc[1]);
        SBAR();
        breload_sc1(of[(s2 + WO) & 7], voffO, sO, (s2 + WO < 8 ? so_c : so_n) + ((s2 + WO) & 7) * 1024u);
        SBAR(); });
      so_c = so_n;
    }
    wait_vm<0>();
#pragma unroll
    for (int i = 0; i < 8; ++i) pin(of[i]); }
  ST(28);
  const srd_t sX2 = mk_srd(ws + X_OFF, X_BYTES);
  const unsigned xb = (u * 8u + jq) * 4u;
  if (wid < 6u) {
#pragma unroll
    for (int i = 0; i < 4; ++i) bstore_sc1(pack8(acc[i >> 1], i & 1), voffO, sX2, (xb + g4) * 4096u + i * 1024u);
    wait_vm<0>();
    if (lane == 0) st_agent(ws + FLAG2_OFF + (xb + g4) * FLAG_STRIDE, tag);
    ST(29); ST(30);
  } else {
    __builtin_amdgcn_s_sleep(PDELAY2);
    poll_flags(ws + FLAG2_OFF + (xb + ((g4 + 1u + lane % 3u) & 3u)) * FLAG_STRIDE, tag);
    ST(29);
    bf16x8 xp[12];
#pragma unroll
    for (int i = 0; i < 12; ++i) bload_sc1(xp[i], voffO, sX2, (xb + ((g4 + 1u + (i >> 2)) & 3u)) * 4096u + (i & 3) * 1024u);
    wait_vm<0>();
#pragma unroll
    for (int j = 0; j < 12; ++j) { const int i = j & 3; pin(xp[j]); const u32x4 v = __builtin_bit_cast(u32x4, xp[j]);
#pragma unroll
      for (int e = 0; e < 4; ++e) { acc[i >> 1][8 * (i & 1) + 2 * e] += __uint_as_float(v[e] << 16); acc[i >> 1][8 * (i & 1) + 2 * e + 1] += __uint_as_float(v[e] & 0xFFFF0000u); } }
    ST(30);
    char* op = (char*)out + (size_t)(G * 32u + r) * (DM * 2u) + ct * 128u + hh * 16u;
#pragma unroll
    for (int i = 0; i < 4; ++i) *(u32x4*)(op + i * 32) = pack8(acc[i >> 1], i & 1);
  }
  ST(31); ST_OUT(ws, 0, 32);
}

extern "C" void kernel_launch(void* const* d_in, const int* in_sizes, int n_in, void* d_out, int out_size, void* d_ws, size_t ws_size, hipStream_t stream) {
  (void)in_sizes; (void)n_in; (void)out_size; (void)ws_size;
  (void)hipFuncSetAttribute((const void*)mha_kernel, hipFuncAttributeMaxDynamicSharedMemorySize, LDS_BYTES);
  const unsigned short* x = (const unsigned short*)d_in[0]; const unsigned short* kc = (const unsigned short*)d_in[1]; const unsigned short* vc = (const unsigned short*)d_in[2];
  const unsigned short* wq = (const unsigned short*)d_in[3]; const unsigned short* wo = (const unsigned short*)d_in[4];
  mha_kernel<<<256, 512, LDS_BYTES, stream>>>(x, kc, vc, wq, wo, (char*)d_ws, (unsigned short*)d_out);
}
```

```cpp
#include <hip/hip_runtime.h>
#include <stdint.h>
#include <utility>

typedef short    bf16x8 __attribute__((ext_vector_type(8)));
typedef short    s16x4  __attribute__((ext_vector_type(4)));
typedef float    f32x16 __attribute__((ext_vector_type(16)));
typedef float    f32x2  __attribute__((ext_vector_type(2)));
typedef __bf16   bf16x2 __attribute__((ext_vector_type(2)));
typedef unsigned u32x4  __attribute__((ext_vector_type(4)));
typedef __attribute__((address_space(3))) const char* lds_cptr;
typedef __attribute__((address_space(1))) unsigned long long gu64;
typedef __amdgpu_buffer_rsrc_t srd_t;

#define DI __device__ __forceinline__
#define MFMA(a, b, c) __builtin_amdgcn_mfma_f32_32x32x16_bf16((a), (b), (c), 0, 0, 0)
#define SBAR() __builtin_amdgcn_sched_barrier(0)

constexpr int NB = 8, SQ = 224, DM = 576, NH = 32, SK = 512, DH = 128;
constexpr int LDS_BYTES = 163840;
constexpr float QSCALE = 0.08838834764831845f * 1.4426950408889634f;
constexpr float SUM_THR = 4096.f;

template <int... Is, class F> DI void static_for_impl(std::integer_sequence<int, Is...>, F&& f) { (f(std::integral_constant<int, Is>{}), ...); }
template <int N, class F> DI void static_for(F&& f) { static_for_impl(std::make_integer_sequence<int, N>{}, f); }

DI unsigned rfl(unsigned x) { return __builtin_amdgcn_readfirstlane(x); }
DI unsigned cvtpk(float lo, float hi) { f32x2 v = {lo, hi}; bf16x2 b = __builtin_convertvector(v, bf16x2); return __builtin_bit_cast(unsigned, b); }
DI srd_t mk_srd(const void* p, unsigned bytes) { return __builtin_amdgcn_make_buffer_rsrc((void*)p, (short)0, (int)bytes, 0x00020000); }
DI void dma4(unsigned lds, unsigned v0, unsigned v1, unsigned v2, unsigned v3, srd_t srd, unsigned soff) {
  asm volatile("s_mov_b32 m0, %0\n\ts_nop 0\n\tbuffer_load_dwordx4 %1, %5, %6 offen lds\n\tbuffer_load_dwordx4 %2, %5, %6 offen offset:1024 lds\n\t"
               "buffer_load_dwordx4 %3, %5, %6 offen offset:2048 lds\n\tbuffer_load_dwordx4 %4, %5, %6 offen offset:3072 lds"
               :: "s"(lds), "v"(v0), "v"(v1), "v"(v2), "v"(v3), "s"(srd), "s"(soff) : "memory");
}
DI void dma4_nt(unsigned lds, unsigned v0, unsigned v1, unsigned v2, unsigned v3, srd_t srd, unsigned soff) {
  asm volatile("s_mov_b32 m0, %0\n\ts_nop 0\n\tbuffer_load_dwordx4 %1, %5, %6 offen nt lds\n\tbuffer_load_dwordx4 %2, %5, %6 offen offset:1024 nt lds\n\t"
               "buffer_load_dwordx4 %3, %5, %6 offen offset:2048 nt lds\n\tbuffer_load_dwordx4 %4, %5, %6 offen offset:3072 nt lds"
               :: "s"(lds), "v"(v0), "v"(v1), "v"(v2), "v"(v3), "s"(srd), "s"(soff) : "memory");
}
DI void bstore_sc1(const u32x4& d, unsigned voff, srd_t srd, unsigned soff) { asm volatile("buffer_store_dwordx4 %0, %1, %2, %3 offen sc1\n\ts_nop 1" :: "v"(d), "v"(voff), "s"(srd), "s"(soff) : "memory"); }
DI void bload_sc1(bf16x8& d, unsigned voff, srd_t srd, unsigned soff) { asm volatile("buffer_load_dwordx4 %0, %1, %2, %3 offen sc1" : "=v"(d) : "v"(voff), "s"(srd), "s"(soff) : "memory"); }
DI void breload_sc1(bf16x8& d, unsigned voff, srd_t srd, unsigned soff) { asm volatile("buffer_load_dwordx4 %0, %1, %2, %3 offen sc1" : "+v"(d) : "v"(voff), "s"(srd), "s"(soff) : "memory"); }
DI void pin(bf16x8& a) { asm volatile("" : "+v"(a)); }
DI void pin16(f32x16& a) { asm volatile("" : "+v"(a)); }
DI void pinf(float& a) { asm volatile("" : "+v"(a)); }
DI void pinu(unsigned& a) { asm volatile("" : "+v"(a)); }
template <int N> DI void wait_vm1(bf16x8& a) { asm volatile("s_waitcnt vmcnt(%1)" : "+v"(a) : "n"(N) : "memory"); }
template <int N> DI void wait_vm() { asm volatile("s_waitcnt vmcnt(%0)" :: "n"(N) : "memory"); }
template <int N> DI void wait_lgkm() { __builtin_amdgcn_s_waitcnt(0xC07F | (N << 8)); SBAR(); }
DI void bar_if(unsigned c) { asm volatile("s_cmp_eq_u32 %0, 0\n\ts_cbranch_scc1 1f\n\ts_waitcnt lgkmcnt(0)\n\ts_barrier\n1:" :: "s"(c) : "memory", "scc"); }
DI void bar() { asm volatile("s_waitcnt lgkmcnt(0)\n\ts_barrier" ::: "memory"); }
typedef short v4i16_t __attribute__((ext_vector_type(4)));
DI s16x4 trd(lds_cptr p) { return __builtin_amdgcn_ds_read_tr16_b64_v4i16((__attribute__((address_space(3))) v4i16_t*)p); }
DI bf16x8 trd2(lds_cptr p, int o0, int o1) { s16x4 a = trd(p + o0), b = trd(p + o1); return __builtin_shufflevector(a, b, 0, 1, 2, 3, 4, 5, 6, 7); }
DI bf16x8 rd128(lds_cptr p) { return *(const __attribute__((address_space(3))) bf16x8*)p; }
DI float swap_max(float m) { auto rr = __builtin_amdgcn_permlane32_swap(__float_as_uint(m), __float_as_uint(m), false, false); return fmaxf(__uint_as_float(rr[0]), __uint_as_float(rr[1])); }
DI float swap_add(float m) { auto rr = __builtin_amdgcn_permlane32_swap(__float_as_uint(m), __float_as_uint(m), false, false); return __uint_as_float(rr[0]) + __uint_as_float(rr[1]); }
DI float max16(const f32x16& s) { float a = fmaxf(fmaxf(s[0], s[1]), s[2]), b = fmaxf(fmaxf(s[3], s[4]), s[5]);
  a = fmaxf(fmaxf(a, s[6]), s[7]); b = fmaxf(fmaxf(b, s[8]), s[9]); a = fmaxf(fmaxf(a, s[10]), s[11]); b = fmaxf(fmaxf(b, s[12]), s[13]); a = fmaxf(fmaxf(a, s[14]), s[15]); return fmaxf(a, b); }
DI u32x4 pack8(const f32x16& a, int s, float f) { return u32x4{cvtpk(a[8 * s] * f, a[8 * s + 1] * f), cvtpk(a[8 * s + 2] * f, a[8 * s + 3] * f), cvtpk(a[8 * s + 4] * f, a[8 * s + 5] * f), cvtpk(a[8 * s + 6] * f, a[8 * s + 7] * f)}; }
DI u32x4 pack8(const f32x16& a, int s) { return u32x4{cvtpk(a[8 * s], a[8 * s + 1]), cvtpk(a[8 * s + 2], a[8 * s + 3]), cvtpk(a[8 * s + 4], a[8 * s + 5]), cvtpk(a[8 * s + 6], a[8 * s + 7])}; }

DI float expo(const f32x16& s, u32x4 (&p)[2]) {
  float e[16]; float s0 = 0.f, s1 = 0.f;
#pragma unroll
  for (int i = 0; i < 16; ++i) e[i] = __builtin_amdgcn_exp2f(s[i]);
#pragma unroll
  for (int i = 0; i < 16; i += 2) { s0 += e[i]; s1 += e[i + 1]; }
#pragma unroll
  for (int i = 0; i < 8; ++i) p[i >> 2][i & 3] = cvtpk(e[2 * i], e[2 * i + 1]);
  return s0 + s1;
}

#define ST_DECL
#define ST(k)
#define ST_OUT(ws, lo, hi)

constexpr unsigned SLOTK = 0x04204209u;
constexpr unsigned SLOTV = 0x31531531u;
constexpr unsigned SLOTW = 0x05429876u;
constexpr unsigned STG = 45056u, STG_W = 28672u;
constexpr unsigned O_BYTES = NB * SQ * NH * DH * 2;
constexpr unsigned X_OFF = O_BYTES, X_BYTES = 63u * 8u * 4u * 4096u;
constexpr unsigned FLAG_OFF = X_OFF + X_BYTES, FLAG_STRIDE = 128, FLAG2_OFF = FLAG_OFF + 256 * FLAG_STRIDE, EPOCH_OFF = FLAG2_OFF + 63 * 32 * FLAG_STRIDE;
#define XOFF 0
#define KA 0u
#define QG 1
#define PACE 0
#define PACEA 0
#define K0S 1
#define NOWUP 0
#define WUPC 3u
#define RAMP 0
#define HOLD 0
#define SRAMP 0
#define RAMPW 5
#define PRE 0u
#define R2 0
#define PD2 25
#define PD1 20
constexpr unsigned SPIN_CAP = 1u << 20;
constexpr int RA = 7, WO = 6, A0BAR = 6, PSLEEP = 4, PDELAY = PD1, PDELAY2 = PD2;
constexpr unsigned SPARE = 3u * STG;
constexpr unsigned CODE_BYTES = 19200u;
constexpr int lds_ops(int a, int b, bool last) { int n = 0; for (int i = a; i < b; ++i) n += i >= 16 ? (last ? 0 : 1) : (i >= 8 ? 2 : 1); return n; }
constexpr int pv_order(int i) { return i < 4 ? i : i < 8 ? i + 4 : i < 12 ? i - 4 : i; }
DI unsigned long long mix64(unsigned long long z) { z += 0x9E3779B97F4A7C15ull; z = (z ^ (z >> 30)) * 0xBF58476D1CE4E5B9ull; z = (z ^ (z >> 27)) * 0x94D049BB133111EBull; return z ^ (z >> 31); }
DI unsigned long long rfl64(unsigned long long v) { return ((unsigned long long)rfl((unsigned)(v >> 32)) << 32) | rfl((unsigned)v); }
DI unsigned long long ld_agent(const char* p) { return __hip_atomic_load((gu64*)p, __ATOMIC_RELAXED, __HIP_MEMORY_SCOPE_AGENT); }
DI void st_agent(char* p, unsigned long long v) { __hip_atomic_store((gu64*)p, v, __ATOMIC_RELAXED, __HIP_MEMORY_SCOPE_AGENT); }
DI void poll_flags(const char* p, unsigned long long tag) {
  _Pragma("nounroll") for (unsigned spins = 0; spins < SPIN_CAP; ++spins) { if (__all(ld_agent(p) == tag)) break; __builtin_amdgcn_s_sleep(PSLEEP); }
  asm volatile("" ::: "memory");
}

__global__ __launch_bounds__(512, 2) void mha_kernel(const unsigned short* __restrict__ x, const unsigned short* __restrict__ kc, const unsigned short* __restrict__ vc,
                                                     const unsigned short* __restrict__ wq, const unsigned short* __restrict__ wo, char* ws, unsigned short* __restrict__ out) {
  extern __shared__ __attribute__((aligned(16))) char lds[];
  unsigned long long pc0; asm volatile("s_getpc_b64 %0" : "=s"(pc0));
  const unsigned tid = threadIdx.x, lane = tid & 63u, r = lane & 31u, hh = lane >> 5;
  const unsigned wid = rfl(tid >> 6);
  ST_DECL ST(0);
  const unsigned bid = blockIdx.x, xcd = bid & 7u, ix = bid >> 3;
  const unsigned b = (xcd & 1u) * 4u + (ix & 3u), h = (xcd >> 1) * 8u + (ix >> 2);
  const unsigned lds0 = (unsigned)(uintptr_t)lds;
  if (SRAMP) { for (unsigned i = 0; i < (ix & 7u); ++i) __builtin_amdgcn_s_sleep(SRAMP); }
  {
    const char* cp = (const char*)(pc0 & ~255ull) + min((ix * 8u + wid) * 128u, CODE_BYTES);
    asm volatile("s_mov_b32 m0, %0\n\ts_nop 0\n\tglobal_load_lds_dword %1, %2" :: "s"(lds0 + SPARE), "v"(0u), "s"(cp) : "memory"); }
  const srd_t sW = mk_srd((const char*)wq - 1024, DM * NH * DH * 2 + 1024);
  const srd_t sX = mk_srd(x + (size_t)b * SQ * DM, SQ * DM * 2);
  const unsigned prow = (lane >> 2) & 7u, pch = 4u * hh + (lane & 3u);
  const unsigned pchx0 = 4u * hh + ((lane & 3u) ^ ((lane >> 4) & 1u)), pchx1 = pchx0 ^ 2u;
  const unsigned vW = prow * 8192u + pch * 16u, vX0 = prow * (DM * 2u) + pchx0 * 16u, vX1 = prow * (DM * 2u) + pchx1 * 16u;
  const unsigned ck0 = (QG == 3 ? 3u * (((ix & 3u) + (ix >> 2)) % 3u) : QG == 2 ? 4u * (((ix & 3u) + (ix >> 2)) & 1u) : 0u) + (XOFF ? (0x10022110u >> (4u * xcd)) & 3u : 0u), ck1 = ck0 == 8u ? 0u : ck0 + 1u;
  unsigned long long ep; asm volatile("global_load_dwordx2 %0, %1, %2 sc1" : "=v"(ep) : "v"(0u), "s"(ws + EPOCH_OFF) : "memory");
  dma4(lds0 + (wid < 7u ? wid * 4096u : SPARE), vX0, vX1 + 8192u, vX0 + 16384u, vX1 + 24576u, sX, ck0 * 128u + min(wid, 6u) * 32u * DM * 2u);
  dma4(lds0 + (wid >> 2) * STG + STG_W + (wid & 3u) * 4096u, vW + 1024u, vW + 128u, vW + 65536u - 1024u, vW + 65664u - 2048u, sW, (wid < 4u ? ck0 : ck1) * 64u * 8192u + h * 256u + (wid & 3u) * 131072u);
  const srd_t sK = mk_srd((const char*)(kc + (size_t)(b * NH + h) * SK * DH) - 1024, SK * DH * 2 + 1024);
  const srd_t sV = mk_srd((const char*)(vc + (size_t)(b * NH + h) * SK * DH) - 1024, SK * DH * 2 + 1024);
  const unsigned vK0 = prow * 256u + pchx0 * 16u, vK1 = prow * 256u + pchx1 * 16u;
  const unsigned q4 = (lane & 15u) >> 2, p4 = lane & 3u, blk = (lane >> 4) & 1u;
  const unsigned trcol = 32u * blk + 16u * (p4 & 1u) + 8u * (p4 >> 1);
  const srd_t sO = mk_srd(ws, O_BYTES);
  unsigned long long tag;
  const unsigned g4 = xcd >> 1, u0 = (xcd & 1u) * 32u + ix;
  const bool has_tile = u0 < 63u;
  const unsigned u = has_tile ? u0 : 62u, rt = u / 9u, ct = u - rt * 9u;

  if (wid == 7u) {
    auto fillQ = [&](unsigned c, unsigned stage) {
      const unsigned lw = lds0 + stage + STG_W, sw = c * 64u * 8192u + h * 256u, lx = lds0 + stage, sx = c * 128u;
#pragma unroll
      for (int i = 0; i < 4; ++i) dma4(lw + i * 4096u, vW + 1024u, vW + 128u, vW + 65536u - 1024u, vW + 65664u - 2048u, sW, sw + i * 131072u);
#pragma unroll
      for (int i = 0; i < 7; ++i) dma4(lx + i * 4096u, vX0, vX1 + 8192u, vX0 + 16384u, vX1 + 24576u, sX, sx + i * 32u * DM * 2u); };
    bar(); ST(2);
    { unsigned stage = 2 * STG, ck = ck1 == 8u ? 0u : ck1 + 1u;
#pragma nounroll
      for (unsigned c = 2; c < 9; ++c) { fillQ(ck, stage); ck = ck == 8u ? 0u : ck + 1u; stage = stage == 2 * STG ? 0u : stage + STG;
        wait_vm<44>(); bar(); ST(1 + c); } }
    const unsigned vV = prow * 256u + pch * 16u;
    const srd_t sWo = mk_srd(wo, NH * DH * DM * 2);
    const unsigned vWo = prow * (DM * 2u) + pch * 16u;
    auto pace = [&](unsigned n) { if (n == 0u) { if (PACEA) __builtin_amdgcn_s_sleep(PACEA); } else if (n < 6u) { if (PACE) __builtin_amdgcn_s_sleep(PACE); } };
    { unsigned j = 1, jw = 0;
#pragma nounroll
      for (unsigned n = 0; n < 10; ++n) {
        if (n == 9u) { wait_vm<0>(); bar(); }
        const unsigned je = ((unsigned)(0xFFFECA8642ull >> (4u * n)) & 15u) + 1u;
#pragma nounroll
        for (; j < je; ++j) {
          if ((j & 1u) && j != 15u) { const unsigned t = (j + 1u) >> 1, ld = lds0 + ((SLOTK >> (4u * t)) & 15u) * 16384u;
#pragma unroll
            for (int i = 0; i < 4; ++i) dma4_nt(ld + i * 4096u, vK0 + 1024u, vK0 + 128u, vK1 + 1024u, vK1 + 128u, sK, t * 16384u + i * 4096u), pace(n);
          } else { const unsigned t = (j - 1u) >> 1, ld = lds0 + ((SLOTV >> (4u * t)) & 15u) * 16384u;
#pragma unroll
            for (int i = 0; i < 4; ++i) dma4_nt(ld + i * 4096u, vV + 1024u, vV + 128u, vV + 1024u, vV + 128u, sV, t * 16384u + i * 4096u), pace(n); } }
        const unsigned we = (unsigned)(0x8741000000ull >> (4u * n)) & 15u;
#pragma nounroll
        for (; jw < we; ++jw) { const unsigned ld = lds0 + ((SLOTW >> (4u * jw)) & 15u) * 16384u, so = (g4 * 8u + jw) * 128u * (DM * 2u) + ct * 128u;
#pragma unroll
          for (int k = 0; k < 4; ++k) dma4(ld + k * 4096u, vWo, vWo + 8192u, vWo + 16384u, vWo + 24576u, sWo, so + k * 32u * (DM * 2u)); }
        if (n == 9u) break;
        if (n < 6u) wait_vm<32>(); else if (n == 6u) wait_vm<48>(); else wait_vm<62>();
        bar(); ST(n == 0u ? 10u : 11u + n); } }
    asm volatile("" : "+v"(ep)); tag = mix64(rfl64(ep));
    ST(20); ST(21);
  } else {
  const unsigned rsw0 = 64u * (r & 7u) + 16u * (hh ^ ((r >> 2) & 3u)), rsw1 = rsw0 ^ 32u;
  f32x16 qa[4] = {}, o[4] = {};
  asm volatile("" : "+v"(qa[0]), "+v"(qa[1]), "+v"(qa[2]), "+v"(qa[3]), "+v"(o[0]), "+v"(o[1]), "+v"(o[2]), "+v"(o[3]));
  if (PRE) {
    unsigned z = (tid + 1u) * 2654435761u; u32x4 ja = {z & 0xBFFFBFFFu, (z * 3u) & 0xBFFFBFFFu, (z * 5u) & 0xBFFFBFFFu, (z * 7u) & 0xBFFFBFFFu}, jb = {(z * 9u) & 0xBFFFBFFFu, (z * 11u) & 0xBFFFBFFFu, (z * 13u) & 0xBFFFBFFFu, (z * 15u) & 0xBFFFBFFFu};
    f32x16 jc = {};
#pragma nounroll
    for (unsigned i = 0; i < PRE; ++i) {
      jc = MFMA(__builtin_bit_cast(bf16x8, ja), __builtin_bit_cast(bf16x8, jb), jc); jc = MFMA(__builtin_bit_cast(bf16x8, jb), __builtin_bit_cast(bf16x8, ja), jc); pin16(jc);
      ja[0] = ja[1]; ja[1] = jb[2]; jb[2] = jb[3] ^ 0x01230456u; jb[3] = ja[0];
      if (i * 3u < PRE) __builtin_amdgcn_s_sleep(4); else if (i * 3u < 2u * PRE) __builtin_amdgcn_s_sleep(2); } }
  wait_lgkm<0>();
  wait_vm<4>(); if (wid < 4u) wait_vm<0>();
  ST(1);
  dma4(lds0 + STG + wid * 4096u, vX0, vX1 + 8192u, vX0 + 16384u, vX1 + 24576u, sX, ck1 * 128u + wid * 32u * DM * 2u);
  bar();
  asm volatile("" : "+v"(ep)); ep = rfl64(ep);
  ST(2);
  const lds_cptr kb0 = (lds_cptr)lds + 2048u * (r >> 3) + rsw0, kb1 = (lds_cptr)lds + 2048u * (r >> 3) + rsw1;
  const lds_cptr vb = (lds_cptr)lds + 64u * (4u * hh + q4) + trcol;
  bf16x8 qf[8], pre[RA];
  f32x16 negm, S0, S1; u32x4 P0[2], P1[2]; float l = 0.f;
  auto rdK = [&](lds_cptr k0, lds_cptr k1, int kb, int i) { return rd128(((i & 1) ? k1 : k0) + 8192 * kb + 512 * (i >> 1)); };
  auto rdV = [&](lds_cptr v, int kb, int j) { return trd2(v, 8192 * kb + 4096 * (j >> 2) + 512 * (j & 3), 8192 * kb + 4096 * (j >> 2) + 2048 + 512 * (j & 3)); };
  auto sm_fix = [&](float ps, f32x16& Sc, f32x16& Sn, u32x4 (&P)[2]) {
    if (__builtin_expect(__any(!(ps <= SUM_THR)), 0)) {
      const float dl = fmaxf(swap_max(max16(Sc)), 0.f), f = __builtin_amdgcn_exp2f(-dl);
#pragma unroll
      for (int i = 0; i < 16; ++i) { Sc[i] -= dl; Sn[i] -= dl; negm[i] -= dl; }
#pragma unroll
      for (int db = 0; db < 4; ++db)
#pragma unroll
        for (int i = 0; i < 16; ++i) o[db][i] *= f;
      l *= f; ps = expo(Sc, P); }
    l += ps; };
  { lds_cptr wp = (lds_cptr)lds + STG_W + 2048u * hh + 64u * q4 + trcol;
    lds_cptr xp0 = (lds_cptr)lds + wid * 4096u + 1024u * (r >> 3) + rsw0, xp1 = (lds_cptr)lds + wid * 4096u + 1024u * (r >> 3) + rsw1;
    bf16x8 xf[4], af[5];
    auto rdA = [&](int i) { return trd2(wp, 4096 * (i >> 2) + 512 * (i & 3), 4096 * (i >> 2) + 512 * (i & 3) + 256); };
    xf[0] = rd128(xp0); af[0] = rdA(0); af[1] = rdA(1); af[2] = rdA(2); af[3] = rdA(3);
    unsigned stage = 0;
#pragma nounroll
    for (unsigned c = 0; c < 8; ++c) {
      if (!NOWUP && __builtin_expect(c == WUPC && wid == 3u, 0)) {
#pragma unroll
        for (int i = 0; i < (K0S ? 0 : 4); ++i) dma4_nt(lds0 + 9u * 16384u + i * 4096u, vK0 + 1024u, vK0 + 128u, vK1 + 1024u, vK1 + 128u, sK, i * 4096u);
        unsigned ln = lane; asm volatile("" : "+v"(ln));
        const unsigned k = ln & 7u, sel = ln >> 3;
        const char* wup = sel == 0u ? (const char*)vc + (size_t)(b * NH + h) * (SK * DH * 2) + k * 16384u
                        : sel == 1u ? (const char*)wo + (size_t)(g4 * 1024u + k * 128u) * (DM * 2u) + ct * 128u
                        : sel == 2u ? (const char*)out + (size_t)((8u * rt + k) * 32u) * (DM * 2u)
                        : ws + (sel == 3u ? ((b * 7u + k) * NH + h) * 8192u : sel == 4u ? ((8u * rt + k) * NH + g4 * 8u) * 8192u : sel == 5u ? X_OFF + u * 131072u + k * 16384u : FLAG_OFF + k * 32768u + 64u);
        asm volatile("s_mov_b32 m0, %0\n\ts_nop 0\n\tglobal_load_lds_dword %1, off sc1" :: "s"(lds0 + SPARE), "v"(wup) : "memory"); }
      if (HOLD && c == 0u) __builtin_amdgcn_s_sleep(HOLD);
      if (RAMPW && c == 0u) { for (unsigned i = 0; i < ((wid + ix) & 3u); ++i) __builtin_amdgcn_s_sleep(RAMPW); }
      if (RAMP && c == 0u) { for (unsigned i = 0; i < ((ix + wid) & 7u); ++i) __builtin_amdgcn_s_sleep(RAMP); }
      if (K0S && __builtin_expect(c >= 3u && c < 7u && wid == 3u, 0)) dma4_nt(lds0 + 9u * 16384u + (c - 3u) * 4096u, vK0 + 1024u, vK0 + 128u, vK1 + 1024u, vK1 + 128u, sK, (c - 3u) * 4096u);
      if (KA && __builtin_expect(c >= 4u && c < 7u && wid == 3u, 0))
        asm volatile("s_mov_b32 m0, %0\n\ts_nop 0\n\tbuffer_load_dword %1, %2, %3 offen nt lds" :: "s"(lds0 + SPARE), "v"((lane & (KA - 1u)) * 128u), "s"(sK), "s"(17408u + (c - 4u) * KA * 128u) : "memory");
      const unsigned nstage = stage == 2 * STG ? 0u : stage + STG; const int dlt = (int)nstage - (int)stage; stage = nstage;
      SBAR();
      static_for<12>([&](auto I) { constexpr int i = I.value;
        qa[i & 3] = MFMA(af[i % 5], xf[i >> 2], qa[i & 3]);
        af[(i + 4) % 5] = rdA(i + 4);
        if constexpr (i == 0) xf[1] = rd128(xp1); if constexpr (i == 3) xf[2] = rd128(xp0 + 512); if constexpr (i == 7) xf[3] = rd128(xp1 + 512);
        SBAR(); });
      qa[0] = MFMA(af[12 % 5], xf[3], qa[0]);
      wp += dlt; xp0 += dlt; xp1 += dlt; asm volatile("" : "+v"(wp), "+v"(xp0), "+v"(xp1));
      SBAR();
      asm volatile("s_bitcmp1_b32 %0, %1\n\ts_cbranch_scc1 1f\n\ts_waitcnt vmcnt(0)\n1:\n\ts_waitcnt lgkmcnt(0)\n\ts_barrier" :: "s"(0x78u), "s"(c) : "memory", "scc");
      const bf16x8 xl = xf[3], a13 = af[13 % 5], a14 = af[14 % 5], a15 = af[15 % 5];
      xf[0] = rd128(xp0); af[0] = rdA(0); af[1] = rdA(1);
      SBAR();
      qa[1] = MFMA(a13, xl, qa[1]); SBAR();
      af[2] = rdA(2);
      qa[2] = MFMA(a14, xl, qa[2]); SBAR();
      af[3] = rdA(3);
      qa[3] = MFMA(a15, xl, qa[3]); SBAR();
      ST(3 + c);
    }
    lds_cptr k0 = kb0 + 9 * 16384, k1 = kb1 + 9 * 16384; asm volatile("" : "+v"(k0), "+v"(k1));
    bf16x8 ag[16], f[16]; u32x4 qw[8]; float qs = QSCALE; asm volatile("" : "+s"(qs));
#pragma unroll
    for (int i = 0; i < 4; ++i) ag[i] = af[i];
    auto cvq = [&](int j) { float lo = qa[j >> 3][2 * (j & 7)] * qs; pinf(lo); float hi = qa[j >> 3][2 * (j & 7) + 1] * qs; unsigned w = cvtpk(lo, hi); pinu(w); qw[j >> 2][j & 3] = w; };
    SBAR();
    static_for<24>([&](auto G) { constexpr int g = G.value, n = g + 4;
      if constexpr (g < 4) qa[g] = MFMA(ag[g], xf[0], qa[g]);
      else if constexpr (g < 16) qa[(g - 4) / 3] = MFMA(ag[g], xf[1 + (g - 4) % 3], qa[(g - 4) / 3]);
      else { qf[g - 16] = __builtin_bit_cast(bf16x8, qw[g - 16]);
        if constexpr (g == 16) { const f32x16 z = {}; S0 = MFMA(f[0], qf[0], z); } else S0 = MFMA(f[g - 16], qf[g - 16], S0); pin16(S0); }
      if constexpr (g == 0) { xf[1] = rd128(xp1); xf[2] = rd128(xp0 + 512); } if constexpr (g == 1) xf[3] = rd128(xp1 + 512);
      if constexpr (n < 16) ag[n] = rdA(4 * (1 + (n - 4) % 3) + (n - 4) / 3); else f[n - 16] = rdK(k0, k1, (n - 16) >> 3, (n - 16) & 7);
      if constexpr (g >= 9 && g < 21) { constexpr int j0 = (g - 9) * 8 / 3, j1 = (g - 8) * 8 / 3; static_for<j1 - j0>([&](auto J) { cvq(j0 + J.value); }); }
      SBAR(); });
    ST(11);
#pragma unroll
    for (int i = 12; i < 16; ++i) f[i] = rdK(k0, k1, 1, i & 7);
    const float m = swap_max(max16(S0));
#pragma unroll
    for (int i = 0; i < 16; ++i) negm[i] = -m;
    SBAR();
    float e[16]; float s0 = 0.f, s1 = 0.f;
    static_for<9>([&](auto G) { constexpr int g = G.value;
      if constexpr (g == 0) { if (wid >= 4u) bar(); }
      if constexpr (g == A0BAR) { if (wid < 4u) bar();
        constexpr unsigned ko = ((SLOTK >> 4) & 15u) * 16384u;
#pragma unroll
        for (int i = 0; i < RA; ++i) pre[i] = rdK(kb0 + ko, kb1 + ko, 0, i);
        SBAR(); }
      if constexpr (g == 0) S1 = MFMA(f[8], qf[0], negm); else if constexpr (g < 8) S1 = MFMA(f[8 + g], qf[g], S1);
      if constexpr (g < 8) pin16(S1);
      if constexpr (g < 8) { e[2 * g] = __builtin_amdgcn_exp2f(S0[2 * g] - m); e[2 * g + 1] = __builtin_amdgcn_exp2f(S0[2 * g + 1] - m); pinf(e[2 * g]); pinf(e[2 * g + 1]); }
      if constexpr (g == 1) { s0 = e[0]; s1 = e[1]; } else if constexpr (g > 1) { s0 += e[2 * g - 2]; pinf(s0); s1 += e[2 * g - 1]; pinf(s1); }
      if constexpr (g >= 1) { unsigned w = cvtpk(e[2 * g - 2], e[2 * g - 1]); pinu(w); P0[(g - 1) >> 2][(g - 1) & 3] = w; }
      SBAR(); });
    l = s0 + s1; }
  ST(12);
  auto unit = [&](auto KB, auto LAST, lds_cptr k0, lds_cptr k1, lds_cptr v, lds_cptr nk0, lds_cptr nk1, f32x16& Sn, f32x16& Sc, const u32x4 (&Pp)[2], u32x4 (&Pc)[2]) {
    constexpr int kb = KB.value; constexpr bool last = LAST.value;
    float e[16]; float s0 = 0.f, s1 = 0.f; bf16x8 f[16 + RA];
#pragma unroll
    for (int i = 0; i < RA; ++i) f[i] = pre[i];
    static_for<16>([&](auto G) { constexpr int g = G.value; constexpr int n = g + RA; constexpr int gb = 12;
      if constexpr (last && g == gb) { bar(); wait_lgkm<0>(); }
      else if constexpr ((g & 3) == 0) wait_lgkm<lds_ops(g + 4, g + RA, last)>();
      if constexpr (g == 0) Sn = MFMA(f[0], qf[0], negm); else if constexpr (g < 8) Sn = MFMA(f[g], qf[g], Sn);
      else o[g & 3] = MFMA(f[g], __builtin_bit_cast(bf16x8, Pp[(g >> 2) & 1]), o[g & 3]);
      if constexpr (n < 8) f[n] = rdK(k0, k1, kb, n); else if constexpr (n < 16) f[n] = rdV(v, kb, n - 8);
      else if constexpr (!last) f[n] = rdK(k0, k1, kb + 1, n - 16);
      else if constexpr (g >= gb) { constexpr int per = (RA + 15 - gb) / (16 - gb); static_for<per>([&](auto J) { constexpr int i = per * (g - gb) + J.value; if constexpr (i < RA) f[16 + i] = rdK(nk0, nk1, 0, i); }); }
      if constexpr (g == 0) { e[0] = __builtin_amdgcn_exp2f(Sc[0]); pinf(e[0]); }
      if constexpr (g < 15) { e[g + 1] = __builtin_amdgcn_exp2f(Sc[g + 1]); pinf(e[g + 1]); }
      if constexpr (g == 1) { s0 = e[0]; s1 = e[1]; } else if constexpr (g > 1) { if constexpr (g & 1) { s1 += e[g]; pinf(s1); } else { s0 += e[g]; pinf(s0); } }
      if constexpr (g & 1) { unsigned w = cvtpk(e[g - 1], e[g]); pinu(w); Pc[g >> 3][(g >> 1) & 3] = w; }
      SBAR(); });
#pragma unroll
    for (int i = 0; i < RA; ++i) pre[i] = f[16 + i];
    sm_fix(s0 + s1, Sc, Sn, Pc); };
#pragma nounroll
  for (unsigned t = 1; t < 7; ++t) {
    const unsigned ko = ((SLOTK >> (4u * t)) & 15u) * 16384u, vo = ((SLOTV >> (4u * (t - 1u))) & 15u) * 16384u, nko = ((SLOTK >> (4u * ((t + 1u) & 7u))) & 15u) * 16384u;
    SBAR();
    unit(std::integral_constant<int, 0>{}, std::false_type{}, kb0 + ko, kb1 + ko, vb + vo, kb0, kb1, S0, S1, P0, P1);
    unit(std::integral_constant<int, 1>{}, std::true_type{}, kb0 + ko, kb1 + ko, vb + vo, kb0 + nko, kb1 + nko, S1, S0, P1, P0);
    if (t == 1u || t == 6u) ST(12 + t);
  }
  const unsigned me = (b * 7u + wid) * NH + h;
  {
    constexpr unsigned ko = ((SLOTK >> 28) & 15u) * 16384u;
    const lds_cptr k0 = kb0 + ko, k1 = kb1 + ko, v6 = vb + ((SLOTV >> 24) & 15u) * 16384u, v7 = vb + ((SLOTV >> 28) & 15u) * 16384u;
    u32x4 P2[2], P3[2];
    auto scaleP = [&](u32x4 (&P)[2], float f) {
#pragma unroll
      for (int i = 0; i < 8; ++i) { const unsigned w = P[i >> 2][i & 3]; P[i >> 2][i & 3] = cvtpk(__uint_as_float(w << 16) * f, __uint_as_float(w & 0xFFFF0000u) * f); } };
    auto fix = [&](auto NP, float ps, f32x16& Sc, f32x16& Sn, u32x4 (&P)[2]) {
      if (__builtin_expect(__any(!(ps <= SUM_THR)), 0)) {
        const float f = __builtin_amdgcn_exp2f(-fmaxf(swap_max(max16(Sc)), 0.f));
        scaleP(P0, f); if constexpr (NP.value > 1) scaleP(P1, f); if constexpr (NP.value > 2) scaleP(P2, f); }
      sm_fix(ps, Sc, Sn, P); };
    auto rdVt = [&](int i) { const int j = pv_order(i), db = j >> 3, un = (j >> 1) & 3, ks = j & 1; return rdV(un < 2 ? v6 : v7, un & 1, 4 * ks + db); };
    auto pvt = [&](auto I, const bf16x8& vf) { constexpr int j = pv_order(I.value), un = (j >> 1) & 3;
      const u32x4 pb = un == 0 ? P0[j & 1] : un == 1 ? P1[j & 1] : un == 2 ? P2[j & 1] : P3[j & 1];
      o[j >> 3] = MFMA(vf, __builtin_bit_cast(bf16x8, pb), o[j >> 3]); pin16(o[j >> 3]); };
    bf16x8 f[16], w[32];
#pragma unroll
    for (int i = 0; i < RA; ++i) f[i] = pre[i];
    auto qsm = [&](auto H, f32x16& Sn, const f32x16& Sc, u32x4 (&Pc)[2]) { constexpr int hf = H.value;
      float e[16]; float s0 = 0.f, s1 = 0.f;
      static_for<9>([&](auto G) { constexpr int g = G.value, n = 8 * hf + g + RA;
        if constexpr (hf < 2 && g < 8) { if constexpr (g == 0) Sn = MFMA(f[8 * hf], qf[0], negm); else Sn = MFMA(f[8 * hf + g], qf[g], Sn);
          pin16(Sn);
          if constexpr (n < 16) f[n] = rdK(k0, k1, n >> 3, n & 7); }
        if constexpr (hf == 1 && g >= 2 && g < 8) w[g - 2] = rdVt(g - 2);
        if constexpr (hf == 2 && g < 8) { pvt(G, w[g]); w[g + 6] = rdVt(g + 6); }
        if constexpr (g < 8) { e[2 * g] = __builtin_amdgcn_exp2f(Sc[2 * g]); e[2 * g + 1] = __builtin_amdgcn_exp2f(Sc[2 * g + 1]); pinf(e[2 * g]); pinf(e[2 * g + 1]); }
        if constexpr (g == 1) { s0 = e[0]; s1 = e[1]; } else if constexpr (g > 1) { s0 += e[2 * g - 2]; pinf(s0); s1 += e[2 * g - 1]; pinf(s1); }
        if constexpr (g >= 1) { unsigned pw = cvtpk(e[2 * g - 2], e[2 * g - 1]); pinu(pw); Pc[(g - 1) >> 2][(g - 1) & 3] = pw; }
        SBAR(); });
      return s0 + s1; };
    if (wid < 4u) __builtin_amdgcn_s_setprio(1);
    SBAR();
    fix(std::integral_constant<int, 1>{}, qsm(std::integral_constant<int, 0>{}, S0, S1, P1), S1, S0, P1);
    bar_if(wid >> 2);
    fix(std::integral_constant<int, 2>{}, qsm(std::integral_constant<int, 1>{}, S1, S0, P2), S0, S1, P2);
    bar_if((wid >> 2) ^ 1u);
    ST(19);
    fix(std::integral_constant<int, 3>{}, qsm(std::integral_constant<int, 2>{}, S0, S1, P3), S1, S0, P3);
    const float inv = __builtin_amdgcn_rcpf(swap_add(l));
    auto put = [&](int k8) { bstore_sc1(pack8(o[k8 >> 1], k8 & 1, inv), lane * 16u, sO, me * 8192u + k8 * 1024u); };
    static_for<24>([&](auto G) { constexpr int g = G.value + 8;
      pvt(std::integral_constant<int, g>{}, w[g]);
      if constexpr (g + 6 < 32) w[g + 6] = rdVt(g + 6);
      if constexpr (g == 14) put(0); if constexpr (g == 16) put(1); if constexpr (g == 19) put(2); if constexpr (g == 22) put(3); if constexpr (g == 26) put(4); if constexpr (g == 29) put(5);
      SBAR(); });
    ST(20);
    put(6); put(7); }
  ST(21);
  tag = mix64(ep);
  __builtin_amdgcn_s_setprio(0);
  }
  if (wid < 7u) { wait_vm<0>();
  ST(22);
  bar(); }
  if (tid == 0) st_agent(ws + FLAG_OFF + (b * NH + h) * FLAG_STRIDE, tag);
  ST(23);
  if (!has_tile) {
    if (bid == 249u && wid == 0u) {
#pragma nounroll
      for (unsigned i = 0; i < 4; ++i) poll_flags(ws + FLAG_OFF + (i * 64u + lane) * FLAG_STRIDE, tag);
      if (lane == 0) st_agent(ws + EPOCH_OFF, tag); }
    ST(24); ST(25); ST(26); ST(27); ST(28); ST(29); ST(30); ST(31); ST_OUT(ws, 0, 32); return; }
  const unsigned jq = (2u * g4 + 2u + wid) & 7u, G = 8u * rt + jq;
  const unsigned voffO = lane * 16u, soO = (G * NH + g4 * 8u) * 8192u;
  const char* fp = ws + FLAG_OFF + (((G * 37u) >> 8) * NH + g4 * 8u + (lane & 7u)) * FLAG_STRIDE;
  unsigned ready = 0;
  auto need = [&](unsigned m) {
    _Pragma("nounroll") for (unsigned spins = 0; (ready & m) != m && spins < SPIN_CAP; ++spins) { ready = (unsigned)__ballot(ld_agent(fp) == tag); if ((ready & m) != m) __builtin_amdgcn_s_sleep(PSLEEP); }
    asm volatile("" ::: "memory"); };
  __builtin_amdgcn_s_sleep(PDELAY);
  if (R2) { for (unsigned i = 0; i < ((wid + ix) & 3u); ++i) __builtin_amdgcn_s_sleep(R2); }
  need(1u);
  ST(24);
  const lds_cptr wb = (lds_cptr)lds + 1024u * hh + 64u * q4 + trcol;
  f32x16 acc[2] = {};
  { unsigned hd = 0, so_c = soO + hd * 8192u; lds_cptr wp = wb + ((SLOTW >> (4u * hd)) & 15u) * 16384u;
    bf16x8 of[8], lf[4][2];
    auto rdW = [&](int s2) { lf[s2 & 3][0] = trd2(wp, (s2 & 7) * 2048, (s2 & 7) * 2048 + 256); lf[s2 & 3][1] = trd2(wp, (s2 & 7) * 2048 + 512, (s2 & 7) * 2048 + 768); };
#pragma unroll
    for (int i = 0; i < 8; ++i) { if (i < WO) bload_sc1(of[i], voffO, sO, so_c + i * 1024u); else of[i] = bf16x8{}; }
    rdW(0); rdW(1);
#pragma nounroll
    for (unsigned p = 0; p < 8; ++p) {
      hd = (hd + 1u) & 7u;
      if (p == 5u) __builtin_amdgcn_s_barrier();
      const unsigned so_n = p < 7u ? soO + hd * 8192u : O_BYTES;
      static_for<8>([&](auto S) { constexpr int s2 = S.value;
        if constexpr (s2 == 8 - WO) need(1u << hd);
        if constexpr (s2 == 6) wp = wb + ((SLOTW >> (4u * hd)) & 15u) * 16384u;
        wait_vm1<WO - 1>(of[s2]);
        wait_lgkm<4>();
        acc[0] = MFMA(lf[s2 & 3][0], of[s2], acc[0]);
        rdW(s2 + 2);
        SBAR();
        acc[1] = MFMA(lf[s2 & 3][1], of[s2], acc[1]);
        SBAR();
        breload_sc1(of[(s2 + WO) & 7], voffO, sO, (s2 + WO < 8 ? so_c : so_n) + ((s2 + WO) & 7) * 1024u);
        SBAR(); });
      so_c = so_n;
    }
    wait_vm<0>();
#pragma unroll
    for (int i = 0; i < 8; ++i) pin(of[i]); }
  ST(28);
  const srd_t sX2 = mk_srd(ws + X_OFF, X_BYTES);
  const unsigned xb = (u * 8u + jq) * 4u;
  if (wid < 6u) {
#pragma unroll
    for (int i = 0; i < 4; ++i) bstore_sc1(pack8(acc[i >> 1], i & 1), voffO, sX2, (xb + g4) * 4096u + i * 1024u);
    wait_vm<0>();
    if (lane == 0) st_agent(ws + FLAG2_OFF + (xb + g4) * FLAG_STRIDE, tag);
    ST(29); ST(30);
  } else {
    __builtin_amdgcn_s_sleep(PDELAY2);
    poll_flags(ws + FLAG2_OFF + (xb + ((g4 + 1u + lane % 3u) & 3u)) * FLAG_STRIDE, tag);
    ST(29);
    bf16x8 xp[12];
#pragma unroll
    for (int i = 0; i < 12; ++i) bload_sc1(xp[i], voffO, sX2, (xb + ((g4 + 1u + (i >> 2)) & 3u)) * 4096u + (i & 3) * 1024u);
    wait_vm<0>();
#pragma unroll
    for (int j = 0; j < 12; ++j) { const int i = j & 3; pin(xp[j]); const u32x4 v = __builtin_bit_cast(u32x4, xp[j]);
#pragma unroll
      for (int e = 0; e < 4; ++e) { acc[i >> 1][8 * (i & 1) + 2 * e] += __uint_as_float(v[e] << 16); acc[i >> 1][8 * (i & 1) + 2 * e + 1] += __uint_as_float(v[e] & 0xFFFF0000u); } }
    ST(30);
    char* op = (char*)out + (size_t)(G * 32u + r) * (DM * 2u) + ct * 128u + hh * 16u;
#pragma unroll
    for (int i = 0; i < 4; ++i) *(u32x4*)(op + i * 32) = pack8(acc[i >> 1], i & 1);
  }
  ST(31); ST_OUT(ws, 0, 32);
}

extern "C" void kernel_launch(void* const* d_in, const int* in_sizes, int n_in, void* d_out, int out_size, void* d_ws, size_t ws_size, hipStream_t stream) {
  (void)in_sizes; (void)n_in; (void)out_size; (void)ws_size;
  (void)hipFuncSetAttribute((const void*)mha_kernel, hipFuncAttributeMaxDynamicSharedMemorySize, LDS_BYTES);
  const unsigned short* x = (const unsigned short*)d_in[0]; const unsigned short* kc = (const unsigned short*)d_in[1]; const unsigned short* vc = (const unsigned short*)d_in[2];
  const unsigned short* wq = (const unsigned short*)d_in[3]; const unsigned short* wo = (const unsigned short*)d_in[4];
  mha_kernel<<<256, 512, LDS_BYTES, stream>>>(x, kc, vc, wq, wo, (char*)d_ws, (unsigned short*)d_out);
}
```

```cpp
#include <hip/hip_runtime.h>
#include <stdint.h>
#include <utility>

typedef short    bf16x8 __attribute__((ext_vector_type(8)));
typedef short    s16x4  __attribute__((ext_vector_type(4)));
typedef float    f32x16 __attribute__((ext_vector_type(16)));
typedef float    f32x2  __attribute__((ext_vector_type(2)));
typedef __bf16   bf16x2 __attribute__((ext_vector_type(2)));
typedef unsigned u32x4  __attribute__((ext_vector_type(4)));
typedef __attribute__((address_space(3))) const char* lds_cptr;
typedef __attribute__((address_space(1))) unsigned long long gu64;
typedef __amdgpu_buffer_rsrc_t srd_t;

#define DI __device__ __forceinline__
#define MFMA(a, b, c) __builtin_amdgcn_mfma_f32_32x32x16_bf16((a), (b), (c), 0, 0, 0)
#define SBAR() __builtin_amdgcn_sched_barrier(0)

constexpr int NB = 8, SQ = 224, DM = 576, NH = 32, SK = 512, DH = 128;
constexpr int LDS_BYTES = 163840;
constexpr float QSCALE = 0.08838834764831845f * 1.4426950408889634f;
constexpr float SUM_THR = 4096.f;

template <int... Is, class F> DI void static_for_impl(std::integer_sequence<int, Is...>, F&& f) { (f(std::integral_constant<int, Is>{}), ...); }
template <int N, class F> DI void static_for(F&& f) { static_for_impl(std::make_integer_sequence<int, N>{}, f); }

DI unsigned rfl(unsigned x) { return __builtin_amdgcn_readfirstlane(x); }
DI unsigned cvtpk(float lo, float hi) { f32x2 v = {lo, hi}; bf16x2 b = __builtin_convertvector(v, bf16x2); return __builtin_bit_cast(unsigned, b); }
DI srd_t mk_srd(const void* p, unsigned bytes) { return __builtin_amdgcn_make_buffer_rsrc((void*)p, (short)0, (int)bytes, 0x00020000); }
DI void dma4(unsigned lds, unsigned v0, unsigned v1, unsigned v2, unsigned v3, srd_t srd, unsigned soff) {
  asm volatile("s_mov_b32 m0, %0\n\ts_nop 0\n\tbuffer_load_dwordx4 %1, %5, %6 offen lds\n\tbuffer_load_dwordx4 %2, %5, %6 offen offset:1024 lds\n\t"
               "buffer_load_dwordx4 %3, %5, %6 offen offset:2048 lds\n\tbuffer_load_dwordx4 %4, %5, %6 offen offset:3072 lds"
               :: "s"(lds), "v"(v0), "v"(v1), "v"(v2), "v"(v3), "s"(srd), "s"(soff) : "memory");
}
DI void dma4_nt(unsigned lds, unsigned v0, unsigned v1, unsigned v2, unsigned v3, srd_t srd, unsigned soff) {
  asm volatile("s_mov_b32 m0, %0\n\ts_nop 0\n\tbuffer_load_dwordx4 %1, %5, %6 offen nt lds\n\tbuffer_load_dwordx4 %2, %5, %6 offen offset:1024 nt lds\n\t"
               "buffer_load_dwordx4 %3, %5, %6 offen offset:2048 nt lds\n\tbuffer_load_dwordx4 %4, %5, %6 offen offset:3072 nt lds"
               :: "s"(lds), "v"(v0), "v"(v1), "v"(v2), "v"(v3), "s"(srd), "s"(soff) : "memory");
}
DI void bstore_sc1(const u32x4& d, unsigned voff, srd_t srd, unsigned soff) { asm volatile("buffer_store_dwordx4 %0, %1, %2, %3 offen sc1\n\ts_nop 1" :: "v"(d), "v"(voff), "s"(srd), "s"(soff) : "memory"); }
DI void bload_sc1(bf16x8& d, unsigned voff, srd_t srd, unsigned soff) { asm volatile("buffer_load_dwordx4 %0, %1, %2, %3 offen sc1" : "=v"(d) : "v"(voff), "s"(srd), "s"(soff) : "memory"); }
DI void breload_sc1(bf16x8& d, unsigned voff, srd_t srd, unsigned soff) { asm volatile("buffer_load_dwordx4 %0, %1, %2, %3 offen sc1" : "+v"(d) : "v"(voff), "s"(srd), "s"(soff) : "memory"); }
DI void pin(bf16x8& a) { asm volatile("" : "+v"(a)); }
DI void pin16(f32x16& a) { asm volatile("" : "+v"(a)); }
DI void pinf(float& a) { asm volatile("" : "+v"(a)); }
DI void pinu(unsigned& a) { asm volatile("" : "+v"(a)); }
template <int N> DI void wait_vm1(bf16x8& a) { asm volatile("s_waitcnt vmcnt(%1)" : "+v"(a) : "n"(N) : "memory"); }
template <int N> DI void wait_vm() { asm volatile("s_waitcnt vmcnt(%0)" :: "n"(N) : "memory"); }
template <int N> DI void wait_lgkm() { __builtin_amdgcn_s_waitcnt(0xC07F | (N << 8)); SBAR(); }
DI void bar_if(unsigned c) { asm volatile("s_cmp_eq_u32 %0, 0\n\ts_cbranch_scc1 1f\n\ts_waitcnt lgkmcnt(0)\n\ts_barrier\n1:" :: "s"(c) : "memory", "scc"); }
DI void bar() { asm volatile("s_waitcnt lgkmcnt(0)\n\ts_barrier" ::: "memory"); }
typedef short v4i16_t __attribute__((ext_vector_type(4)));
DI s16x4 trd(lds_cptr p) { return __builtin_amdgcn_ds_read_tr16_b64_v4i16((__attribute__((address_space(3))) v4i16_t*)p); }
DI bf16x8 trd2(lds_cptr p, int o0, int o1) { s16x4 a = trd(p + o0), b = trd(p + o1); return __builtin_shufflevector(a, b, 0, 1, 2, 3, 4, 5, 6, 7); }
DI bf16x8 rd128(lds_cptr p) { return *(const __attribute__((address_space(3))) bf16x8*)p; }
DI float swap_max(float m) { auto rr = __builtin_amdgcn_permlane32_swap(__float_as_uint(m), __float_as_uint(m), false, false); return fmaxf(__uint_as_float(rr[0]), __uint_as_float(rr[1])); }
DI float swap_add(float m) { auto rr = __builtin_amdgcn_permlane32_swap(__float_as_uint(m), __float_as_uint(m), false, false); return __uint_as_float(rr[0]) + __uint_as_float(rr[1]); }
DI float max16(const f32x16& s) { float a = fmaxf(fmaxf(s[0], s[1]), s[2]), b = fmaxf(fmaxf(s[3], s[4]), s[5]);
  a = fmaxf(fmaxf(a, s[6]), s[7]); b = fmaxf(fmaxf(b, s[8]), s[9]); a = fmaxf(fmaxf(a, s[10]), s[11]); b = fmaxf(fmaxf(b, s[12]), s[13]); a = fmaxf(fmaxf(a, s[14]), s[15]); return fmaxf(a, b); }
DI u32x4 pack8(const f32x16& a, int s, float f) { return u32x4{cvtpk(a[8 * s] * f, a[8 * s + 1] * f), cvtpk(a[8 * s + 2] * f, a[8 * s + 3] * f), cvtpk(a[8 * s + 4] * f, a[8 * s + 5] * f), cvtpk(a[8 * s + 6] * f, a[8 * s + 7] * f)}; }
DI u32x4 pack8(const f32x16& a, int s) { return u32x4{cvtpk(a[8 * s], a[8 * s + 1]), cvtpk(a[8 * s + 2], a[8 * s + 3]), cvtpk(a[8 * s + 4], a[8 * s + 5]), cvtpk(a[8 * s + 6], a[8 * s + 7])}; }

DI float expo(const f32x16& s, u32x4 (&p)[2]) {
  float e[16]; float s0 = 0.f, s1 = 0.f;
#pragma unroll
  for (int i = 0; i < 16; ++i) e[i] = __builtin_amdgcn_exp2f(s[i]);
#pragma unroll
  for (int i = 0; i < 16; i += 2) { s0 += e[i]; s1 += e[i + 1]; }
#pragma unroll
  for (int i = 0; i < 8; ++i) p[i >> 2][i & 3] = cvtpk(e[2 * i], e[2 * i + 1]);
  return s0 + s1;
}

constexpr unsigned SLOTK = 0x04204209u;
constexpr unsigned SLOTV = 0x31531531u;
constexpr unsigned SLOTW = 0x05429876u;
constexpr unsigned STG = 45056u, STG_W = 28672u;
constexpr unsigned O_BYTES = NB * SQ * NH * DH * 2;
constexpr unsigned X_OFF = O_BYTES, X_BYTES = 63u * 8u * 4u * 4096u;
constexpr unsigned FLAG_OFF = X_OFF + X_BYTES, FLAG_STRIDE = 128, FLAG2_OFF = FLAG_OFF + 256 * FLAG_STRIDE, EPOCH_OFF = FLAG2_OFF + 63 * 32 * FLAG_STRIDE;
constexpr unsigned SPIN_CAP = 1u << 20;
constexpr int RA = 7, WO = 6, A0BAR = 6, PSLEEP = 4, PDELAY = 20, PDELAY2 = 25, RAMPW = 8;
constexpr unsigned SPARE = 3u * STG;
constexpr unsigned CODE_BYTES = 19200u;
constexpr int lds_ops(int a, int b, bool last) { int n = 0; for (int i = a; i < b; ++i) n += i >= 16 ? (last ? 0 : 1) : (i >= 8 ? 2 : 1); return n; }
constexpr int pv_order(int i) { return i < 4 ? i : i < 8 ? i + 4 : i < 12 ? i - 4 : i; }
DI unsigned long long mix64(unsigned long long z) { z += 0x9E3779B97F4A7C15ull; z = (z ^ (z >> 30)) * 0xBF58476D1CE4E5B9ull; z = (z ^ (z >> 27)) * 0x94D049BB133111EBull; return z ^ (z >> 31); }
DI unsigned long long rfl64(unsigned long long v) { return ((unsigned long long)rfl((unsigned)(v >> 32)) << 32) | rfl((unsigned)v); }
DI unsigned long long ld_agent(const char* p) { return __hip_atomic_load((gu64*)p, __ATOMIC_RELAXED, __HIP_MEMORY_SCOPE_AGENT); }
DI void st_agent(char* p, unsigned long long v) { __hip_atomic_store((gu64*)p, v, __ATOMIC_RELAXED, __HIP_MEMORY_SCOPE_AGENT); }
DI void poll_flags(const char* p, unsigned long long tag) {
  _Pragma("nounroll") for (unsigned spins = 0; spins < SPIN_CAP; ++spins) { if (__all(ld_agent(p) == tag)) break; __builtin_amdgcn_s_sleep(PSLEEP); }
  asm volatile("" ::: "memory");
}

__global__ __launch_bounds__(512, 2) void mha_kernel(const unsigned short* __restrict__ x, const unsigned short* __restrict__ kc, const unsigned short* __restrict__ vc,
                                                     const unsigned short* __restrict__ wq, const unsigned short* __restrict__ wo, char* ws, unsigned short* __restrict__ out) {
  extern __shared__ __attribute__((aligned(16))) char lds[];
  unsigned long long pc0; asm volatile("s_getpc_b64 %0" : "=s"(pc0));
  const unsigned tid = threadIdx.x, lane = tid & 63u, r = lane & 31u, hh = lane >> 5;
  const unsigned wid = rfl(tid >> 6);
  const unsigned bid = blockIdx.x, xcd = bid & 7u, ix = bid >> 3;
  const unsigned b = (xcd & 1u) * 4u + (ix & 3u), h = (xcd >> 1) * 8u + (ix >> 2);
  const unsigned lds0 = (unsigned)(uintptr_t)lds;
  {
    const char* cp = (const char*)(pc0 & ~255ull) + min((ix * 8u + wid) * 128u, CODE_BYTES);
    asm volatile("s_mov_b32 m0, %0\n\ts_nop 0\n\tglobal_load_lds_dword %1, %2" :: "s"(lds0 + SPARE), "v"(0u), "s"(cp) : "memory"); }
  const srd_t sW = mk_srd((const char*)wq - 1024, DM * NH * DH * 2 + 1024);
  const srd_t sX = mk_srd(x + (size_t)b * SQ * DM, SQ * DM * 2);
  const unsigned prow = (lane >> 2) & 7u, pch = 4u * hh + (lane & 3u);
  const unsigned pchx0 = 4u * hh + ((lane & 3u) ^ ((lane >> 4) & 1u)), pchx1 = pchx0 ^ 2u;
  const unsigned vW = prow * 8192u + pch * 16u, vX0 = prow * (DM * 2u) + pchx0 * 16u, vX1 = prow * (DM * 2u) + pchx1 * 16u;
  unsigned long long ep; asm volatile("global_load_dwordx2 %0, %1, %2 sc1" : "=v"(ep) : "v"(0u), "s"(ws + EPOCH_OFF) : "memory");
  dma4(lds0 + (wid < 7u ? wid * 4096u : SPARE), vX0, vX1 + 8192u, vX0 + 16384u, vX1 + 24576u, sX, min(wid, 6u) * 32u * DM * 2u);
  dma4(lds0 + (wid >> 2) * STG + STG_W + (wid & 3u) * 4096u, vW + 1024u, vW + 128u, vW + 65536u - 1024u, vW + 65664u - 2048u, sW, (wid >> 2) * 64u * 8192u + h * 256u + (wid & 3u) * 131072u);
  const srd_t sK = mk_srd((const char*)(kc + (size_t)(b * NH + h) * SK * DH) - 1024, SK * DH * 2 + 1024);
  const srd_t sV = mk_srd((const char*)(vc + (size_t)(b * NH + h) * SK * DH) - 1024, SK * DH * 2 + 1024);
  const unsigned vK0 = prow * 256u + pchx0 * 16u, vK1 = prow * 256u + pchx1 * 16u;
  const unsigned q4 = (lane & 15u) >> 2, p4 = lane & 3u, blk = (lane >> 4) & 1u;
  const unsigned trcol = 32u * blk + 16u * (p4 & 1u) + 8u * (p4 >> 1);
  const srd_t sO = mk_srd(ws, O_BYTES);
  unsigned long long tag;
  const unsigned g4 = xcd >> 1, u0 = (xcd & 1u) * 32u + ix;
  const bool has_tile = u0 < 63u;
  const unsigned u = has_tile ? u0 : 62u, rt = u / 9u, ct = u - rt * 9u;

  if (wid == 7u) {
    auto fillQ = [&](unsigned c, unsigned stage) {
      const unsigned lw = lds0 + stage + STG_W, sw = c * 64u * 8192u + h * 256u, lx = lds0 + stage, sx = c * 128u;
#pragma unroll
      for (int i = 0; i < 4; ++i) dma4(lw + i * 4096u, vW + 1024u, vW + 128u, vW + 65536u - 1024u, vW + 65664u - 2048u, sW, sw + i * 131072u);
#pragma unroll
      for (int i = 0; i < 7; ++i) dma4(lx + i * 4096u, vX0, vX1 + 8192u, vX0 + 16384u, vX1 + 24576u, sX, sx + i * 32u * DM * 2u); };
    bar();
    { unsigned stage = 2 * STG;
#pragma nounroll
      for (unsigned c = 2; c < 9; ++c) { fillQ(c, stage); stage = stage == 2 * STG ? 0u : stage + STG;
        wait_vm<44>(); bar(); } }
    const unsigned vV = prow * 256u + pch * 16u;
    const srd_t sWo = mk_srd(wo, NH * DH * DM * 2);
    const unsigned vWo = prow * (DM * 2u) + pch * 16u;
    { unsigned j = 1, jw = 0;
#pragma nounroll
      for (unsigned n = 0; n < 10; ++n) {
        if (n == 9u) { wait_vm<0>(); bar(); }
        const unsigned je = ((unsigned)(0xFFFECA8642ull >> (4u * n)) & 15u) + 1u;
#pragma nounroll
        for (; j < je; ++j) {
          if ((j & 1u) && j != 15u) { const unsigned t = (j + 1u) >> 1, ld = lds0 + ((SLOTK >> (4u * t)) & 15u) * 16384u;
#pragma unroll
            for (int i = 0; i < 4; ++i) dma4_nt(ld + i * 4096u, vK0 + 1024u, vK0 + 128u, vK1 + 1024u, vK1 + 128u, sK, t * 16384u + i * 4096u);
          } else { const unsigned t = (j - 1u) >> 1, ld = lds0 + ((SLOTV >> (4u * t)) & 15u) * 16384u;
#pragma unroll
            for (int i = 0; i < 4; ++i) dma4_nt(ld + i * 4096u, vV + 1024u, vV + 128u, vV + 1024u, vV + 128u, sV, t * 16384u + i * 4096u); } }
        const unsigned we = (unsigned)(0x8741000000ull >> (4u * n)) & 15u;
#pragma nounroll
        for (; jw < we; ++jw) { const unsigned ld = lds0 + ((SLOTW >> (4u * jw)) & 15u) * 16384u, so = (g4 * 8u + jw) * 128u * (DM * 2u) + ct * 128u;
#pragma unroll
          for (int k = 0; k < 4; ++k) dma4(ld + k * 4096u, vWo, vWo + 8192u, vWo + 16384u, vWo + 24576u, sWo, so + k * 32u * (DM * 2u)); }
        if (n == 9u) break;
        if (n < 6u) wait_vm<32>(); else if (n == 6u) wait_vm<48>(); else wait_vm<62>();
        bar(); } }
    asm volatile("" : "+v"(ep)); tag = mix64(rfl64(ep));
  } else {
  const unsigned rsw0 = 64u * (r & 7u) + 16u * (hh ^ ((r >> 2) & 3u)), rsw1 = rsw0 ^ 32u;
  f32x16 qa[4] = {}, o[4] = {};
  asm volatile("" : "+v"(qa[0]), "+v"(qa[1]), "+v"(qa[2]), "+v"(qa[3]), "+v"(o[0]), "+v"(o[1]), "+v"(o[2]), "+v"(o[3]));
  wait_lgkm<0>();
  wait_vm<4>(); if (wid < 4u) wait_vm<0>();

  dma4(lds0 + STG + wid * 4096u, vX0, vX1 + 8192u, vX0 + 16384u, vX1 + 24576u, sX, 128u + wid * 32u * DM * 2u);
  bar();
  asm volatile("" : "+v"(ep)); ep = rfl64(ep);

  const lds_cptr kb0 = (lds_cptr)lds + 2048u * (r >> 3) + rsw0, kb1 = (lds_cptr)lds + 2048u * (r >> 3) + rsw1;
  const lds_cptr vb = (lds_cptr)lds + 64u * (4u * hh + q4) + trcol;
  bf16x8 qf[8], pre[RA];
  f32x16 negm, S0, S1; u32x4 P0[2], P1[2]; float l = 0.f;
  auto rdK = [&](lds_cptr k0, lds_cptr k1, int kb, int i) { return rd128(((i & 1) ? k1 : k0) + 8192 * kb + 512 * (i >> 1)); };
  auto rdV = [&](lds_cptr v, int kb, int j) { return trd2(v, 8192 * kb + 4096 * (j >> 2) + 512 * (j & 3), 8192 * kb + 4096 * (j >> 2) + 2048 + 512 * (j & 3)); };
  auto sm_fix = [&](float ps, f32x16& Sc, f32x16& Sn, u32x4 (&P)[2]) {
    if (__builtin_expect(__any(!(ps <= SUM_THR)), 0)) {
      const float dl = fmaxf(swap_max(max16(Sc)), 0.f), f = __builtin_amdgcn_exp2f(-dl);
#pragma unroll
      for (int i = 0; i < 16; ++i) { Sc[i] -= dl; Sn[i] -= dl; negm[i] -= dl; }
#pragma unroll
      for (int db = 0; db < 4; ++db)
#pragma unroll
        for (int i = 0; i < 16; ++i) o[db][i] *= f;
      l *= f; ps = expo(Sc, P); }
    l += ps; };
  { lds_cptr wp = (lds_cptr)lds + STG_W + 2048u * hh + 64u * q4 + trcol;
    lds_cptr xp0 = (lds_cptr)lds + wid * 4096u + 1024u * (r >> 3) + rsw0, xp1 = (lds_cptr)lds + wid * 4096u + 1024u * (r >> 3) + rsw1;
    bf16x8 xf[4], af[5];
    auto rdA = [&](int i) { return trd2(wp, 4096 * (i >> 2) + 512 * (i & 3), 4096 * (i >> 2) + 512 * (i & 3) + 256); };
    xf[0] = rd128(xp0); af[0] = rdA(0); af[1] = rdA(1); af[2] = rdA(2); af[3] = rdA(3);
    unsigned stage = 0, side = wid == 3u ? 0x78u : 0u; asm volatile("" : "+s"(side));
    for (unsigned i = 0; i < ((wid + ix) & 3u); ++i) __builtin_amdgcn_s_sleep(RAMPW);
#pragma nounroll
    for (unsigned c = 0; c < 8; ++c) {
      if (__builtin_expect((side >> c) & 1u, 0)) {
        dma4_nt(lds0 + 9u * 16384u + (c - 3u) * 4096u, vK0 + 1024u, vK0 + 128u, vK1 + 1024u, vK1 + 128u, sK, (c - 3u) * 4096u);
        if (c == 3u) {
        unsigned ln = lane; asm volatile("" : "+v"(ln));
        const unsigned k = ln & 7u, sel = ln >> 3;
        const char* wup = sel == 0u ? (const char*)vc + (size_t)(b * NH + h) * (SK * DH * 2) + k * 16384u
                        : sel == 1u ? (const char*)wo + (size_t)(g4 * 1024u + k * 128u) * (DM * 2u) + ct * 128u
                        : sel == 2u ? (const char*)out + (size_t)((8u * rt + k) * 32u) * (DM * 2u)
                        : ws + (sel == 3u ? ((b * 7u + k) * NH + h) * 8192u : sel == 4u ? ((8u * rt + k) * NH + g4 * 8u) * 8192u : sel == 5u ? X_OFF + u * 131072u + k * 16384u : FLAG_OFF + k * 32768u + 64u);
        asm volatile("s_mov_b32 m0, %0\n\ts_nop 0\n\tglobal_load_lds_dword %1, off sc1" :: "s"(lds0 + SPARE), "v"(wup) : "memory"); } }
      const unsigned nstage = stage == 2 * STG ? 0u : stage + STG; const int dlt = (int)nstage - (int)stage; stage = nstage;
      SBAR();
      static_for<12>([&](auto I) { constexpr int i = I.value;
        qa[i & 3] = MFMA(af[i % 5], xf[i >> 2], qa[i & 3]);
        af[(i + 4) % 5] = rdA(i + 4);
        if constexpr (i == 0) xf[1] = rd128(xp1); if constexpr (i == 3) xf[2] = rd128(xp0 + 512); if constexpr (i == 7) xf[3] = rd128(xp1 + 512);
        SBAR(); });
      qa[0] = MFMA(af[12 % 5], xf[3], qa[0]);
      wp += dlt; xp0 += dlt; xp1 += dlt; asm volatile("" : "+v"(wp), "+v"(xp0), "+v"(xp1));
      SBAR();
      asm volatile("s_bitcmp1_b32 %0, %1\n\ts_cbranch_scc1 1f\n\ts_waitcnt vmcnt(0)\n1:\n\ts_waitcnt lgkmcnt(0)\n\ts_barrier" :: "s"(side), "s"(c) : "memory", "scc");
      const bf16x8 xl = xf[3], a13 = af[13 % 5], a14 = af[14 % 5], a15 = af[15 % 5];
      xf[0] = rd128(xp0); af[0] = rdA(0); af[1] = rdA(1);
      SBAR();
      qa[1] = MFMA(a13, xl, qa[1]); SBAR();
      af[2] = rdA(2);
      qa[2] = MFMA(a14, xl, qa[2]); SBAR();
      af[3] = rdA(3);
      qa[3] = MFMA(a15, xl, qa[3]); SBAR();
    }
    lds_cptr k0 = kb0 + 9 * 16384, k1 = kb1 + 9 * 16384; asm volatile("" : "+v"(k0), "+v"(k1));
    bf16x8 ag[16], f[16]; u32x4 qw[8]; float qs = QSCALE; asm volatile("" : "+s"(qs));
#pragma unroll
    for (int i = 0; i < 4; ++i) ag[i] = af[i];
    auto cvq = [&](int j) { float lo = qa[j >> 3][2 * (j & 7)] * qs; pinf(lo); float hi = qa[j >> 3][2 * (j & 7) + 1] * qs; unsigned w = cvtpk(lo, hi); pinu(w); qw[j >> 2][j & 3] = w; };
    SBAR();
    static_for<24>([&](auto G) { constexpr int g = G.value, n = g + 4;
      if constexpr (g < 4) qa[g] = MFMA(ag[g], xf[0], qa[g]);
      else if constexpr (g < 16) qa[(g - 4) / 3] = MFMA(ag[g], xf[1 + (g - 4) % 3], qa[(g - 4) / 3]);
      else { qf[g - 16] = __builtin_bit_cast(bf16x8, qw[g - 16]);
        if constexpr (g == 16) { const f32x16 z = {}; S0 = MFMA(f[0], qf[0], z); } else S0 = MFMA(f[g - 16], qf[g - 16], S0); pin16(S0); }
      if constexpr (g == 0) { xf[1] = rd128(xp1); xf[2] = rd128(xp0 + 512); } if constexpr (g == 1) xf[3] = rd128(xp1 + 512);
      if constexpr (n < 16) ag[n] = rdA(4 * (1 + (n - 4) % 3) + (n - 4) / 3); else f[n - 16] = rdK(k0, k1, (n - 16) >> 3, (n - 16) & 7);
      if constexpr (g >= 9 && g < 21) { constexpr int j0 = (g - 9) * 8 / 3, j1 = (g - 8) * 8 / 3; static_for<j1 - j0>([&](auto J) { cvq(j0 + J.value); }); }
      SBAR(); });

#pragma unroll
    for (int i = 12; i < 16; ++i) f[i] = rdK(k0, k1, 1, i & 7);
    const float m = swap_max(max16(S0));
#pragma unroll
    for (int i = 0; i < 16; ++i) negm[i] = -m;
    SBAR();
    float e[16]; float s0 = 0.f, s1 = 0.f;
    static_for<9>([&](auto G) { constexpr int g = G.value;
      if constexpr (g == 0) { if (wid >= 4u) bar(); }
      if constexpr (g == A0BAR) { if (wid < 4u) bar();
        constexpr unsigned ko = ((SLOTK >> 4) & 15u) * 16384u;
#pragma unroll
        for (int i = 0; i < RA; ++i) pre[i] = rdK(kb0 + ko, kb1 + ko, 0, i);
        SBAR(); }
      if constexpr (g == 0) S1 = MFMA(f[8], qf[0], negm); else if constexpr (g < 8) S1 = MFMA(f[8 + g], qf[g], S1);
      if constexpr (g < 8) pin16(S1);
      if constexpr (g < 8) { e[2 * g] = __builtin_amdgcn_exp2f(S0[2 * g] - m); e[2 * g + 1] = __builtin_amdgcn_exp2f(S0[2 * g + 1] - m); pinf(e[2 * g]); pinf(e[2 * g + 1]); }
      if constexpr (g == 1) { s0 = e[0]; s1 = e[1]; } else if constexpr (g > 1) { s0 += e[2 * g - 2]; pinf(s0); s1 += e[2 * g - 1]; pinf(s1); }
      if constexpr (g >= 1) { unsigned w = cvtpk(e[2 * g - 2], e[2 * g - 1]); pinu(w); P0[(g - 1) >> 2][(g - 1) & 3] = w; }
      SBAR(); });
    l = s0 + s1; }

  auto unit = [&](auto KB, auto LAST, lds_cptr k0, lds_cptr k1, lds_cptr v, lds_cptr nk0, lds_cptr nk1, f32x16& Sn, f32x16& Sc, const u32x4 (&Pp)[2], u32x4 (&Pc)[2]) {
    constexpr int kb = KB.value; constexpr bool last = LAST.value;
    float e[16]; float s0 = 0.f, s1 = 0.f; bf16x8 f[16 + RA];
#pragma unroll
    for (int i = 0; i < RA; ++i) f[i] = pre[i];
    static_for<16>([&](auto G) { constexpr int g = G.value; constexpr int n = g + RA; constexpr int gb = 12;
      if constexpr (last && g == gb) { bar(); wait_lgkm<0>(); }
      else if constexpr ((g & 3) == 0) wait_lgkm<lds_ops(g + 4, g + RA, last)>();
      if constexpr (g == 0) Sn = MFMA(f[0], qf[0], negm); else if constexpr (g < 8) Sn = MFMA(f[g], qf[g], Sn);
      else o[g & 3] = MFMA(f[g], __builtin_bit_cast(bf16x8, Pp[(g >> 2) & 1]), o[g & 3]);
      if constexpr (n < 8) f[n] = rdK(k0, k1, kb, n); else if constexpr (n < 16) f[n] = rdV(v, kb, n - 8);
      else if constexpr (!last) f[n] = rdK(k0, k1, kb + 1, n - 16);
      else if constexpr (g >= gb) { constexpr int per = (RA + 15 - gb) / (16 - gb); static_for<per>([&](auto J) { constexpr int i = per * (g - gb) + J.value; if constexpr (i < RA) f[16 + i] = rdK(nk0, nk1, 0, i); }); }
      if constexpr (g == 0) { e[0] = __builtin_amdgcn_exp2f(Sc[0]); pinf(e[0]); }
      if constexpr (g < 15) { e[g + 1] = __builtin_amdgcn_exp2f(Sc[g + 1]); pinf(e[g + 1]); }
      if constexpr (g == 1) { s0 = e[0]; s1 = e[1]; } else if constexpr (g > 1) { if constexpr (g & 1) { s1 += e[g]; pinf(s1); } else { s0 += e[g]; pinf(s0); } }
      if constexpr (g & 1) { unsigned w = cvtpk(e[g - 1], e[g]); pinu(w); Pc[g >> 3][(g >> 1) & 3] = w; }
      SBAR(); });
#pragma unroll
    for (int i = 0; i < RA; ++i) pre[i] = f[16 + i];
    sm_fix(s0 + s1, Sc, Sn, Pc); };
#pragma nounroll
  for (unsigned t = 1; t < 7; ++t) {
    const unsigned ko = ((SLOTK >> (4u * t)) & 15u) * 16384u, vo = ((SLOTV >> (4u * (t - 1u))) & 15u) * 16384u, nko = ((SLOTK >> (4u * ((t + 1u) & 7u))) & 15u) * 16384u;
    SBAR();
    unit(std::integral_constant<int, 0>{}, std::false_type{}, kb0 + ko, kb1 + ko, vb + vo, kb0, kb1, S0, S1, P0, P1);
    unit(std::integral_constant<int, 1>{}, std::true_type{}, kb0 + ko, kb1 + ko, vb + vo, kb0 + nko, kb1 + nko, S1, S0, P1, P0);
  }
  const unsigned me = (b * 7u + wid) * NH + h;
  {
    constexpr unsigned ko = ((SLOTK >> 28) & 15u) * 16384u;
    const lds_cptr k0 = kb0 + ko, k1 = kb1 + ko, v6 = vb + ((SLOTV >> 24) & 15u) * 16384u, v7 = vb + ((SLOTV >> 28) & 15u) * 16384u;
    u32x4 P2[2], P3[2];
    auto scaleP = [&](u32x4 (&P)[2], float f) {
#pragma unroll
      for (int i = 0; i < 8; ++i) { const unsigned w = P[i >> 2][i & 3]; P[i >> 2][i & 3] = cvtpk(__uint_as_float(w << 16) * f, __uint_as_float(w & 0xFFFF0000u) * f); } };
    auto fix = [&](auto NP, float ps, f32x16& Sc, f32x16& Sn, u32x4 (&P)[2]) {
      if (__builtin_expect(__any(!(ps <= SUM_THR)), 0)) {
        const float f = __builtin_amdgcn_exp2f(-fmaxf(swap_max(max16(Sc)), 0.f));
        scaleP(P0, f); if constexpr (NP.value > 1) scaleP(P1, f); if constexpr (NP.value > 2) scaleP(P2, f); }
      sm_fix(ps, Sc, Sn, P); };
    auto rdVt = [&](int i) { const int j = pv_order(i), db = j >> 3, un = (j >> 1) & 3, ks = j & 1; return rdV(un < 2 ? v6 : v7, un & 1, 4 * ks + db); };
    auto pvt = [&](auto I, const bf16x8& vf) { constexpr int j = pv_order(I.value), un = (j >> 1) & 3;
      const u32x4 pb = un == 0 ? P0[j & 1] : un == 1 ? P1[j & 1] : un == 2 ? P2[j & 1] : P3[j & 1];
      o[j >> 3] = MFMA(vf, __builtin_bit_cast(bf16x8, pb), o[j >> 3]); pin16(o[j >> 3]); };
    bf16x8 f[16], w[32];
#pragma unroll
    for (int i = 0; i < RA; ++i) f[i] = pre[i];
    auto qsm = [&](auto H, f32x16& Sn, const f32x16& Sc, u32x4 (&Pc)[2]) { constexpr int hf = H.value;
      float e[16]; float s0 = 0.f, s1 = 0.f;
      static_for<9>([&](auto G) { constexpr int g = G.value, n = 8 * hf + g + RA;
        if constexpr (hf < 2 && g < 8) { if constexpr (g == 0) Sn = MFMA(f[8 * hf], qf[0], negm); else Sn = MFMA(f[8 * hf + g], qf[g], Sn);
          pin16(Sn);
          if constexpr (n < 16) f[n] = rdK(k0, k1, n >> 3, n & 7); }
        if constexpr (hf == 1 && g >= 2 && g < 8) w[g - 2] = rdVt(g - 2);
        if constexpr (hf == 2 && g < 8) { pvt(G, w[g]); w[g + 6] = rdVt(g + 6); }
        if constexpr (g < 8) { e[2 * g] = __builtin_amdgcn_exp2f(Sc[2 * g]); e[2 * g + 1] = __builtin_amdgcn_exp2f(Sc[2 * g + 1]); pinf(e[2 * g]); pinf(e[2 * g + 1]); }
        if constexpr (g == 1) { s0 = e[0]; s1 = e[1]; } else if constexpr (g > 1) { s0 += e[2 * g - 2]; pinf(s0); s1 += e[2 * g - 1]; pinf(s1); }
        if constexpr (g >= 1) { unsigned pw = cvtpk(e[2 * g - 2], e[2 * g - 1]); pinu(pw); Pc[(g - 1) >> 2][(g - 1) & 3] = pw; }
        SBAR(); });
      return s0 + s1; };
    if (wid < 4u) __builtin_amdgcn_s_setprio(1);
    SBAR();
    fix(std::integral_constant<int, 1>{}, qsm(std::integral_constant<int, 0>{}, S0, S1, P1), S1, S0, P1);
    bar_if(wid >> 2);
    fix(std::integral_constant<int, 2>{}, qsm(std::integral_constant<int, 1>{}, S1, S0, P2), S0, S1, P2);
    bar_if((wid >> 2) ^ 1u);

    fix(std::integral_constant<int, 3>{}, qsm(std::integral_constant<int, 2>{}, S0, S1, P3), S1, S0, P3);
    const float inv = __builtin_amdgcn_rcpf(swap_add(l));
    auto put = [&](int k8) { bstore_sc1(pack8(o[k8 >> 1], k8 & 1, inv), lane * 16u, sO, me * 8192u + k8 * 1024u); };
    static_for<24>([&](auto G) { constexpr int g = G.value + 8;
      pvt(std::integral_constant<int, g>{}, w[g]);
      if constexpr (g + 6 < 32) w[g + 6] = rdVt(g + 6);
      if constexpr (g == 14) put(0); if constexpr (g == 16) put(1); if constexpr (g == 19) put(2); if constexpr (g == 22) put(3); if constexpr (g == 26) put(4); if constexpr (g == 29) put(5);
      SBAR(); });

    put(6); put(7); }

  tag = mix64(ep);
  __builtin_amdgcn_s_setprio(0);
  }
  if (wid < 7u) { wait_vm<0>(); bar(); }
  if (tid == 0) st_agent(ws + FLAG_OFF + (b * NH + h) * FLAG_STRIDE, tag);

  if (!has_tile) {
    if (bid == 249u && wid == 0u) {
#pragma nounroll
      for (unsigned i = 0; i < 4; ++i) poll_flags(ws + FLAG_OFF + (i * 64u + lane) * FLAG_STRIDE, tag);
      if (lane == 0) st_agent(ws + EPOCH_OFF, tag); }
    return; }
  const unsigned jq = (2u * g4 + 2u + wid) & 7u, G = 8u * rt + jq;
  const unsigned voffO = lane * 16u, soO = (G * NH + g4 * 8u) * 8192u;
  const char* fp = ws + FLAG_OFF + (((G * 37u) >> 8) * NH + g4 * 8u + (lane & 7u)) * FLAG_STRIDE;
  unsigned ready = 0;
  auto need = [&](unsigned m) {
    _Pragma("nounroll") for (unsigned spins = 0; (ready & m) != m && spins < SPIN_CAP; ++spins) { ready = (unsigned)__ballot(ld_agent(fp) == tag); if ((ready & m) != m) __builtin_amdgcn_s_sleep(PSLEEP); }
    asm volatile("" ::: "memory"); };
  __builtin_amdgcn_s_sleep(PDELAY);
  need(1u);

  const lds_cptr wb = (lds_cptr)lds + 1024u * hh + 64u * q4 + trcol;
  f32x16 acc[2] = {};
  { unsigned hd = 0, so_c = soO + hd * 8192u; lds_cptr wp = wb + ((SLOTW >> (4u * hd)) & 15u) * 16384u;
    bf16x8 of[8], lf[4][2];
    auto rdW = [&](int s2) { lf[s2 & 3][0] = trd2(wp, (s2 & 7) * 2048, (s2 & 7) * 2048 + 256); lf[s2 & 3][1] = trd2(wp, (s2 & 7) * 2048 + 512, (s2 & 7) * 2048 + 768); };
#pragma unroll
    for (int i = 0; i < 8; ++i) { if (i < WO) bload_sc1(of[i], voffO, sO, so_c + i * 1024u); else of[i] = bf16x8{}; }
    rdW(0); rdW(1);
#pragma nounroll
    for (unsigned p = 0; p < 8; ++p) {
      hd = (hd + 1u) & 7u;
      if (p == 5u) __builtin_amdgcn_s_barrier();
      const unsigned so_n = p < 7u ? soO + hd * 8192u : O_BYTES;
      static_for<8>([&](auto S) { constexpr int s2 = S.value;
        if constexpr (s2 == 8 - WO) need(1u << hd);
        if constexpr (s2 == 6) wp = wb + ((SLOTW >> (4u * hd)) & 15u) * 16384u;
        wait_vm1<WO - 1>(of[s2]);
        wait_lgkm<4>();
        acc[0] = MFMA(lf[s2 & 3][0], of[s2], acc[0]);
        rdW(s2 + 2);
        SBAR();
        acc[1] = MFMA(lf[s2 & 3][1], of[s2], acc[1]);
        SBAR();
        breload_sc1(of[(s2 + WO) & 7], voffO, sO, (s2 + WO < 8 ? so_c : so_n) + ((s2 + WO) & 7) * 1024u);
        SBAR(); });
      so_c = so_n;
    }
    wait_vm<0>();
#pragma unroll
    for (int i = 0; i < 8; ++i) pin(of[i]); }

  const srd_t sX2 = mk_srd(ws + X_OFF, X_BYTES);
  const unsigned xb = (u * 8u + jq) * 4u;
  if (wid < 6u) {
#pragma unroll
    for (int i = 0; i < 4; ++i) bstore_sc1(pack8(acc[i >> 1], i & 1), voffO, sX2, (xb + g4) * 4096u + i * 1024u);
    wait_vm<0>();
    if (lane == 0) st_agent(ws + FLAG2_OFF + (xb + g4) * FLAG_STRIDE, tag);
  } else {
    __builtin_amdgcn_s_sleep(PDELAY2);
    poll_flags(ws + FLAG2_OFF + (xb + ((g4 + 1u + lane % 3u) & 3u)) * FLAG_STRIDE, tag);

    bf16x8 xp[12];
#pragma unroll
    for (int i = 0; i < 12; ++i) bload_sc1(xp[i], voffO, sX2, (xb + ((g4 + 1u + (i >> 2)) & 3u)) * 4096u + (i & 3) * 1024u);
    wait_vm<0>();
#pragma unroll
    for (int j = 0; j < 12; ++j) { const int i = j & 3; pin(xp[j]); const u32x4 v = __builtin_bit_cast(u32x4, xp[j]);
#pragma unroll
      for (int e = 0; e < 4; ++e) { acc[i >> 1][8 * (i & 1) + 2 * e] += __uint_as_float(v[e] << 16); acc[i >> 1][8 * (i & 1) + 2 * e + 1] += __uint_as_float(v[e] & 0xFFFF0000u); } }

    char* op = (char*)out + (size_t)(G * 32u + r) * (DM * 2u) + ct * 128u + hh * 16u;
#pragma unroll
    for (int i = 0; i < 4; ++i) *(u32x4*)(op + i * 32) = pack8(acc[i >> 1], i & 1);
  }
}

extern "C" void kernel_launch(void* const* d_in, const int* in_sizes, int n_in, void* d_out, int out_size, void* d_ws, size_t ws_size, hipStream_t stream) {
  (void)in_sizes; (void)n_in; (void)out_size; (void)ws_size;
  (void)hipFuncSetAttribute((const void*)mha_kernel, hipFuncAttributeMaxDynamicSharedMemorySize, LDS_BYTES);
  const unsigned short* x = (const unsigned short*)d_in[0]; const unsigned short* kc = (const unsigned short*)d_in[1]; const unsigned short* vc = (const unsigned short*)d_in[2];
  const unsigned short* wq = (const unsigned short*)d_in[3]; const unsigned short* wo = (const unsigned short*)d_in[4];
  mha_kernel<<<256, 512, LDS_BYTES, stream>>>(x, kc, vc, wq, wo, (char*)d_ws, (unsigned short*)d_out);
}
```

```cpp
#define SM 0x0Eu
#define SL 3
#define RW 5
#include <hip/hip_runtime.h>
#include <stdint.h>
#include <utility>

typedef short    bf16x8 __attribute__((ext_vector_type(8)));
typedef short    s16x4  __attribute__((ext_vector_type(4)));
typedef float    f32x16 __attribute__((ext_vector_type(16)));
typedef float    f32x2  __attribute__((ext_vector_type(2)));
typedef __bf16   bf16x2 __attribute__((ext_vector_type(2)));
typedef unsigned u32x4  __attribute__((ext_vector_type(4)));
typedef __attribute__((address_space(3))) const char* lds_cptr;
typedef __attribute__((address_space(1))) unsigned long long gu64;
typedef __amdgpu_buffer_rsrc_t srd_t;

#define DI __device__ __forceinline__
#define MFMA(a, b, c) __builtin_amdgcn_mfma_f32_32x32x16_bf16((a), (b), (c), 0, 0, 0)
#define SBAR() __builtin_amdgcn_sched_barrier(0)

constexpr int NB = 8, SQ = 224, DM = 576, NH = 32, SK = 512, DH = 128;
constexpr int LDS_BYTES = 163840;
constexpr float QSCALE = 0.08838834764831845f * 1.4426950408889634f;
constexpr float SUM_THR = 4096.f;

template <int... Is, class F> DI void static_for_impl(std::integer_sequence<int, Is...>, F&& f) { (f(std::integral_constant<int, Is>{}), ...); }
template <int N, class F> DI void static_for(F&& f) { static_for_impl(std::make_integer_sequence<int, N>{}, f); }

DI unsigned rfl(unsigned x) { return __builtin_amdgcn_readfirstlane(x); }
DI unsigned cvtpk(float lo, float hi) { f32x2 v = {lo, hi}; bf16x2 b = __builtin_convertvector(v, bf16x2); return __builtin_bit_cast(unsigned, b); }
DI srd_t mk_srd(const void* p, unsigned bytes) { return __builtin_amdgcn_make_buffer_rsrc((void*)p, (short)0, (int)bytes, 0x00020000); }
DI void dma4(unsigned lds, unsigned v0, unsigned v1, unsigned v2, unsigned v3, srd_t srd, unsigned soff) {
  asm volatile("s_mov_b32 m0, %0\n\ts_nop 0\n\tbuffer_load_dwordx4 %1, %5, %6 offen lds\n\tbuffer_load_dwordx4 %2, %5, %6 offen offset:1024 lds\n\t"
               "buffer_load_dwordx4 %3, %5, %6 offen offset:2048 lds\n\tbuffer_load_dwordx4 %4, %5, %6 offen offset:3072 lds"
               :: "s"(lds), "v"(v0), "v"(v1), "v"(v2), "v"(v3), "s"(srd), "s"(soff) : "memory");
}
DI void dma4_nt(unsigned lds, unsigned v0, unsigned v1, unsigned v2, unsigned v3, srd_t srd, unsigned soff) {
  asm volatile("s_mov_b32 m0, %0\n\ts_nop 0\n\tbuffer_load_dwordx4 %1, %5, %6 offen nt lds\n\tbuffer_load_dwordx4 %2, %5, %6 offen offset:1024 nt lds\n\t"
               "buffer_load_dwordx4 %3, %5, %6 offen offset:2048 nt lds\n\tbuffer_load_dwordx4 %4, %5, %6 offen offset:3072 nt lds"
               :: "s"(lds), "v"(v0), "v"(v1), "v"(v2), "v"(v3), "s"(srd), "s"(soff) : "memory");
}
DI void bstore_sc1(const u32x4& d, unsigned voff, srd_t srd, unsigned soff) { asm volatile("buffer_store_dwordx4 %0, %1, %2, %3 offen sc1\n\ts_nop 1" :: "v"(d), "v"(voff), "s"(srd), "s"(soff) : "memory"); }
DI void bload_sc1(bf16x8& d, unsigned voff, srd_t srd, unsigned soff) { asm volatile("buffer_load_dwordx4 %0, %1, %2, %3 offen sc1" : "=v"(d) : "v"(voff), "s"(srd), "s"(soff) : "memory"); }
DI void breload_sc1(bf16x8& d, unsigned voff, srd_t srd, unsigned soff) { asm volatile("buffer_load_dwordx4 %0, %1, %2, %3 offen sc1" : "+v"(d) : "v"(voff), "s"(srd), "s"(soff) : "memory"); }
DI void pin(bf16x8& a) { asm volatile("" : "+v"(a)); }
DI void pin16(f32x16& a) { asm volatile("" : "+v"(a)); }
DI void pinf(float& a) { asm volatile("" : "+v"(a)); }
DI void pinu(unsigned& a) { asm volatile("" : "+v"(a)); }
template <int N> DI void wait_vm1(bf16x8& a) { asm volatile("s_waitcnt vmcnt(%1)" : "+v"(a) : "n"(N) : "memory"); }
template <int N> DI void wait_vm() { asm volatile("s_waitcnt vmcnt(%0)" :: "n"(N) : "memory"); }
template <int N> DI void wait_lgkm() { __builtin_amdgcn_s_waitcnt(0xC07F | (N << 8)); SBAR(); }
DI void bar_if(unsigned c) { asm volatile("s_cmp_eq_u32 %0, 0\n\ts_cbranch_scc1 1f\n\ts_waitcnt lgkmcnt(0)\n\ts_barrier\n1:" :: "s"(c) : "memory", "scc"); }
DI void bar() { asm volatile("s_waitcnt lgkmcnt(0)\n\ts_barrier" ::: "memory"); }
typedef short v4i16_t __attribute__((ext_vector_type(4)));
DI s16x4 trd(lds_cptr p) { return __builtin_amdgcn_ds_read_tr16_b64_v4i16((__attribute__((address_space(3))) v4i16_t*)p); }
DI bf16x8 trd2(lds_cptr p, int o0, int o1) { s16x4 a = trd(p + o0), b = trd(p + o1); return __builtin_shufflevector(a, b, 0, 1, 2, 3, 4, 5, 6, 7); }
DI bf16x8 rd128(lds_cptr p) { return *(const __attribute__((address_space(3))) bf16x8*)p; }
DI float swap_max(float m) { auto rr = __builtin_amdgcn_permlane32_swap(__float_as_uint(m), __float_as_uint(m), false, false); return fmaxf(__uint_as_float(rr[0]), __uint_as_float(rr[1])); }
DI float swap_add(float m) { auto rr = __builtin_amdgcn_permlane32_swap(__float_as_uint(m), __float_as_uint(m), false, false); return __uint_as_float(rr[0]) + __uint_as_float(rr[1]); }
DI float max16(const f32x16& s) { float a = fmaxf(fmaxf(s[0], s[1]), s[2]), b = fmaxf(fmaxf(s[3], s[4]), s[5]);
  a = fmaxf(fmaxf(a, s[6]), s[7]); b = fmaxf(fmaxf(b, s[8]), s[9]); a = fmaxf(fmaxf(a, s[10]), s[11]); b = fmaxf(fmaxf(b, s[12]), s[13]); a = fmaxf(fmaxf(a, s[14]), s[15]); return fmaxf(a, b); }
DI u32x4 pack8(const f32x16& a, int s, float f) { return u32x4{cvtpk(a[8 * s] * f, a[8 * s + 1] * f), cvtpk(a[8 * s + 2] * f, a[8 * s + 3] * f), cvtpk(a[8 * s + 4] * f, a[8 * s + 5] * f), cvtpk(a[8 * s + 6] * f, a[8 * s + 7] * f)}; }
DI u32x4 pack8(const f32x16& a, int s) { return u32x4{cvtpk(a[8 * s], a[8 * s + 1]), cvtpk(a[8 * s + 2], a[8 * s + 3]), cvtpk(a[8 * s + 4], a[8 * s + 5]), cvtpk(a[8 * s + 6], a[8 * s + 7])}; }

DI float expo(const f32x16& s, u32x4 (&p)[2]) {
  float e[16]; float s0 = 0.f, s1 = 0.f;
#pragma unroll
  for (int i = 0; i < 16; ++i) e[i] = __builtin_amdgcn_exp2f(s[i]);
#pragma unroll
  for (int i = 0; i < 16; i += 2) { s0 += e[i]; s1 += e[i + 1]; }
#pragma unroll
  for (int i = 0; i < 8; ++i) p[i >> 2][i & 3] = cvtpk(e[2 * i], e[2 * i + 1]);
  return s0 + s1;
}

#define ST_DECL
#define ST(k)
#define ST_OUT(ws, lo, hi)

constexpr unsigned SLOTK = 0x04204209u;
constexpr unsigned SLOTV = 0x31531531u;
constexpr unsigned SLOTW = 0x05429876u;
constexpr unsigned STG = 45056u, STG_W = 28672u;
constexpr unsigned O_BYTES = NB * SQ * NH * DH * 2;
constexpr unsigned X_OFF = O_BYTES, X_BYTES = 63u * 8u * 4u * 4096u;
constexpr unsigned FLAG_OFF = X_OFF + X_BYTES, FLAG_STRIDE = 128, FLAG2_OFF = FLAG_OFF + 256 * FLAG_STRIDE, EPOCH_OFF = FLAG2_OFF + 63 * 32 * FLAG_STRIDE;
#define STRESS 0
#define R2 0
#define PD1 20
#define P2S 0
#define P2N 3u
#define FILL2 0u
#define FILL 30u
#define FILLS 0
#define MS 0u
#define PACEA 0
constexpr unsigned SPIN_CAP = 1u << 20;
constexpr int RA = 7, WO = 6, A0BAR = 6, PSLEEP = 4, PDELAY = PD1, PDELAY2 = 25, RAMPW = RW;
constexpr unsigned SPARE = 3u * STG;
constexpr unsigned CODE_BYTES = 19200u;
constexpr int lds_ops(int a, int b, bool last) { int n = 0; for (int i = a; i < b; ++i) n += i >= 16 ? (last ? 0 : 1) : (i >= 8 ? 2 : 1); return n; }
constexpr int pv_order(int i) { return i < 4 ? i : i < 8 ? i + 4 : i < 12 ? i - 4 : i; }
DI unsigned long long mix64(unsigned long long z) { z += 0x9E3779B97F4A7C15ull; z = (z ^ (z >> 30)) * 0xBF58476D1CE4E5B9ull; z = (z ^ (z >> 27)) * 0x94D049BB133111EBull; return z ^ (z >> 31); }
DI unsigned long long rfl64(unsigned long long v) { return ((unsigned long long)rfl((unsigned)(v >> 32)) << 32) | rfl((unsigned)v); }
DI unsigned long long ld_agent(const char* p) { return __hip_atomic_load((gu64*)p, __ATOMIC_RELAXED, __HIP_MEMORY_SCOPE_AGENT); }
DI void st_agent(char* p, unsigned long long v) { __hip_atomic_store((gu64*)p, v, __ATOMIC_RELAXED, __HIP_MEMORY_SCOPE_AGENT); }
DI void poll_flags(const char* p, unsigned long long tag) {
  _Pragma("nounroll") for (unsigned spins = 0; spins < SPIN_CAP; ++spins) { if (__all(ld_agent(p) == tag)) break; __builtin_amdgcn_s_sleep(PSLEEP); }
  asm volatile("" ::: "memory");
}

__global__ __launch_bounds__(512, 2) void mha_kernel(const unsigned short* __restrict__ x, const unsigned short* __restrict__ kc, const unsigned short* __restrict__ vc,
                                                     const unsigned short* __restrict__ wq, const unsigned short* __restrict__ wo, char* ws, unsigned short* __restrict__ out) {
  extern __shared__ __attribute__((aligned(16))) char lds[];
  unsigned long long pc0; asm volatile("s_getpc_b64 %0" : "=s"(pc0));
  const unsigned tid = threadIdx.x, lane = tid & 63u, r = lane & 31u, hh = lane >> 5;
  const unsigned wid = rfl(tid >> 6);
  ST_DECL ST(0);
  const unsigned bid = blockIdx.x, xcd = bid & 7u, ix = bid >> 3;
  const unsigned b = (xcd & 1u) * 4u + (ix & 3u), h = (xcd >> 1) * 8u + (ix >> 2);
  const unsigned lds0 = (unsigned)(uintptr_t)lds;
  {
    const char* cp = (const char*)(pc0 & ~255ull) + min((ix * 8u + wid) * 128u, CODE_BYTES);
    asm volatile("s_mov_b32 m0, %0\n\ts_nop 0\n\tglobal_load_lds_dword %1, %2" :: "s"(lds0 + SPARE), "v"(0u), "s"(cp) : "memory"); }
  const srd_t sW = mk_srd((const char*)wq - 1024, DM * NH * DH * 2 + 1024);
  const srd_t sX = mk_srd(x + (size_t)b * SQ * DM, SQ * DM * 2);
  const unsigned prow = (lane >> 2) & 7u, pch = 4u * hh + (lane & 3u);
  const unsigned pchx0 = 4u * hh + ((lane & 3u) ^ ((lane >> 4) & 1u)), pchx1 = pchx0 ^ 2u;
  const unsigned vW = prow * 8192u + pch * 16u, vX0 = prow * (DM * 2u) + pchx0 * 16u, vX1 = prow * (DM * 2u) + pchx1 * 16u;
  const unsigned ck0 = STRESS ? 3u * (((ix & 3u) + (ix >> 2)) % 3u) : 0u;
  unsigned long long ep; asm volatile("global_load_dwordx2 %0, %1, %2 sc1" : "=v"(ep) : "v"(0u), "s"(ws + EPOCH_OFF) : "memory");
  dma4(lds0 + (wid < 7u ? wid * 4096u : SPARE), vX0, vX1 + 8192u, vX0 + 16384u, vX1 + 24576u, sX, ck0 * 128u + min(wid, 6u) * 32u * DM * 2u);
  dma4(lds0 + (wid >> 2) * STG + STG_W + (wid & 3u) * 4096u, vW + 1024u, vW + 128u, vW + 65536u - 1024u, vW + 65664u - 2048u, sW, (ck0 + (wid >> 2)) * 64u * 8192u + h * 256u + (wid & 3u) * 131072u);
  const srd_t sK = mk_srd((const char*)(kc + (size_t)(b * NH + h) * SK * DH) - 1024, SK * DH * 2 + 1024);
  const srd_t sV = mk_srd((const char*)(vc + (size_t)(b * NH + h) * SK * DH) - 1024, SK * DH * 2 + 1024);
  const unsigned vK0 = prow * 256u + pchx0 * 16u, vK1 = prow * 256u + pchx1 * 16u;
  const unsigned q4 = (lane & 15u) >> 2, p4 = lane & 3u, blk = (lane >> 4) & 1u;
  const unsigned trcol = 32u * blk + 16u * (p4 & 1u) + 8u * (p4 >> 1);
  const srd_t sO = mk_srd(ws, O_BYTES);
  unsigned long long tag;
  const unsigned g4 = xcd >> 1, u0 = (xcd & 1u) * 32u + ix;
  const bool has_tile = u0 < 63u;
  const unsigned u = has_tile ? u0 : 62u, rt = u / 9u, ct = u - rt * 9u;

  if (wid == 7u) {
    auto fillQ = [&](unsigned c, unsigned stage) {
      const unsigned lw = lds0 + stage + STG_W, sw = c * 64u * 8192u + h * 256u, lx = lds0 + stage, sx = c * 128u;
#pragma unroll
      for (int i = 0; i < 4; ++i) dma4(lw + i * 4096u, vW + 1024u, vW + 128u, vW + 65536u - 1024u, vW + 65664u - 2048u, sW, sw + i * 131072u);
#pragma unroll
      for (int i = 0; i < 7; ++i) dma4(lx + i * 4096u, vX0, vX1 + 8192u, vX0 + 16384u, vX1 + 24576u, sX, sx + i * 32u * DM * 2u); };
    bar();
    { unsigned stage = 2 * STG;
#pragma nounroll
      for (unsigned c = 2; c < 9; ++c) { fillQ(c + ck0 >= 9u ? c + ck0 - 9u : c + ck0, stage); stage = stage == 2 * STG ? 0u : stage + STG;
        wait_vm<44>(); bar(); } }
    const unsigned vV = prow * 256u + pch * 16u;
    const srd_t sWo = mk_srd(wo, NH * DH * DM * 2);
    const unsigned vWo = prow * (DM * 2u) + pch * 16u;
    auto pace = [&](unsigned n) { if (PACEA && n < 6u) { if (n == 0u) __builtin_amdgcn_s_sleep(1); else __builtin_amdgcn_s_sleep(PACEA); } };
    { unsigned j = 1, jw = 0;
#pragma nounroll
      for (unsigned n = 0; n < 10; ++n) {
        if (n == 9u) { wait_vm<0>(); bar(); }
        const unsigned je = ((unsigned)(0xFFFECA8642ull >> (4u * n)) & 15u) + 1u;
#pragma nounroll
        for (; j < je; ++j) {
          if ((j & 1u) && j != 15u) { const unsigned t = (j + 1u) >> 1, ld = lds0 + ((SLOTK >> (4u * t)) & 15u) * 16384u;
#pragma unroll
            for (int i = 0; i < 4; ++i) dma4_nt(ld + i * 4096u, vK0 + 1024u, vK0 + 128u, vK1 + 1024u, vK1 + 128u, sK, t * 16384u + i * 4096u), pace(n);
          } else { const unsigned t = (j - 1u) >> 1, ld = lds0 + ((SLOTV >> (4u * t)) & 15u) * 16384u;
#pragma unroll
            for (int i = 0; i < 4; ++i) dma4_nt(ld + i * 4096u, vV + 1024u, vV + 128u, vV + 1024u, vV + 128u, sV, t * 16384u + i * 4096u), pace(n); } }
        const unsigned we = (unsigned)(0x8741000000ull >> (4u * n)) & 15u;
#pragma nounroll
        for (; jw < we; ++jw) { const unsigned ld = lds0 + ((SLOTW >> (4u * jw)) & 15u) * 16384u, so = (g4 * 8u + jw) * 128u * (DM * 2u) + ct * 128u;
#pragma unroll
          for (int k = 0; k < 4; ++k) dma4(ld + k * 4096u, vWo, vWo + 8192u, vWo + 16384u, vWo + 24576u, sWo, so + k * 32u * (DM * 2u)); }
        if (n == 9u) break;
        if (n < 6u) wait_vm<32>(); else if (n == 6u) wait_vm<48>(); else wait_vm<62>();
        bar(); } }
    asm volatile("" : "+v"(ep)); tag = mix64(rfl64(ep));
  } else {
  const unsigned rsw0 = 64u * (r & 7u) + 16u * (hh ^ ((r >> 2) & 3u)), rsw1 = rsw0 ^ 32u;
  f32x16 qa[4] = {}, o[4] = {};
  asm volatile("" : "+v"(qa[0]), "+v"(qa[1]), "+v"(qa[2]), "+v"(qa[3]), "+v"(o[0]), "+v"(o[1]), "+v"(o[2]), "+v"(o[3]));
  wait_lgkm<0>();
  wait_vm<4>(); if (wid < 4u) wait_vm<0>();

  dma4(lds0 + STG + wid * 4096u, vX0, vX1 + 8192u, vX0 + 16384u, vX1 + 24576u, sX, (ck0 + 1u) * 128u + wid * 32u * DM * 2u);
  bar();
  asm volatile("" : "+v"(ep)); ep = rfl64(ep);

  const lds_cptr kb0 = (lds_cptr)lds + 2048u * (r >> 3) + rsw0, kb1 = (lds_cptr)lds + 2048u * (r >> 3) + rsw1;
  const lds_cptr vb = (lds_cptr)lds + 64u * (4u * hh + q4) + trcol;
  bf16x8 qf[8], pre[RA];
  f32x16 negm, S0, S1; u32x4 P0[2], P1[2]; float l = 0.f;
  auto rdK = [&](lds_cptr k0, lds_cptr k1, int kb, int i) { return rd128(((i & 1) ? k1 : k0) + 8192 * kb + 512 * (i >> 1)); };
  auto rdV = [&](lds_cptr v, int kb, int j) { return trd2(v, 8192 * kb + 4096 * (j >> 2) + 512 * (j & 3), 8192 * kb + 4096 * (j >> 2) + 2048 + 512 * (j & 3)); };
  auto sm_fix = [&](float ps, f32x16& Sc, f32x16& Sn, u32x4 (&P)[2]) {
    if (__builtin_expect(__any(!(ps <= SUM_THR)), 0)) {
      const float dl = fmaxf(swap_max(max16(Sc)), 0.f), f = __builtin_amdgcn_exp2f(-dl);
#pragma unroll
      for (int i = 0; i < 16; ++i) { Sc[i] -= dl; Sn[i] -= dl; negm[i] -= dl; }
#pragma unroll
      for (int db = 0; db < 4; ++db)
#pragma unroll
        for (int i = 0; i < 16; ++i) o[db][i] *= f;
      l *= f; ps = expo(Sc, P); }
    l += ps; };
  { lds_cptr wp = (lds_cptr)lds + STG_W + 2048u * hh + 64u * q4 + trcol;
    lds_cptr xp0 = (lds_cptr)lds + wid * 4096u + 1024u * (r >> 3) + rsw0, xp1 = (lds_cptr)lds + wid * 4096u + 1024u * (r >> 3) + rsw1;
    bf16x8 xf[4], af[5];
    auto rdA = [&](int i) { return trd2(wp, 4096 * (i >> 2) + 512 * (i & 3), 4096 * (i >> 2) + 512 * (i & 3) + 256); };
    xf[0] = rd128(xp0); af[0] = rdA(0); af[1] = rdA(1); af[2] = rdA(2); af[3] = rdA(3);
    unsigned stage = 0, side = wid == 3u ? 0x78u : 0u; asm volatile("" : "+s"(side));
    for (unsigned i = 0; i < ((wid + ix) & 3u); ++i) __builtin_amdgcn_s_sleep(RAMPW);
#pragma nounroll
    for (unsigned c = 0; c < 8; ++c) {
      if (__builtin_expect((side >> c) & 1u, 0)) {
        dma4_nt(lds0 + 9u * 16384u + (c - 3u) * 4096u, vK0 + 1024u, vK0 + 128u, vK1 + 1024u, vK1 + 128u, sK, (c - 3u) * 4096u);
        if (c == 3u) {
        unsigned ln = lane; asm volatile("" : "+v"(ln));
        const unsigned k = ln & 7u, sel = ln >> 3;
        const char* wup = sel == 0u ? (const char*)vc + (size_t)(b * NH + h) * (SK * DH * 2) + k * 16384u
                        : sel == 1u ? (const char*)wo + (size_t)(g4 * 1024u + k * 128u) * (DM * 2u) + ct * 128u
                        : sel == 2u ? (const char*)out + (size_t)((8u * rt + k) * 32u) * (DM * 2u)
                        : ws + (sel == 3u ? ((b * 7u + k) * NH + h) * 8192u : sel == 4u ? ((8u * rt + k) * NH + g4 * 8u) * 8192u : sel == 5u ? X_OFF + u * 131072u + k * 16384u : FLAG_OFF + k * 32768u + 64u);
        asm volatile("s_mov_b32 m0, %0\n\ts_nop 0\n\tglobal_load_lds_dword %1, off sc1" :: "s"(lds0 + SPARE), "v"(wup) : "memory"); } }
      if (SM) asm volatile("s_bitcmp0_b32 %0, %1\n\ts_cbranch_scc1 1f\n\ts_sleep %2\n1:" :: "s"(SM), "s"(c), "n"(SL) : "scc");
      const unsigned nstage = stage == 2 * STG ? 0u : stage + STG; const int dlt = (int)nstage - (int)stage; stage = nstage;
      SBAR();
      static_for<12>([&](auto I) { constexpr int i = I.value; const unsigned cc = c;
        qa[i & 3] = MFMA(af[i % 5], xf[i >> 2], qa[i & 3]);
        if constexpr (MS != 0 && (i == 3 || i == 9)) asm volatile("s_bitcmp0_b32 %0, %1\n\ts_cbranch_scc1 1f\n\ts_sleep 1\n1:" :: "s"(MS), "s"(cc) : "scc");
        af[(i + 4) % 5] = rdA(i + 4);
        if constexpr (i == 0) xf[1] = rd128(xp1); if constexpr (i == 3) xf[2] = rd128(xp0 + 512); if constexpr (i == 7) xf[3] = rd128(xp1 + 512);
        SBAR(); });
      qa[0] = MFMA(af[12 % 5], xf[3], qa[0]);
      wp += dlt; xp0 += dlt; xp1 += dlt; asm volatile("" : "+v"(wp), "+v"(xp0), "+v"(xp1));
      SBAR();
      asm volatile("s_bitcmp1_b32 %0, %1\n\ts_cbranch_scc1 1f\n\ts_waitcnt vmcnt(0)\n1:\n\ts_waitcnt lgkmcnt(0)\n\ts_barrier" :: "s"(side), "s"(c) : "memory", "scc");
      const bf16x8 xl = xf[3], a13 = af[13 % 5], a14 = af[14 % 5], a15 = af[15 % 5];
      xf[0] = rd128(xp0); af[0] = rdA(0); af[1] = rdA(1);
      SBAR();
      qa[1] = MFMA(a13, xl, qa[1]); SBAR();
      af[2] = rdA(2);
      qa[2] = MFMA(a14, xl, qa[2]); SBAR();
      af[3] = rdA(3);
      qa[3] = MFMA(a15, xl, qa[3]); SBAR();
    }
    lds_cptr k0 = kb0 + 9 * 16384, k1 = kb1 + 9 * 16384; asm volatile("" : "+v"(k0), "+v"(k1));
    bf16x8 ag[16], f[16]; u32x4 qw[8]; float qs = QSCALE; asm volatile("" : "+s"(qs));
#pragma unroll
    for (int i = 0; i < 4; ++i) ag[i] = af[i];
    auto cvq = [&](int j) { float lo = qa[j >> 3][2 * (j & 7)] * qs; pinf(lo); float hi = qa[j >> 3][2 * (j & 7) + 1] * qs; unsigned w = cvtpk(lo, hi); pinu(w); qw[j >> 2][j & 3] = w; };
    SBAR();
    static_for<24>([&](auto G) { constexpr int g = G.value, n = g + 4;
      if constexpr (g < 4) qa[g] = MFMA(ag[g], xf[0], qa[g]);
      else if constexpr (g < 16) qa[(g - 4) / 3] = MFMA(ag[g], xf[1 + (g - 4) % 3], qa[(g - 4) / 3]);
      else { qf[g - 16] = __builtin_bit_cast(bf16x8, qw[g - 16]);
        if constexpr (g == 16) { const f32x16 z = {}; S0 = MFMA(f[0], qf[0], z); } else S0 = MFMA(f[g - 16], qf[g - 16], S0); pin16(S0); }
      if constexpr (g == 0) { xf[1] = rd128(xp1); xf[2] = rd128(xp0 + 512); } if constexpr (g == 1) xf[3] = rd128(xp1 + 512);
      if constexpr (n < 16) ag[n] = rdA(4 * (1 + (n - 4) % 3) + (n - 4) / 3); else f[n - 16] = rdK(k0, k1, (n - 16) >> 3, (n - 16) & 7);
      if constexpr (g >= 9 && g < 21) { constexpr int j0 = (g - 9) * 8 / 3, j1 = (g - 8) * 8 / 3; static_for<j1 - j0>([&](auto J) { cvq(j0 + J.value); }); }
      SBAR(); });

#pragma unroll
    for (int i = 12; i < 16; ++i) f[i] = rdK(k0, k1, 1, i & 7);
    const float m = swap_max(max16(S0));
#pragma unroll
    for (int i = 0; i < 16; ++i) negm[i] = -m;
    SBAR();
    float e[16]; float s0 = 0.f, s1 = 0.f;
    static_for<9>([&](auto G) { constexpr int g = G.value;
      if constexpr (g == 0) { if (wid >= 4u) bar(); }
      if constexpr (g == A0BAR) { if (wid < 4u) bar();
        constexpr unsigned ko = ((SLOTK >> 4) & 15u) * 16384u;
#pragma unroll
        for (int i = 0; i < RA; ++i) pre[i] = rdK(kb0 + ko, kb1 + ko, 0, i);
        SBAR(); }
      if constexpr (g == 0) S1 = MFMA(f[8], qf[0], negm); else if constexpr (g < 8) S1 = MFMA(f[8 + g], qf[g], S1);
      if constexpr (g < 8) pin16(S1);
      if constexpr (g < 8) { e[2 * g] = __builtin_amdgcn_exp2f(S0[2 * g] - m); e[2 * g + 1] = __builtin_amdgcn_exp2f(S0[2 * g + 1] - m); pinf(e[2 * g]); pinf(e[2 * g + 1]); }
      if constexpr (g == 1) { s0 = e[0]; s1 = e[1]; } else if constexpr (g > 1) { s0 += e[2 * g - 2]; pinf(s0); s1 += e[2 * g - 1]; pinf(s1); }
      if constexpr (g >= 1) { unsigned w = cvtpk(e[2 * g - 2], e[2 * g - 1]); pinu(w); P0[(g - 1) >> 2][(g - 1) & 3] = w; }
      SBAR(); });
    l = s0 + s1; }

  auto unit = [&](auto KB, auto LAST, lds_cptr k0, lds_cptr k1, lds_cptr v, lds_cptr nk0, lds_cptr nk1, f32x16& Sn, f32x16& Sc, const u32x4 (&Pp)[2], u32x4 (&Pc)[2]) {
    constexpr int kb = KB.value; constexpr bool last = LAST.value;
    float e[16]; float s0 = 0.f, s1 = 0.f; bf16x8 f[16 + RA];
#pragma unroll
    for (int i = 0; i < RA; ++i) f[i] = pre[i];
    static_for<16>([&](auto G) { constexpr int g = G.value; constexpr int n = g + RA; constexpr int gb = 12;
      if constexpr (last && g == gb) { bar(); wait_lgkm<0>(); }
      else if constexpr ((g & 3) == 0) wait_lgkm<lds_ops(g + 4, g + RA, last)>();
      if constexpr (g == 0) Sn = MFMA(f[0], qf[0], negm); else if constexpr (g < 8) Sn = MFMA(f[g], qf[g], Sn);
      else o[g & 3] = MFMA(f[g], __builtin_bit_cast(bf16x8, Pp[(g >> 2) & 1]), o[g & 3]);
      if constexpr (n < 8) f[n] = rdK(k0, k1, kb, n); else if constexpr (n < 16) f[n] = rdV(v, kb, n - 8);
      else if constexpr (!last) f[n] = rdK(k0, k1, kb + 1, n - 16);
      else if constexpr (g >= gb) { constexpr int per = (RA + 15 - gb) / (16 - gb); static_for<per>([&](auto J) { constexpr int i = per * (g - gb) + J.value; if constexpr (i < RA) f[16 + i] = rdK(nk0, nk1, 0, i); }); }
      if constexpr (g == 0) { e[0] = __builtin_amdgcn_exp2f(Sc[0]); pinf(e[0]); }
      if constexpr (g < 15) { e[g + 1] = __builtin_amdgcn_exp2f(Sc[g + 1]); pinf(e[g + 1]); }
      if constexpr (g == 1) { s0 = e[0]; s1 = e[1]; } else if constexpr (g > 1) { if constexpr (g & 1) { s1 += e[g]; pinf(s1); } else { s0 += e[g]; pinf(s0); } }
      if constexpr (g & 1) { unsigned w = cvtpk(e[g - 1], e[g]); pinu(w); Pc[g >> 3][(g >> 1) & 3] = w; }
      SBAR(); });
#pragma unroll
    for (int i = 0; i < RA; ++i) pre[i] = f[16 + i];
    sm_fix(s0 + s1, Sc, Sn, Pc); };
#pragma nounroll
  for (unsigned t = 1; t < 7; ++t) {
    const unsigned ko = ((SLOTK >> (4u * t)) & 15u) * 16384u, vo = ((SLOTV >> (4u * (t - 1u))) & 15u) * 16384u, nko = ((SLOTK >> (4u * ((t + 1u) & 7u))) & 15u) * 16384u;
    SBAR();
    unit(std::integral_constant<int, 0>{}, std::false_type{}, kb0 + ko, kb1 + ko, vb + vo, kb0, kb1, S0, S1, P0, P1);
    unit(std::integral_constant<int, 1>{}, std::true_type{}, kb0 + ko, kb1 + ko, vb + vo, kb0 + nko, kb1 + nko, S1, S0, P1, P0);
  }
  const unsigned me = (b * 7u + wid) * NH + h;
  {
    constexpr unsigned ko = ((SLOTK >> 28) & 15u) * 16384u;
    const lds_cptr k0 = kb0 + ko, k1 = kb1 + ko, v6 = vb + ((SLOTV >> 24) & 15u) * 16384u, v7 = vb + ((SLOTV >> 28) & 15u) * 16384u;
    u32x4 P2[2], P3[2];
    auto scaleP = [&](u32x4 (&P)[2], float f) {
#pragma unroll
      for (int i = 0; i < 8; ++i) { const unsigned w = P[i >> 2][i & 3]; P[i >> 2][i & 3] = cvtpk(__uint_as_float(w << 16) * f, __uint_as_float(w & 0xFFFF0000u) * f); } };
    auto fix = [&](auto NP, float ps, f32x16& Sc, f32x16& Sn, u32x4 (&P)[2]) {
      if (__builtin_expect(__any(!(ps <= SUM_THR)), 0)) {
        const float f = __builtin_amdgcn_exp2f(-fmaxf(swap_max(max16(Sc)), 0.f));
        scaleP(P0, f); if constexpr (NP.value > 1) scaleP(P1, f); if constexpr (NP.value > 2) scaleP(P2, f); }
      sm_fix(ps, Sc, Sn, P); };
    auto rdVt = [&](int i) { const int j = pv_order(i), db = j >> 3, un = (j >> 1) & 3, ks = j & 1; return rdV(un < 2 ? v6 : v7, un & 1, 4 * ks + db); };
    auto pvt = [&](auto I, const bf16x8& vf) { constexpr int j = pv_order(I.value), un = (j >> 1) & 3;
      const u32x4 pb = un == 0 ? P0[j & 1] : un == 1 ? P1[j & 1] : un == 2 ? P2[j & 1] : P3[j & 1];
      o[j >> 3] = MFMA(vf, __builtin_bit_cast(bf16x8, pb), o[j >> 3]); pin16(o[j >> 3]); };
    bf16x8 f[16], w[32];
#pragma unroll
    for (int i = 0; i < RA; ++i) f[i] = pre[i];
    auto qsm = [&](auto H, f32x16& Sn, const f32x16& Sc, u32x4 (&Pc)[2]) { constexpr int hf = H.value;
      float e[16]; float s0 = 0.f, s1 = 0.f;
      static_for<9>([&](auto G) { constexpr int g = G.value, n = 8 * hf + g + RA;
        if constexpr (hf < 2 && g < 8) { if constexpr (g == 0) Sn = MFMA(f[8 * hf], qf[0], negm); else Sn = MFMA(f[8 * hf + g], qf[g], Sn);
          pin16(Sn);
          if constexpr (n < 16) f[n] = rdK(k0, k1, n >> 3, n & 7); }
        if constexpr (hf == 1 && g >= 2 && g < 8) w[g - 2] = rdVt(g - 2);
        if constexpr (hf == 2 && g < 8) { pvt(G, w[g]); w[g + 6] = rdVt(g + 6); }
        if constexpr (g < 8) { e[2 * g] = __builtin_amdgcn_exp2f(Sc[2 * g]); e[2 * g + 1] = __builtin_amdgcn_exp2f(Sc[2 * g + 1]); pinf(e[2 * g]); pinf(e[2 * g + 1]); }
        if constexpr (g == 1) { s0 = e[0]; s1 = e[1]; } else if constexpr (g > 1) { s0 += e[2 * g - 2]; pinf(s0); s1 += e[2 * g - 1]; pinf(s1); }
        if constexpr (g >= 1) { unsigned pw = cvtpk(e[2 * g - 2], e[2 * g - 1]); pinu(pw); Pc[(g - 1) >> 2][(g - 1) & 3] = pw; }
        SBAR(); });
      return s0 + s1; };
    if (wid < 4u) __builtin_amdgcn_s_setprio(1);
    SBAR();
    fix(std::integral_constant<int, 1>{}, qsm(std::integral_constant<int, 0>{}, S0, S1, P1), S1, S0, P1);
    bar_if(wid >> 2);
    fix(std::integral_constant<int, 2>{}, qsm(std::integral_constant<int, 1>{}, S1, S0, P2), S0, S1, P2);
    bar_if((wid >> 2) ^ 1u);
    ST(19);

    fix(std::integral_constant<int, 3>{}, qsm(std::integral_constant<int, 2>{}, S0, S1, P3), S1, S0, P3);
    const float inv = __builtin_amdgcn_rcpf(swap_add(l));
    auto put = [&](int k8) { bstore_sc1(pack8(o[k8 >> 1], k8 & 1, inv), lane * 16u, sO, me * 8192u + k8 * 1024u); };
    static_for<24>([&](auto G) { constexpr int g = G.value + 8;
      pvt(std::integral_constant<int, g>{}, w[g]);
      if constexpr (g + 6 < 32) w[g + 6] = rdVt(g + 6);
      if constexpr (g == 14) put(0); if constexpr (g == 16) put(1); if constexpr (g == 19) put(2); if constexpr (g == 22) put(3); if constexpr (g == 26) put(4); if constexpr (g == 29) put(5);
      SBAR(); });

    put(6); put(7); }

  tag = mix64(ep);
  __builtin_amdgcn_s_setprio(0);
  }
  if (wid < 7u) { wait_vm<0>(); bar(); }
  if (tid == 0) st_agent(ws + FLAG_OFF + (b * NH + h) * FLAG_STRIDE, tag);

  if (!has_tile) {
    if (bid == 249u && wid == 0u) {
#pragma nounroll
      for (unsigned i = 0; i < 4; ++i) poll_flags(ws + FLAG_OFF + (i * 64u + lane) * FLAG_STRIDE, tag);
      if (lane == 0) st_agent(ws + EPOCH_OFF, tag); }
    ST(31); ST_OUT(ws, 0, 32); return; }
  const unsigned jq = (2u * g4 + 2u + wid) & 7u, G = 8u * rt + jq;
  const unsigned voffO = lane * 16u, soO = (G * NH + g4 * 8u) * 8192u;
  const char* fp = ws + FLAG_OFF + (((G * 37u) >> 8) * NH + g4 * 8u + (lane & 7u)) * FLAG_STRIDE;
  unsigned ready = 0;
  auto need = [&](unsigned m) {
    _Pragma("nounroll") for (unsigned spins = 0; (ready & m) != m && spins < SPIN_CAP; ++spins) { ready = (unsigned)__ballot(ld_agent(fp) == tag); if ((ready & m) != m) __builtin_amdgcn_s_sleep(PSLEEP); }
    asm volatile("" ::: "memory"); };
  auto fill = [&](unsigned n) { const lds_cptr jp = (lds_cptr)lds + 6u * 16384u + lane * 16u; bf16x8 ja = rd128(jp), jb = rd128(jp + 1024), jd = rd128(jp + 2048); f32x16 jc = {};
#pragma nounroll
    for (unsigned i = 0; i < n; i += 2) { jc = MFMA(ja, jb, jc); jc = MFMA(jd, ja, jc); pin16(jc); if (FILLS) __builtin_amdgcn_s_sleep(FILLS); } };
  if (FILL && wid < 4u) fill(FILL); else __builtin_amdgcn_s_sleep(PDELAY);
  if (R2) { for (unsigned i = 0; i < ((wid + ix) & 3u); ++i) __builtin_amdgcn_s_sleep(R2); }
  need(1u);

  const lds_cptr wb = (lds_cptr)lds + 1024u * hh + 64u * q4 + trcol;
  f32x16 acc[2] = {};
  { unsigned hd = 0, so_c = soO + hd * 8192u; lds_cptr wp = wb + ((SLOTW >> (4u * hd)) & 15u) * 16384u;
    bf16x8 of[8], lf[4][2];
    auto rdW = [&](int s2) { lf[s2 & 3][0] = trd2(wp, (s2 & 7) * 2048, (s2 & 7) * 2048 + 256); lf[s2 & 3][1] = trd2(wp, (s2 & 7) * 2048 + 512, (s2 & 7) * 2048 + 768); };
#pragma unroll
    for (int i = 0; i < 8; ++i) { if (i < WO) bload_sc1(of[i], voffO, sO, so_c + i * 1024u); else of[i] = bf16x8{}; }
    if (FILL2 && wid >= 4u) fill(FILL2);
    rdW(0); rdW(1);
#pragma nounroll
    for (unsigned p = 0; p < 8; ++p) {
      hd = (hd + 1u) & 7u;
      if (P2S && p < P2N) __builtin_amdgcn_s_sleep(P2S);
      if (p == 5u) __builtin_amdgcn_s_barrier();
      const unsigned so_n = p < 7u ? soO + hd * 8192u : O_BYTES;
      static_for<8>([&](auto S) { constexpr int s2 = S.value;
        if constexpr (s2 == 8 - WO) need(1u << hd);
        if constexpr (s2 == 6) wp = wb + ((SLOTW >> (4u * hd)) & 15u) * 16384u;
        wait_vm1<WO - 1>(of[s2]);
        wait_lgkm<4>();
        acc[0] = MFMA(lf[s2 & 3][0], of[s2], acc[0]);
        rdW(s2 + 2);
        SBAR();
        acc[1] = MFMA(lf[s2 & 3][1], of[s2], acc[1]);
        SBAR();
        breload_sc1(of[(s2 + WO) & 7], voffO, sO, (s2 + WO < 8 ? so_c : so_n) + ((s2 + WO) & 7) * 1024u);
        SBAR(); });
      so_c = so_n;
    }
    wait_vm<0>();
#pragma unroll
    for (int i = 0; i < 8; ++i) pin(of[i]); }

  const srd_t sX2 = mk_srd(ws + X_OFF, X_BYTES);
  const unsigned xb = (u * 8u + jq) * 4u;
  if (wid < 6u) {
#pragma unroll
    for (int i = 0; i < 4; ++i) bstore_sc1(pack8(acc[i >> 1], i & 1), voffO, sX2, (xb + g4) * 4096u + i * 1024u);
    wait_vm<0>();
    if (lane == 0) st_agent(ws + FLAG2_OFF + (xb + g4) * FLAG_STRIDE, tag);
  } else {
    __builtin_amdgcn_s_sleep(PDELAY2);
    poll_flags(ws + FLAG2_OFF + (xb + ((g4 + 1u + lane % 3u) & 3u)) * FLAG_STRIDE, tag);

    bf16x8 xp[12];
#pragma unroll
    for (int i = 0; i < 12; ++i) bload_sc1(xp[i], voffO, sX2, (xb + ((g4 + 1u + (i >> 2)) & 3u)) * 4096u + (i & 3) * 1024u);
    wait_vm<0>();
#pragma unroll
    for (int j = 0; j < 12; ++j) { const int i = j & 3; pin(xp[j]); const u32x4 v = __builtin_bit_cast(u32x4, xp[j]);
#pragma unroll
      for (int e = 0; e < 4; ++e) { acc[i >> 1][8 * (i & 1) + 2 * e] += __uint_as_float(v[e] << 16); acc[i >> 1][8 * (i & 1) + 2 * e + 1] += __uint_as_float(v[e] & 0xFFFF0000u); } }

    char* op = (char*)out + (size_t)(G * 32u + r) * (DM * 2u) + ct * 128u + hh * 16u;
#pragma unroll
    for (int i = 0; i < 4; ++i) *(u32x4*)(op + i * 32) = pack8(acc[i >> 1], i & 1);
  }
  ST(31); ST_OUT(ws, 0, 32);
}

extern "C" void kernel_launch(void* const* d_in, const int* in_sizes, int n_in, void* d_out, int out_size, void* d_ws, size_t ws_size, hipStream_t stream) {
  (void)in_sizes; (void)n_in; (void)out_size; (void)ws_size;
  (void)hipFuncSetAttribute((const void*)mha_kernel, hipFuncAttributeMaxDynamicSharedMemorySize, LDS_BYTES);
  const unsigned short* x = (const unsigned short*)d_in[0]; const unsigned short* kc = (const unsigned short*)d_in[1]; const unsigned short* vc = (const unsigned short*)d_in[2];
  const unsigned short* wq = (const unsigned short*)d_in[3]; const unsigned short* wo = (const unsigned short*)d_in[4];
  mha_kernel<<<256, 512, LDS_BYTES, stream>>>(x, kc, vc, wq, wo, (char*)d_ws + ((ws_size - (24u << 20)) & ~(size_t)0x1FFFFF), (unsigned short*)d_out);
}
```

```cpp
#include <hip/hip_runtime.h>
#include <stdint.h>
#include <utility>

typedef short    bf16x8 __attribute__((ext_vector_type(8)));
typedef short    s16x4  __attribute__((ext_vector_type(4)));
typedef float    f32x16 __attribute__((ext_vector_type(16)));
typedef float    f32x2  __attribute__((ext_vector_type(2)));
typedef __bf16   bf16x2 __attribute__((ext_vector_type(2)));
typedef unsigned u32x4  __attribute__((ext_vector_type(4)));
typedef __attribute__((address_space(3))) const char* lds_cptr;
typedef __attribute__((address_space(1))) unsigned long long gu64;
typedef __amdgpu_buffer_rsrc_t srd_t;

#define DI __device__ __forceinline__
#define MFMA(a, b, c) __builtin_amdgcn_mfma_f32_32x32x16_bf16((a), (b), (c), 0, 0, 0)
#define SBAR() __builtin_amdgcn_sched_barrier(0)

constexpr int NB = 8, SQ = 224, DM = 576, NH = 32, SK = 512, DH = 128;
constexpr int LDS_BYTES = 163840;
constexpr float QSCALE = 0.08838834764831845f * 1.4426950408889634f;
constexpr float SUM_THR = 4096.f;

template <int... Is, class F> DI void static_for_impl(std::integer_sequence<int, Is...>, F&& f) { (f(std::integral_constant<int, Is>{}), ...); }
template <int N, class F> DI void static_for(F&& f) { static_for_impl(std::make_integer_sequence<int, N>{}, f); }

DI unsigned rfl(unsigned x) { return __builtin_amdgcn_readfirstlane(x); }
DI unsigned cvtpk(float lo, float hi) { f32x2 v = {lo, hi}; bf16x2 b = __builtin_convertvector(v, bf16x2); return __builtin_bit_cast(unsigned, b); }
DI srd_t mk_srd(const void* p, unsigned bytes) { return __builtin_amdgcn_make_buffer_rsrc((void*)p, (short)0, (int)bytes, 0x00020000); }
DI void dma4(unsigned lds, unsigned v0, unsigned v1, unsigned v2, unsigned v3, srd_t srd, unsigned soff) {
  asm volatile("s_mov_b32 m0, %0\n\ts_nop 0\n\tbuffer_load_dwordx4 %1, %5, %6 offen lds\n\tbuffer_load_dwordx4 %2, %5, %6 offen offset:1024 lds\n\t"
               "buffer_load_dwordx4 %3, %5, %6 offen offset:2048 lds\n\tbuffer_load_dwordx4 %4, %5, %6 offen offset:3072 lds"
               :: "s"(lds), "v"(v0), "v"(v1), "v"(v2), "v"(v3), "s"(srd), "s"(soff) : "memory");
}
DI void dma4_nt(unsigned lds, unsigned v0, unsigned v1, unsigned v2, unsigned v3, srd_t srd, unsigned soff) {
  asm volatile("s_mov_b32 m0, %0\n\ts_nop 0\n\tbuffer_load_dwordx4 %1, %5, %6 offen nt lds\n\tbuffer_load_dwordx4 %2, %5, %6 offen offset:1024 nt lds\n\t"
               "buffer_load_dwordx4 %3, %5, %6 offen offset:2048 nt lds\n\tbuffer_load_dwordx4 %4, %5, %6 offen offset:3072 nt lds"
               :: "s"(lds), "v"(v0), "v"(v1), "v"(v2), "v"(v3), "s"(srd), "s"(soff) : "memory");
}
DI void bstore_sc1(const u32x4& d, unsigned voff, srd_t srd, unsigned soff) { asm volatile("buffer_store_dwordx4 %0, %1, %2, %3 offen sc1\n\ts_nop 1" :: "v"(d), "v"(voff), "s"(srd), "s"(soff) : "memory"); }
DI void bload_sc1(bf16x8& d, unsigned voff, srd_t srd, unsigned soff) { asm volatile("buffer_load_dwordx4 %0, %1, %2, %3 offen sc1" : "=v"(d) : "v"(voff), "s"(srd), "s"(soff) : "memory"); }
DI void breload_sc1(bf16x8& d, unsigned voff, srd_t srd, unsigned soff) { asm volatile("buffer_load_dwordx4 %0, %1, %2, %3 offen sc1" : "+v"(d) : "v"(voff), "s"(srd), "s"(soff) : "memory"); }
DI void pin(bf16x8& a) { asm volatile("" : "+v"(a)); }
DI void pin16(f32x16& a) { asm volatile("" : "+v"(a)); }
DI void pinf(float& a) { asm volatile("" : "+v"(a)); }
DI void pinu(unsigned& a) { asm volatile("" : "+v"(a)); }
template <int N> DI void wait_vm1(bf16x8& a) { asm volatile("s_waitcnt vmcnt(%1)" : "+v"(a) : "n"(N) : "memory"); }
template <int N> DI void wait_vm() { asm volatile("s_waitcnt vmcnt(%0)" :: "n"(N) : "memory"); }
template <int N> DI void wait_lgkm() { __builtin_amdgcn_s_waitcnt(0xC07F | (N << 8)); SBAR(); }
DI void bar_if(unsigned c) { asm volatile("s_cmp_eq_u32 %0, 0\n\ts_cbranch_scc1 1f\n\ts_waitcnt lgkmcnt(0)\n\ts_barrier\n1:" :: "s"(c) : "memory", "scc"); }
DI void bar() { asm volatile("s_waitcnt lgkmcnt(0)\n\ts_barrier" ::: "memory"); }
typedef short v4i16_t __attribute__((ext_vector_type(4)));
DI s16x4 trd(lds_cptr p) { return __builtin_amdgcn_ds_read_tr16_b64_v4i16((__attribute__((address_space(3))) v4i16_t*)p); }
DI bf16x8 trd2(lds_cptr p, int o0, int o1) { s16x4 a = trd(p + o0), b = trd(p + o1); return __builtin_shufflevector(a, b, 0, 1, 2, 3, 4, 5, 6, 7); }
DI bf16x8 rd128(lds_cptr p) { return *(const __attribute__((address_space(3))) bf16x8*)p; }
DI float swap_max(float m) { auto rr = __builtin_amdgcn_permlane32_swap(__float_as_uint(m), __float_as_uint(m), false, false); return fmaxf(__uint_as_float(rr[0]), __uint_as_float(rr[1])); }
DI float swap_add(float m) { auto rr = __builtin_amdgcn_permlane32_swap(__float_as_uint(m), __float_as_uint(m), false, false); return __uint_as_float(rr[0]) + __uint_as_float(rr[1]); }
DI float max16(const f32x16& s) { float a = fmaxf(fmaxf(s[0], s[1]), s[2]), b = fmaxf(fmaxf(s[3], s[4]), s[5]);
  a = fmaxf(fmaxf(a, s[6]), s[7]); b = fmaxf(fmaxf(b, s[8]), s[9]); a = fmaxf(fmaxf(a, s[10]), s[11]); b = fmaxf(fmaxf(b, s[12]), s[13]); a = fmaxf(fmaxf(a, s[14]), s[15]); return fmaxf(a, b); }
DI u32x4 pack8(const f32x16& a, int s, float f) { return u32x4{cvtpk(a[8 * s] * f, a[8 * s + 1] * f), cvtpk(a[8 * s + 2] * f, a[8 * s + 3] * f), cvtpk(a[8 * s + 4] * f, a[8 * s + 5] * f), cvtpk(a[8 * s + 6] * f, a[8 * s + 7] * f)}; }
DI u32x4 pack8(const f32x16& a, int s) { return u32x4{cvtpk(a[8 * s], a[8 * s + 1]), cvtpk(a[8 * s + 2], a[8 * s + 3]), cvtpk(a[8 * s + 4], a[8 * s + 5]), cvtpk(a[8 * s + 6], a[8 * s + 7])}; }

DI float expo(const f32x16& s, u32x4 (&p)[2]) {
  float e[16]; float s0 = 0.f, s1 = 0.f;
#pragma unroll
  for (int i = 0; i < 16; ++i) e[i] = __builtin_amdgcn_exp2f(s[i]);
#pragma unroll
  for (int i = 0; i < 16; i += 2) { s0 += e[i]; s1 += e[i + 1]; }
#pragma unroll
  for (int i = 0; i < 8; ++i) p[i >> 2][i & 3] = cvtpk(e[2 * i], e[2 * i + 1]);
  return s0 + s1;
}

constexpr unsigned SLOTK = 0x04204209u;
constexpr unsigned SLOTV = 0x31531531u;
constexpr unsigned SLOTW = 0x05429876u;
constexpr unsigned STG = 45056u, STG_W = 28672u;
constexpr unsigned O_BYTES = NB * SQ * NH * DH * 2;
constexpr unsigned X_OFF = O_BYTES, X_BYTES = 63u * 8u * 4u * 4096u;
constexpr unsigned FLAG_OFF = X_OFF + X_BYTES, FLAG_STRIDE = 128, FLAG2_OFF = FLAG_OFF + 256 * FLAG_STRIDE, EPOCH_OFF = FLAG2_OFF + 63 * 32 * FLAG_STRIDE;
constexpr unsigned SPIN_CAP = 1u << 20;
constexpr int RA = 7, WO = 6, A0BAR = 6, PSLEEP = 4, PDELAY = 20, PDELAY2 = 25, RAMPW = 5;
constexpr unsigned SPARE = 3u * STG;
constexpr unsigned CODE_BYTES = 19200u;
constexpr int lds_ops(int a, int b, bool last) { int n = 0; for (int i = a; i < b; ++i) n += i >= 16 ? (last ? 0 : 1) : (i >= 8 ? 2 : 1); return n; }
constexpr int pv_order(int i) { return i < 4 ? i : i < 8 ? i + 4 : i < 12 ? i - 4 : i; }
DI unsigned long long mix64(unsigned long long z) { z += 0x9E3779B97F4A7C15ull; z = (z ^ (z >> 30)) * 0xBF58476D1CE4E5B9ull; z = (z ^ (z >> 27)) * 0x94D049BB133111EBull; return z ^ (z >> 31); }
DI unsigned long long rfl64(unsigned long long v) { return ((unsigned long long)rfl((unsigned)(v >> 32)) << 32) | rfl((unsigned)v); }
DI unsigned long long ld_agent(const char* p) { return __hip_atomic_load((gu64*)p, __ATOMIC_RELAXED, __HIP_MEMORY_SCOPE_AGENT); }
DI void st_agent(char* p, unsigned long long v) { __hip_atomic_store((gu64*)p, v, __ATOMIC_RELAXED, __HIP_MEMORY_SCOPE_AGENT); }
DI void poll_flags(const char* p, unsigned long long tag) {
  _Pragma("nounroll") for (unsigned spins = 0; spins < SPIN_CAP; ++spins) { if (__all(ld_agent(p) == tag)) break; __builtin_amdgcn_s_sleep(PSLEEP); }
  asm volatile("" ::: "memory");
}

__global__ __launch_bounds__(512, 2) void mha_kernel(const unsigned short* __restrict__ x, const unsigned short* __restrict__ kc, const unsigned short* __restrict__ vc,
                                                     const unsigned short* __restrict__ wq, const unsigned short* __restrict__ wo, char* ws, unsigned short* __restrict__ out) {
  extern __shared__ __attribute__((aligned(16))) char lds[];
  unsigned long long pc0; asm volatile("s_getpc_b64 %0" : "=s"(pc0));
  const unsigned tid = threadIdx.x, lane = tid & 63u, r = lane & 31u, hh = lane >> 5;
  const unsigned wid = rfl(tid >> 6);
  const unsigned bid = blockIdx.x, xcd = bid & 7u, ix = bid >> 3;
  const unsigned b = (xcd & 1u) * 4u + (ix & 3u), h = (xcd >> 1) * 8u + (ix >> 2);
  const unsigned lds0 = (unsigned)(uintptr_t)lds;
  {
    const char* cp = (const char*)(pc0 & ~255ull) + min((ix * 8u + wid) * 128u, CODE_BYTES);
    asm volatile("s_mov_b32 m0, %0\n\ts_nop 0\n\tglobal_load_lds_dword %1, %2" :: "s"(lds0 + SPARE), "v"(0u), "s"(cp) : "memory"); }
  const srd_t sW = mk_srd((const char*)wq - 1024, DM * NH * DH * 2 + 1024);
  const srd_t sX = mk_srd(x + (size_t)b * SQ * DM, SQ * DM * 2);
  const unsigned prow = (lane >> 2) & 7u, pch = 4u * hh + (lane & 3u);
  const unsigned pchx0 = 4u * hh + ((lane & 3u) ^ ((lane >> 4) & 1u)), pchx1 = pchx0 ^ 2u;
  const unsigned vW = prow * 8192u + pch * 16u, vX0 = prow * (DM * 2u) + pchx0 * 16u, vX1 = prow * (DM * 2u) + pchx1 * 16u;
  unsigned long long ep; asm volatile("global_load_dwordx2 %0, %1, %2 sc1" : "=v"(ep) : "v"(0u), "s"(ws + EPOCH_OFF) : "memory");
  dma4(lds0 + (wid < 7u ? wid * 4096u : SPARE), vX0, vX1 + 8192u, vX0 + 16384u, vX1 + 24576u, sX, min(wid, 6u) * 32u * DM * 2u);
  dma4(lds0 + (wid >> 2) * STG + STG_W + (wid & 3u) * 4096u, vW + 1024u, vW + 128u, vW + 65536u - 1024u, vW + 65664u - 2048u, sW, (wid >> 2) * 64u * 8192u + h * 256u + (wid & 3u) * 131072u);
  const srd_t sK = mk_srd((const char*)(kc + (size_t)(b * NH + h) * SK * DH) - 1024, SK * DH * 2 + 1024);
  const srd_t sV = mk_srd((const char*)(vc + (size_t)(b * NH + h) * SK * DH) - 1024, SK * DH * 2 + 1024);
  const unsigned vK0 = prow * 256u + pchx0 * 16u, vK1 = prow * 256u + pchx1 * 16u;
  const unsigned q4 = (lane & 15u) >> 2, p4 = lane & 3u, blk = (lane >> 4) & 1u;
  const unsigned trcol = 32u * blk + 16u * (p4 & 1u) + 8u * (p4 >> 1);
  const srd_t sO = mk_srd(ws, O_BYTES);
  unsigned long long tag;
  const unsigned g4 = xcd >> 1, u0 = (xcd & 1u) * 32u + ix;
  const bool has_tile = u0 < 63u;
  const unsigned u = has_tile ? u0 : 62u, rt = u / 9u, ct = u - rt * 9u;

  if (wid == 7u) {
    auto fillQ = [&](unsigned c, unsigned stage) {
      const unsigned lw = lds0 + stage + STG_W, sw = c * 64u * 8192u + h * 256u, lx = lds0 + stage, sx = c * 128u;
#pragma unroll
      for (int i = 0; i < 4; ++i) dma4(lw + i * 4096u, vW + 1024u, vW + 128u, vW + 65536u - 1024u, vW + 65664u - 2048u, sW, sw + i * 131072u);
#pragma unroll
      for (int i = 0; i < 7; ++i) dma4(lx + i * 4096u, vX0, vX1 + 8192u, vX0 + 16384u, vX1 + 24576u, sX, sx + i * 32u * DM * 2u); };
    bar();
    { unsigned stage = 2 * STG;
#pragma nounroll
      for (unsigned c = 2; c < 9; ++c) { fillQ(c, stage); stage = stage == 2 * STG ? 0u : stage + STG;
        wait_vm<44>(); bar(); } }
    const unsigned vV = prow * 256u + pch * 16u;
    const srd_t sWo = mk_srd(wo, NH * DH * DM * 2);
    const unsigned vWo = prow * (DM * 2u) + pch * 16u;
    { unsigned j = 1, jw = 0;
#pragma nounroll
      for (unsigned n = 0; n < 10; ++n) {
        if (n == 9u) { wait_vm<0>(); bar(); }
        const unsigned je = ((unsigned)(0xFFFECA8642ull >> (4u * n)) & 15u) + 1u;
#pragma nounroll
        for (; j < je; ++j) {
          if ((j & 1u) && j != 15u) { const unsigned t = (j + 1u) >> 1, ld = lds0 + ((SLOTK >> (4u * t)) & 15u) * 16384u;
#pragma unroll
            for (int i = 0; i < 4; ++i) dma4_nt(ld + i * 4096u, vK0 + 1024u, vK0 + 128u, vK1 + 1024u, vK1 + 128u, sK, t * 16384u + i * 4096u);
          } else { const unsigned t = (j - 1u) >> 1, ld = lds0 + ((SLOTV >> (4u * t)) & 15u) * 16384u;
#pragma unroll
            for (int i = 0; i < 4; ++i) dma4_nt(ld + i * 4096u, vV + 1024u, vV + 128u, vV + 1024u, vV + 128u, sV, t * 16384u + i * 4096u); } }
        const unsigned we = (unsigned)(0x8741000000ull >> (4u * n)) & 15u;
#pragma nounroll
        for (; jw < we; ++jw) { const unsigned ld = lds0 + ((SLOTW >> (4u * jw)) & 15u) * 16384u, so = (g4 * 8u + jw) * 128u * (DM * 2u) + ct * 128u;
#pragma unroll
          for (int k = 0; k < 4; ++k) dma4(ld + k * 4096u, vWo, vWo + 8192u, vWo + 16384u, vWo + 24576u, sWo, so + k * 32u * (DM * 2u)); }
        if (n == 9u) break;
        if (n < 6u) wait_vm<32>(); else if (n == 6u) wait_vm<48>(); else wait_vm<62>();
        bar(); } }
    asm volatile("" : "+v"(ep)); tag = mix64(rfl64(ep));
  } else {
  const unsigned rsw0 = 64u * (r & 7u) + 16u * (hh ^ ((r >> 2) & 3u)), rsw1 = rsw0 ^ 32u;
  f32x16 qa[4] = {}, o[4] = {};
  asm volatile("" : "+v"(qa[0]), "+v"(qa[1]), "+v"(qa[2]), "+v"(qa[3]), "+v"(o[0]), "+v"(o[1]), "+v"(o[2]), "+v"(o[3]));
  wait_lgkm<0>();
  wait_vm<4>(); if (wid < 4u) wait_vm<0>();

  dma4(lds0 + STG + wid * 4096u, vX0, vX1 + 8192u, vX0 + 16384u, vX1 + 24576u, sX, 128u + wid * 32u * DM * 2u);
  bar();
  asm volatile("" : "+v"(ep)); ep = rfl64(ep);

  const lds_cptr kb0 = (lds_cptr)lds + 2048u * (r >> 3) + rsw0, kb1 = (lds_cptr)lds + 2048u * (r >> 3) + rsw1;
  const lds_cptr vb = (lds_cptr)lds + 64u * (4u * hh + q4) + trcol;
  bf16x8 qf[8], pre[RA];
  f32x16 negm, S0, S1; u32x4 P0[2], P1[2]; float l = 0.f;
  auto rdK = [&](lds_cptr k0, lds_cptr k1, int kb, int i) { return rd128(((i & 1) ? k1 : k0) + 8192 * kb + 512 * (i >> 1)); };
  auto rdV = [&](lds_cptr v, int kb, int j) { return trd2(v, 8192 * kb + 4096 * (j >> 2) + 512 * (j & 3), 8192 * kb + 4096 * (j >> 2) + 2048 + 512 * (j & 3)); };
  auto sm_fix = [&](float ps, f32x16& Sc, f32x16& Sn, u32x4 (&P)[2]) {
    if (__builtin_expect(__any(!(ps <= SUM_THR)), 0)) {
      const float dl = fmaxf(swap_max(max16(Sc)), 0.f), f = __builtin_amdgcn_exp2f(-dl);
#pragma unroll
      for (int i = 0; i < 16; ++i) { Sc[i] -= dl; Sn[i] -= dl; negm[i] -= dl; }
#pragma unroll
      for (int db = 0; db < 4; ++db)
#pragma unroll
        for (int i = 0; i < 16; ++i) o[db][i] *= f;
      l *= f; ps = expo(Sc, P); }
    l += ps; };
  { lds_cptr wp = (lds_cptr)lds + STG_W + 2048u * hh + 64u * q4 + trcol;
    lds_cptr xp0 = (lds_cptr)lds + wid * 4096u + 1024u * (r >> 3) + rsw0, xp1 = (lds_cptr)lds + wid * 4096u + 1024u * (r >> 3) + rsw1;
    bf16x8 xf[4], af[5];
    auto rdA = [&](int i) { return trd2(wp, 4096 * (i >> 2) + 512 * (i & 3), 4096 * (i >> 2) + 512 * (i & 3) + 256); };
    xf[0] = rd128(xp0); af[0] = rdA(0); af[1] = rdA(1); af[2] = rdA(2); af[3] = rdA(3);
    unsigned stage = 0, side = wid == 3u ? 0x78u : 0u; asm volatile("" : "+s"(side));
    for (unsigned i = 0; i < ((wid + ix) & 3u); ++i) __builtin_amdgcn_s_sleep(RAMPW);
#pragma nounroll
    for (unsigned c = 0; c < 8; ++c) {
      if (__builtin_expect((side >> c) & 1u, 0)) {
        dma4_nt(lds0 + 9u * 16384u + (c - 3u) * 4096u, vK0 + 1024u, vK0 + 128u, vK1 + 1024u, vK1 + 128u, sK, (c - 3u) * 4096u);
        if (c == 3u) {
        unsigned ln = lane; asm volatile("" : "+v"(ln));
        const unsigned k = ln & 7u, sel = ln >> 3;
        const char* wup = sel == 0u ? (const char*)vc + (size_t)(b * NH + h) * (SK * DH * 2) + k * 16384u
                        : sel == 1u ? (const char*)wo + (size_t)(g4 * 1024u + k * 128u) * (DM * 2u) + ct * 128u
                        : sel == 2u ? (const char*)out + (size_t)((8u * rt + k) * 32u) * (DM * 2u)
                        : ws + (sel == 3u ? ((b * 7u + k) * NH + h) * 8192u : sel == 4u ? ((8u * rt + k) * NH + g4 * 8u) * 8192u : sel == 5u ? X_OFF + u * 131072u + k * 16384u : FLAG_OFF + k * 32768u + 64u);
        asm volatile("s_mov_b32 m0, %0\n\ts_nop 0\n\tglobal_load_lds_dword %1, off sc1" :: "s"(lds0 + SPARE), "v"(wup) : "memory"); } }
      const unsigned nstage = stage == 2 * STG ? 0u : stage + STG; const int dlt = (int)nstage - (int)stage; stage = nstage;
      SBAR();
      static_for<12>([&](auto I) { constexpr int i = I.value;
        qa[i & 3] = MFMA(af[i % 5], xf[i >> 2], qa[i & 3]);
        af[(i + 4) % 5] = rdA(i + 4);
        if constexpr (i == 0) xf[1] = rd128(xp1); if constexpr (i == 3) xf[2] = rd128(xp0 + 512); if constexpr (i == 7) xf[3] = rd128(xp1 + 512);
        SBAR(); });
      qa[0] = MFMA(af[12 % 5], xf[3], qa[0]);
      wp += dlt; xp0 += dlt; xp1 += dlt; asm volatile("" : "+v"(wp), "+v"(xp0), "+v"(xp1));
      SBAR();
      asm volatile("s_bitcmp1_b32 %0, %1\n\ts_cbranch_scc1 1f\n\ts_waitcnt vmcnt(0)\n1:\n\ts_waitcnt lgkmcnt(0)\n\ts_barrier" :: "s"(side), "s"(c) : "memory", "scc");
      const bf16x8 xl = xf[3], a13 = af[13 % 5], a14 = af[14 % 5], a15 = af[15 % 5];
      xf[0] = rd128(xp0); af[0] = rdA(0); af[1] = rdA(1);
      SBAR();
      qa[1] = MFMA(a13, xl, qa[1]); SBAR();
      af[2] = rdA(2);
      qa[2] = MFMA(a14, xl, qa[2]); SBAR();
      af[3] = rdA(3);
      qa[3] = MFMA(a15, xl, qa[3]); SBAR();
    }
    lds_cptr k0 = kb0 + 9 * 16384, k1 = kb1 + 9 * 16384; asm volatile("" : "+v"(k0), "+v"(k1));
    bf16x8 ag[16], f[16]; u32x4 qw[8]; float qs = QSCALE; asm volatile("" : "+s"(qs));
#pragma unroll
    for (int i = 0; i < 4; ++i) ag[i] = af[i];
    auto cvq = [&](int j) { float lo = qa[j >> 3][2 * (j & 7)] * qs; pinf(lo); float hi = qa[j >> 3][2 * (j & 7) + 1] * qs; unsigned w = cvtpk(lo, hi); pinu(w); qw[j >> 2][j & 3] = w; };
    SBAR();
    static_for<24>([&](auto G) { constexpr int g = G.value, n = g + 4;
      if constexpr (g < 4) qa[g] = MFMA(ag[g], xf[0], qa[g]);
      else if constexpr (g < 16) qa[(g - 4) / 3] = MFMA(ag[g], xf[1 + (g - 4) % 3], qa[(g - 4) / 3]);
      else { qf[g - 16] = __builtin_bit_cast(bf16x8, qw[g - 16]);
        if constexpr (g == 16) { const f32x16 z = {}; S0 = MFMA(f[0], qf[0], z); } else S0 = MFMA(f[g - 16], qf[g - 16], S0); pin16(S0); }
      if constexpr (g == 0) { xf[1] = rd128(xp1); xf[2] = rd128(xp0 + 512); } if constexpr (g == 1) xf[3] = rd128(xp1 + 512);
      if constexpr (n < 16) ag[n] = rdA(4 * (1 + (n - 4) % 3) + (n - 4) / 3); else f[n - 16] = rdK(k0, k1, (n - 16) >> 3, (n - 16) & 7);
      if constexpr (g >= 9 && g < 21) { constexpr int j0 = (g - 9) * 8 / 3, j1 = (g - 8) * 8 / 3; static_for<j1 - j0>([&](auto J) { cvq(j0 + J.value); }); }
      SBAR(); });

#pragma unroll
    for (int i = 12; i < 16; ++i) f[i] = rdK(k0, k1, 1, i & 7);
    const float m = swap_max(max16(S0));
#pragma unroll
    for (int i = 0; i < 16; ++i) negm[i] = -m;
    SBAR();
    float e[16]; float s0 = 0.f, s1 = 0.f;
    static_for<9>([&](auto G) { constexpr int g = G.value;
      if constexpr (g == 0) { if (wid >= 4u) bar(); }
      if constexpr (g == A0BAR) { if (wid < 4u) bar();
        constexpr unsigned ko = ((SLOTK >> 4) & 15u) * 16384u;
#pragma unroll
        for (int i = 0; i < RA; ++i) pre[i] = rdK(kb0 + ko, kb1 + ko, 0, i);
        SBAR(); }
      if constexpr (g == 0) S1 = MFMA(f[8], qf[0], negm); else if constexpr (g < 8) S1 = MFMA(f[8 + g], qf[g], S1);
      if constexpr (g < 8) pin16(S1);
      if constexpr (g < 8) { e[2 * g] = __builtin_amdgcn_exp2f(S0[2 * g] - m); e[2 * g + 1] = __builtin_amdgcn_exp2f(S0[2 * g + 1] - m); pinf(e[2 * g]); pinf(e[2 * g + 1]); }
      if constexpr (g == 1) { s0 = e[0]; s1 = e[1]; } else if constexpr (g > 1) { s0 += e[2 * g - 2]; pinf(s0); s1 += e[2 * g - 1]; pinf(s1); }
      if constexpr (g >= 1) { unsigned w = cvtpk(e[2 * g - 2], e[2 * g - 1]); pinu(w); P0[(g - 1) >> 2][(g - 1) & 3] = w; }
      SBAR(); });
    l = s0 + s1; }

  auto unit = [&](auto KB, auto LAST, lds_cptr k0, lds_cptr k1, lds_cptr v, lds_cptr nk0, lds_cptr nk1, f32x16& Sn, f32x16& Sc, const u32x4 (&Pp)[2], u32x4 (&Pc)[2]) {
    constexpr int kb = KB.value; constexpr bool last = LAST.value;
    float e[16]; float s0 = 0.f, s1 = 0.f; bf16x8 f[16 + RA];
#pragma unroll
    for (int i = 0; i < RA; ++i) f[i] = pre[i];
    static_for<16>([&](auto G) { constexpr int g = G.value; constexpr int n = g + RA; constexpr int gb = 12;
      if constexpr (last && g == gb) { bar(); wait_lgkm<0>(); }
      else if constexpr ((g & 3) == 0) wait_lgkm<lds_ops(g + 4, g + RA, last)>();
      if constexpr (g == 0) Sn = MFMA(f[0], qf[0], negm); else if constexpr (g < 8) Sn = MFMA(f[g], qf[g], Sn);
      else o[g & 3] = MFMA(f[g], __builtin_bit_cast(bf16x8, Pp[(g >> 2) & 1]), o[g & 3]);
      if constexpr (n < 8) f[n] = rdK(k0, k1, kb, n); else if constexpr (n < 16) f[n] = rdV(v, kb, n - 8);
      else if constexpr (!last) f[n] = rdK(k0, k1, kb + 1, n - 16);
      else if constexpr (g >= gb) { constexpr int per = (RA + 15 - gb) / (16 - gb); static_for<per>([&](auto J) { constexpr int i = per * (g - gb) + J.value; if constexpr (i < RA) f[16 + i] = rdK(nk0, nk1, 0, i); }); }
      if constexpr (g == 0) { e[0] = __builtin_amdgcn_exp2f(Sc[0]); pinf(e[0]); }
      if constexpr (g < 15) { e[g + 1] = __builtin_amdgcn_exp2f(Sc[g + 1]); pinf(e[g + 1]); }
      if constexpr (g == 1) { s0 = e[0]; s1 = e[1]; } else if constexpr (g > 1) { if constexpr (g & 1) { s1 += e[g]; pinf(s1); } else { s0 += e[g]; pinf(s0); } }
      if constexpr (g & 1) { unsigned w = cvtpk(e[g - 1], e[g]); pinu(w); Pc[g >> 3][(g >> 1) & 3] = w; }
      SBAR(); });
#pragma unroll
    for (int i = 0; i < RA; ++i) pre[i] = f[16 + i];
    sm_fix(s0 + s1, Sc, Sn, Pc); };
#pragma nounroll
  for (unsigned t = 1; t < 7; ++t) {
    const unsigned ko = ((SLOTK >> (4u * t)) & 15u) * 16384u, vo = ((SLOTV >> (4u * (t - 1u))) & 15u) * 16384u, nko = ((SLOTK >> (4u * ((t + 1u) & 7u))) & 15u) * 16384u;
    SBAR();
    unit(std::integral_constant<int, 0>{}, std::false_type{}, kb0 + ko, kb1 + ko, vb + vo, kb0, kb1, S0, S1, P0, P1);
    unit(std::integral_constant<int, 1>{}, std::true_type{}, kb0 + ko, kb1 + ko, vb + vo, kb0 + nko, kb1 + nko, S1, S0, P1, P0);
  }
  const unsigned me = (b * 7u + wid) * NH + h;
  {
    constexpr unsigned ko = ((SLOTK >> 28) & 15u) * 16384u;
    const lds_cptr k0 = kb0 + ko, k1 = kb1 + ko, v6 = vb + ((SLOTV >> 24) & 15u) * 16384u, v7 = vb + ((SLOTV >> 28) & 15u) * 16384u;
    u32x4 P2[2], P3[2];
    auto scaleP = [&](u32x4 (&P)[2], float f) {
#pragma unroll
      for (int i = 0; i < 8; ++i) { const unsigned w = P[i >> 2][i & 3]; P[i >> 2][i & 3] = cvtpk(__uint_as_float(w << 16) * f, __uint_as_float(w & 0xFFFF0000u) * f); } };
    auto fix = [&](auto NP, float ps, f32x16& Sc, f32x16& Sn, u32x4 (&P)[2]) {
      if (__builtin_expect(__any(!(ps <= SUM_THR)), 0)) {
        const float f = __builtin_amdgcn_exp2f(-fmaxf(swap_max(max16(Sc)), 0.f));
        scaleP(P0, f); if constexpr (NP.value > 1) scaleP(P1, f); if constexpr (NP.value > 2) scaleP(P2, f); }
      sm_fix(ps, Sc, Sn, P); };
    auto rdVt = [&](int i) { const int j = pv_order(i), db = j >> 3, un = (j >> 1) & 3, ks = j & 1; return rdV(un < 2 ? v6 : v7, un & 1, 4 * ks + db); };
    auto pvt = [&](auto I, const bf16x8& vf) { constexpr int j = pv_order(I.value), un = (j >> 1) & 3;
      const u32x4 pb = un == 0 ? P0[j & 1] : un == 1 ? P1[j & 1] : un == 2 ? P2[j & 1] : P3[j & 1];
      o[j >> 3] = MFMA(vf, __builtin_bit_cast(bf16x8, pb), o[j >> 3]); pin16(o[j >> 3]); };
    bf16x8 f[16], w[32];
#pragma unroll
    for (int i = 0; i < RA; ++i) f[i] = pre[i];
    auto qsm = [&](auto H, f32x16& Sn, const f32x16& Sc, u32x4 (&Pc)[2]) { constexpr int hf = H.value;
      float e[16]; float s0 = 0.f, s1 = 0.f;
      static_for<9>([&](auto G) { constexpr int g = G.value, n = 8 * hf + g + RA;
        if constexpr (hf < 2 && g < 8) { if constexpr (g == 0) Sn = MFMA(f[8 * hf], qf[0], negm); else Sn = MFMA(f[8 * hf + g], qf[g], Sn);
          pin16(Sn);
          if constexpr (n < 16) f[n] = rdK(k0, k1, n >> 3, n & 7); }
        if constexpr (hf == 1 && g >= 2 && g < 8) w[g - 2] = rdVt(g - 2);
        if constexpr (hf == 2 && g < 8) { pvt(G, w[g]); w[g + 6] = rdVt(g + 6); }
        if constexpr (g < 8) { e[2 * g] = __builtin_amdgcn_exp2f(Sc[2 * g]); e[2 * g + 1] = __builtin_amdgcn_exp2f(Sc[2 * g + 1]); pinf(e[2 * g]); pinf(e[2 * g + 1]); }
        if constexpr (g == 1) { s0 = e[0]; s1 = e[1]; } else if constexpr (g > 1) { s0 += e[2 * g - 2]; pinf(s0); s1 += e[2 * g - 1]; pinf(s1); }
        if constexpr (g >= 1) { unsigned pw = cvtpk(e[2 * g - 2], e[2 * g - 1]); pinu(pw); Pc[(g - 1) >> 2][(g - 1) & 3] = pw; }
        SBAR(); });
      return s0 + s1; };
    if (wid < 4u) __builtin_amdgcn_s_setprio(1);
    SBAR();
    fix(std::integral_constant<int, 1>{}, qsm(std::integral_constant<int, 0>{}, S0, S1, P1), S1, S0, P1);
    bar_if(wid >> 2);
    fix(std::integral_constant<int, 2>{}, qsm(std::integral_constant<int, 1>{}, S1, S0, P2), S0, S1, P2);
    bar_if((wid >> 2) ^ 1u);

    fix(std::integral_constant<int, 3>{}, qsm(std::integral_constant<int, 2>{}, S0, S1, P3), S1, S0, P3);
    const float inv = __builtin_amdgcn_rcpf(swap_add(l));
    auto put = [&](int k8) { bstore_sc1(pack8(o[k8 >> 1], k8 & 1, inv), lane * 16u, sO, me * 8192u + k8 * 1024u); };
    static_for<24>([&](auto G) { constexpr int g = G.value + 8;
      pvt(std::integral_constant<int, g>{}, w[g]);
      if constexpr (g + 6 < 32) w[g + 6] = rdVt(g + 6);
      if constexpr (g == 14) put(0); if constexpr (g == 16) put(1); if constexpr (g == 19) put(2); if constexpr (g == 22) put(3); if constexpr (g == 26) put(4); if constexpr (g == 29) put(5);
      SBAR(); });

    put(6); put(7); }

  tag = mix64(ep);
  __builtin_amdgcn_s_setprio(0);
  }
  if (wid < 7u) { wait_vm<0>(); bar(); }
  if (tid == 0) st_agent(ws + FLAG_OFF + (b * NH + h) * FLAG_STRIDE, tag);

  if (!has_tile) {
    if (bid == 249u && wid == 0u) {
#pragma nounroll
      for (unsigned i = 0; i < 4; ++i) poll_flags(ws + FLAG_OFF + (i * 64u + lane) * FLAG_STRIDE, tag);
      if (lane == 0) st_agent(ws + EPOCH_OFF, tag); }
    return; }
  const unsigned jq = (2u * g4 + 2u + wid) & 7u, G = 8u * rt + jq;
  const unsigned voffO = lane * 16u, soO = (G * NH + g4 * 8u) * 8192u;
  const char* fp = ws + FLAG_OFF + (((G * 37u) >> 8) * NH + g4 * 8u + (lane & 7u)) * FLAG_STRIDE;
  unsigned ready = 0;
  auto need = [&](unsigned m) {
    _Pragma("nounroll") for (unsigned spins = 0; (ready & m) != m && spins < SPIN_CAP; ++spins) { ready = (unsigned)__ballot(ld_agent(fp) == tag); if ((ready & m) != m) __builtin_amdgcn_s_sleep(PSLEEP); }
    asm volatile("" ::: "memory"); };
  __builtin_amdgcn_s_sleep(PDELAY);
  need(1u);

  const lds_cptr wb = (lds_cptr)lds + 1024u * hh + 64u * q4 + trcol;
  f32x16 acc[2] = {};
  { unsigned hd = 0, so_c = soO + hd * 8192u; lds_cptr wp = wb + ((SLOTW >> (4u * hd)) & 15u) * 16384u;
    bf16x8 of[8], lf[4][2];
    auto rdW = [&](int s2) { lf[s2 & 3][0] = trd2(wp, (s2 & 7) * 2048, (s2 & 7) * 2048 + 256); lf[s2 & 3][1] = trd2(wp, (s2 & 7) * 2048 + 512, (s2 & 7) * 2048 + 768); };
#pragma unroll
    for (int i = 0; i < 8; ++i) { if (i < WO) bload_sc1(of[i], voffO, sO, so_c + i * 1024u); else of[i] = bf16x8{}; }
    rdW(0); rdW(1);
#pragma nounroll
    for (unsigned p = 0; p < 8; ++p) {
      hd = (hd + 1u) & 7u;
      if (p == 5u) __builtin_amdgcn_s_barrier();
      const unsigned so_n = p < 7u ? soO + hd * 8192u : O_BYTES;
      static_for<8>([&](auto S) { constexpr int s2 = S.value;
        if constexpr (s2 == 8 - WO) need(1u << hd);
        if constexpr (s2 == 6) wp = wb + ((SLOTW >> (4u * hd)) & 15u) * 16384u;
        wait_vm1<WO - 1>(of[s2]);
        wait_lgkm<4>();
        acc[0] = MFMA(lf[s2 & 3][0], of[s2], acc[0]);
        rdW(s2 + 2);
        SBAR();
        acc[1] = MFMA(lf[s2 & 3][1], of[s2], acc[1]);
        SBAR();
        breload_sc1(of[(s2 + WO) & 7], voffO, sO, (s2 + WO < 8 ? so_c : so_n) + ((s2 + WO) & 7) * 1024u);
        SBAR(); });
      so_c = so_n;
    }
    wait_vm<0>();
#pragma unroll
    for (int i = 0; i < 8; ++i) pin(of[i]); }

  const srd_t sX2 = mk_srd(ws + X_OFF, X_BYTES);
  const unsigned xb = (u * 8u + jq) * 4u;
  if (wid < 6u) {
#pragma unroll
    for (int i = 0; i < 4; ++i) bstore_sc1(pack8(acc[i >> 1], i & 1), voffO, sX2, (xb + g4) * 4096u + i * 1024u);
    wait_vm<0>();
    if (lane == 0) st_agent(ws + FLAG2_OFF + (xb + g4) * FLAG_STRIDE, tag);
  } else {
    __builtin_amdgcn_s_sleep(PDELAY2);
    poll_flags(ws + FLAG2_OFF + (xb + ((g4 + 1u + lane % 3u) & 3u)) * FLAG_STRIDE, tag);

    bf16x8 xp[12];
#pragma unroll
    for (int i = 0; i < 12; ++i) bload_sc1(xp[i], voffO, sX2, (xb + ((g4 + 1u + (i >> 2)) & 3u)) * 4096u + (i & 3) * 1024u);
    wait_vm<0>();
#pragma unroll
    for (int j = 0; j < 12; ++j) { const int i = j & 3; pin(xp[j]); const u32x4 v = __builtin_bit_cast(u32x4, xp[j]);
#pragma unroll
      for (int e = 0; e < 4; ++e) { acc[i >> 1][8 * (i & 1) + 2 * e] += __uint_as_float(v[e] << 16); acc[i >> 1][8 * (i & 1) + 2 * e + 1] += __uint_as_float(v[e] & 0xFFFF0000u); } }

    char* op = (char*)out + (size_t)(G * 32u + r) * (DM * 2u) + ct * 128u + hh * 16u;
#pragma unroll
    for (int i = 0; i < 4; ++i) *(u32x4*)(op + i * 32) = pack8(acc[i >> 1], i & 1);
  }
}

extern "C" void kernel_launch(void* const* d_in, const int* in_sizes, int n_in, void* d_out, int out_size, void* d_ws, size_t ws_size, hipStream_t stream) {
  (void)in_sizes; (void)n_in; (void)out_size; (void)ws_size;
  (void)hipFuncSetAttribute((const void*)mha_kernel, hipFuncAttributeMaxDynamicSharedMemorySize, LDS_BYTES);
  const unsigned short* x = (const unsigned short*)d_in[0]; const unsigned short* kc = (const unsigned short*)d_in[1]; const unsigned short* vc = (const unsigned short*)d_in[2];
  const unsigned short* wq = (const unsigned short*)d_in[3]; const unsigned short* wo = (const unsigned short*)d_in[4];
  mha_kernel<<<256, 512, LDS_BYTES, stream>>>(x, kc, vc, wq, wo, (char*)d_ws, (unsigned short*)d_out);
}
```

```cpp
#include <hip/hip_runtime.h>
#include <stdint.h>
#include <utility>

typedef short    bf16x8 __attribute__((ext_vector_type(8)));
typedef short    s16x4  __attribute__((ext_vector_type(4)));
typedef float    f32x16 __attribute__((ext_vector_type(16)));
typedef float    f32x2  __attribute__((ext_vector_type(2)));
typedef __bf16   bf16x2 __attribute__((ext_vector_type(2)));
typedef unsigned u32x4  __attribute__((ext_vector_type(4)));
typedef __attribute__((address_space(3))) const char* lds_cptr;
typedef __attribute__((address_space(1))) unsigned long long gu64;
typedef __amdgpu_buffer_rsrc_t srd_t;

#define DI __device__ __forceinline__
#define MFMA(a, b, c) __builtin_amdgcn_mfma_f32_32x32x16_bf16((a), (b), (c), 0, 0, 0)
#define SBAR() __builtin_amdgcn_sched_barrier(0)

constexpr int NB = 8, SQ = 224, DM = 576, NH = 32, SK = 512, DH = 128;
constexpr int LDS_BYTES = 163840;
constexpr float QSCALE = 0.08838834764831845f * 1.4426950408889634f;
constexpr float SUM_THR = 4096.f;

template <int... Is, class F> DI void static_for_impl(std::integer_sequence<int, Is...>, F&& f) { (f(std::integral_constant<int, Is>{}), ...); }
template <int N, class F> DI void static_for(F&& f) { static_for_impl(std::make_integer_sequence<int, N>{}, f); }

DI unsigned rfl(unsigned x) { return __builtin_amdgcn_readfirstlane(x); }
DI unsigned cvtpk(float lo, float hi) { f32x2 v = {lo, hi}; bf16x2 b = __builtin_convertvector(v, bf16x2); return __builtin_bit_cast(unsigned, b); }
DI srd_t mk_srd(const void* p, unsigned bytes) { return __builtin_amdgcn_make_buffer_rsrc((void*)p, (short)0, (int)bytes, 0x00020000); }
DI void dma4(unsigned lds, unsigned v0, unsigned v1, unsigned v2, unsigned v3, srd_t srd, unsigned soff) {
  asm volatile("s_mov_b32 m0, %0\n\ts_nop 0\n\tbuffer_load_dwordx4 %1, %5, %6 offen lds\n\tbuffer_load_dwordx4 %2, %5, %6 offen offset:1024 lds\n\t"
               "buffer_load_dwordx4 %3, %5, %6 offen offset:2048 lds\n\tbuffer_load_dwordx4 %4, %5, %6 offen offset:3072 lds"
               :: "s"(lds), "v"(v0), "v"(v1), "v"(v2), "v"(v3), "s"(srd), "s"(soff) : "memory");
}
DI void dma4_nt(unsigned lds, unsigned v0, unsigned v1, unsigned v2, unsigned v3, srd_t srd, unsigned soff) {
  asm volatile("s_mov_b32 m0, %0\n\ts_nop 0\n\tbuffer_load_dwordx4 %1, %5, %6 offen nt lds\n\tbuffer_load_dwordx4 %2, %5, %6 offen offset:1024 nt lds\n\t"
               "buffer_load_dwordx4 %3, %5, %6 offen offset:2048 nt lds\n\tbuffer_load_dwordx4 %4, %5, %6 offen offset:3072 nt lds"
               :: "s"(lds), "v"(v0), "v"(v1), "v"(v2), "v"(v3), "s"(srd), "s"(soff) : "memory");
}
DI void bstore_sc1(const u32x4& d, unsigned voff, srd_t srd, unsigned soff) { asm volatile("buffer_store_dwordx4 %0, %1, %2, %3 offen sc1\n\ts_nop 1" :: "v"(d), "v"(voff), "s"(srd), "s"(soff) : "memory"); }
DI void bload_sc1(bf16x8& d, unsigned voff, srd_t srd, unsigned soff) { asm volatile("buffer_load_dwordx4 %0, %1, %2, %3 offen sc1" : "=v"(d) : "v"(voff), "s"(srd), "s"(soff) : "memory"); }
DI void breload_sc1(bf16x8& d, unsigned voff, srd_t srd, unsigned soff) { asm volatile("buffer_load_dwordx4 %0, %1, %2, %3 offen sc1" : "+v"(d) : "v"(voff), "s"(srd), "s"(soff) : "memory"); }
DI void pin(bf16x8& a) { asm volatile("" : "+v"(a)); }
DI void pin16(f32x16& a) { asm volatile("" : "+v"(a)); }
DI void pinf(float& a) { asm volatile("" : "+v"(a)); }
DI void pinu(unsigned& a) { asm volatile("" : "+v"(a)); }
template <int N> DI void wait_vm1(bf16x8& a) { asm volatile("s_waitcnt vmcnt(%1)" : "+v"(a) : "n"(N) : "memory"); }
template <int N> DI void wait_vm() { asm volatile("s_waitcnt vmcnt(%0)" :: "n"(N) : "memory"); }
template <int N> DI void wait_lgkm() { __builtin_amdgcn_s_waitcnt(0xC07F | (N << 8)); SBAR(); }
DI void bar_if(unsigned c) { asm volatile("s_cmp_eq_u32 %0, 0\n\ts_cbranch_scc1 1f\n\ts_waitcnt lgkmcnt(0)\n\ts_barrier\n1:" :: "s"(c) : "memory", "scc"); }
DI void bar() { asm volatile("s_waitcnt lgkmcnt(0)\n\ts_barrier" ::: "memory"); }
typedef short v4i16_t __attribute__((ext_vector_type(4)));
DI s16x4 trd(lds_cptr p) { return __builtin_amdgcn_ds_read_tr16_b64_v4i16((__attribute__((address_space(3))) v4i16_t*)p); }
DI bf16x8 trd2(lds_cptr p, int o0, int o1) { s16x4 a = trd(p + o0), b = trd(p + o1); return __builtin_shufflevector(a, b, 0, 1, 2, 3, 4, 5, 6, 7); }
DI bf16x8 rd128(lds_cptr p) { return *(const __attribute__((address_space(3))) bf16x8*)p; }
DI float swap_max(float m) { auto rr = __builtin_amdgcn_permlane32_swap(__float_as_uint(m), __float_as_uint(m), false, false); return fmaxf(__uint_as_float(rr[0]), __uint_as_float(rr[1])); }
DI float swap_add(float m) { auto rr = __builtin_amdgcn_permlane32_swap(__float_as_uint(m), __float_as_uint(m), false, false); return __uint_as_float(rr[0]) + __uint_as_float(rr[1]); }
DI float max16(const f32x16& s) { float a = fmaxf(fmaxf(s[0], s[1]), s[2]), b = fmaxf(fmaxf(s[3], s[4]), s[5]);
  a = fmaxf(fmaxf(a, s[6]), s[7]); b = fmaxf(fmaxf(b, s[8]), s[9]); a = fmaxf(fmaxf(a, s[10]), s[11]); b = fmaxf(fmaxf(b, s[12]), s[13]); a = fmaxf(fmaxf(a, s[14]), s[15]); return fmaxf(a, b); }
DI u32x4 pack8(const f32x16& a, int s, float f) { return u32x4{cvtpk(a[8 * s] * f, a[8 * s + 1] * f), cvtpk(a[8 * s + 2] * f, a[8 * s + 3] * f), cvtpk(a[8 * s + 4] * f, a[8 * s + 5] * f), cvtpk(a[8 * s + 6] * f, a[8 * s + 7] * f)}; }
DI u32x4 pack8(const f32x16& a, int s) { return u32x4{cvtpk(a[8 * s], a[8 * s + 1]), cvtpk(a[8 * s + 2], a[8 * s + 3]), cvtpk(a[8 * s + 4], a[8 * s + 5]), cvtpk(a[8 * s + 6], a[8 * s + 7])}; }

DI float expo(const f32x16& s, u32x4 (&p)[2]) {
  float e[16]; float s0 = 0.f, s1 = 0.f;
#pragma unroll
  for (int i = 0; i < 16; ++i) e[i] = __builtin_amdgcn_exp2f(s[i]);
#pragma unroll
  for (int i = 0; i < 16; i += 2) { s0 += e[i]; s1 += e[i + 1]; }
#pragma unroll
  for (int i = 0; i < 8; ++i) p[i >> 2][i & 3] = cvtpk(e[2 * i], e[2 * i + 1]);
  return s0 + s1;
}

constexpr unsigned SLOTK = 0x04204209u;
constexpr unsigned SLOTV = 0x31531531u;
constexpr unsigned SLOTW = 0x05429876u;
constexpr unsigned STG = 45056u, STG_W = 28672u;
constexpr unsigned O_BYTES = NB * SQ * NH * DH * 2;
constexpr unsigned X_OFF = O_BYTES, X_BYTES = 63u * 8u * 4u * 4096u;
constexpr unsigned FLAG_OFF = X_OFF + X_BYTES, FLAG_STRIDE = 128, FLAG2_OFF = FLAG_OFF + 256 * FLAG_STRIDE, EPOCH_OFF = FLAG2_OFF + 63 * 32 * FLAG_STRIDE;
constexpr unsigned SPIN_CAP = 1u << 20;
constexpr int RA = 7, WO = 6, A0BAR = 6, PSLEEP = 4, PDELAY = 20, PDELAY2 = 25, RAMP0 = 3, RAMP1 = 8;
constexpr unsigned SPARE = 3u * STG;
constexpr unsigned CODE_BYTES = 19200u;
constexpr int lds_ops(int a, int b, bool last) { int n = 0; for (int i = a; i < b; ++i) n += i >= 16 ? (last ? 0 : 1) : (i >= 8 ? 2 : 1); return n; }
constexpr int pv_order(int i) { return i < 4 ? i : i < 8 ? i + 4 : i < 12 ? i - 4 : i; }
DI unsigned long long mix64(unsigned long long z) { z += 0x9E3779B97F4A7C15ull; z = (z ^ (z >> 30)) * 0xBF58476D1CE4E5B9ull; z = (z ^ (z >> 27)) * 0x94D049BB133111EBull; return z ^ (z >> 31); }
DI unsigned long long rfl64(unsigned long long v) { return ((unsigned long long)rfl((unsigned)(v >> 32)) << 32) | rfl((unsigned)v); }
DI unsigned long long ld_agent(const char* p) { return __hip_atomic_load((gu64*)p, __ATOMIC_RELAXED, __HIP_MEMORY_SCOPE_AGENT); }
DI void st_agent(char* p, unsigned long long v) { __hip_atomic_store((gu64*)p, v, __ATOMIC_RELAXED, __HIP_MEMORY_SCOPE_AGENT); }
DI void poll_flags(const char* p, unsigned long long tag) {
  _Pragma("nounroll") for (unsigned spins = 0; spins < SPIN_CAP; ++spins) { if (__all(ld_agent(p) == tag)) break; __builtin_amdgcn_s_sleep(PSLEEP); }
  asm volatile("" ::: "memory");
}

__global__ __launch_bounds__(512, 2) void mha_kernel(const unsigned short* __restrict__ x, const unsigned short* __restrict__ kc, const unsigned short* __restrict__ vc,
                                                     const unsigned short* __restrict__ wq, const unsigned short* __restrict__ wo, char* ws, unsigned short* __restrict__ out) {
  extern __shared__ __attribute__((aligned(16))) char lds[];
  unsigned long long pc0; asm volatile("s_getpc_b64 %0" : "=s"(pc0));
  const unsigned tid = threadIdx.x, lane = tid & 63u, r = lane & 31u, hh = lane >> 5;
  const unsigned wid = rfl(tid >> 6);
  const unsigned bid = blockIdx.x, xcd = bid & 7u, ix = bid >> 3;
  const unsigned b = (xcd & 1u) * 4u + (ix & 3u), h = (xcd >> 1) * 8u + (ix >> 2);
  const unsigned lds0 = (unsigned)(uintptr_t)lds;
  {
    const char* cp = (const char*)(pc0 & ~255ull) + min((ix * 8u + wid) * 128u, CODE_BYTES);
    asm volatile("s_mov_b32 m0, %0\n\ts_nop 0\n\tglobal_load_lds_dword %1, %2" :: "s"(lds0 + SPARE), "v"(0u), "s"(cp) : "memory"); }
  const srd_t sW = mk_srd((const char*)wq - 1024, DM * NH * DH * 2 + 1024);
  const srd_t sX = mk_srd(x + (size_t)b * SQ * DM, SQ * DM * 2);
  const unsigned prow = (lane >> 2) & 7u, pch = 4u * hh + (lane & 3u);
  const unsigned pchx0 = 4u * hh + ((lane & 3u) ^ ((lane >> 4) & 1u)), pchx1 = pchx0 ^ 2u;
  const unsigned vW = prow * 8192u + pch * 16u, vX0 = prow * (DM * 2u) + pchx0 * 16u, vX1 = prow * (DM * 2u) + pchx1 * 16u;
  unsigned long long ep; asm volatile("global_load_dwordx2 %0, %1, %2 sc1" : "=v"(ep) : "v"(0u), "s"(ws + EPOCH_OFF) : "memory");
  dma4(lds0 + (wid < 7u ? wid * 4096u : SPARE), vX0, vX1 + 8192u, vX0 + 16384u, vX1 + 24576u, sX, min(wid, 6u) * 32u * DM * 2u);
  dma4(lds0 + (wid >> 2) * STG + STG_W + (wid & 3u) * 4096u, vW + 1024u, vW + 128u, vW + 65536u - 1024u, vW + 65664u - 2048u, sW, (wid >> 2) * 64u * 8192u + h * 256u + (wid & 3u) * 131072u);
  const srd_t sK = mk_srd((const char*)(kc + (size_t)(b * NH + h) * SK * DH) - 1024, SK * DH * 2 + 1024);
  const srd_t sV = mk_srd((const char*)(vc + (size_t)(b * NH + h) * SK * DH) - 1024, SK * DH * 2 + 1024);
  const unsigned vK0 = prow * 256u + pchx0 * 16u, vK1 = prow * 256u + pchx1 * 16u;
  const unsigned q4 = (lane & 15u) >> 2, p4 = lane & 3u, blk = (lane >> 4) & 1u;
  const unsigned trcol = 32u * blk + 16u * (p4 & 1u) + 8u * (p4 >> 1);
  const srd_t sO = mk_srd(ws, O_BYTES);
  unsigned long long tag;
  const unsigned g4 = xcd >> 1, u0 = (xcd & 1u) * 32u + ix;
  const bool has_tile = u0 < 63u;
  const unsigned u = has_tile ? u0 : 62u, rt = u / 9u, ct = u - rt * 9u;

  if (wid == 7u) {
    auto fillQ = [&](unsigned c, unsigned stage) {
      const unsigned lw = lds0 + stage + STG_W, sw = c * 64u * 8192u + h * 256u, lx = lds0 + stage, sx = c * 128u;
#pragma unroll
      for (int i = 0; i < 4; ++i) dma4(lw + i * 4096u, vW + 1024u, vW + 128u, vW + 65536u - 1024u, vW + 65664u - 2048u, sW, sw + i * 131072u);
#pragma unroll
      for (int i = 0; i < 7; ++i) dma4(lx + i * 4096u, vX0, vX1 + 8192u, vX0 + 16384u, vX1 + 24576u, sX, sx + i * 32u * DM * 2u); };
    bar();
    { unsigned stage = 2 * STG;
#pragma nounroll
      for (unsigned c = 2; c < 9; ++c) { fillQ(c, stage); stage = stage == 2 * STG ? 0u : stage + STG;
        wait_vm<44>(); bar(); } }
    const unsigned vV = prow * 256u + pch * 16u;
    const srd_t sWo = mk_srd(wo, NH * DH * DM * 2);
    const unsigned vWo = prow * (DM * 2u) + pch * 16u;
    { unsigned j = 1, jw = 0;
#pragma nounroll
      for (unsigned n = 0; n < 10; ++n) {
        if (n == 9u) { wait_vm<0>(); bar(); }
        const unsigned je = ((unsigned)(0xFFFECA8642ull >> (4u * n)) & 15u) + 1u;
#pragma nounroll
        for (; j < je; ++j) {
          if ((j & 1u) && j != 15u) { const unsigned t = (j + 1u) >> 1, ld = lds0 + ((SLOTK >> (4u * t)) & 15u) * 16384u;
#pragma unroll
            for (int i = 0; i < 4; ++i) dma4_nt(ld + i * 4096u, vK0 + 1024u, vK0 + 128u, vK1 + 1024u, vK1 + 128u, sK, t * 16384u + i * 4096u);
          } else { const unsigned t = (j - 1u) >> 1, ld = lds0 + ((SLOTV >> (4u * t)) & 15u) * 16384u;
#pragma unroll
            for (int i = 0; i < 4; ++i) dma4_nt(ld + i * 4096u, vV + 1024u, vV + 128u, vV + 1024u, vV + 128u, sV, t * 16384u + i * 4096u); } }
        const unsigned we = (unsigned)(0x8741000000ull >> (4u * n)) & 15u;
#pragma nounroll
        for (; jw < we; ++jw) { const unsigned ld = lds0 + ((SLOTW >> (4u * jw)) & 15u) * 16384u, so = (g4 * 8u + jw) * 128u * (DM * 2u) + ct * 128u;
#pragma unroll
          for (int k = 0; k < 4; ++k) dma4(ld + k * 4096u, vWo, vWo + 8192u, vWo + 16384u, vWo + 24576u, sWo, so + k * 32u * (DM * 2u)); }
        if (n == 9u) break;
        if (n < 6u) wait_vm<32>(); else if (n == 6u) wait_vm<48>(); else wait_vm<62>();
        bar(); } }
    asm volatile("" : "+v"(ep)); tag = mix64(rfl64(ep));
  } else {
  const unsigned rsw0 = 64u * (r & 7u) + 16u * (hh ^ ((r >> 2) & 3u)), rsw1 = rsw0 ^ 32u;
  f32x16 qa[4] = {}, o[4] = {};
  asm volatile("" : "+v"(qa[0]), "+v"(qa[1]), "+v"(qa[2]), "+v"(qa[3]), "+v"(o[0]), "+v"(o[1]), "+v"(o[2]), "+v"(o[3]));
  wait_lgkm<0>();
  wait_vm<4>(); if (wid < 4u) wait_vm<0>();

  dma4(lds0 + STG + wid * 4096u, vX0, vX1 + 8192u, vX0 + 16384u, vX1 + 24576u, sX, 128u + wid * 32u * DM * 2u);
  bar();
  asm volatile("" : "+v"(ep)); ep = rfl64(ep);

  const lds_cptr kb0 = (lds_cptr)lds + 2048u * (r >> 3) + rsw0, kb1 = (lds_cptr)lds + 2048u * (r >> 3) + rsw1;
  const lds_cptr vb = (lds_cptr)lds + 64u * (4u * hh + q4) + trcol;
  bf16x8 qf[8], pre[RA];
  f32x16 negm, S0, S1; u32x4 P0[2], P1[2]; float l = 0.f;
  auto rdK = [&](lds_cptr k0, lds_cptr k1, int kb, int i) { return rd128(((i & 1) ? k1 : k0) + 8192 * kb + 512 * (i >> 1)); };
  auto rdV = [&](lds_cptr v, int kb, int j) { return trd2(v, 8192 * kb + 4096 * (j >> 2) + 512 * (j & 3), 8192 * kb + 4096 * (j >> 2) + 2048 + 512 * (j & 3)); };
  auto sm_fix = [&](float ps, f32x16& Sc, f32x16& Sn, u32x4 (&P)[2]) {
    if (__builtin_expect(__any(!(ps <= SUM_THR)), 0)) {
      const float dl = fmaxf(swap_max(max16(Sc)), 0.f), f = __builtin_amdgcn_exp2f(-dl);
#pragma unroll
      for (int i = 0; i < 16; ++i) { Sc[i] -= dl; Sn[i] -= dl; negm[i] -= dl; }
#pragma unroll
      for (int db = 0; db < 4; ++db)
#pragma unroll
        for (int i = 0; i < 16; ++i) o[db][i] *= f;
      l *= f; ps = expo(Sc, P); }
    l += ps; };
  { lds_cptr wp = (lds_cptr)lds + STG_W + 2048u * hh + 64u * q4 + trcol;
    lds_cptr xp0 = (lds_cptr)lds + wid * 4096u + 1024u * (r >> 3) + rsw0, xp1 = (lds_cptr)lds + wid * 4096u + 1024u * (r >> 3) + rsw1;
    bf16x8 xf[4], af[5];
    auto rdA = [&](int i) { return trd2(wp, 4096 * (i >> 2) + 512 * (i & 3), 4096 * (i >> 2) + 512 * (i & 3) + 256); };
    xf[0] = rd128(xp0); af[0] = rdA(0); af[1] = rdA(1); af[2] = rdA(2); af[3] = rdA(3);
    unsigned stage = 0, side = wid == 3u ? 0x78u : 0u; asm volatile("" : "+s"(side));
    for (unsigned i = 0; i < ((wid + ix) & 3u); ++i) __builtin_amdgcn_s_sleep(RAMP0);
#pragma nounroll
    for (unsigned c = 0; c < 8; ++c) {
      if (__builtin_expect((side >> c) & 1u, 0)) {
        dma4_nt(lds0 + 9u * 16384u + (c - 3u) * 4096u, vK0 + 1024u, vK0 + 128u, vK1 + 1024u, vK1 + 128u, sK, (c - 3u) * 4096u);
        if (c == 3u) {
          unsigned ln = lane; asm volatile("" : "+v"(ln));
          const unsigned k = ln & 7u, sel = ln >> 3;
          const char* wup = sel == 0u ? (const char*)vc + (size_t)(b * NH + h) * (SK * DH * 2) + k * 16384u
                          : sel == 1u ? (const char*)wo + (size_t)(g4 * 1024u + k * 128u) * (DM * 2u) + ct * 128u
                          : sel == 2u ? (const char*)out + (size_t)((8u * rt + k) * 32u) * (DM * 2u)
                          : ws + (sel == 3u ? ((b * 7u + k) * NH + h) * 8192u : sel == 4u ? ((8u * rt + k) * NH + g4 * 8u) * 8192u : sel == 5u ? X_OFF + u * 131072u + k * 16384u : FLAG_OFF + k * 32768u + 64u);
          asm volatile("s_mov_b32 m0, %0\n\ts_nop 0\n\tglobal_load_lds_dword %1, off sc1" :: "s"(lds0 + SPARE), "v"(wup) : "memory"); } }
      asm volatile("s_bitcmp0_b32 %0, %1\n\ts_cbranch_scc1 1f\n\ts_sleep %2\n1:" :: "s"(0x0Eu), "s"(c), "n"(RAMP1) : "scc");
      const unsigned nstage = stage == 2 * STG ? 0u : stage + STG; const int dlt = (int)nstage - (int)stage; stage = nstage;
      SBAR();
      static_for<12>([&](auto I) { constexpr int i = I.value;
        qa[i & 3] = MFMA(af[i % 5], xf[i >> 2], qa[i & 3]);
        af[(i + 4) % 5] = rdA(i + 4);
        if constexpr (i == 0) xf[1] = rd128(xp1); if constexpr (i == 3) xf[2] = rd128(xp0 + 512); if constexpr (i == 7) xf[3] = rd128(xp1 + 512);
        SBAR(); });
      qa[0] = MFMA(af[12 % 5], xf[3], qa[0]);
      wp += dlt; xp0 += dlt; xp1 += dlt; asm volatile("" : "+v"(wp), "+v"(xp0), "+v"(xp1));
      SBAR();
      asm volatile("s_bitcmp1_b32 %0, %1\n\ts_cbranch_scc1 1f\n\ts_waitcnt vmcnt(0)\n1:\n\ts_waitcnt lgkmcnt(0)\n\ts_barrier" :: "s"(side), "s"(c) : "memory", "scc");
      const bf16x8 xl = xf[3], a13 = af[13 % 5], a14 = af[14 % 5], a15 = af[15 % 5];
      xf[0] = rd128(xp0); af[0] = rdA(0); af[1] = rdA(1);
      SBAR();
      qa[1] = MFMA(a13, xl, qa[1]); SBAR();
      af[2] = rdA(2);
      qa[2] = MFMA(a14, xl, qa[2]); SBAR();
      af[3] = rdA(3);
      qa[3] = MFMA(a15, xl, qa[3]); SBAR();
    }
    lds_cptr k0 = kb0 + 9 * 16384, k1 = kb1 + 9 * 16384; asm volatile("" : "+v"(k0), "+v"(k1));
    bf16x8 ag[16], f[16]; u32x4 qw[8]; float qs = QSCALE; asm volatile("" : "+s"(qs));
#pragma unroll
    for (int i = 0; i < 4; ++i) ag[i] = af[i];
    auto cvq = [&](int j) { float lo = qa[j >> 3][2 * (j & 7)] * qs; pinf(lo); float hi = qa[j >> 3][2 * (j & 7) + 1] * qs; unsigned w = cvtpk(lo, hi); pinu(w); qw[j >> 2][j & 3] = w; };
    SBAR();
    static_for<24>([&](auto G) { constexpr int g = G.value, n = g + 4;
      if constexpr (g < 4) qa[g] = MFMA(ag[g], xf[0], qa[g]);
      else if constexpr (g < 16) qa[(g - 4) / 3] = MFMA(ag[g], xf[1 + (g - 4) % 3], qa[(g - 4) / 3]);
      else { qf[g - 16] = __builtin_bit_cast(bf16x8, qw[g - 16]);
        if constexpr (g == 16) { const f32x16 z = {}; S0 = MFMA(f[0], qf[0], z); } else S0 = MFMA(f[g - 16], qf[g - 16], S0); pin16(S0); }
      if constexpr (g == 0) { xf[1] = rd128(xp1); xf[2] = rd128(xp0 + 512); } if constexpr (g == 1) xf[3] = rd128(xp1 + 512);
      if constexpr (n < 16) ag[n] = rdA(4 * (1 + (n - 4) % 3) + (n - 4) / 3); else f[n - 16] = rdK(k0, k1, (n - 16) >> 3, (n - 16) & 7);
      if constexpr (g >= 9 && g < 21) { constexpr int j0 = (g - 9) * 8 / 3, j1 = (g - 8) * 8 / 3; static_for<j1 - j0>([&](auto J) { cvq(j0 + J.value); }); }
      SBAR(); });

#pragma unroll
    for (int i = 12; i < 16; ++i) f[i] = rdK(k0, k1, 1, i & 7);
    const float m = swap_max(max16(S0));
#pragma unroll
    for (int i = 0; i < 16; ++i) negm[i] = -m;
    SBAR();
    float e[16]; float s0 = 0.f, s1 = 0.f;
    static_for<9>([&](auto G) { constexpr int g = G.value;
      if constexpr (g == 0) { if (wid >= 4u) bar(); }
      if constexpr (g == A0BAR) { if (wid < 4u) bar();
        constexpr unsigned ko = ((SLOTK >> 4) & 15u) * 16384u;
#pragma unroll
        for (int i = 0; i < RA; ++i) pre[i] = rdK(kb0 + ko, kb1 + ko, 0, i);
        SBAR(); }
      if constexpr (g == 0) S1 = MFMA(f[8], qf[0], negm); else if constexpr (g < 8) S1 = MFMA(f[8 + g], qf[g], S1);
      if constexpr (g < 8) pin16(S1);
      if constexpr (g < 8) { e[2 * g] = __builtin_amdgcn_exp2f(S0[2 * g] - m); e[2 * g + 1] = __builtin_amdgcn_exp2f(S0[2 * g + 1] - m); pinf(e[2 * g]); pinf(e[2 * g + 1]); }
      if constexpr (g == 1) { s0 = e[0]; s1 = e[1]; } else if constexpr (g > 1) { s0 += e[2 * g - 2]; pinf(s0); s1 += e[2 * g - 1]; pinf(s1); }
      if constexpr (g >= 1) { unsigned w = cvtpk(e[2 * g - 2], e[2 * g - 1]); pinu(w); P0[(g - 1) >> 2][(g - 1) & 3] = w; }
      SBAR(); });
    l = s0 + s1; }

  auto unit = [&](auto KB, auto LAST, lds_cptr k0, lds_cptr k1, lds_cptr v, lds_cptr nk0, lds_cptr nk1, f32x16& Sn, f32x16& Sc, const u32x4 (&Pp)[2], u32x4 (&Pc)[2]) {
    constexpr int kb = KB.value; constexpr bool last = LAST.value;
    float e[16]; float s0 = 0.f, s1 = 0.f; bf16x8 f[16 + RA];
#pragma unroll
    for (int i = 0; i < RA; ++i) f[i] = pre[i];
    static_for<16>([&](auto G) { constexpr int g = G.value; constexpr int n = g + RA; constexpr int gb = 12;
      if constexpr (last && g == gb) { bar(); wait_lgkm<0>(); }
      else if constexpr ((g & 3) == 0) wait_lgkm<lds_ops(g + 4, g + RA, last)>();
      if constexpr (g == 0) Sn = MFMA(f[0], qf[0], negm); else if constexpr (g < 8) Sn = MFMA(f[g], qf[g], Sn);
      else o[g & 3] = MFMA(f[g], __builtin_bit_cast(bf16x8, Pp[(g >> 2) & 1]), o[g & 3]);
      if constexpr (n < 8) f[n] = rdK(k0, k1, kb, n); else if constexpr (n < 16) f[n] = rdV(v, kb, n - 8);
      else if constexpr (!last) f[n] = rdK(k0, k1, kb + 1, n - 16);
      else if constexpr (g >= gb) { constexpr int per = (RA + 15 - gb) / (16 - gb); static_for<per>([&](auto J) { constexpr int i = per * (g - gb) + J.value; if constexpr (i < RA) f[16 + i] = rdK(nk0, nk1, 0, i); }); }
      if constexpr (g == 0) { e[0] = __builtin_amdgcn_exp2f(Sc[0]); pinf(e[0]); }
      if constexpr (g < 15) { e[g + 1] = __builtin_amdgcn_exp2f(Sc[g + 1]); pinf(e[g + 1]); }
      if constexpr (g == 1) { s0 = e[0]; s1 = e[1]; } else if constexpr (g > 1) { if constexpr (g & 1) { s1 += e[g]; pinf(s1); } else { s0 += e[g]; pinf(s0); } }
      if constexpr (g & 1) { unsigned w = cvtpk(e[g - 1], e[g]); pinu(w); Pc[g >> 3][(g >> 1) & 3] = w; }
      SBAR(); });
#pragma unroll
    for (int i = 0; i < RA; ++i) pre[i] = f[16 + i];
    sm_fix(s0 + s1, Sc, Sn, Pc); };
#pragma nounroll
  for (unsigned t = 1; t < 7; ++t) {
    const unsigned ko = ((SLOTK >> (4u * t)) & 15u) * 16384u, vo = ((SLOTV >> (4u * (t - 1u))) & 15u) * 16384u, nko = ((SLOTK >> (4u * ((t + 1u) & 7u))) & 15u) * 16384u;
    SBAR();
    unit(std::integral_constant<int, 0>{}, std::false_type{}, kb0 + ko, kb1 + ko, vb + vo, kb0, kb1, S0, S1, P0, P1);
    unit(std::integral_constant<int, 1>{}, std::true_type{}, kb0 + ko, kb1 + ko, vb + vo, kb0 + nko, kb1 + nko, S1, S0, P1, P0);
  }
  const unsigned me = (b * 7u + wid) * NH + h;
  {
    constexpr unsigned ko = ((SLOTK >> 28) & 15u) * 16384u;
    const lds_cptr k0 = kb0 + ko, k1 = kb1 + ko, v6 = vb + ((SLOTV >> 24) & 15u) * 16384u, v7 = vb + ((SLOTV >> 28) & 15u) * 16384u;
    u32x4 P2[2], P3[2];
    auto scaleP = [&](u32x4 (&P)[2], float f) {
#pragma unroll
      for (int i = 0; i < 8; ++i) { const unsigned w = P[i >> 2][i & 3]; P[i >> 2][i & 3] = cvtpk(__uint_as_float(w << 16) * f, __uint_as_float(w & 0xFFFF0000u) * f); } };
    auto fix = [&](auto NP, float ps, f32x16& Sc, f32x16& Sn, u32x4 (&P)[2]) {
      if (__builtin_expect(__any(!(ps <= SUM_THR)), 0)) {
        const float f = __builtin_amdgcn_exp2f(-fmaxf(swap_max(max16(Sc)), 0.f));
        scaleP(P0, f); if constexpr (NP.value > 1) scaleP(P1, f); if constexpr (NP.value > 2) scaleP(P2, f); }
      sm_fix(ps, Sc, Sn, P); };
    auto rdVt = [&](int i) { const int j = pv_order(i), db = j >> 3, un = (j >> 1) & 3, ks = j & 1; return rdV(un < 2 ? v6 : v7, un & 1, 4 * ks + db); };
    auto pvt = [&](auto I, const bf16x8& vf) { constexpr int j = pv_order(I.value), un = (j >> 1) & 3;
      const u32x4 pb = un == 0 ? P0[j & 1] : un == 1 ? P1[j & 1] : un == 2 ? P2[j & 1] : P3[j & 1];
      o[j >> 3] = MFMA(vf, __builtin_bit_cast(bf16x8, pb), o[j >> 3]); pin16(o[j >> 3]); };
    bf16x8 f[16], w[32];
#pragma unroll
    for (int i = 0; i < RA; ++i) f[i] = pre[i];
    auto qsm = [&](auto H, f32x16& Sn, const f32x16& Sc, u32x4 (&Pc)[2]) { constexpr int hf = H.value;
      float e[16]; float s0 = 0.f, s1 = 0.f;
      static_for<9>([&](auto G) { constexpr int g = G.value, n = 8 * hf + g + RA;
        if constexpr (hf < 2 && g < 8) { if constexpr (g == 0) Sn = MFMA(f[8 * hf], qf[0], negm); else Sn = MFMA(f[8 * hf + g], qf[g], Sn);
          pin16(Sn);
          if constexpr (n < 16) f[n] = rdK(k0, k1, n >> 3, n & 7); }
        if constexpr (hf == 1 && g >= 2 && g < 8) w[g - 2] = rdVt(g - 2);
        if constexpr (hf == 2 && g < 8) { pvt(G, w[g]); w[g + 6] = rdVt(g + 6); }
        if constexpr (g < 8) { e[2 * g] = __builtin_amdgcn_exp2f(Sc[2 * g]); e[2 * g + 1] = __builtin_amdgcn_exp2f(Sc[2 * g + 1]); pinf(e[2 * g]); pinf(e[2 * g + 1]); }
        if constexpr (g == 1) { s0 = e[0]; s1 = e[1]; } else if constexpr (g > 1) { s0 += e[2 * g - 2]; pinf(s0); s1 += e[2 * g - 1]; pinf(s1); }
        if constexpr (g >= 1) { unsigned pw = cvtpk(e[2 * g - 2], e[2 * g - 1]); pinu(pw); Pc[(g - 1) >> 2][(g - 1) & 3] = pw; }
        SBAR(); });
      return s0 + s1; };
    if (wid < 4u) __builtin_amdgcn_s_setprio(1);
    SBAR();
    fix(std::integral_constant<int, 1>{}, qsm(std::integral_constant<int, 0>{}, S0, S1, P1), S1, S0, P1);
    bar_if(wid >> 2);
    fix(std::integral_constant<int, 2>{}, qsm(std::integral_constant<int, 1>{}, S1, S0, P2), S0, S1, P2);
    bar_if((wid >> 2) ^ 1u);

    fix(std::integral_constant<int, 3>{}, qsm(std::integral_constant<int, 2>{}, S0, S1, P3), S1, S0, P3);
    const float inv = __builtin_amdgcn_rcpf(swap_add(l));
    auto put = [&](int k8) { bstore_sc1(pack8(o[k8 >> 1], k8 & 1, inv), lane * 16u, sO, me * 8192u + k8 * 1024u); };
    static_for<24>([&](auto G) { constexpr int g = G.value + 8;
      pvt(std::integral_constant<int, g>{}, w[g]);
      if constexpr (g + 6 < 32) w[g + 6] = rdVt(g + 6);
      if constexpr (g == 14) put(0); if constexpr (g == 16) put(1); if constexpr (g == 19) put(2); if constexpr (g == 22) put(3); if constexpr (g == 26) put(4); if constexpr (g == 29) put(5);
      SBAR(); });

    put(6); put(7); }

  tag = mix64(ep);
  __builtin_amdgcn_s_setprio(0);
  }
  if (wid < 7u) { wait_vm<0>(); bar(); }
  if (tid == 0) st_agent(ws + FLAG_OFF + (b * NH + h) * FLAG_STRIDE, tag);

  if (!has_tile) {
    if (bid == 249u && wid == 0u) {
#pragma nounroll
      for (unsigned i = 0; i < 4; ++i) poll_flags(ws + FLAG_OFF + (i * 64u + lane) * FLAG_STRIDE, tag);
      if (lane == 0) st_agent(ws + EPOCH_OFF, tag); }
    return; }
  const unsigned jq = (2u * g4 + 2u + wid) & 7u, G = 8u * rt + jq;
  const unsigned voffO = lane * 16u, soO = (G * NH + g4 * 8u) * 8192u;
  const char* fp = ws + FLAG_OFF + (((G * 37u) >> 8) * NH + g4 * 8u + (lane & 7u)) * FLAG_STRIDE;
  unsigned ready = 0;
  auto need = [&](unsigned m) {
    _Pragma("nounroll") for (unsigned spins = 0; (ready & m) != m && spins < SPIN_CAP; ++spins) { ready = (unsigned)__ballot(ld_agent(fp) == tag); if ((ready & m) != m) __builtin_amdgcn_s_sleep(PSLEEP); }
    asm volatile("" ::: "memory"); };
  __builtin_amdgcn_s_sleep(PDELAY);
  need(1u);

  const lds_cptr wb = (lds_cptr)lds + 1024u * hh + 64u * q4 + trcol;
  f32x16 acc[2] = {};
  { unsigned hd = 0, so_c = soO + hd * 8192u; lds_cptr wp = wb + ((SLOTW >> (4u * hd)) & 15u) * 16384u;
    bf16x8 of[8], lf[4][2];
    auto rdW = [&](int s2) { lf[s2 & 3][0] = trd2(wp, (s2 & 7) * 2048, (s2 & 7) * 2048 + 256); lf[s2 & 3][1] = trd2(wp, (s2 & 7) * 2048 + 512, (s2 & 7) * 2048 + 768); };
#pragma unroll
    for (int i = 0; i < 8; ++i) { if (i < WO) bload_sc1(of[i], voffO, sO, so_c + i * 1024u); else of[i] = bf16x8{}; }
    rdW(0); rdW(1);
#pragma nounroll
    for (unsigned p = 0; p < 8; ++p) {
      hd = (hd + 1u) & 7u;
      if (p == 5u) __builtin_amdgcn_s_barrier();
      const unsigned so_n = p < 7u ? soO + hd * 8192u : O_BYTES;
      static_for<8>([&](auto S) { constexpr int s2 = S.value;
        if constexpr (s2 == 8 - WO) need(1u << hd);
        if constexpr (s2 == 6) wp = wb + ((SLOTW >> (4u * hd)) & 15u) * 16384u;
        wait_vm1<WO - 1>(of[s2]);
        wait_lgkm<4>();
        acc[0] = MFMA(lf[s2 & 3][0], of[s2], acc[0]);
        rdW(s2 + 2);
        SBAR();
        acc[1] = MFMA(lf[s2 & 3][1], of[s2], acc[1]);
        SBAR();
        breload_sc1(of[(s2 + WO) & 7], voffO, sO, (s2 + WO < 8 ? so_c : so_n) + ((s2 + WO) & 7) * 1024u);
        SBAR(); });
      so_c = so_n;
    }
    wait_vm<0>();
#pragma unroll
    for (int i = 0; i < 8; ++i) pin(of[i]); }

  const srd_t sX2 = mk_srd(ws + X_OFF, X_BYTES);
  const unsigned xb = (u * 8u + jq) * 4u;
  if (wid < 6u) {
#pragma unroll
    for (int i = 0; i < 4; ++i) bstore_sc1(pack8(acc[i >> 1], i & 1), voffO, sX2, (xb + g4) * 4096u + i * 1024u);
    wait_vm<0>();
    if (lane == 0) st_agent(ws + FLAG2_OFF + (xb + g4) * FLAG_STRIDE, tag);
  } else {
    __builtin_amdgcn_s_sleep(PDELAY2);
    poll_flags(ws + FLAG2_OFF + (xb + ((g4 + 1u + lane % 3u) & 3u)) * FLAG_STRIDE, tag);

    bf16x8 xp[12];
#pragma unroll
    for (int i = 0; i < 12; ++i) bload_sc1(xp[i], voffO, sX2, (xb + ((g4 + 1u + (i >> 2)) & 3u)) * 4096u + (i & 3) * 1024u);
    wait_vm<0>();
#pragma unroll
    for (int j = 0; j < 12; ++j) { const int i = j & 3; pin(xp[j]); const u32x4 v = __builtin_bit_cast(u32x4, xp[j]);
#pragma unroll
      for (int e = 0; e < 4; ++e) { acc[i >> 1][8 * (i & 1) + 2 * e] += __uint_as_float(v[e] << 16); acc[i >> 1][8 * (i & 1) + 2 * e + 1] += __uint_as_float(v[e] & 0xFFFF0000u); } }

    char* op = (char*)out + (size_t)(G * 32u + r) * (DM * 2u) + ct * 128u + hh * 16u;
#pragma unroll
    for (int i = 0; i < 4; ++i) *(u32x4*)(op + i * 32) = pack8(acc[i >> 1], i & 1);
  }
}

extern "C" void kernel_launch(void* const* d_in, const int* in_sizes, int n_in, void* d_out, int out_size, void* d_ws, size_t ws_size, hipStream_t stream) {
  (void)in_sizes; (void)n_in; (void)out_size; (void)ws_size;
  (void)hipFuncSetAttribute((const void*)mha_kernel, hipFuncAttributeMaxDynamicSharedMemorySize, LDS_BYTES);
  const unsigned short* x = (const unsigned short*)d_in[0]; const unsigned short* kc = (const unsigned short*)d_in[1]; const unsigned short* vc = (const unsigned short*)d_in[2];
  const unsigned short* wq = (const unsigned short*)d_in[3]; const unsigned short* wo = (const unsigned short*)d_in[4];
  mha_kernel<<<256, 512, LDS_BYTES, stream>>>(x, kc, vc, wq, wo, (char*)d_ws, (unsigned short*)d_out);
}
```
